```python
import torch

def _softmax(x, axis=-1):
    return torch.softmax(x, dim=axis)

def _gelu(x):
    return torch.nn.functional.gelu(x, approximate='tanh')

def _silu(x):
    return torch.nn.functional.silu(x)

def _sigmoid(x):
    return torch.sigmoid(x)

def _layernorm(x, axis=-1, eps=1e-05):
    m = x.mean(dim=axis, keepdim=True)
    v = x.var(dim=axis, keepdim=True, unbiased=False)
    return (x - m) / torch.sqrt(v + eps)

def _scatter_add(tgt, idx, src, axis=0):
    return tgt.clone().index_add_(axis, idx, src)

def _segment_sum(src, seg, n):
    out = torch.zeros((n,) + src.shape[1:], dtype=src.dtype)
    return out.index_add_(0, seg, src)

def _pad(x, pw):
    flat = tuple((v for pair in reversed(pw) for v in pair))
    return torch.nn.functional.pad(x, flat)
kernel_name = 'adaLN_cond__memory_aug_recurrent_mem_tr__mixer_s_mlp__prompt'

def setup_inputs():

    _w = lambda a: (lambda t: t.to(torch.bfloat16) if t.is_floating_point() else t)(torch.as_tensor(a))

    def _A_setup():
        B, S, Dm = (1, 2048, 1024)
        H, Dh = (32, 128)
        M, seg = (8, 32)
        seg = min(seg, S)
        S = S // seg * seg
        return {'x': _w((lambda *s: torch.randn(*s, dtype=torch.bfloat16) * 0.05)(B, S, Dm)), 'mem0': _w((lambda *s: torch.randn(*s, dtype=torch.bfloat16) * 0.4)(B, M, Dm)), 'Wq': _w((lambda *s: torch.randn(*s, dtype=torch.bfloat16) * 3.2)(Dm, H, Dh) * Dm ** (-0.5)), 'Wk': _w((lambda *s: torch.randn(*s, dtype=torch.bfloat16) * 3.2)(Dm, H, Dh) * Dm ** (-0.5)), 'Wv': _w((lambda *s: torch.randn(*s, dtype=torch.bfloat16) * 0.4)(Dm, H, Dh) * Dm ** (-0.5)), 'Wo': _w((lambda *s: torch.randn(*s, dtype=torch.bfloat16) * 0.4)(H, Dh, Dm) * (H * Dh) ** (-0.5)), 'seg_len': seg}
    _A_in = _A_setup()

    def _B_setup():
        B, S, D, F = (1, 2048, 1024, 3584)
        return {'x': _w((lambda *s: torch.randn(*s, dtype=torch.bfloat16) * 0.05)(B, S, D)), 'w1': _w((lambda *s: torch.randn(*s, dtype=torch.bfloat16) * 3.2)(D, D) * D ** (-0.5)), 'w2': _w((lambda *s: torch.randn(*s, dtype=torch.bfloat16) * 3.2)(D, D) * D ** (-0.5)), 'w3': _w((lambda *s: torch.randn(*s, dtype=torch.bfloat16) * 3.2)(D, F) * D ** (-0.5)), 'w4': _w((lambda *s: torch.randn(*s, dtype=torch.bfloat16) * 3.2)(F, D) * F ** (-0.5))}
    _B_in = _B_setup()
    return {'A_x': _A_in['x'], 'A_mem0': _A_in['mem0'], 'A_Wq': _A_in['Wq'], 'A_Wk': _A_in['Wk'], 'A_Wv': _A_in['Wv'], 'A_Wo': _A_in['Wo'], 'A_seg_len': _A_in['seg_len'], 'B_x': _B_in['x'], 'B_w1': _B_in['w1'], 'B_w2': _B_in['w2'], 'B_w3': _B_in['w3'], 'B_w4': _B_in['w4'], 'c': _w((lambda *s: torch.randn(*s, dtype=torch.bfloat16) * 0.1)(1, 1024) * 8.0), 'w_mod': _w((lambda *s: torch.randn(*s, dtype=torch.bfloat16) * 0.1)(1024, 6 * 1024) * 1024 ** (-0.5) * 8.0)}

def reference(A_x, A_mem0, A_Wq, A_Wk, A_Wv, A_Wo, A_seg_len, B_x, B_w1, B_w2, B_w3, B_w4, c, w_mod):

    def _A_ref(x, mem0, Wq, Wk, Wv, Wo, seg_len):
        B, S, Dm = x.shape
        H, Dh = (Wq.shape[1], Wq.shape[2])
        M = mem0.shape[1]
        scale = Dh ** (-0.5)
        n_seg = S // seg_len
        L = M + seg_len
        idx = torch.arange(L)
        mask = (idx[:, None] < M) | (idx[:, None] >= idx[None, :])
        mem = mem0
        outs = []
        for s in range(n_seg):
            xs = x[:, s * seg_len:(s + 1) * seg_len, :].clone()
            h = torch.concatenate([mem, xs], axis=1)
            q = torch.einsum('btd,dhe->bhte', h, Wq)
            k = torch.einsum('btd,dhe->bhte', h, Wk)
            v = torch.einsum('btd,dhe->bhte', h, Wv)
            scores = torch.einsum('bhqe,bhke->bhqk', q, k) * scale
            scores = torch.where(mask[None, None], scores, -1000000000.0)
            p = _softmax(scores, axis=-1)
            o = torch.einsum('bhqk,bhke->bhqe', p, v)
            o = torch.einsum('bhte,hed->btd', o, Wo)
            mem = o[:, :M, :].clone()
            outs.append(o[:, M:, :])
        return torch.concatenate(outs, axis=1)

    def _B_ref(x, w1, w2, w3, w4):
        B, S, D = x.shape
        h = _gelu(_layernorm(x, axis=-1) @ w1)
        c = D // 4
        g0 = _pad(h[:, :, 0 * c:1 * c], ((0, 0), (1, 0), (0, 0)))[:, :S, :]
        g1 = _pad(h[:, :, 1 * c:2 * c], ((0, 0), (0, 1), (0, 0)))[:, 1:, :]
        g2 = _pad(h[:, :, 2 * c:3 * c], ((0, 0), (2, 0), (0, 0)))[:, :S, :]
        g3 = _pad(h[:, :, 3 * c:], ((0, 0), (0, 2), (0, 0)))[:, 2:, :]
        hs = torch.concatenate([g0, g1, g2, g3], axis=-1) @ w2
        x1 = x + hs
        h2 = _gelu(_layernorm(x1, axis=-1) @ w3) @ w4
        return x1 + h2
    x = A_x
    mod = (_silu(c) @ w_mod).reshape(c.shape[0], 6, 1, -1)
    sa, ca, ga, sb, cb, gb = (mod[:, 0].clone(), mod[:, 1].clone(), mod[:, 2].clone(), mod[:, 3].clone(), mod[:, 4].clone(), mod[:, 5].clone())
    h = _layernorm(x, axis=-1) * (1 + ca) + sa
    x = x + ga * _A_ref(h, A_mem0, A_Wq, A_Wk, A_Wv, A_Wo, A_seg_len)
    h = _layernorm(x, axis=-1) * (1 + cb) + sb
    x = x + gb * _B_ref(h, B_w1, B_w2, B_w3, B_w4)
    return x
```

```cpp
#include <hip/hip_runtime.h>
#include <hip/hip_bf16.h>
#include <cstdlib>
#pragma clang diagnostic ignored "-Winline-asm"

#define LAS __attribute__((address_space(3)))
#define GAS __attribute__((address_space(1)))
typedef unsigned short u16;
typedef __bf16 bf16x2 __attribute__((ext_vector_type(2)));
typedef __bf16 bf16x4 __attribute__((ext_vector_type(4)));
typedef __bf16 bf16x8 __attribute__((ext_vector_type(8)));
typedef __bf16 bf16x16 __attribute__((ext_vector_type(16)));
typedef short s16x4 __attribute__((ext_vector_type(4)));
typedef float f32x2 __attribute__((ext_vector_type(2)));
typedef float f32x4 __attribute__((ext_vector_type(4)));
typedef unsigned u32x2 __attribute__((ext_vector_type(2)));
typedef unsigned u32x4 __attribute__((ext_vector_type(4)));

constexpr int S_ = 2048, D_ = 1024, NH = 32, DH = 128, HD = 4096, NSEG = 64, FF = 3584;

constexpr size_t MB = 1u << 20;
constexpr size_t WS_X = 0;
constexpr size_t X_MEMP = 0;
constexpr size_t X_OX = 4 * MB;
constexpr size_t X_VM = 8 * MB;
constexpr size_t X_CTL = 12 * MB;
constexpr size_t X_BYTES = 12 * MB + 4096;
constexpr size_t WS_MOD = 15 * MB;
constexpr size_t WS_KM = 124 * MB;
constexpr size_t WS_H = 16 * MB;
constexpr size_t WS_Q = 20 * MB;
constexpr size_t WS_K = 36 * MB;
constexpr size_t WS_VT = 52 * MB;
constexpr size_t WS_O = 68 * MB;
constexpr size_t WS_X1 = 84 * MB;
constexpr size_t WS_H2 = 88 * MB;
constexpr size_t WS_G = 92 * MB;
constexpr size_t WS_H1S = 96 * MB;
constexpr size_t WS_X2 = 100 * MB;
constexpr size_t WS_L2 = 104 * MB;
constexpr size_t WS_U = 108 * MB;

__device__ __forceinline__ float bf2f(unsigned short b) { return __builtin_bit_cast(float, (unsigned)b << 16); }
__device__ __forceinline__ unsigned f2u(float f) { return __builtin_bit_cast(unsigned, f); }
__device__ __forceinline__ float u2f(unsigned u) { return __builtin_bit_cast(float, u); }
__device__ __forceinline__ float lo2f(unsigned w) { return __builtin_bit_cast(float, w << 16); }
__device__ __forceinline__ float hi2f(unsigned w) { return __builtin_bit_cast(float, w & 0xFFFF0000u); }
__device__ __forceinline__ unsigned pack2(float a, float b) { bf16x2 v; v[0] = (__bf16)a; v[1] = (__bf16)b; return __builtin_bit_cast(unsigned, v); }
__device__ __forceinline__ float rbf(float a) { return (float)(__bf16)a; }
__device__ __forceinline__ unsigned nz(unsigned w) { return w == 0u ? 0x80000000u : w; }
__device__ __forceinline__ float gelu_tanh(float x) {
  const float z = 0.7978845608028654f * (x + 0.044715f * x * x * x);
  const float e = __builtin_amdgcn_exp2f(-2.885390081777927f * z);
  return x * __builtin_amdgcn_rcpf(1.0f + e);
}
__device__ __forceinline__ float wave_sum(float v) {
#pragma unroll
  for (int o = 32; o >= 1; o >>= 1) v += __shfl_xor(v, o);
  return v;
}
#define WAIT_V(n) asm volatile("s_waitcnt vmcnt(" #n ")" ::: "memory")
#define WAIT_L0() asm volatile("s_waitcnt lgkmcnt(0)" ::: "memory")
#define BAR() do { asm volatile("" ::: "memory"); __builtin_amdgcn_s_barrier(); asm volatile("" ::: "memory"); } while (0)
#define LBAR() do { WAIT_L0(); BAR(); } while (0)
template <int N> __device__ __forceinline__ void wait_v() { asm volatile("s_waitcnt vmcnt(%0)" :: "n"(N) : "memory"); }
__device__ __forceinline__ void dma16(__amdgpu_buffer_rsrc_t rs, unsigned voff, unsigned soff, unsigned ldsa) {
  asm volatile("s_mov_b32 m0, %0\n\ts_nop 3\n\tbuffer_load_dwordx4 %1, %2, %3 offen lds" :: "s"(ldsa), "v"(voff), "s"(rs), "s"(soff) : "m0", "memory");
}
__device__ __forceinline__ __amdgpu_buffer_rsrc_t mk_rs(const void* p, unsigned bytes) { return __builtin_amdgcn_make_buffer_rsrc((void*)p, 0, (int)bytes, 0x00020000); }
#define SCHED() __builtin_amdgcn_sched_barrier(0)
template <int X = 0> __device__ __forceinline__ LAS unsigned char* lad(unsigned v, unsigned u) {
  unsigned a;
  if (X == 0) asm volatile("v_add_u32 %0, %1, %2" : "=v"(a) : "s"(u), "v"(v));
  else asm volatile("v_xor_b32 %0, %3, %2\n\tv_add_u32 %0, %1, %0" : "=&v"(a) : "s"(u), "v"(v), "n"(X));
  return (LAS unsigned char*)(size_t)a;
}
__device__ __forceinline__ bf16x4 trread(LAS unsigned char* p) { return __builtin_amdgcn_ds_read_tr16_b64_v4bf16((LAS bf16x4*)p); }
__device__ __forceinline__ bf16x8 cat8(bf16x4 a, bf16x4 b) { return __builtin_shufflevector(a, b, 0, 1, 2, 3, 4, 5, 6, 7); }
__device__ __forceinline__ bf16x16 cat16(bf16x8 a, bf16x8 b) { return __builtin_shufflevector(a, b, 0, 1, 2, 3, 4, 5, 6, 7, 8, 9, 10, 11, 12, 13, 14, 15); }
__device__ __forceinline__ f32x4 mfma32(bf16x8 a, bf16x8 b, f32x4 c) { return __builtin_amdgcn_mfma_f32_16x16x32_bf16(a, b, c, 0, 0, 0); }
__device__ __forceinline__ f32x4 mfma16(u32x2 a, u32x2 b, f32x4 c) { return __builtin_amdgcn_mfma_f32_16x16x16bf16_1k(__builtin_bit_cast(s16x4, a), __builtin_bit_cast(s16x4, b), c, 0, 0, 0); }
__device__ __forceinline__ f32x4 smf(bf16x8 a, bf16x16 b, f32x4 c, int idx) { return __builtin_amdgcn_smfmac_f32_16x16x64_bf16(a, b, c, idx, 0, 0); }

__device__ __forceinline__ void ln_row(float (&x)[16]) {
  float s = 0.f;
#pragma unroll
  for (int i = 0; i < 16; ++i) s += x[i];
  const float mean = wave_sum(s) * (1.0f / 1024.0f);
  float q = 0.f;
#pragma unroll
  for (int i = 0; i < 16; ++i) { const float d = x[i] - mean; q += d * d; }
  const float var = wave_sum(q) * (1.0f / 1024.0f);
  const float m = rbf(mean), sd = rbf(sqrtf(rbf(rbf(var) + 1e-5f)));
#pragma unroll
  for (int i = 0; i < 16; ++i) x[i] = rbf(rbf(x[i] - m) / sd);
}
__device__ __forceinline__ void load_row16(const u16* rowp, int lane, float (&x)[16]) {
  const u32x4 a = *(const u32x4*)(rowp + lane * 8), b = *(const u32x4*)(rowp + 512 + lane * 8);
#pragma unroll
  for (int i = 0; i < 4; ++i) { x[2 * i] = lo2f(a[i]); x[2 * i + 1] = hi2f(a[i]); x[8 + 2 * i] = lo2f(b[i]); x[9 + 2 * i] = hi2f(b[i]); }
}
__device__ __forceinline__ void store_row16(u16* rowp, int lane, const float (&x)[16]) {
  u32x4 a, b;
#pragma unroll
  for (int i = 0; i < 4; ++i) { a[i] = pack2(x[2 * i], x[2 * i + 1]); b[i] = pack2(x[8 + 2 * i], x[9 + 2 * i]); }
  *(u32x4*)(rowp + lane * 8) = a; *(u32x4*)(rowp + 512 + lane * 8) = b;
}
__device__ __forceinline__ void modulate(float (&x)[16], const float* sc, const float* sh, int lane) {
#pragma unroll
  for (int hh = 0; hh < 2; ++hh)
#pragma unroll
    for (int j = 0; j < 2; ++j) {
      const f32x4 c = *(const f32x4*)(sc + hh * 512 + lane * 8 + j * 4), t = *(const f32x4*)(sh + hh * 512 + lane * 8 + j * 4);
#pragma unroll
      for (int i = 0; i < 4; ++i) { float& v = x[hh * 8 + j * 4 + i]; v = rbf(rbf(v * rbf(1.0f + c[i])) + t[i]); }
    }
}

__host__ __device__ __forceinline__ int mem_slot(int dl) {
  return ((dl >> 2) & 1) * 32 + (((dl >> 5) & 1) * 2 + ((dl >> 3) & 1)) * 8 + (((dl >> 4) & 1) * 2 + (dl & 1)) * 2 + ((dl >> 1) & 1);
}

__global__ void __launch_bounds__(512) k_prep(const u16* __restrict__ c, const u16* __restrict__ wmod, const u16* __restrict__ mem0, unsigned char* ws) {
  __shared__ float red[128][33];
  __shared__ float sc[1024];
  const int b = blockIdx.x, tid = threadIdx.x;
  {
    u32x4* z = (u32x4*)(ws + WS_X);
    const u32x4 zero = {0u, 0u, 0u, 0u};
    for (size_t i = (size_t)65536 / 16 + (size_t)b * 512 + tid; i < X_BYTES / 16; i += (size_t)256 * 512) z[i] = zero;
  }
  if (b == 255) {
    for (int d = tid; d < 1024; d += 512) {
      u32x4 v;
#pragma unroll
      for (int i = 0; i < 4; ++i) v[i] = nz((unsigned)mem0[(2 * i) * 1024 + d] | ((unsigned)mem0[(2 * i + 1) * 1024 + d] << 16));
      const u32x4 mz = {0x80008000u, 0x80008000u, 0x80008000u, 0x80008000u};
#pragma unroll
      for (int r = 0; r < 2; ++r) {
        *(u32x4*)(ws + WS_X + X_MEMP + r * 32768 + (d >> 6) * 1024 + mem_slot(d & 63) * 16) = v;
        *(u32x4*)(ws + WS_X + X_MEMP + r * 32768 + 16384 + d * 16) = mz;
      }
    }
  }
  if (b < 192) {
    for (int i = tid; i < 1024; i += 512) { const float v = bf2f(c[i]); sc[i] = rbf(v / (1.0f + __expf(-v))); }
    __syncthreads();
    const int kg = tid >> 2, c8 = tid & 3;
    float acc[8] = {0.f, 0.f, 0.f, 0.f, 0.f, 0.f, 0.f, 0.f};
    u32x4 w[8];
#pragma unroll
    for (int i = 0; i < 8; ++i) w[i] = *(const u32x4*)(wmod + (size_t)(kg + 128 * i) * 6144 + b * 32 + c8 * 8);
#pragma unroll
    for (int i = 0; i < 8; ++i) {
      const float s = sc[kg + 128 * i];
#pragma unroll
      for (int j = 0; j < 4; ++j) { acc[2 * j] += s * lo2f(w[i][j]); acc[2 * j + 1] += s * hi2f(w[i][j]); }
    }
#pragma unroll
    for (int j = 0; j < 8; ++j) red[kg][c8 * 8 + j] = acc[j];
    __syncthreads();
    if (tid < 256) {
      const int col = tid & 31, part = tid >> 5;
      float s = 0.f;
#pragma unroll
      for (int i = 0; i < 16; ++i) s += red[part * 16 + i][col];
      __syncthreads();
      red[part][col] = s;
    } else __syncthreads();
    __syncthreads();
    if (tid < 32) {
      float s = 0.f;
#pragma unroll
      for (int i = 0; i < 8; ++i) s += red[i][tid];
      ((float*)(ws + WS_MOD))[b * 32 + tid] = rbf(s);
    }
  }
}

template <int MODE>
__global__ void __launch_bounds__(256) k_rows(const u16* __restrict__ in, u16* __restrict__ o1, u16* __restrict__ o2, const float* __restrict__ mod) {
  const int lane = threadIdx.x & 63, row = blockIdx.x * 4 + (threadIdx.x >> 6);
  float x[16];
  load_row16(in + (size_t)row * D_, lane, x);
  ln_row(x);
  if (MODE == 0) modulate(x, mod + 1024, mod, lane);
  if (MODE == 1) modulate(x, mod + 4096, mod + 3072, lane);
  store_row16(o1 + (size_t)row * D_, lane, x);
  if (MODE == 1) { ln_row(x); store_row16(o2 + (size_t)row * D_, lane, x); }
}

enum { EPI_FRAG = 0, EPI_VT = 1, EPI_WO = 2, EPI_W1 = 3, EPI_W2 = 4, EPI_W3 = 5, EPI_W4 = 6 };
struct GemmP {
  const u16* A; const u16* W; u16* C;
  const u16* r1; const u16* r2; const float* gate;
  int lda, ldw, ldc, K;
};

template <int EPI> __device__ __forceinline__ void epi8(const GemmP& p, int row, int col, u32x4 v) {
  if (EPI == EPI_FRAG) {
    *(u32x4*)(p.C + ((size_t)(((row >> 5) * NH + (col >> 7)) * 8 + ((row >> 4) & 1) * 4 + ((col >> 5) & 3)) * 64 + ((col >> 3) & 3) * 16 + (row & 15)) * 8) = v;
    return;
  }
  float a[8];
#pragma unroll
  for (int i = 0; i < 4; ++i) { a[2 * i] = lo2f(v[i]); a[2 * i + 1] = hi2f(v[i]); }
  if (EPI == EPI_WO) {
    const u32x4 xr = *(const u32x4*)(p.r1 + (size_t)row * D_ + col);
    const f32x4 g0 = *(const f32x4*)(p.gate + col), g1 = *(const f32x4*)(p.gate + col + 4);
#pragma unroll
    for (int i = 0; i < 4; ++i) {
      const float ga = (2 * i < 4) ? g0[(2 * i) & 3] : g1[(2 * i) & 3], gb = (2 * i + 1 < 4) ? g0[(2 * i + 1) & 3] : g1[(2 * i + 1) & 3];
      v[i] = pack2(lo2f(xr[i]) + rbf(ga * a[2 * i]), hi2f(xr[i]) + rbf(gb * a[2 * i + 1]));
    }
    *(u32x4*)(p.C + (size_t)row * p.ldc + col) = v;
  } else if (EPI == EPI_W1 || EPI == EPI_W3) {
#pragma unroll
    for (int i = 0; i < 4; ++i) v[i] = pack2(gelu_tanh(a[2 * i]), gelu_tanh(a[2 * i + 1]));
    if (EPI == EPI_W3) *(u32x4*)(p.C + (size_t)row * p.ldc + col) = v;
    else {
      const int grp = col >> 8, sh = (grp & 1) ? -(1 + (grp >> 1)) : (1 + (grp >> 1));
      const int dr = row + sh;
      if (dr >= 0 && dr < S_) *(u32x4*)(p.C + (size_t)dr * p.ldc + col) = v;
      const int sr = row - sh;
      if (sr < 0 || sr >= S_) { const u32x4 z = {0u, 0u, 0u, 0u}; *(u32x4*)(p.C + (size_t)row * p.ldc + col) = z; }
    }
  } else if (EPI == EPI_W2) {
    const u32x4 hr = *(const u32x4*)(p.r1 + (size_t)row * D_ + col);
#pragma unroll
    for (int i = 0; i < 4; ++i) v[i] = pack2(lo2f(hr[i]) + a[2 * i], hi2f(hr[i]) + a[2 * i + 1]);
    *(u32x4*)(p.C + (size_t)row * p.ldc + col) = v;
  } else if (EPI == EPI_W4) {
    const u32x4 x2 = *(const u32x4*)(p.r1 + (size_t)row * D_ + col), x1 = *(const u32x4*)(p.r2 + (size_t)row * D_ + col);
    const f32x4 g0 = *(const f32x4*)(p.gate + col), g1 = *(const f32x4*)(p.gate + col + 4);
#pragma unroll
    for (int i = 0; i < 4; ++i) {
      const float ga = (2 * i < 4) ? g0[(2 * i) & 3] : g1[(2 * i) & 3], gb = (2 * i + 1 < 4) ? g0[(2 * i + 1) & 3] : g1[(2 * i + 1) & 3];
      const float b0 = rbf(lo2f(x2[i]) + a[2 * i]), b1 = rbf(hi2f(x2[i]) + a[2 * i + 1]);
      v[i] = pack2(lo2f(x1[i]) + rbf(ga * b0), hi2f(x1[i]) + rbf(gb * b1));
    }
    *(u32x4*)(p.C + (size_t)row * p.ldc + col) = v;
  }
}

template <int BM, int BN, int WM, int WN, int NST, int EPI>
__device__ __forceinline__ void gemm_tile(LAS unsigned char* lds, const GemmP& p, int row0, int col0) {
  constexpr int WTM = BM / WM, WTN = BN / WN, MT = WTM / 16, NT = WTN / 16;
  constexpr int PA = BM / 64, PB = BN / 64, PW = PA + PB;
  constexpr int STB = (BM + BN) * 128, BOFF = BM * 128;
  constexpr bool SWAP = (EPI != EPI_VT);
  const int tid = threadIdx.x, lane = tid & 63, w = __builtin_amdgcn_readfirstlane(tid >> 6);
  const int wm = w / WN, wn = w % WN, fr = lane & 15, g = lane >> 4;
  const int nt = p.K / 64;
  const __amdgpu_buffer_rsrc_t rsA = mk_rs(p.A, 0x7FFFFFFFu), rsW = mk_rs(p.W, 0x7FFFFFFFu);
  const unsigned lbase = (unsigned)(size_t)lds;
  const int ub = lane >> 3;
  const unsigned vA = (unsigned)(ub * p.lda + (((lane & 7) ^ (ub & 7)) * 8)) * 2u;
  const unsigned vB = (unsigned)(((ub & 3) + 8 * (ub >> 2)) * p.ldw + 16 * (((lane >> 1) & 3) ^ (ub >> 1)) + 8 * (lane & 1)) * 2u;
  const unsigned sA = (unsigned)((row0 + 8 * w) * p.lda) * 2u;
  const unsigned sB = (unsigned)((32 * (w >> 2) + 16 * ((w >> 1) & 1) + 4 * (w & 1)) * p.ldw + col0) * 2u;
  auto stage = [&](int kt, int slot) {
    const unsigned dst = lbase + slot * STB + w * 1024;
#pragma unroll
    for (int i = 0; i < PA; ++i) dma16(rsA, vA, sA + (unsigned)(i * 64 * p.lda + kt * 64) * 2u, dst + i * 8192);
#pragma unroll
    for (int i = 0; i < PB; ++i) dma16(rsW, vB, sB + (unsigned)(kt * 64 * p.ldw + i * 64) * 2u, dst + BOFF + i * 8192);
  };
  const int aoff = (wm * WTM + fr) * 128 + ((g ^ (fr & 7)) << 4);
  const int tu = 4 * (g & 1) + (fr >> 2);
  const int boff = BOFF + (g >> 1) * 2048 + tu * 128 + ((tu >> 1) << 5) + (fr & 3) * 8;
  f32x4 acc[MT][NT];
#pragma unroll
  for (int m = 0; m < MT; ++m)
#pragma unroll
    for (int n = 0; n < NT; ++n) acc[m][n] = f32x4{0.f, 0.f, 0.f, 0.f};
#pragma unroll
  for (int s = 0; s < NST - 1; ++s) stage(s, s);
  int slot = 0;
  for (int kt = 0; kt < nt; ++kt) {
    if (kt + NST - 2 < nt) wait_v<PW * (NST - 2)>(); else wait_v<0>();
    BAR();
    if (kt + NST - 1 < nt) { int ns = slot + NST - 1; if (ns >= NST) ns -= NST; stage(kt + NST - 1, ns); }
    LAS unsigned char* sb = lds + slot * STB;
#pragma unroll
    for (int ks = 0; ks < 2; ++ks) {
      bf16x8 af[MT], bfr[NT];
#pragma unroll
      for (int n = 0; n < NT; ++n) {
        const int T = wn * NT + n;
        LAS unsigned char* q = sb + ((boff ^ ((T & 3) << 5)) + (T >> 2) * 8192 + ks * 4096);
        bfr[n] = cat8(trread(q), trread(q + 1024));
      }
#pragma unroll
      for (int m = 0; m < MT; ++m) af[m] = *(const LAS bf16x8*)(sb + ((aoff ^ (ks * 64)) + m * 2048));
#pragma unroll
      for (int m = 0; m < MT; ++m)
#pragma unroll
        for (int n = 0; n < NT; ++n) acc[m][n] = SWAP ? mfma32(bfr[n], af[m], acc[m][n]) : mfma32(af[m], bfr[n], acc[m][n]);
    }
    if (++slot == NST) slot = 0;
  }
  if (SWAP) {
#pragma unroll
    for (int m = 0; m < MT; ++m) {
      const int row = row0 + wm * WTM + 16 * m + fr;
#pragma unroll
      for (int u = 0; u < NT / 2; ++u) {
        const f32x4 a = acc[m][2 * u], b = acc[m][2 * u + 1];
        const auto r0 = __builtin_amdgcn_permlane16_swap(pack2(a[0], a[1]), pack2(b[0], b[1]), false, false);
        const auto r1 = __builtin_amdgcn_permlane16_swap(pack2(a[2], a[3]), pack2(b[2], b[3]), false, false);
        const u32x4 v = {r0[0], r1[0], r0[1], r1[1]};
        epi8<EPI>(p, row, col0 + wn * WTN + 32 * u + 16 * (g & 1) + 8 * (g >> 1), v);
      }
    }
  } else {
#pragma unroll
    for (int mp = 0; mp < MT / 2; ++mp) {
      const int seg = (row0 + wm * WTM + 32 * mp) >> 5;
#pragma unroll
      for (int n = 0; n < NT; ++n) {
        const f32x4 a = acc[2 * mp][n], b = acc[2 * mp + 1][n];
        const u32x4 v = {pack2(a[0], a[1]), pack2(a[2], a[3]), pack2(b[0], b[1]), pack2(b[2], b[3])};
        const int ct = (col0 + wn * WTN) / 16 + n;
        *(u32x4*)(p.C + ((size_t)(seg * 256 + ct) * 64 + lane) * 8) = v;
      }
    }
  }
  BAR();
}

template <int BM, int BN, int WM, int WN, int NST, int EPI>
__global__ void __launch_bounds__(512, 2) k_gemm(GemmP p, int tiles_m, int tiles_n) {
  extern __shared__ __attribute__((aligned(16))) unsigned char smem[];
  const int nwg = tiles_m * tiles_n, b = blockIdx.x;
  const int q = nwg / 8, r = nwg % 8, x = b % 8, o = b / 8;
  const int t = (x < r ? x * (q + 1) : r * (q + 1) + (x - r) * q) + o;
  const int tm = t % tiles_m, tn = t / tiles_m;
  gemm_tile<BM, BN, WM, WN, NST, EPI>((LAS unsigned char*)smem, p, tm * BM, tn * BN);
}

__global__ void __launch_bounds__(256) k_attn(const u16* __restrict__ Q, const u16* __restrict__ Kk, const u16* __restrict__ Vt,
                                              const unsigned char* __restrict__ km, const unsigned char* __restrict__ vm, u16* __restrict__ O) {
  const int lane = threadIdx.x & 63, unit = blockIdx.x * 4 + (threadIdx.x >> 6);
  const int seg = unit >> 5, h = unit & 31, fr = lane & 15, g = lane >> 4;
  f32x4 s0[2], s1[2], s2;
  s0[0] = s0[1] = s1[0] = s1[1] = s2 = f32x4{0.f, 0.f, 0.f, 0.f};
  const u16* qp = Q + (size_t)unit * 4096 + lane * 8;
  const u16* kp = Kk + (size_t)unit * 4096 + lane * 8;
  const unsigned char* kmp = km + (size_t)unit * 4096 + lane * 16;
#pragma unroll
  for (int ks = 0; ks < 4; ++ks) {
    const bf16x8 q0 = *(const bf16x8*)(qp + 512 * ks), q1 = *(const bf16x8*)(qp + 2048 + 512 * ks);
    const bf16x8 k0 = *(const bf16x8*)(kp + 512 * ks), k1 = *(const bf16x8*)(kp + 2048 + 512 * ks);
    const bf16x8 kmf = *(const bf16x8*)(kmp + ks * 1024);
    s0[0] = mfma32(kmf, q0, s0[0]); s0[1] = mfma32(kmf, q1, s0[1]);
    s1[0] = mfma32(k0, q0, s1[0]); s1[1] = mfma32(k0, q1, s1[1]);
    s2 = mfma32(k1, q1, s2);
  }
  const float scale = 0.08838834764831845f, L2E = 1.4426950408889634f;
  u32x2 pm[2]; u32x4 pt[2];
#pragma unroll
  for (int t = 0; t < 2; ++t) {
    float a[4], b[4], c[4];
    float mx = -3.0e38f;
#pragma unroll
    for (int r = 0; r < 4; ++r) {
      const int j = 4 * g + r;
      a[r] = (g < 2) ? rbf(rbf(s0[t][r]) * scale) : -3.0e38f;
      b[r] = (t == 1 || j <= fr) ? rbf(rbf(s1[t][r]) * scale) : -3.0e38f;
      c[r] = (t == 1 && j <= fr) ? rbf(rbf(s2[r]) * scale) : -3.0e38f;
      mx = fmaxf(mx, fmaxf(a[r], fmaxf(b[r], c[r])));
    }
    mx = fmaxf(mx, __shfl_xor(mx, 16)); mx = fmaxf(mx, __shfl_xor(mx, 32));
    float sum = 0.f;
#pragma unroll
    for (int r = 0; r < 4; ++r) {
      a[r] = __builtin_amdgcn_exp2f((a[r] - mx) * L2E); b[r] = __builtin_amdgcn_exp2f((b[r] - mx) * L2E); c[r] = __builtin_amdgcn_exp2f((c[r] - mx) * L2E);
      sum += a[r] + b[r] + c[r];
    }
    sum += __shfl_xor(sum, 16); sum += __shfl_xor(sum, 32);
    const float inv = 1.0f / sum;
    pm[t] = u32x2{pack2(a[0] * inv, a[1] * inv), pack2(a[2] * inv, a[3] * inv)};
    pt[t] = u32x4{pack2(b[0] * inv, b[1] * inv), pack2(b[2] * inv, b[3] * inv), pack2(c[0] * inv, c[1] * inv), pack2(c[2] * inv, c[3] * inv)};
  }
  const u16* vp = Vt + (size_t)unit * 4096 + g * 128;
  const unsigned char* vmp = vm + (size_t)unit * 2048 + (g & 1) * 128;
#pragma unroll
  for (int u = 0; u < 4; ++u) {
    f32x4 o[2][2];
#pragma unroll
    for (int bq = 0; bq < 2; ++bq) {
      const int e = 32 * u + 8 * (fr >> 2) + 4 * bq + (fr & 3);
      const bf16x8 vf = *(const bf16x8*)(vp + (e >> 4) * 512 + (e & 15) * 8);
      const u32x2 vmf = *(const u32x2*)(vmp + (e >> 4) * 256 + (e & 15) * 8);
#pragma unroll
      for (int t = 0; t < 2; ++t) {
        f32x4 z = {0.f, 0.f, 0.f, 0.f};
        z = mfma16(vmf, pm[t], z);
        o[bq][t] = mfma32(vf, __builtin_bit_cast(bf16x8, pt[t]), z);
      }
    }
#pragma unroll
    for (int t = 0; t < 2; ++t) {
      const u32x4 v = {pack2(o[0][t][0], o[0][t][1]), pack2(o[0][t][2], o[0][t][3]), pack2(o[1][t][0], o[1][t][1]), pack2(o[1][t][2], o[1][t][3])};
      *(u32x4*)(O + (size_t)(seg * 32 + 16 * t + fr) * HD + h * DH + 32 * u + 8 * g) = v;
    }
  }
}

#define ST_DECL
#define ST(i)
#define ST_FLUSH(cu, s)
#define POLLC 1
struct RecP { const u16 *Wq, *Wk, *Wv, *Wo; unsigned char* ws; int d1, d2; };
__device__ __forceinline__ unsigned now32() { unsigned long long t; asm volatile("s_memtime %0\n\ts_waitcnt lgkmcnt(0)" : "=s"(t) :: "memory"); return (unsigned)t; }
__device__ __forceinline__ void wait_until(unsigned t) { while ((int)(now32() - t) < 0) {} }
constexpr unsigned SPIN_CAP = 1u << 22;
constexpr int R_W = 0, R_MEM = 131072, R_QK = R_MEM + 16384, R_KT = R_QK + 4096, R_RED = R_MEM + 16384, R_LDS = 163840;
__device__ __forceinline__ unsigned umin3(unsigned a, unsigned b, unsigned c) { return min(min(a, b), c); }
__device__ __forceinline__ unsigned umin4(u32x4 v) { return min(umin3(v[0], v[1], v[2]), v[3]); }
__device__ __forceinline__ u32x4 ldx16(__amdgpu_buffer_rsrc_t rs, unsigned voff, unsigned soff) { return __builtin_amdgcn_raw_buffer_load_b128(rs, voff, soff, 16); }
__device__ __forceinline__ u32x2 ldx8(__amdgpu_buffer_rsrc_t rs, unsigned voff, unsigned soff) { return __builtin_amdgcn_raw_buffer_load_b64(rs, voff, soff, 16); }
__device__ __forceinline__ void stx16(__amdgpu_buffer_rsrc_t rs, unsigned voff, unsigned soff, u32x4 v) { __builtin_amdgcn_raw_buffer_store_b128(v, rs, voff, soff, 16); }
__device__ __forceinline__ void stx8(__amdgpu_buffer_rsrc_t rs, unsigned voff, unsigned soff, u32x2 v) { __builtin_amdgcn_raw_buffer_store_b64(v, rs, voff, soff, 16); }
#define PSLEEP ""
#define PIECE(R0, R1, R2, R3, SO, CNT) \
  "s_waitcnt vmcnt(" #CNT ")\n\t" \
  "v_min3_u32 %[t], v" #R0 ", v" #R1 ", v" #R2 "\n\t" \
  "v_min_u32 %[t], %[t], v" #R3 "\n\t" \
  "v_cmp_eq_u32 vcc, 0, %[t]\n\t" \
  "s_or_b64 %[acc], %[acc], vcc\n\t" \
  "s_mov_b64 exec, vcc\n\t" \
  "buffer_load_dwordx4 v[" #R0 ":" #R3 "], %[vo], %[rs], %[" SO "] offen sc1\n\t" \
  "s_mov_b64 exec, -1\n\t"
#define FIRST(R0, R3, SO) "buffer_load_dwordx4 v[" #R0 ":" #R3 "], %[vo], %[rs], %[" SO "] offen sc1\n\t"
#define POLL_TAIL \
  "s_cmp_eq_u64 %[acc], 0\n\t" \
  "s_cbranch_scc1 2f\n\t" \
  PSLEEP \
  "s_sub_u32 %[cap], %[cap], 1\n\t" \
  "s_cbranch_scc0 1b\n\t" \
  "s_mov_b32 %[cap], 0\n\t" \
  "2:\n\t" \
  "s_waitcnt vmcnt(0)"
template <int HI> __device__ __forceinline__ void poll2(__amdgpu_buffer_rsrc_t rs, unsigned vo, unsigned s0, u32x4& a0, u32x4& a1, unsigned& cap) {
  unsigned t; unsigned long long acc; const unsigned s1 = s0 + 1024;
  if (HI) asm volatile("s_nop 4\n\t" FIRST(248, 251, "s0") FIRST(252, 255, "s1") "1:\n\ts_mov_b64 %[acc], 0\n\t" PIECE(248, 249, 250, 251, "s0", 1) PIECE(252, 253, 254, 255, "s1", 1) POLL_TAIL
               : "=&{v[248:251]}"(a0), "=&{v[252:255]}"(a1), [t] "=&v"(t), [acc] "=&s"(acc), [cap] "+s"(cap) : [vo] "v"(vo), [rs] "s"(rs), [s0] "s"(s0), [s1] "s"(s1) : "vcc", "scc", "memory");
  else asm volatile("s_nop 4\n\t" FIRST(240, 243, "s0") FIRST(244, 247, "s1") "1:\n\ts_mov_b64 %[acc], 0\n\t" PIECE(240, 241, 242, 243, "s0", 1) PIECE(244, 245, 246, 247, "s1", 1) POLL_TAIL
               : "=&{v[240:243]}"(a0), "=&{v[244:247]}"(a1), [t] "=&v"(t), [acc] "=&s"(acc), [cap] "+s"(cap) : [vo] "v"(vo), [rs] "s"(rs), [s0] "s"(s0), [s1] "s"(s1) : "vcc", "scc", "memory");
}
__device__ __forceinline__ void poll4(__amdgpu_buffer_rsrc_t rs, unsigned vo, unsigned s0, unsigned s1, unsigned s2, unsigned s3, u32x4& a0, u32x4& a1, u32x4& a2, u32x4& a3, unsigned& cap) {
  unsigned t; unsigned long long acc;
  asm volatile("s_nop 4\n\t" FIRST(240, 243, "s0") FIRST(244, 247, "s1") FIRST(248, 251, "s2") FIRST(252, 255, "s3") "1:\n\ts_mov_b64 %[acc], 0\n\t"
               PIECE(240, 241, 242, 243, "s0", 3) PIECE(244, 245, 246, 247, "s1", 3) PIECE(248, 249, 250, 251, "s2", 3) PIECE(252, 253, 254, 255, "s3", 3) POLL_TAIL
               : "=&{v[240:243]}"(a0), "=&{v[244:247]}"(a1), "=&{v[248:251]}"(a2), "=&{v[252:255]}"(a3), [t] "=&v"(t), [acc] "=&s"(acc), [cap] "+s"(cap)
               : [vo] "v"(vo), [rs] "s"(rs), [s0] "s"(s0), [s1] "s"(s1), [s2] "s"(s2), [s3] "s"(s3) : "vcc", "scc", "memory");
}
__device__ __forceinline__ void poll1_finish(__amdgpu_buffer_rsrc_t rs, unsigned vo, unsigned s0, u32x2& a, unsigned& cap) {
  unsigned t;
  asm volatile("1:\n\t"
               "v_min_u32 %[t], v254, v255\n\t"
               "v_cmp_eq_u32 vcc, 0, %[t]\n\t"
               "s_cmp_eq_u64 vcc, 0\n\t"
               "s_cbranch_scc1 2f\n\t"
               "buffer_load_dwordx2 v[254:255], %[vo], %[rs], %[s0] offen sc1\n\t"
               "s_waitcnt vmcnt(0)\n\t"
               "s_sub_u32 %[cap], %[cap], 1\n\t"
               "s_cbranch_scc0 1b\n\t"
               "s_mov_b32 %[cap], 0\n\t"
               "2:\n\ts_nop 1"
               : "+{v[254:255]}"(a), [t] "=&v"(t), [cap] "+s"(cap) : [vo] "v"(vo), [rs] "s"(rs), [s0] "s"(s0) : "vcc", "scc", "memory");
}
__device__ __forceinline__ void gather_mem(__amdgpu_buffer_rsrc_t rs, unsigned lbase, int s, int half, int w, unsigned l16, unsigned& cap) {
  const unsigned so = (unsigned)X_MEMP + s * 65536 + half * 32768 + w * 2048;
  u32x4 a0, a1, b0, b1;
  poll4(rs, l16, so + half * 16384, so + half * 16384 + 1024, so + (half ^ 1) * 16384, so + (half ^ 1) * 16384 + 1024, a0, a1, b0, b1, cap);
  u32x4 r0, r1;
#pragma unroll
  for (int i = 0; i < 4; ++i) {
    const f32x2 x = f32x2{lo2f(a0[i]), hi2f(a0[i])} + f32x2{lo2f(b0[i]), hi2f(b0[i])};
    const f32x2 y = f32x2{lo2f(a1[i]), hi2f(a1[i])} + f32x2{lo2f(b1[i]), hi2f(b1[i])};
    r0[i] = pack2(x[0], x[1]); r1[i] = pack2(y[0], y[1]);
  }
  LAS unsigned char* d = lad(l16, lbase + R_MEM + w * 2048);
  *(LAS u32x4*)(d) = r0;
  *(LAS u32x4*)(d + 1024) = r1;
}
__device__ __forceinline__ bf16x8 mem_frag(LAS unsigned char* mp, int chunk) { return cat8(trread(mp + chunk * 1024), trread(mp + chunk * 1024 + 512)); }
__device__ __forceinline__ bf16x16 w_frag(LAS unsigned char* p) {
  return cat16(cat8(trread(p), trread(p + 2048)), cat8(trread(p + 4096), trread(p + 6144)));
}
__device__ __forceinline__ f32x4 fold(f32x4 a, f32x4 b) {
  f32x4 r;
#pragma unroll
  for (int i = 0; i < 4; ++i) {
    const auto t = __builtin_amdgcn_permlane32_swap(f2u(a[i]), f2u(b[i]), false, false);
    r[i] = u2f(t[0]) + u2f(t[1]);
  }
  return r;
}
__device__ __forceinline__ float gmax(float v) {
  auto t = __builtin_amdgcn_permlane16_swap(f2u(v), f2u(v), false, false);
  v = fmaxf(u2f(t[0]), u2f(t[1]));
  t = __builtin_amdgcn_permlane32_swap(f2u(v), f2u(v), false, false);
  return fmaxf(u2f(t[0]), u2f(t[1]));
}
__device__ __forceinline__ float gsum(float v) {
  auto t = __builtin_amdgcn_permlane16_swap(f2u(v), f2u(v), false, false);
  v = u2f(t[0]) + u2f(t[1]);
  t = __builtin_amdgcn_permlane32_swap(f2u(v), f2u(v), false, false);
  return u2f(t[0]) + u2f(t[1]);
}

__device__ __forceinline__ void role_a(const RecP& p, LAS unsigned char* lds, int half, int hl) {
  const int tid = threadIdx.x, lane = tid & 63, w = __builtin_amdgcn_readfirstlane(tid >> 6);
  const int h = half * 16 + hl;
  const __amdgpu_buffer_rsrc_t rs = mk_rs(p.ws, 0x7FFFFFFFu);
  const unsigned lbase = (unsigned)(size_t)lds;
  const unsigned l16 = lane * 16, l8 = lane * 8;
  const int sidx = (lane & 8) ? 0xEEEE : 0x4444;
  bf16x16 W[24];
  unsigned woff, qoff;
  {
    const int fr = lane & 15, g = lane >> 4, tu = 4 * (g & 1) + (fr >> 2), ub = lane >> 3;
    woff = (w >> 1) * 8192 + (g >> 1) * 1024 + tu * 128 + (((2 * (w & 1)) ^ (tu >> 1)) << 5) + (fr & 3) * 8;
    qoff = R_QK + (g >> 1) * 256 + (fr & 1) * 128 + (8 * (g & 1) + (fr >> 2)) * 8;
    const __amdgpu_buffer_rsrc_t rq = mk_rs(p.Wq, 0x7FFFFFFFu), rk = mk_rs(p.Wk, 0x7FFFFFFFu);
    const unsigned vo = (unsigned)(ub * HD + 16 * (((lane >> 1) & 3) ^ (ub >> 1)) + 8 * (lane & 1)) * 2u;
    const unsigned so = (unsigned)(8 * w * HD + h * DH) * 2u;
    auto stage = [&](int c) {
      const unsigned d = lbase + R_W + (c & 3) * 32768 + w * 1024, sc = so + (unsigned)c * 64 * HD * 2;
      dma16(rq, vo, sc, d); dma16(rq, vo, sc + 128, d + 8192); dma16(rk, vo, sc, d + 16384); dma16(rk, vo, sc + 128, d + 24576);
    };
    stage(0); stage(1); stage(2); stage(3);
#pragma unroll
    for (int c = 0; c < 12; ++c) {
      WAIT_V(12); BAR();
      W[2 * c] = w_frag(lds + R_W + (c & 3) * 32768 + woff);
      W[2 * c + 1] = w_frag(lds + R_W + (c & 3) * 32768 + (woff ^ 32));
      LBAR();
      stage(c + 4);
    }
    dma16(rs, l16, (unsigned)WS_K + h * 8192 + w * 1024, lbase + R_KT + w * 1024);
    WAIT_V(0); BAR();
  }
  unsigned cap = SPIN_CAP, tref = now32() - p.d2;
  ST_DECL
  for (int s = 0; s < NSEG; ++s) {
    ST(0);
    bf16x16 L0 = w_frag(lad(woff, lbase + R_W)), L1 = w_frag(lad<32>(woff, lbase + R_W));
    SCHED();
    wait_until(tref + p.d2);
    gather_mem(rs, lbase, s, half, w, l16, cap);
    ST(1);
    LBAR();
    tref = now32();
    ST(2);
    f32x4 c0 = {0.f, 0.f, 0.f, 0.f}, c1 = {0.f, 0.f, 0.f, 0.f};
    LAS unsigned char* mp = lad(l8, lbase + R_MEM);
    bf16x8 af[3];
    u32x4 vt0, vt1;
    u32x2 vmv;
    af[0] = mem_frag(mp, 12); af[1] = mem_frag(mp, 0);
#pragma unroll
    for (int i = 0; i < 16; ++i) {
      if (i + 2 < 16) { const int j = i + 2; af[j % 3] = mem_frag(mp, (j & 3) == 0 ? 12 + (j >> 2) : 3 * (j >> 2) + (j & 3) - 1); }
      if ((i & 3) == 0) {
        c0 = smf(af[i % 3], L0, c0, sidx); c1 = smf(af[i % 3], L1, c1, sidx);
        if (i < 12) { L0 = w_frag(lad(woff, lbase + R_W + ((i >> 2) + 1) * 32768)); L1 = w_frag(lad<32>(woff, lbase + R_W + ((i >> 2) + 1) * 32768)); }
        else {
          const unsigned vo = (unsigned)WS_VT + (unsigned)(s * NH + h) * 8192 + (w & 3) * 2048;
          vt0 = __builtin_amdgcn_raw_buffer_load_b128(rs, l16, vo, 0); vt1 = __builtin_amdgcn_raw_buffer_load_b128(rs, l16, vo + 1024, 0);
        }
      } else {
        const int c = 3 * (i >> 2) + (i & 3) - 1;
        c0 = smf(af[i % 3], W[2 * c], c0, sidx); c1 = smf(af[i % 3], W[2 * c + 1], c1, sidx);
      }
      SCHED();
    }
    {
      const f32x4 f = fold(c0, c1);
      *(LAS u32x2*)(lad(l8, lbase + R_QK + w * 512)) = u32x2{pack2(f[0], f[1]), pack2(f[2], f[3])};
    }
    ST(3);
    if (w < 4) {
      const unsigned vso = (unsigned)X_VM + (unsigned)(s * NH + h) * 2048 + w * 512;
      bf16x8 k1[4], k2[4];
      LAS unsigned char* kb = lad(l16, lbase + R_KT);
#pragma unroll
      for (int ks = 0; ks < 2; ++ks) { k1[ks] = *(const LAS bf16x8*)(kb + ks * 1024); k2[ks] = *(const LAS bf16x8*)(kb + 4096 + ks * 1024); }
      LBAR();
      ST(4);
      vmv = ldx8(rs, l8, vso);
      f32x4 t0 = {0.f, 0.f, 0.f, 0.f}, t1 = t0, t2 = t0;
      LAS unsigned char* qb = lad(qoff, lbase);
      bf16x8 qf[4], kf[4];
#pragma unroll
      for (int ks = 0; ks < 2; ++ks) {
        qf[ks] = cat8(trread(qb + ks * 512), trread(qb + ks * 512 + 32));
        kf[ks] = cat8(trread(qb + 2048 + ks * 512), trread(qb + 2048 + ks * 512 + 32));
      }
      SCHED();
#pragma unroll
      for (int ks = 0; ks < 4; ++ks) {
        t1 = mfma32(k1[ks], qf[ks], t1); t2 = mfma32(k2[ks], qf[ks], t2); t0 = mfma32(kf[ks], qf[ks], t0);
        if (ks < 2) {
          qf[ks + 2] = cat8(trread(qb + (ks + 2) * 512), trread(qb + (ks + 2) * 512 + 32));
          k1[ks + 2] = *(const LAS bf16x8*)(kb + (ks + 2) * 1024); k2[ks + 2] = *(const LAS bf16x8*)(kb + 4096 + (ks + 2) * 1024);
          kf[ks + 2] = cat8(trread(qb + 2048 + (ks + 2) * 512), trread(qb + 2048 + (ks + 2) * 512 + 32));
        }
        SCHED();
      }
      ST(5);
      if (s + 1 < NSEG) {
        const float sl = 0.08838834764831845f * 1.4426950408889634f;
        float mx = fmaxf(fmaxf(fmaxf(t1[0], t1[1]), fmaxf(t1[2], t1[3])), fmaxf(fmaxf(t2[0], t2[1]), fmaxf(t2[2], t2[3])));
        mx = fmaxf(mx, fmaxf(fmaxf(t0[0], t0[1]), fmaxf(t0[2], t0[3])));
        const float nm = -gmax(mx) * sl;
#pragma unroll
        for (int r = 0; r < 4; ++r) { t1[r] = __builtin_amdgcn_exp2f(fmaf(t1[r], sl, nm)); t2[r] = __builtin_amdgcn_exp2f(fmaf(t2[r], sl, nm)); }
        const u32x4 pt = {pack2(t1[0], t1[1]), pack2(t1[2], t1[3]), pack2(t2[0], t2[1]), pack2(t2[2], t2[3])};
        f32x4 o0 = {0.f, 0.f, 0.f, 0.f}, o1 = o0;
        o0 = mfma32(__builtin_bit_cast(bf16x8, vt0), __builtin_bit_cast(bf16x8, pt), o0);
        o1 = mfma32(__builtin_bit_cast(bf16x8, vt1), __builtin_bit_cast(bf16x8, pt), o1);
#pragma unroll
        for (int r = 0; r < 4; ++r) t0[r] = __builtin_amdgcn_exp2f(fmaf(t0[r], sl, nm));
        const u32x2 pmv = {pack2(t0[0], t0[1]), pack2(t0[2], t0[3])};
        const u32x2 zz = {0u, 0u};
        const u32x2 pm0 = (l16 < 512) ? pmv : zz, pm1 = (l16 < 512) ? zz : pmv;
        const float ssum = ((t1[0] + t1[1]) + (t1[2] + t1[3])) + ((t2[0] + t2[1]) + (t2[2] + t2[3])) + 0.5f * ((t0[0] + t0[1]) + (t0[2] + t0[3]));
        const float inv = __builtin_amdgcn_rcpf(gsum(ssum));
        ST(6);
        poll1_finish(rs, l8, vso, vmv, cap);
        ST(7);
        o0 = mfma16(vmv, pm0, o0);
        o1 = mfma16(vmv, pm1, o1);
        if ((l16 & 128) == 0) {
          const u32x4 v = {nz(pack2(o0[0] * inv, o0[1] * inv)), nz(pack2(o0[2] * inv, o0[3] * inv)), nz(pack2(o1[0] * inv, o1[1] * inv)), nz(pack2(o1[2] * inv, o1[3] * inv))};
          stx16(rs, l16, (unsigned)X_OX + s * 65536 + half * 32768 + (hl * 2 + (w >> 1)) * 1024 + (w & 1) * 128, v);
        }
        ST(8);
      }
    } else {
      LBAR();
      LAS unsigned char* qb = lad(qoff, lbase + 2048 + (w - 4) * 512);
      const bf16x8 kf = cat8(trread(qb), trread(qb + 32));
      __builtin_amdgcn_raw_buffer_store_b128(__builtin_bit_cast(u32x4, kf), rs, l16, (unsigned)WS_KM + (unsigned)(s * NH + h) * 4096 + (w - 4) * 1024, 0);
    }
    LBAR();
    ST(9);
    ST_FLUSH(half * 32 + hl, s);
    if (s + 1 < NSEG) dma16(rs, l16, (unsigned)WS_K + (unsigned)((s + 1) * NH + h) * 8192 + w * 1024, lbase + R_KT + w * 1024);
  }
  if (cap == 0u && l16 == 0) __hip_atomic_store((GAS unsigned*)(p.ws + WS_X + X_CTL), 1u, __ATOMIC_RELAXED, __HIP_MEMORY_SCOPE_AGENT);
}

__device__ __forceinline__ void role_vb(const RecP& p, LAS unsigned char* lds, int half, int jb) {
  const int tid = threadIdx.x, lane = tid & 63, w = __builtin_amdgcn_readfirstlane(tid >> 6);
  const int h = half * 16 + jb;
  const __amdgpu_buffer_rsrc_t rs = mk_rs(p.ws, 0x7FFFFFFFu);
  const unsigned lbase = (unsigned)(size_t)lds;
  const unsigned l16 = lane * 16, l8 = lane * 8;
  const int sidx = (lane & 8) ? 0xEEEE : 0x4444;
  bf16x16 WV[16], WB[8];
  unsigned lo, roff;
  {
    const int fr = lane & 15, g = lane >> 4, tu = 4 * (g & 1) + (fr >> 2), ub = lane >> 3;
    lo = (g >> 1) * 1024 + tu * 128 + ((tu >> 1) << 5) + (fr & 3) * 8;
    roff = R_RED + w * 2048 + mem_slot(16 * (g >> 1) + fr) * 32 + (g & 1) * 16;
    const unsigned vcol = (unsigned)(16 * (((lane >> 1) & 3) ^ (ub >> 1)) + 8 * (lane & 1)) * 2u;
    const __amdgpu_buffer_rsrc_t rv = mk_rs(p.Wv, 0x7FFFFFFFu), ro = mk_rs(p.Wo, 0x7FFFFFFFu);
    const unsigned vv = (unsigned)(ub * HD) * 2u + vcol, sv = (unsigned)(8 * w * HD + h * DH) * 2u;
    auto stage = [&](int c) {
      const unsigned d = lbase + R_W + (c & 7) * 16384 + w * 1024, sc = sv + (unsigned)c * 64 * HD * 2;
      dma16(rv, vv, sc, d); dma16(rv, vv, sc + 128, d + 8192);
    };
#pragma unroll
    for (int c = 0; c < 8; ++c) stage(c);
    const int vo = (w >> 2) * 8192 + (lo ^ ((w & 3) << 5));
#pragma unroll
    for (int c = 0; c < 16; ++c) {
      if (c < 8) WAIT_V(14); else wait_v<0>();
      BAR();
      WV[c] = w_frag(lds + R_W + (c & 7) * 16384 + vo);
      LBAR();
      if (c < 8) stage(c + 8);
    }
    const unsigned vw = (unsigned)((32 * ((ub >> 1) & 1) + 16 * (ub >> 2) + (ub & 1)) * D_) * 2u + vcol;
    const unsigned sw = (unsigned)((half * 2048 + w * 256) * D_ + jb * 64) * 2u;
    auto stageo = [&](int c) {
      const unsigned d = lbase + R_W + w * 16384 + (c & 1) * 8192;
#pragma unroll
      for (int rho = 0; rho < 8; ++rho) {
        const int r = rho >> 1, hb = rho & 1;
        dma16(ro, vw, sw + (unsigned)((c * 64 + 8 * (r >> 1) + 4 * hb + 2 * (r & 1)) * D_) * 2u, d + rho * 1024);
      }
    };
    stageo(0); stageo(1);
    WAIT_V(0);
#pragma unroll
    for (int c = 0; c < 2; ++c)
#pragma unroll
      for (int t = 0; t < 4; ++t) WB[c * 4 + t] = w_frag(lds + R_W + w * 16384 + c * 8192 + (lo ^ (t << 5)));
    WAIT_L0();
    stageo(2); stageo(3);
    WAIT_V(0);
  }
  unsigned cap = SPIN_CAP, tref = now32() - p.d2;
  ST_DECL
  for (int s = 0; s < NSEG; ++s) {
    ST(0);
    wait_until(tref + p.d2);
    gather_mem(rs, lbase, s, half, w, l16, cap);
    ST(1);
    LBAR();
    tref = now32();
    ST(2);
    {
      f32x4 c0 = {0.f, 0.f, 0.f, 0.f}, c1 = c0;
      LAS unsigned char* mp = lad(l8, lbase + R_MEM);
      bf16x8 af[6];
#pragma unroll
      for (int c = 0; c < 5; ++c) af[c] = mem_frag(mp, c);
#pragma unroll
      for (int c = 0; c < 16; ++c) {
        if (c + 5 < 16) af[(c + 5) % 6] = mem_frag(mp, c + 5);
        if (c & 1) c1 = smf(af[c % 6], WV[c], c1, sidx); else c0 = smf(af[c % 6], WV[c], c0, sidx);
        SCHED();
      }
      c0 += c1;
      const f32x4 f = fold(c0, c0);
      if (l16 < 512) stx8(rs, l8, (unsigned)X_VM + (unsigned)(s * NH + h) * 2048 + w * 256, u32x2{nz(pack2(f[0], f[1])), nz(pack2(f[2], f[3]))});
    }
    ST(3);
    if (s + 1 < NSEG) {
      const unsigned wb = lbase + R_W + w * 16384;
      bf16x16 L0 = w_frag(lad(lo, wb)), L1 = w_frag(lad<32>(lo, wb));
      SCHED();
      u32x4 a[4];
      const unsigned so = (unsigned)X_OX + s * 65536 + half * 32768 + w * 4096;
      wait_until(tref + p.d1);
      poll4(rs, l16, so, so + 1024, so + 2048, so + 3072, a[0], a[1], a[2], a[3], cap);
      ST(4);
      f32x4 c[4];
#pragma unroll
      for (int t = 0; t < 4; ++t) c[t] = f32x4{0.f, 0.f, 0.f, 0.f};
#pragma unroll
      for (int q = 0; q < 4; ++q) {
        const int ci = 2 + (q >> 1), tb = 2 * (q & 1);
        c[tb] = smf(__builtin_bit_cast(bf16x8, a[ci]), L0, c[tb], sidx);
        c[tb + 1] = smf(__builtin_bit_cast(bf16x8, a[ci]), L1, c[tb + 1], sidx);
        if (q < 3) {
          const int nq = q + 1;
          if (nq & 1) { L0 = w_frag(lad<64>(lo, wb + (nq >> 1) * 8192)); L1 = w_frag(lad<96>(lo, wb + (nq >> 1) * 8192)); }
          else { L0 = w_frag(lad(lo, wb + (nq >> 1) * 8192)); L1 = w_frag(lad<32>(lo, wb + (nq >> 1) * 8192)); }
        }
        const int ri = q >> 1, rt = 2 * ((q & 1) ^ 1);
        c[rt] = smf(__builtin_bit_cast(bf16x8, a[ri]), WB[ri * 4 + rt], c[rt], sidx);
        c[rt + 1] = smf(__builtin_bit_cast(bf16x8, a[ri]), WB[ri * 4 + rt + 1], c[rt + 1], sidx);
        SCHED();
      }
      ST(5);
      LAS unsigned char* rw = lad(roff, lbase);
      *(LAS f32x4*)(rw) = fold(c[0], c[1]);
      *(LAS f32x4*)(rw + 512) = fold(c[2], c[3]);
      ST(6);
      LBAR();
      ST(7);
      if (w < 2) {
        LAS unsigned char* rp = lad(l16, lbase + R_RED + w * 1024);
        f32x4 v[8];
#pragma unroll
        for (int i = 0; i < 8; ++i) v[i] = *(const LAS f32x4*)(rp + i * 2048);
        const f32x4 sum = ((v[0] + v[1]) + (v[2] + v[3])) + ((v[4] + v[5]) + (v[6] + v[7]));
        const u32x2 o = {nz(pack2(sum[0], sum[1])), nz(pack2(sum[2], sum[3]))};
        const unsigned mo = (unsigned)X_MEMP + (s + 1) * 65536 + half * 16384 + jb * 1024 + w * 512;
        stx8(rs, l8, mo + (half ^ 1) * 32768, o);
        stx8(rs, l8, mo + half * 32768, o);
      }
      ST(8);
    }
    ST(9);
    ST_FLUSH(half * 32 + 16 + jb, s);
  }
  if (cap == 0u && l16 == 0) __hip_atomic_store((GAS unsigned*)(p.ws + WS_X + X_CTL), 1u, __ATOMIC_RELAXED, __HIP_MEMORY_SCOPE_AGENT);
}

__global__ void __launch_bounds__(512, 2) k_rec(RecP p) {
  extern __shared__ __attribute__((aligned(16))) unsigned char smem[];
  const int b = blockIdx.x, x = b & 7, j = b >> 3;
  if (x >= 2) return;
  if (j < 16) role_a(p, (LAS unsigned char*)smem, x, j);
  else role_vb(p, (LAS unsigned char*)smem, x, j - 16);
}

template <int BM, int BN, int WM, int WN, int NST, int EPI>
static void launch_gemm(const GemmP& p, int M, int N, hipStream_t st) {
  constexpr int lds = NST * (BM + BN) * 128;
  (void)hipFuncSetAttribute((const void*)k_gemm<BM, BN, WM, WN, NST, EPI>, hipFuncAttributeMaxDynamicSharedMemorySize, lds);
  hipLaunchKernelGGL((k_gemm<BM, BN, WM, WN, NST, EPI>), dim3((M / BM) * (N / BN)), dim3(512), lds, st, p, M / BM, N / BN);
}

extern "C" void kernel_launch(void* const* d_in, const int* in_sizes, int n_in, void* d_out, int out_size, void* d_ws, size_t ws_size,
                              hipStream_t stream) {
  const u16* x = (const u16*)d_in[0];
  const u16* mem0 = (const u16*)d_in[1];
  const u16 *Wq = (const u16*)d_in[2], *Wk = (const u16*)d_in[3], *Wv = (const u16*)d_in[4], *Wo = (const u16*)d_in[5];
  const u16 *w1 = (const u16*)d_in[8], *w2 = (const u16*)d_in[9], *w3 = (const u16*)d_in[10], *w4 = (const u16*)d_in[11];
  const u16 *c = (const u16*)d_in[12], *wmod = (const u16*)d_in[13];
  unsigned char* ws = (unsigned char*)d_ws;
  u16* out = (u16*)d_out;
  const float* mod = (const float*)(ws + WS_MOD);
  u16 *H = (u16*)(ws + WS_H), *Q = (u16*)(ws + WS_Q), *K = (u16*)(ws + WS_K), *VT = (u16*)(ws + WS_VT), *O = (u16*)(ws + WS_O);
  u16 *X1 = (u16*)(ws + WS_X1), *H2 = (u16*)(ws + WS_H2), *G = (u16*)(ws + WS_G), *H1S = (u16*)(ws + WS_H1S), *X2 = (u16*)(ws + WS_X2),
      *L2 = (u16*)(ws + WS_L2), *U = (u16*)(ws + WS_U);

  k_prep<<<256, 512, 0, stream>>>(c, wmod, mem0, ws);
  k_rows<0><<<S_ / 4, 256, 0, stream>>>(x, H, nullptr, mod);
  { GemmP p{H, Wk, K, nullptr, nullptr, nullptr, D_, HD, HD, D_}; launch_gemm<256, 128, 4, 2, 3, EPI_FRAG>(p, S_, HD, stream); }
  { GemmP p{H, Wv, VT, nullptr, nullptr, nullptr, D_, HD, HD, D_}; launch_gemm<256, 128, 4, 2, 3, EPI_VT>(p, S_, HD, stream); }
  { GemmP p{H, Wq, Q, nullptr, nullptr, nullptr, D_, HD, HD, D_}; launch_gemm<256, 128, 4, 2, 3, EPI_FRAG>(p, S_, HD, stream); }
  {
    RecP p{Wq, Wk, Wv, Wo, ws, 3400, 7000};
    (void)hipFuncSetAttribute((const void*)k_rec, hipFuncAttributeMaxDynamicSharedMemorySize, R_LDS);
    k_rec<<<256, 512, R_LDS, stream>>>(p);
  }
  k_attn<<<NSEG * NH / 4, 256, 0, stream>>>(Q, K, VT, ws + WS_KM, ws + WS_X + X_VM, O);
  { GemmP p{O, Wo, X1, x, nullptr, mod + 2048, HD, D_, D_, HD}; launch_gemm<128, 64, 4, 2, 4, EPI_WO>(p, S_, D_, stream); }
  k_rows<1><<<S_ / 4, 256, 0, stream>>>(X1, H2, G, mod);
  { GemmP p{G, w1, H1S, nullptr, nullptr, nullptr, D_, D_, D_, D_}; launch_gemm<128, 64, 4, 2, 4, EPI_W1>(p, S_, D_, stream); }
  { GemmP p{H1S, w2, X2, H2, nullptr, nullptr, D_, D_, D_, D_}; launch_gemm<128, 64, 4, 2, 4, EPI_W2>(p, S_, D_, stream); }
  k_rows<2><<<S_ / 4, 256, 0, stream>>>(X2, L2, nullptr, mod);
  { GemmP p{L2, w3, U, nullptr, nullptr, nullptr, D_, FF, FF, D_}; launch_gemm<256, 128, 4, 2, 3, EPI_W3>(p, S_, FF, stream); }
  { GemmP p{U, w4, out, X2, X1, mod + 5120, FF, D_, D_, FF}; launch_gemm<128, 64, 4, 2, 4, EPI_W4>(p, S_, D_, stream); }
}
```

```cpp
#include <hip/hip_runtime.h>
#include <hip/hip_bf16.h>
#include <cstdlib>
#pragma clang diagnostic ignored "-Winline-asm"

#define LAS __attribute__((address_space(3)))
#define GAS __attribute__((address_space(1)))
typedef unsigned short u16;
typedef __bf16 bf16x2 __attribute__((ext_vector_type(2)));
typedef __bf16 bf16x4 __attribute__((ext_vector_type(4)));
typedef __bf16 bf16x8 __attribute__((ext_vector_type(8)));
typedef __bf16 bf16x16 __attribute__((ext_vector_type(16)));
typedef short s16x4 __attribute__((ext_vector_type(4)));
typedef float f32x2 __attribute__((ext_vector_type(2)));
typedef float f32x4 __attribute__((ext_vector_type(4)));
typedef unsigned u32x2 __attribute__((ext_vector_type(2)));
typedef unsigned u32x4 __attribute__((ext_vector_type(4)));

constexpr int S_ = 2048, D_ = 1024, NH = 32, DH = 128, HD = 4096, NSEG = 64, FF = 3584;

constexpr unsigned MB = 1u << 20;
constexpr unsigned X_MEMP = 0;
constexpr unsigned X_OX = 4 * MB;
constexpr unsigned X_VM = 8 * MB;
constexpr unsigned F_LN = 12 * MB;
constexpr unsigned F_ST = F_LN + 32768;
constexpr unsigned F_CH = F_ST + 65536;
constexpr unsigned F_TL = F_CH + 4096;
constexpr unsigned X_CTL = F_TL + 8192;
constexpr unsigned X_BYTES = X_CTL + 4096;
constexpr unsigned WS_MOD = 15 * MB;
constexpr unsigned WS_H = 16 * MB;
constexpr unsigned WS_Q = 20 * MB;
constexpr unsigned WS_K = 36 * MB;
constexpr unsigned WS_VT = 52 * MB;
constexpr unsigned WS_O = 68 * MB;
constexpr unsigned WS_X1 = 84 * MB;
constexpr unsigned WS_H2 = 88 * MB;
constexpr unsigned WS_G = 92 * MB;
constexpr unsigned WS_H1S = 96 * MB;
constexpr unsigned WS_X2 = 101 * MB;
constexpr unsigned WS_ST5 = 106 * MB;
constexpr unsigned WS_U = 108 * MB;
constexpr unsigned WS_KM = 124 * MB;
constexpr unsigned WS_P2 = 132 * MB;
constexpr unsigned WS_P8 = 164 * MB;
constexpr unsigned WS_END = 198 * MB;

__device__ __forceinline__ float bf2f(unsigned short b) { return __builtin_bit_cast(float, (unsigned)b << 16); }
__device__ __forceinline__ unsigned f2u(float f) { return __builtin_bit_cast(unsigned, f); }
__device__ __forceinline__ float u2f(unsigned u) { return __builtin_bit_cast(float, u); }
__device__ __forceinline__ float lo2f(unsigned w) { return __builtin_bit_cast(float, w << 16); }
__device__ __forceinline__ float hi2f(unsigned w) { return __builtin_bit_cast(float, w & 0xFFFF0000u); }
__device__ __forceinline__ unsigned pack2(float a, float b) { bf16x2 v; v[0] = (__bf16)a; v[1] = (__bf16)b; return __builtin_bit_cast(unsigned, v); }
__device__ __forceinline__ float rbf(float a) { return (float)(__bf16)a; }
__device__ __forceinline__ unsigned nz(unsigned w) { return w == 0u ? 0x80000000u : w; }
__device__ __forceinline__ float gelu_tanh(float x) {
  const float z = 0.7978845608028654f * (x + 0.044715f * x * x * x);
  const float e = __builtin_amdgcn_exp2f(-2.885390081777927f * z);
  return x * __builtin_amdgcn_rcpf(1.0f + e);
}
template <int CTRL> __device__ __forceinline__ float dpp_add(float v) { return v + u2f(__builtin_amdgcn_update_dpp(0u, f2u(v), CTRL, 0xF, 0xF, true)); }
__device__ __forceinline__ float wave_sum(float v) {
  v = dpp_add<0xB1>(v); v = dpp_add<0x4E>(v); v = dpp_add<0x141>(v); v = dpp_add<0x140>(v);
  auto t = __builtin_amdgcn_permlane16_swap(f2u(v), f2u(v), false, false);
  v = u2f(t[0]) + u2f(t[1]);
  t = __builtin_amdgcn_permlane32_swap(f2u(v), f2u(v), false, false);
  return u2f(t[0]) + u2f(t[1]);
}
#define WAIT_V(n) asm volatile("s_waitcnt vmcnt(" #n ")" ::: "memory")
#define WAIT_L0() asm volatile("s_waitcnt lgkmcnt(0)" ::: "memory")
#define BAR() do { asm volatile("" ::: "memory"); __builtin_amdgcn_s_barrier(); asm volatile("" ::: "memory"); } while (0)
#define LBAR() do { WAIT_L0(); BAR(); } while (0)
template <int N> __device__ __forceinline__ void wait_v() { asm volatile("s_waitcnt vmcnt(%0)" :: "n"(N) : "memory"); }
__device__ __forceinline__ void dma16(__amdgpu_buffer_rsrc_t rs, unsigned voff, unsigned soff, unsigned ldsa) {
  asm volatile("s_mov_b32 m0, %0\n\ts_nop 3\n\tbuffer_load_dwordx4 %1, %2, %3 offen lds" :: "s"(ldsa), "v"(voff), "s"(rs), "s"(soff) : "m0", "memory");
}
__device__ __forceinline__ void dma16x(__amdgpu_buffer_rsrc_t rs, unsigned voff, unsigned soff, unsigned ldsa) {
  asm volatile("s_mov_b32 m0, %0\n\ts_nop 3\n\tbuffer_load_dwordx4 %1, %2, %3 offen sc1 lds" :: "s"(ldsa), "v"(voff), "s"(rs), "s"(soff) : "m0", "memory");
}
__device__ __forceinline__ u32x4 ldx16(__amdgpu_buffer_rsrc_t rs, unsigned voff, unsigned soff) { return __builtin_amdgcn_raw_buffer_load_b128(rs, voff, soff, 16); }
__device__ __forceinline__ u32x2 ldx8(__amdgpu_buffer_rsrc_t rs, unsigned voff, unsigned soff) { return __builtin_amdgcn_raw_buffer_load_b64(rs, voff, soff, 16); }
__device__ __forceinline__ unsigned ldx4(__amdgpu_buffer_rsrc_t rs, unsigned voff, unsigned soff) { return __builtin_amdgcn_raw_buffer_load_b32(rs, voff, soff, 16); }
__device__ __forceinline__ void stx16(__amdgpu_buffer_rsrc_t rs, unsigned voff, unsigned soff, u32x4 v) {
  __builtin_amdgcn_raw_buffer_store_b128(v, rs, voff, soff, 16);
  asm volatile("s_nop 1" :: "v"(v) : "memory");
}
__device__ __forceinline__ void stx8(__amdgpu_buffer_rsrc_t rs, unsigned voff, unsigned soff, u32x2 v) { __builtin_amdgcn_raw_buffer_store_b64(v, rs, voff, soff, 16); }
__device__ __forceinline__ void stx4(__amdgpu_buffer_rsrc_t rs, unsigned voff, unsigned soff, unsigned v) { __builtin_amdgcn_raw_buffer_store_b32(v, rs, voff, soff, 16); }
__device__ __forceinline__ void wait_ge(__amdgpu_buffer_rsrc_t rs, unsigned voff, unsigned soff, unsigned need, unsigned& cap) {
  unsigned t;
  asm volatile("s_nop 4\n\t"
               "1:\n\t"
               "buffer_load_dword %[t], %[vo], %[rs], %[so] offen sc1\n\t"
               "s_waitcnt vmcnt(0)\n\t"
               "v_cmp_gt_u32 vcc, %[need], %[t]\n\t"
               "s_cmp_eq_u64 vcc, 0\n\t"
               "s_cbranch_scc1 2f\n\t"
               "s_sleep 2\n\t"
               "s_sub_u32 %[cap], %[cap], 1\n\t"
               "s_cbranch_scc0 1b\n\t"
               "s_mov_b32 %[cap], 0\n\t"
               "2:"
               : [t] "=&v"(t), [cap] "+s"(cap) : [vo] "v"(voff), [rs] "s"(rs), [so] "s"(soff), [need] "s"(need) : "vcc", "scc", "memory");
}
__device__ __forceinline__ __amdgpu_buffer_rsrc_t mk_rs(const void* p, unsigned bytes) { return __builtin_amdgcn_make_buffer_rsrc((void*)p, 0, (int)bytes, 0x00020000); }
#define SCHED() __builtin_amdgcn_sched_barrier(0)
__device__ __forceinline__ unsigned now32() { unsigned long long t; asm volatile("s_memtime %0\n\ts_waitcnt lgkmcnt(0)" : "=s"(t) :: "memory"); return (unsigned)t; }
__device__ __forceinline__ void wait_until(unsigned t) { while ((int)(now32() - t) < 0) {} }
template <int X = 0> __device__ __forceinline__ LAS unsigned char* lad(unsigned v, unsigned u) {
  unsigned a;
  if (X == 0) asm volatile("v_add_u32 %0, %1, %2" : "=v"(a) : "s"(u), "v"(v));
  else asm volatile("v_xor_b32 %0, %3, %2\n\tv_add_u32 %0, %1, %0" : "=&v"(a) : "s"(u), "v"(v), "n"(X));
  return (LAS unsigned char*)(size_t)a;
}
__device__ __forceinline__ bf16x4 trread(LAS unsigned char* p) { return __builtin_amdgcn_ds_read_tr16_b64_v4bf16((LAS bf16x4*)p); }
__device__ __forceinline__ bf16x8 cat8(bf16x4 a, bf16x4 b) { return __builtin_shufflevector(a, b, 0, 1, 2, 3, 4, 5, 6, 7); }
__device__ __forceinline__ bf16x16 cat16(bf16x8 a, bf16x8 b) { return __builtin_shufflevector(a, b, 0, 1, 2, 3, 4, 5, 6, 7, 8, 9, 10, 11, 12, 13, 14, 15); }
__device__ __forceinline__ f32x4 mfma32(bf16x8 a, bf16x8 b, f32x4 c) { return __builtin_amdgcn_mfma_f32_16x16x32_bf16(a, b, c, 0, 0, 0); }
__device__ __forceinline__ f32x4 mfma16(u32x2 a, u32x2 b, f32x4 c) {
  return mfma32(__builtin_bit_cast(bf16x8, u32x4{a[0], a[1], 0u, 0u}), __builtin_bit_cast(bf16x8, u32x4{b[0], b[1], 0u, 0u}), c);
}
__device__ __forceinline__ f32x4 mfma16n(u32x2 a, u32x2 b, f32x4 c) { return __builtin_amdgcn_mfma_f32_16x16x16bf16_1k(__builtin_bit_cast(s16x4, a), __builtin_bit_cast(s16x4, b), c, 0, 0, 0); }
__device__ __forceinline__ f32x4 smf(bf16x8 a, bf16x16 b, f32x4 c, int idx) { return __builtin_amdgcn_smfmac_f32_16x16x64_bf16(a, b, c, idx, 0, 0); }

__device__ __forceinline__ void ln_row(float (&x)[16]) {
  float s = 0.f;
#pragma unroll
  for (int i = 0; i < 16; ++i) s += x[i];
  const float mean = wave_sum(s) * (1.0f / 1024.0f);
  float q = 0.f;
#pragma unroll
  for (int i = 0; i < 16; ++i) { const float d = x[i] - mean; q += d * d; }
  const float var = wave_sum(q) * (1.0f / 1024.0f);
  const float m = rbf(mean), isd = __builtin_amdgcn_rcpf(rbf(__builtin_amdgcn_sqrtf(rbf(rbf(var) + 1e-5f))));
#pragma unroll
  for (int i = 0; i < 16; ++i) x[i] = rbf(rbf(x[i] - m) * isd);
}
__device__ __forceinline__ void load_row16(const u16* rowp, int lane, float (&x)[16]) {
  const u32x4 a = *(const u32x4*)(rowp + lane * 8), b = *(const u32x4*)(rowp + 512 + lane * 8);
#pragma unroll
  for (int i = 0; i < 4; ++i) { x[2 * i] = lo2f(a[i]); x[2 * i + 1] = hi2f(a[i]); x[8 + 2 * i] = lo2f(b[i]); x[9 + 2 * i] = hi2f(b[i]); }
}
__device__ __forceinline__ void store_row16(u16* rowp, int lane, const float (&x)[16]) {
  u32x4 a, b;
#pragma unroll
  for (int i = 0; i < 4; ++i) { a[i] = pack2(x[2 * i], x[2 * i + 1]); b[i] = pack2(x[8 + 2 * i], x[9 + 2 * i]); }
  *(u32x4*)(rowp + lane * 8) = a; *(u32x4*)(rowp + 512 + lane * 8) = b;
}
__device__ __forceinline__ void modulate(float (&x)[16], const float* sc, const float* sh, int lane) {
#pragma unroll
  for (int hh = 0; hh < 2; ++hh)
#pragma unroll
    for (int j = 0; j < 2; ++j) {
      const f32x4 c = *(const f32x4*)(sc + hh * 512 + lane * 8 + j * 4), t = *(const f32x4*)(sh + hh * 512 + lane * 8 + j * 4);
#pragma unroll
      for (int i = 0; i < 4; ++i) { float& v = x[hh * 8 + j * 4 + i]; v = rbf(rbf(v * rbf(1.0f + c[i])) + t[i]); }
    }
}

__host__ __device__ __forceinline__ int mem_slot(int dl) {
  return ((dl >> 2) & 1) * 32 + (((dl >> 5) & 1) * 2 + ((dl >> 3) & 1)) * 8 + (((dl >> 4) & 1) * 2 + (dl & 1)) * 2 + ((dl >> 1) & 1);
}

__global__ void __launch_bounds__(512) k_prep(const u16* __restrict__ c, const u16* __restrict__ wmod, const u16* __restrict__ mem0, unsigned char* ws) {
  __shared__ float red[128][33];
  __shared__ float sc[1024];
  const int b = blockIdx.x, tid = threadIdx.x;
  {
    u32x4* z = (u32x4*)ws;
    const u32x4 zero = {0u, 0u, 0u, 0u};
    for (size_t i = (size_t)65536 / 16 + (size_t)b * 512 + tid; i < X_BYTES / 16; i += (size_t)256 * 512) z[i] = zero;
    if (b >= 192 && b < 208) {
      u32x4* y = (u32x4*)(ws + WS_H1S + ((b & 8) ? 64 * 65536 : 0));
      y[(b & 7) * 512 + tid] = zero;
    }
  }
  if (b == 255) {
    for (int d = tid; d < 1024; d += 512) {
      u32x4 v;
#pragma unroll
      for (int i = 0; i < 4; ++i) v[i] = nz((unsigned)mem0[(2 * i) * 1024 + d] | ((unsigned)mem0[(2 * i + 1) * 1024 + d] << 16));
      const u32x4 mz = {0x80008000u, 0x80008000u, 0x80008000u, 0x80008000u};
#pragma unroll
      for (int r = 0; r < 2; ++r) {
        *(u32x4*)(ws + X_MEMP + r * 32768 + (d >> 6) * 1024 + mem_slot(d & 63) * 16) = v;
        *(u32x4*)(ws + X_MEMP + r * 32768 + 16384 + d * 16) = mz;
      }
    }
  }
  if (b < 192) {
    for (int i = tid; i < 1024; i += 512) { const float v = bf2f(c[i]); sc[i] = rbf(v / (1.0f + __expf(-v))); }
    __syncthreads();
    const int kg = tid >> 2, c8 = tid & 3;
    float acc[8] = {0.f, 0.f, 0.f, 0.f, 0.f, 0.f, 0.f, 0.f};
    u32x4 w[8];
#pragma unroll
    for (int i = 0; i < 8; ++i) w[i] = *(const u32x4*)(wmod + (size_t)(kg + 128 * i) * 6144 + b * 32 + c8 * 8);
#pragma unroll
    for (int i = 0; i < 8; ++i) {
      const float s = sc[kg + 128 * i];
#pragma unroll
      for (int j = 0; j < 4; ++j) { acc[2 * j] += s * lo2f(w[i][j]); acc[2 * j + 1] += s * hi2f(w[i][j]); }
    }
#pragma unroll
    for (int j = 0; j < 8; ++j) red[kg][c8 * 8 + j] = acc[j];
    __syncthreads();
    if (tid < 256) {
      const int col = tid & 31, part = tid >> 5;
      float s = 0.f;
#pragma unroll
      for (int i = 0; i < 16; ++i) s += red[part * 16 + i][col];
      __syncthreads();
      red[part][col] = s;
    } else __syncthreads();
    __syncthreads();
    if (tid < 32) {
      float s = 0.f;
#pragma unroll
      for (int i = 0; i < 8; ++i) s += red[i][tid];
      ((float*)(ws + WS_MOD))[b * 32 + tid] = rbf(s);
    }
  }
}

#define WST(rs, wk, i)
struct MainP { const u16 *x, *Wq, *Wk, *Wv, *Wo, *w1, *w2, *w3, *w4; u16* out; unsigned char* ws; int d1, d2; };
constexpr unsigned SPIN_CAP = 1u << 18;

__device__ __forceinline__ float gmax(float v) {
  auto t = __builtin_amdgcn_permlane16_swap(f2u(v), f2u(v), false, false);
  v = fmaxf(u2f(t[0]), u2f(t[1]));
  t = __builtin_amdgcn_permlane32_swap(f2u(v), f2u(v), false, false);
  return fmaxf(u2f(t[0]), u2f(t[1]));
}
__device__ __forceinline__ float gsum(float v) {
  auto t = __builtin_amdgcn_permlane16_swap(f2u(v), f2u(v), false, false);
  v = u2f(t[0]) + u2f(t[1]);
  t = __builtin_amdgcn_permlane32_swap(f2u(v), f2u(v), false, false);
  return u2f(t[0]) + u2f(t[1]);
}
__device__ __forceinline__ u32x4 widen(f32x4 a, f32x4 b) {
  const auto r0 = __builtin_amdgcn_permlane16_swap(pack2(a[0], a[1]), pack2(b[0], b[1]), false, false);
  const auto r1 = __builtin_amdgcn_permlane16_swap(pack2(a[2], a[3]), pack2(b[2], b[3]), false, false);
  return u32x4{r0[0], r1[0], r0[1], r1[1]};
}
__device__ __forceinline__ void publish(__amdgpu_buffer_rsrc_t rs, unsigned word, unsigned val) {
  WAIT_V(0); BAR();
  if (threadIdx.x == 0) stx4(rs, 0, word, val);
}

template <bool VT>
__device__ __forceinline__ void proj_tile(LAS unsigned char* lds, __amdgpu_buffer_rsrc_t rs, __amdgpu_buffer_rsrc_t rsW, unsigned dst, int rb, int hd) {
  constexpr int BM = 128, BN = 128, WN = 2, WTM = 32, WTN = 64, MT = 2, NT = 4, NST = 4, PW = 4, STB = (BM + BN) * 128, BOFF = BM * 128;
  int tid = threadIdx.x; asm volatile("" : "+v"(tid));
  const int lane = tid & 63, w = __builtin_amdgcn_readfirstlane(tid >> 6);
  const int wm = w / WN, wn = w % WN, fr = lane & 15, g = lane >> 4;
  const unsigned lbase = (unsigned)(size_t)lds;
  const int ub = lane >> 3;
  const unsigned vA = (unsigned)(ub * D_ + (((lane & 7) ^ (ub & 7)) * 8)) * 2u;
  const unsigned vB = (unsigned)(((ub & 3) + 8 * (ub >> 2)) * HD + 16 * (((lane >> 1) & 3) ^ (ub >> 1)) + 8 * (lane & 1)) * 2u;
  const unsigned sA = WS_H + (unsigned)((rb * BM + 8 * w) * D_) * 2u;
  const unsigned sB = (unsigned)((32 * (w >> 2) + 16 * ((w >> 1) & 1) + 4 * (w & 1)) * HD + hd * BN) * 2u;
  auto stage = [&](int kt, int slot) {
    const unsigned d = lbase + slot * STB + w * 1024;
#pragma unroll
    for (int i = 0; i < 2; ++i) dma16x(rs, vA, sA + (unsigned)(i * 64 * D_ + kt * 64) * 2u, d + i * 8192);
#pragma unroll
    for (int i = 0; i < 2; ++i) dma16(rsW, vB, sB + (unsigned)(kt * 64 * HD + i * 64) * 2u, d + BOFF + i * 8192);
  };
  const int aoff = (wm * WTM + fr) * 128 + ((g ^ (fr & 7)) << 4);
  const int tu = 4 * (g & 1) + (fr >> 2);
  const int boff = BOFF + wn * 8192 + (g >> 1) * 2048 + tu * 128 + ((tu >> 1) << 5) + (fr & 3) * 8;
  f32x4 acc[MT][NT];
#pragma unroll
  for (int m = 0; m < MT; ++m)
#pragma unroll
    for (int n = 0; n < NT; ++n) acc[m][n] = f32x4{0.f, 0.f, 0.f, 0.f};
#pragma unroll
  for (int s = 0; s < NST - 1; ++s) stage(s, s);
  for (int kt = 0; kt < 16; ++kt) {
    if (kt < 14) wait_v<PW * 2>(); else if (kt == 14) wait_v<PW>(); else wait_v<0>();
    BAR();
    if (kt + NST - 1 < 16) stage(kt + NST - 1, (kt + NST - 1) & 3);
    LAS unsigned char* sb = lds + (kt & 3) * STB;
#pragma unroll
    for (int ks = 0; ks < 2; ++ks) {
      bf16x8 af[MT], bfr[NT];
#pragma unroll
      for (int n = 0; n < NT; ++n) { LAS unsigned char* q = sb + ((boff ^ (n << 5)) + ks * 4096); bfr[n] = cat8(trread(q), trread(q + 1024)); }
#pragma unroll
      for (int m = 0; m < MT; ++m) af[m] = *(const LAS bf16x8*)(sb + ((aoff ^ (ks * 64)) + m * 2048));
#pragma unroll
      for (int m = 0; m < MT; ++m)
#pragma unroll
        for (int n = 0; n < NT; ++n) acc[m][n] = VT ? mfma32(af[m], bfr[n], acc[m][n]) : mfma32(bfr[n], af[m], acc[m][n]);
    }
  }
  const unsigned ob = dst + (unsigned)((rb * 4 + wm) * NH + hd) * 8192;
  if (!VT) {
    const unsigned vo = (unsigned)((2 * (g & 1) + (g >> 1)) * 16 + fr) * 16;
#pragma unroll
    for (int m = 0; m < MT; ++m)
#pragma unroll
      for (int u = 0; u < 2; ++u) stx16(rs, vo, ob + (m * 4 + wn * 2 + u) * 1024, widen(acc[m][2 * u], acc[m][2 * u + 1]));
  } else {
#pragma unroll
    for (int n = 0; n < NT; ++n) {
      const f32x4 a = acc[0][n], b = acc[1][n];
      stx16(rs, lane * 16, ob + (wn * 4 + n) * 1024, u32x4{pack2(a[0], a[1]), pack2(a[2], a[3]), pack2(b[0], b[1]), pack2(b[2], b[3])});
    }
  }
}

constexpr int P_S2 = 0, P_S8 = 32, P_S7 = 64, P_S4 = 120, P_S5 = 136, P_S1 = 160, P_S3 = 288;
__device__ __forceinline__ void stage_attn(__amdgpu_buffer_rsrc_t rs, int wk, int grp, int kq, unsigned& cap) {
  const int tid = threadIdx.x, lane = tid & 63, w = __builtin_amdgcn_readfirstlane(tid >> 6), fr = lane & 15, g = lane >> 4;
  const int h = kq * 8 + w;
  const unsigned l16 = lane * 16;
  for (int seg = grp; seg < NSEG; seg += 4) {
    wait_ge(rs, (unsigned)(lane % 3) * 2048, F_TL + (unsigned)((seg >> 2) * 32 + h) * 4, 1u, cap);
    const unsigned ub = (unsigned)(seg * NH + h);
    f32x4 s0[2], s1[2], s2;
    s0[0] = s0[1] = s1[0] = s1[1] = s2 = f32x4{0.f, 0.f, 0.f, 0.f};
    bf16x8 q0[4], q1[4], vf[8];
#pragma unroll
    for (int ks = 0; ks < 4; ++ks) {
      q0[ks] = __builtin_bit_cast(bf16x8, ldx16(rs, l16, WS_Q + ub * 8192 + ks * 1024)); q1[ks] = __builtin_bit_cast(bf16x8, ldx16(rs, l16, WS_Q + ub * 8192 + 4096 + ks * 1024));
      const bf16x8 k0 = __builtin_bit_cast(bf16x8, ldx16(rs, l16, WS_K + ub * 8192 + ks * 1024)), k1 = __builtin_bit_cast(bf16x8, ldx16(rs, l16, WS_K + ub * 8192 + 4096 + ks * 1024));
      s1[0] = mfma32(k0, q0[ks], s1[0]); s1[1] = mfma32(k0, q1[ks], s1[1]); s2 = mfma32(k1, q1[ks], s2);
    }
    const unsigned ev = (unsigned)(fr >> 3) * 1024 + (unsigned)(g * 16 + 8 * ((fr >> 2) & 1) + (fr & 3)) * 16;
#pragma unroll
    for (int i = 0; i < 8; ++i) vf[i] = __builtin_bit_cast(bf16x8, ldx16(rs, ev, WS_VT + ub * 8192 + (i >> 1) * 2048 + (i & 1) * 64));
    wait_ge(rs, 0, F_CH + h * 128, (unsigned)seg + 1u, cap);
    u32x2 vmf[8];
    const unsigned em = (unsigned)(fr >> 3) * 256 + (unsigned)(g & 1) * 128 + (unsigned)(8 * ((fr >> 2) & 1) + (fr & 3)) * 8;
    for (;;) {
      asm volatile("" ::: "memory");
      unsigned mn = 0xFFFFFFFFu;
#pragma unroll
      for (int i = 0; i < 8; ++i) { vmf[i] = ldx8(rs, em, X_VM + ub * 2048 + (i >> 1) * 512 + (i & 1) * 32); }
#pragma unroll
      for (int ks = 0; ks < 4; ++ks) {
        const bf16x8 kmf = __builtin_bit_cast(bf16x8, ldx16(rs, l16, WS_KM + ub * 4096 + ks * 1024));
        if (ks == 0) { s0[0] = mfma32(kmf, q0[ks], f32x4{0.f, 0.f, 0.f, 0.f}); s0[1] = mfma32(kmf, q1[ks], f32x4{0.f, 0.f, 0.f, 0.f}); }
        else { s0[0] = mfma32(kmf, q0[ks], s0[0]); s0[1] = mfma32(kmf, q1[ks], s0[1]); }
      }
#pragma unroll
      for (int i = 0; i < 8; ++i) mn = min(mn, min(vmf[i][0], vmf[i][1]));
      if (__all(mn != 0u) || cap == 0u) break;
      cap = __builtin_amdgcn_readfirstlane(cap - 1u);
    }
    cap = __builtin_amdgcn_readfirstlane(cap);
    const float scale = 0.08838834764831845f, L2E = 1.4426950408889634f;
    u32x2 pm[2]; u32x4 pt[2];
#pragma unroll
    for (int t = 0; t < 2; ++t) {
      float a[4], b[4], c[4];
      float mx = -3.0e38f;
#pragma unroll
      for (int r = 0; r < 4; ++r) {
        const int j = 4 * g + r;
        a[r] = (g < 2) ? rbf(rbf(s0[t][r]) * scale) : -3.0e38f;
        b[r] = (t == 1 || j <= fr) ? rbf(rbf(s1[t][r]) * scale) : -3.0e38f;
        c[r] = (t == 1 && j <= fr) ? rbf(rbf(s2[r]) * scale) : -3.0e38f;
        mx = fmaxf(mx, fmaxf(a[r], fmaxf(b[r], c[r])));
      }
      mx = gmax(mx);
      float sum = 0.f;
#pragma unroll
      for (int r = 0; r < 4; ++r) {
        a[r] = __builtin_amdgcn_exp2f((a[r] - mx) * L2E); b[r] = __builtin_amdgcn_exp2f((b[r] - mx) * L2E); c[r] = __builtin_amdgcn_exp2f((c[r] - mx) * L2E);
        sum += a[r] + b[r] + c[r];
      }
      const float inv = __builtin_amdgcn_rcpf(gsum(sum));
      pm[t] = u32x2{pack2(a[0] * inv, a[1] * inv), pack2(a[2] * inv, a[3] * inv)};
      pt[t] = u32x4{pack2(b[0] * inv, b[1] * inv), pack2(b[2] * inv, b[3] * inv), pack2(c[0] * inv, c[1] * inv), pack2(c[2] * inv, c[3] * inv)};
    }
#pragma unroll
    for (int u = 0; u < 4; ++u) {
      f32x4 o[2][2];
#pragma unroll
      for (int bq = 0; bq < 2; ++bq)
#pragma unroll
        for (int t = 0; t < 2; ++t) {
          f32x4 z = {0.f, 0.f, 0.f, 0.f};
          z = mfma16(vmf[2 * u + bq], pm[t], z);
          o[bq][t] = mfma32(vf[2 * u + bq], __builtin_bit_cast(bf16x8, pt[t]), z);
        }
#pragma unroll
      for (int t = 0; t < 2; ++t)
        stx16(rs, l16, WS_O + (unsigned)seg * 262144 + (unsigned)((h * 4 + u) * 2 + t) * 1024,
              u32x4{pack2(o[0][t][0], o[0][t][1]), pack2(o[0][t][2], o[0][t][3]), pack2(o[1][t][0], o[1][t][1]), pack2(o[1][t][2], o[1][t][3])});
    }
    WAIT_V(0);
    if (lane == 0) stx4(rs, 0, F_ST + (unsigned)(P_S1 + grp * 32 + h) * 128, (unsigned)seg + 1u);
    WST(rs, wk, 16 + seg);
  }
}

enum { SG_WO = 0, SG_W1 = 1, SG_W2 = 2, SG_W3 = 3, SG_W4 = 4 };
constexpr int G_XCH = 131072, G_RDY = G_XCH + 24576;
template <int SG>
__device__ __forceinline__ void stage_gemm(LAS unsigned char* lds, __amdgpu_buffer_rsrc_t rs, const u16* Wg, int wk, int kq, int cb, unsigned& cap) {
  constexpr bool WIDE = (SG == SG_WO || SG == SG_W4);
  constexpr int KST = (SG == SG_W4) ? 28 : 32;
  constexpr int NP = WIDE ? 2 : 4, KSW = KST / NP;
  constexpr int CH = WIDE ? 4 : 2;
  constexpr int NCOL = WIDE ? 128 : 64;
  constexpr int LDW = (SG == SG_W3) ? FF : D_;
  constexpr int WROWS = (SG == SG_WO) ? HD : (SG == SG_W4 ? FF : D_);
  constexpr int NBLK = (SG == SG_WO || SG == SG_W1) ? 64 : 65;
  constexpr unsigned ABASE = SG == SG_WO ? WS_O : SG == SG_W1 ? WS_G : SG == SG_W2 ? WS_H1S : SG == SG_W3 ? WS_X2 : WS_U;
  constexpr unsigned ASTR = SG == SG_WO ? 262144u : SG == SG_W4 ? 229376u : 65536u;
  const unsigned myword = F_ST + (unsigned)((SG == SG_WO ? P_S2 + kq * 8 : SG == SG_W4 ? P_S8 + kq * 8 : SG == SG_W1 ? P_S4 : SG == SG_W2 ? P_S5 : P_S7) + cb) * 128u;
  const int tid = threadIdx.x, lane = tid & 63, w = __builtin_amdgcn_readfirstlane(tid >> 6), fr = lane & 15, g = lane >> 4;
  const int cg = WIDE ? (w & 3) : (w & 1), kp = WIDE ? (w >> 2) : (w >> 1);
  const unsigned lbase = (unsigned)(size_t)lds, l16 = lane * 16;
  const int col0 = cb * NCOL + cg * 32;
  bf16x8 Wf[2][KSW];
  {
    const __amdgpu_buffer_rsrc_t rsW = mk_rs(Wg, (unsigned)WROWS * LDW * 2u);
    const int ub = lane >> 3, tu = 4 * (g & 1) + (fr >> 2);
    const unsigned vB = (unsigned)(((ub & 3) + 8 * (ub >> 2)) * LDW + 16 * (((lane >> 1) & 3) ^ (ub >> 1)) + 8 * (lane & 1)) * 2u;
    const unsigned sB = (unsigned)((kq * KST * 32 + 32 * (w >> 2) + 16 * ((w >> 1) & 1) + 4 * (w & 1)) * LDW + cb * NCOL) * 2u;
    constexpr int TB = NCOL * 128;
    const int boff = kp * 4 * TB + (cg >> 1) * 8192 + (g >> 1) * 2048 + tu * 128 + (((tu >> 1) ^ (2 * (cg & 1))) << 5) + (fr & 3) * 8;
#pragma unroll
    for (int ps = 0; ps < (WIDE ? 2 : 1); ++ps) {
      BAR();
#pragma unroll
      for (int hh = 0; hh < NP; ++hh)
#pragma unroll
        for (int i = 0; i < 4; ++i)
          if (4 * ps + i < KSW / 2) {
            const unsigned so = sB + (unsigned)((hh * (KSW / 2) + 4 * ps + i) * 64 * LDW) * 2u, d = lbase + (hh * 4 + i) * TB + w * 1024;
            dma16(rsW, vB, so, d);
            if (WIDE) dma16(rsW, vB, so + 128, d + 8192);
          }
      WAIT_V(0); BAR();
#pragma unroll
      for (int i = 0; i < 4; ++i)
        if (4 * ps + i < KSW / 2) {
#pragma unroll
          for (int ks = 0; ks < 2; ++ks)
#pragma unroll
            for (int n = 0; n < 2; ++n) {
              LAS unsigned char* q = lds + ((boff ^ (n << 5)) + i * TB + ks * 4096);
              Wf[n][(4 * ps + i) * 2 + ks] = cat8(trread(q), trread(q + 1024));
            }
        }
      WAIT_L0();
    }
    BAR();
  }
  f32x4 cs[2];
  if (SG == SG_W3) {
    const u32x4 one4 = {0x3F803F80u, 0x3F803F80u, 0x3F803F80u, 0x3F803F80u};
    cs[0] = cs[1] = f32x4{0.f, 0.f, 0.f, 0.f};
#pragma unroll
    for (int k = 0; k < KSW; ++k) { cs[0] = mfma32(Wf[0][k], __builtin_bit_cast(bf16x8, one4), cs[0]); cs[1] = mfma32(Wf[1][k], __builtin_bit_cast(bf16x8, one4), cs[1]); }
    *(LAS f32x4*)(lds + w * 2048 + l16) = cs[0]; *(LAS f32x4*)(lds + w * 2048 + 1024 + l16) = cs[1];
    LBAR();
    cs[0] = cs[1] = f32x4{0.f, 0.f, 0.f, 0.f};
#pragma unroll
    for (int i = 0; i < 4; ++i) { cs[0] += *(const LAS f32x4*)(lds + (i * 2 + cg) * 2048 + l16); cs[1] += *(const LAS f32x4*)(lds + (i * 2 + cg) * 2048 + 1024 + l16); }
    LBAR();
  }
  auto pvo = [&](int b) -> unsigned {
    if (SG == SG_WO) return (unsigned)(P_S1 + (b & 3) * 32 + kq * 8 + (lane & 7)) * 128u;
    if (SG == SG_W1) return (unsigned)(P_S3 + (b & 3) * 32 + (lane & 31)) * 128u;
    if (SG == SG_W2) return (unsigned)(P_S4 + (lane & 15)) * 128u;
    if (SG == SG_W3) return (unsigned)(P_S5 + (lane & 15)) * 128u;
    return (unsigned)(P_S7 + kq * 14 + ((lane & 31) % 14)) * 128u;
  };
  auto need = [&](int b) -> unsigned { return (SG == SG_W2 && b == 64) ? 64u : (unsigned)b + 1u; };
  const unsigned vA = (SG == SG_W1) ? (unsigned)((lane >> 3) * 2048 + (((lane & 7) ^ (lane >> 3)) << 4)) : l16;
  auto piece = [&](int b, int i) {
    unsigned kb;
    if (WIDE) kb = (unsigned)((i & 1) * KSW * 2 + min((i >> 1) * 8 + w, KSW * 2 - 1));
    else kb = (unsigned)(((i & 1) * 2 + (w >> 2)) * 16 + (i >> 1) * 4 + (w & 3));
    const unsigned so = (SG == SG_W1) ? (kb & 3) * 16384u + (kb >> 2) * 128u : kb * 1024u;
    dma16x(rs, vA, ABASE + (unsigned)b * ASTR + (unsigned)kq * (KST * 2048) + so, lbase + (b & 1) * 65536 + kb * 1024);
  };
  auto fetch = [&](int b) {
#pragma unroll
    for (int i = 0; i < 8; ++i) piece(b, i);
  };
  constexpr int NS = (SG == SG_W1 || SG == SG_W3) ? 1 : 2;
  const int row = 16 * (kp & 1) + fr, co = 16 * (g & 1) + 8 * (g >> 1), c8 = col0 + co;
  const unsigned aoff = (SG == SG_W1) ? (unsigned)(fr * 128 + ((g ^ (fr & 7)) << 4)) : l16;
  WST(rs, wk, 10);
#define SEG(i)
  int mode = 0; bool ahead = false;
  for (int b = 0; b < NBLK; ++b) {
    if (mode == 2) { if (WIDE || w < 4) wait_v<NS>(); else wait_v<0>(); BAR(); }
    else if (mode == 0) {
      if (w == 0) wait_ge(rs, pvo(b), F_ST, need(b), cap);
      BAR();
      fetch(b);
    }
    SEG(0);
    f32x4 acc[2][2];
    acc[0][0] = acc[0][1] = acc[1][0] = acc[1][1] = f32x4{0.f, 0.f, 0.f, 0.f};
    LAS unsigned char* ab = lad(aoff, lbase + (b & 1) * 65536 + kp * (KSW * 2048));
    LAS unsigned char* ab1 = ab;
    if (SG == SG_W1) ab1 = lad<64>(aoff, lbase + (b & 1) * 65536 + kp * (KSW * 2048));
    bf16x8 af[2][CH][2];
    unsigned peek; u32x4 res, st[4];
#pragma unroll
    for (int c = 0; c < 5; ++c) {
      if (c < 4) {
        if (mode != 2) { if (c == 0) wait_v<6>(); else if (c == 1) wait_v<4>(); else if (c == 2) wait_v<2>(); else wait_v<0>(); BAR(); }
#pragma unroll
        for (int k = 0; k < CH; ++k) {
          const int kk = CH * c + k;
          if (kk < KSW) {
            if (SG == SG_W1) { LAS unsigned char* q = ((kk & 1) ? ab1 : ab) + (kk >> 1) * 4096; af[c & 1][k][0] = *(const LAS bf16x8*)(q); af[c & 1][k][1] = *(const LAS bf16x8*)(q + 2048); }
            else { af[c & 1][k][0] = *(const LAS bf16x8*)(ab + kk * 2048); af[c & 1][k][1] = *(const LAS bf16x8*)(ab + kk * 2048 + 1024); }
          }
        }
      }
      if (c == 3) {
        asm volatile("buffer_load_dword %0, %1, %2, %3 offen sc1" : "=v"(peek) : "v"(w == 0 ? (lane < 32 ? pvo(b + 1) : pvo(b + 2)) : 0xF0000000u), "s"(rs), "s"(F_ST) : "memory");
        if (SG == SG_W2) {
          const int t = 32 * b - 2 + row;
          asm volatile("buffer_load_dwordx4 %0, %1, %2, %3 offen sc1" : "=v"(res) : "v"((t >= 0 && t < S_ && w < 4) ? (unsigned)(t * D_ + c8) * 2u : 0xF0000000u), "s"(rs), "s"(WS_H2) : "memory");
        }
        if (SG == SG_W3) {
          const unsigned vo = w < 4 ? (unsigned)(g * 2048 + row * 16) : 0xF0000000u, so = WS_ST5 + (unsigned)b * 8192;
          asm volatile("buffer_load_dwordx4 %0, %4, %5, %6 offen sc1\n\tbuffer_load_dwordx4 %1, %4, %5, %6 offen offset:512 sc1\n\t"
                       "buffer_load_dwordx4 %2, %4, %5, %6 offen offset:1024 sc1\n\tbuffer_load_dwordx4 %3, %4, %5, %6 offen offset:1536 sc1"
                       : "=&v"(st[0]), "=&v"(st[1]), "=&v"(st[2]), "=&v"(st[3]) : "v"(vo), "s"(rs), "s"(so) : "memory");
        }
      }
      SCHED();
      if (c > 0) {
#pragma unroll
        for (int k = 0; k < CH; ++k) {
          const int kk = CH * (c - 1) + k;
          if (kk < KSW) {
            acc[0][0] = mfma32(Wf[0][kk], af[(c - 1) & 1][k][0], acc[0][0]); acc[0][1] = mfma32(Wf[1][kk], af[(c - 1) & 1][k][0], acc[0][1]);
            acc[1][0] = mfma32(Wf[0][kk], af[(c - 1) & 1][k][1], acc[1][0]); acc[1][1] = mfma32(Wf[1][kk], af[(c - 1) & 1][k][1], acc[1][1]);
          }
          SCHED();
          if (ahead && (mode == 2) == (c < 3)) {
            const int q = ((c - 1) & 1) * 4;
            if (WIDE) piece(b + 1, q + k); else { piece(b + 1, q + 2 * k); piece(b + 1, q + 2 * k + 1); }
          }
          SCHED();
        }
        SCHED();
      }
    }
    SEG(1);
    if (WIDE) {
      const unsigned so = (SG == SG_WO ? WS_P2 : WS_P8) + (unsigned)(b * 8 + kq * 2 + kp) * 65536u, vo = (unsigned)(fr * D_ + c8) * 2u;
      stx16(rs, vo, so, widen(acc[0][0], acc[0][1])); stx16(rs, vo, so + 32768, widen(acc[1][0], acc[1][1]));
      if (ahead && mode != 2) wait_v<8 + NS>(); else wait_v<NS>();
    } else {
#pragma unroll
      for (int mt = 0; mt < 2; ++mt)
        if (kp != mt) {
          LAS unsigned char* xw = lad(l16, lbase + G_XCH + (((kp > mt ? kp - 1 : kp) * 2 + mt) * 2 + cg) * 2048);
          *(LAS f32x4*)(xw) = acc[mt][0]; *(LAS f32x4*)(xw + 1024) = acc[mt][1];
        }
      if (ahead && mode != 2) wait_v<8>(); else wait_v<0>();
    }
    asm volatile("" : "+v"(peek));
    if (SG == SG_W2) asm volatile("" : "+v"(res));
    if (SG == SG_W3) asm volatile("" : "+v"(st[0]), "+v"(st[1]), "+v"(st[2]), "+v"(st[3]));
    if (w == 0) {
      const unsigned long long ok = __ballot(peek >= (lane < 32 ? need(b + 1) : need(b + 2)));
      const unsigned r = (((unsigned)ok == 0xFFFFFFFFu && b + 1 < NBLK) ? 1u : 0u) | (((unsigned)(ok >> 32) == 0xFFFFFFFFu && b + 2 < NBLK) ? 2u : 0u);
      if (lane == 0) *(LAS unsigned*)(lds + G_RDY) = r;
    }
    SEG(2);
    LBAR();
    SEG(3);
    if (mode == 2 && tid == 448) stx4(rs, 0, myword, (unsigned)b);
    const unsigned rdy = __builtin_amdgcn_readfirstlane(*(const LAS unsigned*)(lds + G_RDY));
    if (!WIDE && w < 4) {
      LAS unsigned char* xr = lad(l16, lbase + G_XCH + w * 2048);
      f32x4 y0 = kp ? acc[1][0] : acc[0][0], y1 = kp ? acc[1][1] : acc[0][1];
#pragma unroll
      for (int i = 0; i < 3; ++i) { y0 += *(const LAS f32x4*)(xr + i * 8192); y1 += *(const LAS f32x4*)(xr + i * 8192 + 1024); }
      const unsigned fo = (unsigned)((c8 >> 5) * 2048 + ((c8 >> 3) & 3) * 256);
      if (SG == SG_W1) {
        u32x4 v = widen(y0, y1);
#pragma unroll
        for (int i = 0; i < 4; ++i) v[i] = pack2(gelu_tanh(lo2f(v[i])), gelu_tanh(hi2f(v[i])));
        const int grp = cb >> 2, sh = (grp & 1) ? -(1 + (grp >> 1)) : (1 + (grp >> 1));
        const int rho = 32 * b + row + sh + 2;
        const unsigned vo = (rho >= 2 && rho < S_ + 2) ? (unsigned)(rho >> 5) * 65536u + fo + (unsigned)((rho >> 4) & 1) * 1024u + (unsigned)(rho & 15) * 16u : 0xF0000000u;
        stx16(rs, vo, WS_H1S, v);
      } else if (SG == SG_W2) {
        u32x4 v = widen(y0, y1);
        float z[8];
#pragma unroll
        for (int i = 0; i < 4; ++i) { v[i] = pack2(lo2f(res[i]) + lo2f(v[i]), hi2f(res[i]) + hi2f(v[i])); z[2 * i] = lo2f(v[i]); z[2 * i + 1] = hi2f(v[i]); }
        stx16(rs, fo + kp * 1024 + fr * 16, WS_X2 + (unsigned)b * 65536u, v);
        const float mw = gsum(((z[0] + z[1]) + (z[2] + z[3])) + ((z[4] + z[5]) + (z[6] + z[7]))) * (1.0f / 32.0f);
        float q = 0.f;
#pragma unroll
        for (int i = 0; i < 8; ++i) { const float d = z[i] - mw; q += d * d; }
        q = gsum(q);
        if (g == 0) stx8(rs, (unsigned)(cb * 512 + row * 16 + cg * 8), WS_ST5 + (unsigned)b * 8192, u32x2{f2u(mw), f2u(q)});
      } else {
        float ms = 0.f;
#pragma unroll
        for (int i = 0; i < 4; ++i) ms += u2f(st[i][0]) + u2f(st[i][2]);
        const float mean = gsum(ms) * (1.0f / 32.0f);
        float q = 0.f;
#pragma unroll
        for (int i = 0; i < 4; ++i) { const float d0 = u2f(st[i][0]) - mean, d1 = u2f(st[i][2]) - mean; q += u2f(st[i][1]) + u2f(st[i][3]) + 32.0f * (d0 * d0 + d1 * d1); }
        const float var = gsum(q) * (1.0f / 1024.0f);
        const float m = rbf(mean), isd = __builtin_amdgcn_rcpf(rbf(__builtin_amdgcn_sqrtf(rbf(rbf(var) + 1e-5f))));
        y0 = (y0 - m * cs[0]) * isd; y1 = (y1 - m * cs[1]) * isd;
        u32x4 v = widen(y0, y1);
#pragma unroll
        for (int i = 0; i < 4; ++i) v[i] = pack2(gelu_tanh(lo2f(v[i])), gelu_tanh(hi2f(v[i])));
        stx16(rs, fo + kp * 1024 + fr * 16, WS_U + (unsigned)b * 229376u, v);
      }
    }
    if (ahead) mode = 2;
    else {
      if (rdy & 1u) { fetch(b + 1); wait_v<8>(); mode = 1; } else { wait_v<0>(); mode = 0; }
      BAR();
      if (tid == 448) stx4(rs, 0, myword, (unsigned)b + 1u);
    }
    ahead = mode != 0 && (rdy & 2u);
    WST(rs, wk, 16 + b);
    SEG(4);
  }
}

__device__ __forceinline__ void wait32(__amdgpu_buffer_rsrc_t rs, int first, int mine, int lane, unsigned need, unsigned& cap) {
  wait_ge(rs, 0, F_ST + (unsigned)(first + mine) * 128u, need, cap);
  wait_ge(rs, (unsigned)(lane & 31) * 128u, F_ST + (unsigned)first * 128u, need, cap);
}
__device__ __forceinline__ void sum_partials(__amdgpu_buffer_rsrc_t rs, unsigned l16, unsigned so, float (&a)[16]) {
  u32x4 pp[8][2];
#pragma unroll
  for (int k = 0; k < 8; ++k) { pp[k][0] = ldx16(rs, l16, so + k * 65536u); pp[k][1] = ldx16(rs, l16, so + k * 65536u + 1024); }
#pragma unroll
  for (int i = 0; i < 8; ++i) {
    f32x2 t = {0.f, 0.f};
#pragma unroll
    for (int k = 0; k < 8; ++k) t += f32x2{lo2f(pp[k][i >> 2][i & 3]), hi2f(pp[k][i >> 2][i & 3])};
    a[2 * i] = rbf(t[0]); a[2 * i + 1] = rbf(t[1]);
  }
}
__device__ __forceinline__ void put_row(__amdgpu_buffer_rsrc_t rs, unsigned l16, unsigned so, const float (&v)[16]) {
  stx16(rs, l16, so, u32x4{pack2(v[0], v[1]), pack2(v[2], v[3]), pack2(v[4], v[5]), pack2(v[6], v[7])});
  stx16(rs, l16, so + 1024, u32x4{pack2(v[8], v[9]), pack2(v[10], v[11]), pack2(v[12], v[13]), pack2(v[14], v[15])});
}
__device__ __forceinline__ void load_vec16(const float* p, int lane, float (&v)[16]) {
#pragma unroll
  for (int i = 0; i < 4; ++i) { const f32x4 t = *(const f32x4*)(p + (i >> 1) * 512 + lane * 8 + (i & 1) * 4); v[4 * i] = t[0]; v[4 * i + 1] = t[1]; v[4 * i + 2] = t[2]; v[4 * i + 3] = t[3]; }
}

__device__ __forceinline__ void stage_mid(__amdgpu_buffer_rsrc_t rs, const u16* x, const float* mod, int wk, int cu, unsigned& cap) {
  const int tid = threadIdx.x, lane = tid & 63, w = __builtin_amdgcn_readfirstlane(tid >> 6);
  const int grp = w >> 1, rl = cu * 2 + (w & 1);
  const unsigned l16 = lane * 16;
  float ga[16], sc[16], sh[16];
  load_vec16(mod + 2048, lane, ga); load_vec16(mod + 4096, lane, sc); load_vec16(mod + 3072, lane, sh);
#pragma unroll
  for (int i = 0; i < 16; ++i) sc[i] = rbf(1.0f + sc[i]);
  for (int seg = grp; seg < NSEG; seg += 4) {
    const int row = seg * 32 + rl;
    float v[16], a[16];
    load_row16(x + (size_t)row * D_, lane, v);
    wait32(rs, P_S2, rl, lane, (unsigned)seg + 1u, cap);
    sum_partials(rs, l16, WS_P2 + (unsigned)seg * 524288u + (unsigned)rl * 2048, a);
#pragma unroll
    for (int i = 0; i < 16; ++i) v[i] = rbf(v[i] + rbf(ga[i] * a[i]));
    put_row(rs, l16, WS_X1 + (unsigned)row * 2048, v);
    ln_row(v);
#pragma unroll
    for (int i = 0; i < 16; ++i) v[i] = rbf(rbf(v[i] * sc[i]) + sh[i]);
    put_row(rs, l16, WS_H2 + (unsigned)row * 2048, v);
    ln_row(v);
    put_row(rs, l16, WS_G + (unsigned)row * 2048, v);
    WAIT_V(0);
    if (lane == 0) stx4(rs, 0, F_ST + (unsigned)(P_S3 + grp * 32 + rl) * 128, (unsigned)seg + 1u);
    WST(rs, wk, 16 + seg);
  }
}

__device__ __forceinline__ void stage_out(__amdgpu_buffer_rsrc_t rs, u16* out, const float* mod, int wk, int cu, unsigned& cap) {
  const int tid = threadIdx.x, lane = tid & 63, w = __builtin_amdgcn_readfirstlane(tid >> 6);
  const int grp = w >> 2, rl = cu * 4 + (w & 3);
  const unsigned l16 = lane * 16;
  float gb[16];
  load_vec16(mod + 5120, lane, gb);
  for (int b = grp; b < 65; b += 2) {
    const int t = 32 * b - 2 + rl;
    if (t < 0 || t >= S_) continue;
    wait32(rs, P_S8, rl, lane, (unsigned)b + 1u, cap);
    const unsigned xo = (unsigned)((lane >> 2) * 2048 + (lane & 3) * 256), xs = WS_X2 + (unsigned)b * 65536u + (unsigned)((rl >> 4) * 1024 + (rl & 15) * 16);
    const u32x4 x2a = ldx16(rs, xo, xs), x2b = ldx16(rs, xo, xs + 32768);
    const u32x4 x1a = ldx16(rs, l16, WS_X1 + (unsigned)t * 2048), x1b = ldx16(rs, l16, WS_X1 + (unsigned)t * 2048 + 1024);
    float a[16];
    sum_partials(rs, l16, WS_P8 + (unsigned)b * 524288u + (unsigned)rl * 2048, a);
#pragma unroll
    for (int i = 0; i < 4; ++i) {
      a[2 * i] = lo2f(x1a[i]) + rbf(gb[2 * i] * rbf(lo2f(x2a[i]) + a[2 * i])); a[2 * i + 1] = hi2f(x1a[i]) + rbf(gb[2 * i + 1] * rbf(hi2f(x2a[i]) + a[2 * i + 1]));
      a[8 + 2 * i] = lo2f(x1b[i]) + rbf(gb[8 + 2 * i] * rbf(lo2f(x2b[i]) + a[8 + 2 * i])); a[9 + 2 * i] = hi2f(x1b[i]) + rbf(gb[9 + 2 * i] * rbf(hi2f(x2b[i]) + a[9 + 2 * i]));
    }
    store_row16(out + (size_t)t * D_, lane, a);
    WST(rs, wk, 16 + b);
  }
}

__device__ __forceinline__ void worker(const MainP& p, LAS unsigned char* lds, int xw, int j) {
  const int tid = threadIdx.x, lane = tid & 63, w = __builtin_amdgcn_readfirstlane(tid >> 6);
  const __amdgpu_buffer_rsrc_t rs = mk_rs(p.ws, WS_END);
  const float* mod = (const float*)(p.ws + WS_MOD);
  unsigned cap = SPIN_CAP;
  const int wk = xw * 32 + j;
  WST(rs, wk, 0);
  {
    const int c = j * 6 + xw;
    for (int row = c * 8 + w; row < S_; row += 1536) {
      float v[16];
      load_row16(p.x + (size_t)row * D_, lane, v);
      ln_row(v);
      modulate(v, mod + 1024, mod, lane);
      stx16(rs, (unsigned)lane * 16, WS_H + (unsigned)row * 2048, u32x4{pack2(v[0], v[1]), pack2(v[2], v[3]), pack2(v[4], v[5]), pack2(v[6], v[7])});
      stx16(rs, (unsigned)lane * 16, WS_H + (unsigned)row * 2048 + 1024, u32x4{pack2(v[8], v[9]), pack2(v[10], v[11]), pack2(v[12], v[13]), pack2(v[14], v[15])});
    }
    publish(rs, F_LN + c * 128, 1u);
    WST(rs, wk, 1);
  }
  for (int r = 0; r < 8; ++r) {
    int rb, mat, hd;
    if (r < 5) { rb = 3 * r + wk % 3; mat = (wk / 3) & 1; hd = (wk / 3) >> 1; }
    else {
      const int i = (r - 5) * 192 + wk;
      if (i < 384) { rb = 6 * (r - 5) + wk % 6; mat = 2; hd = wk / 6; }
      else if (i < 512) { rb = 12 + (wk & 3); mat = 2; hd = wk >> 2; }
      else { rb = 15; mat = wk & 1; hd = (wk - 128) >> 1; }
    }
    if (w == 0) wait_ge(rs, (unsigned)((rb % 12) * 16 + (lane & 15)) * 128u, F_LN, 1u, cap);
    BAR();
    if (mat == 1) proj_tile<true>(lds, rs, mk_rs(p.Wv, D_ * HD * 2), WS_VT, rb, hd);
    else proj_tile<false>(lds, rs, mk_rs(mat == 0 ? p.Wk : p.Wq, D_ * HD * 2), mat == 0 ? WS_K : WS_Q, rb, hd);
    publish(rs, F_TL + (unsigned)(mat * 512 + rb * 32 + hd) * 4, 1u);
    WST(rs, wk, 2 + r);
  }
  if (xw == 0) stage_gemm<SG_WO>(lds, rs, p.Wo, wk, j >> 3, j & 7, cap);
  else if (xw == 1) stage_gemm<SG_W4>(lds, rs, p.w4, wk, j >> 3, j & 7, cap);
  else if (xw < 4) {
    if (j < 28) stage_gemm<SG_W3>(lds, rs, p.w3, wk, 0, (xw - 2) * 28 + j, cap);
    else stage_out(rs, p.out, mod, wk, (xw - 2) * 4 + j - 28, cap);
  } else if (xw == 4) {
    if (j < 16) stage_gemm<SG_W1>(lds, rs, p.w1, wk, 0, j, cap);
    else stage_gemm<SG_W2>(lds, rs, p.w2, wk, 0, j - 16, cap);
  } else {
    if (j < 16) stage_attn(rs, wk, j >> 2, j & 3, cap);
    else stage_mid(rs, p.x, mod, wk, j - 16, cap);
  }
  if (cap == 0u && lane == 0) stx4(rs, 0, X_CTL, 1u);
}

#define ST_DECL
#define ST(i)
#define ST_FLUSH(cu, s)
#define POLLC 1
constexpr int R_W = 0, R_MEM = 131072, R_QK = R_MEM + 16384, R_KT = R_QK + 4096, R_RED = R_MEM + 16384, R_LDS = 163840;
__device__ __forceinline__ unsigned umin3(unsigned a, unsigned b, unsigned c) { return min(min(a, b), c); }
__device__ __forceinline__ unsigned umin4(u32x4 v) { return min(umin3(v[0], v[1], v[2]), v[3]); }
#define PSLEEP ""
#define PIECE(R0, R1, R2, R3, SO, CNT) \
  "s_waitcnt vmcnt(" #CNT ")\n\t" \
  "v_min3_u32 %[t], v" #R0 ", v" #R1 ", v" #R2 "\n\t" \
  "v_min_u32 %[t], %[t], v" #R3 "\n\t" \
  "v_cmp_eq_u32 vcc, 0, %[t]\n\t" \
  "s_or_b64 %[acc], %[acc], vcc\n\t" \
  "s_mov_b64 exec, vcc\n\t" \
  "buffer_load_dwordx4 v[" #R0 ":" #R3 "], %[vo], %[rs], %[" SO "] offen sc1\n\t" \
  "s_mov_b64 exec, -1\n\t"
#define FIRST(R0, R3, SO) "buffer_load_dwordx4 v[" #R0 ":" #R3 "], %[vo], %[rs], %[" SO "] offen sc1\n\t"
#define POLL_TAIL \
  "s_cmp_eq_u64 %[acc], 0\n\t" \
  "s_cbranch_scc1 2f\n\t" \
  PSLEEP \
  "s_sub_u32 %[cap], %[cap], 1\n\t" \
  "s_cbranch_scc0 1b\n\t" \
  "s_mov_b32 %[cap], 0\n\t" \
  "2:\n\t" \
  "s_waitcnt vmcnt(0)"
template <int HI> __device__ __forceinline__ void poll2(__amdgpu_buffer_rsrc_t rs, unsigned vo, unsigned s0, u32x4& a0, u32x4& a1, unsigned& cap) {
  unsigned t; unsigned long long acc; const unsigned s1 = s0 + 1024;
  if (HI) asm volatile("s_nop 4\n\t" FIRST(248, 251, "s0") FIRST(252, 255, "s1") "1:\n\ts_mov_b64 %[acc], 0\n\t" PIECE(248, 249, 250, 251, "s0", 1) PIECE(252, 253, 254, 255, "s1", 1) POLL_TAIL
               : "=&{v[248:251]}"(a0), "=&{v[252:255]}"(a1), [t] "=&v"(t), [acc] "=&s"(acc), [cap] "+s"(cap) : [vo] "v"(vo), [rs] "s"(rs), [s0] "s"(s0), [s1] "s"(s1) : "vcc", "scc", "memory");
  else asm volatile("s_nop 4\n\t" FIRST(240, 243, "s0") FIRST(244, 247, "s1") "1:\n\ts_mov_b64 %[acc], 0\n\t" PIECE(240, 241, 242, 243, "s0", 1) PIECE(244, 245, 246, 247, "s1", 1) POLL_TAIL
               : "=&{v[240:243]}"(a0), "=&{v[244:247]}"(a1), [t] "=&v"(t), [acc] "=&s"(acc), [cap] "+s"(cap) : [vo] "v"(vo), [rs] "s"(rs), [s0] "s"(s0), [s1] "s"(s1) : "vcc", "scc", "memory");
}
__device__ __forceinline__ void poll4(__amdgpu_buffer_rsrc_t rs, unsigned vo, unsigned s0, unsigned s1, unsigned s2, unsigned s3, u32x4& a0, u32x4& a1, u32x4& a2, u32x4& a3, unsigned& cap) {
  unsigned t; unsigned long long acc;
  asm volatile("s_nop 4\n\t" FIRST(240, 243, "s0") FIRST(244, 247, "s1") FIRST(248, 251, "s2") FIRST(252, 255, "s3") "1:\n\ts_mov_b64 %[acc], 0\n\t"
               PIECE(240, 241, 242, 243, "s0", 3) PIECE(244, 245, 246, 247, "s1", 3) PIECE(248, 249, 250, 251, "s2", 3) PIECE(252, 253, 254, 255, "s3", 3) POLL_TAIL
               : "=&{v[240:243]}"(a0), "=&{v[244:247]}"(a1), "=&{v[248:251]}"(a2), "=&{v[252:255]}"(a3), [t] "=&v"(t), [acc] "=&s"(acc), [cap] "+s"(cap)
               : [vo] "v"(vo), [rs] "s"(rs), [s0] "s"(s0), [s1] "s"(s1), [s2] "s"(s2), [s3] "s"(s3) : "vcc", "scc", "memory");
}
__device__ __forceinline__ void poll1_finish(__amdgpu_buffer_rsrc_t rs, unsigned vo, unsigned s0, u32x2& a, unsigned& cap) {
  unsigned t;
  asm volatile("1:\n\t"
               "v_min_u32 %[t], v254, v255\n\t"
               "v_cmp_eq_u32 vcc, 0, %[t]\n\t"
               "s_cmp_eq_u64 vcc, 0\n\t"
               "s_cbranch_scc1 2f\n\t"
               "buffer_load_dwordx2 v[254:255], %[vo], %[rs], %[s0] offen sc1\n\t"
               "s_waitcnt vmcnt(0)\n\t"
               "s_sub_u32 %[cap], %[cap], 1\n\t"
               "s_cbranch_scc0 1b\n\t"
               "s_mov_b32 %[cap], 0\n\t"
               "2:\n\ts_nop 1"
               : "+{v[254:255]}"(a), [t] "=&v"(t), [cap] "+s"(cap) : [vo] "v"(vo), [rs] "s"(rs), [s0] "s"(s0) : "vcc", "scc", "memory");
}
__device__ __forceinline__ void gather_mem(__amdgpu_buffer_rsrc_t rs, unsigned lbase, int s, int half, int w, unsigned l16, unsigned& cap) {
  const unsigned so = (unsigned)X_MEMP + s * 65536 + half * 32768 + w * 2048;
  u32x4 a0, a1, b0, b1;
  poll4(rs, l16, so + half * 16384, so + half * 16384 + 1024, so + (half ^ 1) * 16384, so + (half ^ 1) * 16384 + 1024, a0, a1, b0, b1, cap);
  u32x4 r0, r1;
#pragma unroll
  for (int i = 0; i < 4; ++i) {
    const f32x2 x = f32x2{lo2f(a0[i]), hi2f(a0[i])} + f32x2{lo2f(b0[i]), hi2f(b0[i])};
    const f32x2 y = f32x2{lo2f(a1[i]), hi2f(a1[i])} + f32x2{lo2f(b1[i]), hi2f(b1[i])};
    r0[i] = pack2(x[0], x[1]); r1[i] = pack2(y[0], y[1]);
  }
  LAS unsigned char* d = lad(l16, lbase + R_MEM + w * 2048);
  *(LAS u32x4*)(d) = r0;
  *(LAS u32x4*)(d + 1024) = r1;
}
__device__ __forceinline__ bf16x8 mem_frag(LAS unsigned char* mp, int chunk) { return cat8(trread(mp + chunk * 1024), trread(mp + chunk * 1024 + 512)); }
__device__ __forceinline__ bf16x16 w_frag(LAS unsigned char* p) {
  return cat16(cat8(trread(p), trread(p + 2048)), cat8(trread(p + 4096), trread(p + 6144)));
}
__device__ __forceinline__ f32x4 fold(f32x4 a, f32x4 b) {
  f32x4 r;
#pragma unroll
  for (int i = 0; i < 4; ++i) {
    const auto t = __builtin_amdgcn_permlane32_swap(f2u(a[i]), f2u(b[i]), false, false);
    r[i] = u2f(t[0]) + u2f(t[1]);
  }
  return r;
}

__device__ __forceinline__ void role_a(const MainP& p, LAS unsigned char* lds, int half, int hl) {
  const int tid = threadIdx.x, lane = tid & 63, w = __builtin_amdgcn_readfirstlane(tid >> 6);
  const int h = half * 16 + hl;
  const __amdgpu_buffer_rsrc_t rs = mk_rs(p.ws, WS_END);
  const unsigned lbase = (unsigned)(size_t)lds;
  const unsigned l16 = lane * 16, l8 = lane * 8;
  const int sidx = (lane & 8) ? 0xEEEE : 0x4444;
  bf16x16 W[24];
  unsigned woff, qoff;
  {
    const int fr = lane & 15, g = lane >> 4, tu = 4 * (g & 1) + (fr >> 2), ub = lane >> 3;
    woff = (w >> 1) * 8192 + (g >> 1) * 1024 + tu * 128 + (((2 * (w & 1)) ^ (tu >> 1)) << 5) + (fr & 3) * 8;
    qoff = R_QK + (g >> 1) * 256 + (fr & 1) * 128 + (8 * (g & 1) + (fr >> 2)) * 8;
    const __amdgpu_buffer_rsrc_t rq = mk_rs(p.Wq, D_ * HD * 2), rk = mk_rs(p.Wk, D_ * HD * 2);
    const unsigned vo = (unsigned)(ub * HD + 16 * (((lane >> 1) & 3) ^ (ub >> 1)) + 8 * (lane & 1)) * 2u;
    const unsigned so = (unsigned)(8 * w * HD + h * DH) * 2u;
    auto stage = [&](int c) {
      const unsigned d = lbase + R_W + (c & 3) * 32768 + w * 1024, sc = so + (unsigned)c * 64 * HD * 2;
      dma16(rq, vo, sc, d); dma16(rq, vo, sc + 128, d + 8192); dma16(rk, vo, sc, d + 16384); dma16(rk, vo, sc + 128, d + 24576);
    };
    stage(0); stage(1); stage(2); stage(3);
#pragma unroll
    for (int c = 0; c < 12; ++c) {
      WAIT_V(12); BAR();
      W[2 * c] = w_frag(lds + R_W + (c & 3) * 32768 + woff);
      W[2 * c + 1] = w_frag(lds + R_W + (c & 3) * 32768 + (woff ^ 32));
      LBAR();
      stage(c + 4);
    }
    WAIT_V(0); BAR();
  }
  unsigned cap = SPIN_CAP;
  wait_ge(rs, (l16 & 16) * 128, F_TL + h * 4, 1u, cap);
  dma16x(rs, l16, WS_K + h * 8192 + w * 1024, lbase + R_KT + w * 1024);
  unsigned tref = now32() - p.d2;
  ST_DECL
  for (int s = 0; s < NSEG; ++s) {
    ST(0);
    bf16x16 L0 = w_frag(lad(woff, lbase + R_W)), L1 = w_frag(lad<32>(woff, lbase + R_W));
    SCHED();
    wait_until(tref + p.d2);
    gather_mem(rs, lbase, s, half, w, l16, cap);
    ST(1);
    LBAR();
    tref = now32();
    ST(2);
    f32x4 c0 = {0.f, 0.f, 0.f, 0.f}, c1 = {0.f, 0.f, 0.f, 0.f};
    LAS unsigned char* mp = lad(l8, lbase + R_MEM);
    bf16x8 af[3];
    u32x4 vt0, vt1;
    u32x2 vmv;
    af[0] = mem_frag(mp, 12); af[1] = mem_frag(mp, 0);
#pragma unroll
    for (int i = 0; i < 16; ++i) {
      if (i + 2 < 16) { const int j = i + 2; af[j % 3] = mem_frag(mp, (j & 3) == 0 ? 12 + (j >> 2) : 3 * (j >> 2) + (j & 3) - 1); }
      if ((i & 3) == 0) {
        c0 = smf(af[i % 3], L0, c0, sidx); c1 = smf(af[i % 3], L1, c1, sidx);
        if (i < 12) { L0 = w_frag(lad(woff, lbase + R_W + ((i >> 2) + 1) * 32768)); L1 = w_frag(lad<32>(woff, lbase + R_W + ((i >> 2) + 1) * 32768)); }
        else {
          const unsigned vo = (unsigned)WS_VT + (unsigned)(s * NH + h) * 8192 + (w & 3) * 2048;
          vt0 = ldx16(rs, l16, vo); vt1 = ldx16(rs, l16, vo + 1024);
        }
      } else {
        const int c = 3 * (i >> 2) + (i & 3) - 1;
        c0 = smf(af[i % 3], W[2 * c], c0, sidx); c1 = smf(af[i % 3], W[2 * c + 1], c1, sidx);
      }
      SCHED();
    }
    {
      const f32x4 f = fold(c0, c1);
      *(LAS u32x2*)(lad(l8, lbase + R_QK + w * 512)) = u32x2{pack2(f[0], f[1]), pack2(f[2], f[3])};
    }
    ST(3);
    if (w < 4) {
      const unsigned vso = (unsigned)X_VM + (unsigned)(s * NH + h) * 2048 + w * 512;
      bf16x8 k1[4], k2[4];
      LAS unsigned char* kb = lad(l16, lbase + R_KT);
#pragma unroll
      for (int ks = 0; ks < 2; ++ks) { k1[ks] = *(const LAS bf16x8*)(kb + ks * 1024); k2[ks] = *(const LAS bf16x8*)(kb + 4096 + ks * 1024); }
      LBAR();
      ST(4);
      vmv = ldx8(rs, l8, vso);
      f32x4 t0 = {0.f, 0.f, 0.f, 0.f}, t1 = t0, t2 = t0;
      LAS unsigned char* qb = lad(qoff, lbase);
      bf16x8 qf[4], kf[4];
#pragma unroll
      for (int ks = 0; ks < 2; ++ks) {
        qf[ks] = cat8(trread(qb + ks * 512), trread(qb + ks * 512 + 32));
        kf[ks] = cat8(trread(qb + 2048 + ks * 512), trread(qb + 2048 + ks * 512 + 32));
      }
      SCHED();
#pragma unroll
      for (int ks = 0; ks < 4; ++ks) {
        t1 = mfma32(k1[ks], qf[ks], t1); t2 = mfma32(k2[ks], qf[ks], t2); t0 = mfma32(kf[ks], qf[ks], t0);
        if (ks < 2) {
          qf[ks + 2] = cat8(trread(qb + (ks + 2) * 512), trread(qb + (ks + 2) * 512 + 32));
          k1[ks + 2] = *(const LAS bf16x8*)(kb + (ks + 2) * 1024); k2[ks + 2] = *(const LAS bf16x8*)(kb + 4096 + (ks + 2) * 1024);
          kf[ks + 2] = cat8(trread(qb + 2048 + (ks + 2) * 512), trread(qb + 2048 + (ks + 2) * 512 + 32));
        }
        SCHED();
      }
      ST(5);
      if (s + 1 < NSEG) {
        const float sl = 0.08838834764831845f * 1.4426950408889634f;
        float mx = fmaxf(fmaxf(fmaxf(t1[0], t1[1]), fmaxf(t1[2], t1[3])), fmaxf(fmaxf(t2[0], t2[1]), fmaxf(t2[2], t2[3])));
        mx = fmaxf(mx, fmaxf(fmaxf(t0[0], t0[1]), fmaxf(t0[2], t0[3])));
        const float nm = -gmax(mx) * sl;
#pragma unroll
        for (int r = 0; r < 4; ++r) { t1[r] = __builtin_amdgcn_exp2f(fmaf(t1[r], sl, nm)); t2[r] = __builtin_amdgcn_exp2f(fmaf(t2[r], sl, nm)); }
        const u32x4 pt = {pack2(t1[0], t1[1]), pack2(t1[2], t1[3]), pack2(t2[0], t2[1]), pack2(t2[2], t2[3])};
        f32x4 o0 = {0.f, 0.f, 0.f, 0.f}, o1 = o0;
        o0 = mfma32(__builtin_bit_cast(bf16x8, vt0), __builtin_bit_cast(bf16x8, pt), o0);
        o1 = mfma32(__builtin_bit_cast(bf16x8, vt1), __builtin_bit_cast(bf16x8, pt), o1);
#pragma unroll
        for (int r = 0; r < 4; ++r) t0[r] = __builtin_amdgcn_exp2f(fmaf(t0[r], sl, nm));
        const u32x2 pmv = {pack2(t0[0], t0[1]), pack2(t0[2], t0[3])};
        const u32x2 zz = {0u, 0u};
        const u32x2 pm0 = (l16 < 512) ? pmv : zz, pm1 = (l16 < 512) ? zz : pmv;
        const float ssum = ((t1[0] + t1[1]) + (t1[2] + t1[3])) + ((t2[0] + t2[1]) + (t2[2] + t2[3])) + 0.5f * ((t0[0] + t0[1]) + (t0[2] + t0[3]));
        const float inv = __builtin_amdgcn_rcpf(gsum(ssum));
        ST(6);
        poll1_finish(rs, l8, vso, vmv, cap);
        ST(7);
        o0 = mfma16n(vmv, pm0, o0);
        o1 = mfma16n(vmv, pm1, o1);
        if ((l16 & 128) == 0) {
          const u32x4 v = {nz(pack2(o0[0] * inv, o0[1] * inv)), nz(pack2(o0[2] * inv, o0[3] * inv)), nz(pack2(o1[0] * inv, o1[1] * inv)), nz(pack2(o1[2] * inv, o1[3] * inv))};
          stx16(rs, l16, (unsigned)X_OX + s * 65536 + half * 32768 + (hl * 2 + (w >> 1)) * 1024 + (w & 1) * 128, v);
        }
        ST(8);
      }
    } else {
      LBAR();
      LAS unsigned char* qb = lad(qoff, lbase + 2048 + (w - 4) * 512);
      const bf16x8 kf = cat8(trread(qb), trread(qb + 32));
      stx16(rs, l16, WS_KM + (unsigned)(s * NH + h) * 4096 + (w - 4) * 1024, __builtin_bit_cast(u32x4, kf));
      WAIT_V(0);
    }
    LBAR();
    if (w == 4 && l16 == 0) stx4(rs, 0, F_CH + h * 128, (unsigned)s + 1u);
    ST(9);
    ST_FLUSH(half * 32 + hl, s);
    if (s + 1 < NSEG) {
      if (((s + 1) & 3) == 0) wait_ge(rs, (l16 & 16) * 128, F_TL + (unsigned)(((s + 1) >> 2) * 32 + h) * 4, 1u, cap);
      dma16x(rs, l16, WS_K + (unsigned)((s + 1) * NH + h) * 8192 + w * 1024, lbase + R_KT + w * 1024);
    }
  }
  if (cap == 0u && l16 == 0) __hip_atomic_store((GAS unsigned*)(p.ws + X_CTL), 1u, __ATOMIC_RELAXED, __HIP_MEMORY_SCOPE_AGENT);
}

__device__ __forceinline__ void role_vb(const MainP& p, LAS unsigned char* lds, int half, int jb) {
  const int tid = threadIdx.x, lane = tid & 63, w = __builtin_amdgcn_readfirstlane(tid >> 6);
  const int h = half * 16 + jb;
  const __amdgpu_buffer_rsrc_t rs = mk_rs(p.ws, WS_END);
  const unsigned lbase = (unsigned)(size_t)lds;
  const unsigned l16 = lane * 16, l8 = lane * 8;
  const int sidx = (lane & 8) ? 0xEEEE : 0x4444;
  bf16x16 WV[16], WB[8];
  unsigned lo, roff;
  {
    const int fr = lane & 15, g = lane >> 4, tu = 4 * (g & 1) + (fr >> 2), ub = lane >> 3;
    lo = (g >> 1) * 1024 + tu * 128 + ((tu >> 1) << 5) + (fr & 3) * 8;
    roff = R_RED + w * 2048 + mem_slot(16 * (g >> 1) + fr) * 32 + (g & 1) * 16;
    const unsigned vcol = (unsigned)(16 * (((lane >> 1) & 3) ^ (ub >> 1)) + 8 * (lane & 1)) * 2u;
    const __amdgpu_buffer_rsrc_t rv = mk_rs(p.Wv, D_ * HD * 2), ro = mk_rs(p.Wo, D_ * HD * 2);
    const unsigned vv = (unsigned)(ub * HD) * 2u + vcol, sv = (unsigned)(8 * w * HD + h * DH) * 2u;
    auto stage = [&](int c) {
      const unsigned d = lbase + R_W + (c & 7) * 16384 + w * 1024, sc = sv + (unsigned)c * 64 * HD * 2;
      dma16(rv, vv, sc, d); dma16(rv, vv, sc + 128, d + 8192);
    };
#pragma unroll
    for (int c = 0; c < 8; ++c) stage(c);
    const int vo = (w >> 2) * 8192 + (lo ^ ((w & 3) << 5));
#pragma unroll
    for (int c = 0; c < 16; ++c) {
      if (c < 8) WAIT_V(14); else wait_v<0>();
      BAR();
      WV[c] = w_frag(lds + R_W + (c & 7) * 16384 + vo);
      LBAR();
      if (c < 8) stage(c + 8);
    }
    const unsigned vw = (unsigned)((32 * ((ub >> 1) & 1) + 16 * (ub >> 2) + (ub & 1)) * D_) * 2u + vcol;
    const unsigned sw = (unsigned)((half * 2048 + w * 256) * D_ + jb * 64) * 2u;
    auto stageo = [&](int c) {
      const unsigned d = lbase + R_W + w * 16384 + (c & 1) * 8192;
#pragma unroll
      for (int rho = 0; rho < 8; ++rho) {
        const int r = rho >> 1, hb = rho & 1;
        dma16(ro, vw, sw + (unsigned)((c * 64 + 8 * (r >> 1) + 4 * hb + 2 * (r & 1)) * D_) * 2u, d + rho * 1024);
      }
    };
    stageo(0); stageo(1);
    WAIT_V(0);
#pragma unroll
    for (int c = 0; c < 2; ++c)
#pragma unroll
      for (int t = 0; t < 4; ++t) WB[c * 4 + t] = w_frag(lds + R_W + w * 16384 + c * 8192 + (lo ^ (t << 5)));
    WAIT_L0();
    stageo(2); stageo(3);
    WAIT_V(0);
  }
  unsigned cap = SPIN_CAP, tref = now32() - p.d2;
  ST_DECL
  for (int s = 0; s < NSEG; ++s) {
    ST(0);
    wait_until(tref + p.d2);
    gather_mem(rs, lbase, s, half, w, l16, cap);
    ST(1);
    LBAR();
    tref = now32();
    ST(2);
    {
      f32x4 c0 = {0.f, 0.f, 0.f, 0.f}, c1 = c0;
      LAS unsigned char* mp = lad(l8, lbase + R_MEM);
      bf16x8 af[6];
#pragma unroll
      for (int c = 0; c < 5; ++c) af[c] = mem_frag(mp, c);
#pragma unroll
      for (int c = 0; c < 16; ++c) {
        if (c + 5 < 16) af[(c + 5) % 6] = mem_frag(mp, c + 5);
        if (c & 1) c1 = smf(af[c % 6], WV[c], c1, sidx); else c0 = smf(af[c % 6], WV[c], c0, sidx);
        SCHED();
      }
      c0 += c1;
      const f32x4 f = fold(c0, c0);
      if (l16 < 512) stx8(rs, l8, (unsigned)X_VM + (unsigned)(s * NH + h) * 2048 + w * 256, u32x2{nz(pack2(f[0], f[1])), nz(pack2(f[2], f[3]))});
    }
    ST(3);
    if (s + 1 < NSEG) {
      const unsigned wb = lbase + R_W + w * 16384;
      bf16x16 L0 = w_frag(lad(lo, wb)), L1 = w_frag(lad<32>(lo, wb));
      SCHED();
      u32x4 a[4];
      const unsigned so = (unsigned)X_OX + s * 65536 + half * 32768 + w * 4096;
      wait_until(tref + p.d1);
      poll4(rs, l16, so, so + 1024, so + 2048, so + 3072, a[0], a[1], a[2], a[3], cap);
      ST(4);
      f32x4 c[4];
#pragma unroll
      for (int t = 0; t < 4; ++t) c[t] = f32x4{0.f, 0.f, 0.f, 0.f};
#pragma unroll
      for (int q = 0; q < 4; ++q) {
        const int ci = 2 + (q >> 1), tb = 2 * (q & 1);
        c[tb] = smf(__builtin_bit_cast(bf16x8, a[ci]), L0, c[tb], sidx);
        c[tb + 1] = smf(__builtin_bit_cast(bf16x8, a[ci]), L1, c[tb + 1], sidx);
        if (q < 3) {
          const int nq = q + 1;
          if (nq & 1) { L0 = w_frag(lad<64>(lo, wb + (nq >> 1) * 8192)); L1 = w_frag(lad<96>(lo, wb + (nq >> 1) * 8192)); }
          else { L0 = w_frag(lad(lo, wb + (nq >> 1) * 8192)); L1 = w_frag(lad<32>(lo, wb + (nq >> 1) * 8192)); }
        }
        const int ri = q >> 1, rt = 2 * ((q & 1) ^ 1);
        c[rt] = smf(__builtin_bit_cast(bf16x8, a[ri]), WB[ri * 4 + rt], c[rt], sidx);
        c[rt + 1] = smf(__builtin_bit_cast(bf16x8, a[ri]), WB[ri * 4 + rt + 1], c[rt + 1], sidx);
        SCHED();
      }
      ST(5);
      LAS unsigned char* rw = lad(roff, lbase);
      *(LAS f32x4*)(rw) = fold(c[0], c[1]);
      *(LAS f32x4*)(rw + 512) = fold(c[2], c[3]);
      ST(6);
      LBAR();
      ST(7);
      if (w < 2) {
        LAS unsigned char* rp = lad(l16, lbase + R_RED + w * 1024);
        f32x4 v[8];
#pragma unroll
        for (int i = 0; i < 8; ++i) v[i] = *(const LAS f32x4*)(rp + i * 2048);
        const f32x4 sum = ((v[0] + v[1]) + (v[2] + v[3])) + ((v[4] + v[5]) + (v[6] + v[7]));
        const u32x2 o = {nz(pack2(sum[0], sum[1])), nz(pack2(sum[2], sum[3]))};
        const unsigned mo = (unsigned)X_MEMP + (s + 1) * 65536 + half * 16384 + jb * 1024 + w * 512;
        stx8(rs, l8, mo + (half ^ 1) * 32768, o);
        stx8(rs, l8, mo + half * 32768, o);
      }
      ST(8);
    }
    ST(9);
    ST_FLUSH(half * 32 + 16 + jb, s);
  }
  if (cap == 0u && l16 == 0) __hip_atomic_store((GAS unsigned*)(p.ws + X_CTL), 1u, __ATOMIC_RELAXED, __HIP_MEMORY_SCOPE_AGENT);
}

__global__ void __launch_bounds__(512, 2) k_main(MainP p) {
  extern __shared__ __attribute__((aligned(16))) unsigned char smem[];
  const int b = blockIdx.x, x = b & 7, j = b >> 3;
  if (x >= 2) worker(p, (LAS unsigned char*)smem, x - 2, j);
  else if (j < 16) role_a(p, (LAS unsigned char*)smem, x, j);
  else role_vb(p, (LAS unsigned char*)smem, x, j - 16);
}

extern "C" void kernel_launch(void* const* d_in, const int* in_sizes, int n_in, void* d_out, int out_size, void* d_ws, size_t ws_size,
                              hipStream_t stream) {
  unsigned char* ws = (unsigned char*)d_ws;
  k_prep<<<256, 512, 0, stream>>>((const u16*)d_in[12], (const u16*)d_in[13], (const u16*)d_in[1], ws);
  MainP p;
  p.x = (const u16*)d_in[0]; p.Wq = (const u16*)d_in[2]; p.Wk = (const u16*)d_in[3]; p.Wv = (const u16*)d_in[4]; p.Wo = (const u16*)d_in[5];
  p.w1 = (const u16*)d_in[8]; p.w2 = (const u16*)d_in[9]; p.w3 = (const u16*)d_in[10]; p.w4 = (const u16*)d_in[11];
  p.out = (u16*)d_out; p.ws = ws; p.d1 = 3400; p.d2 = 7000;
  (void)hipFuncSetAttribute((const void*)k_main, hipFuncAttributeMaxDynamicSharedMemorySize, R_LDS);
  k_main<<<256, 512, R_LDS, stream>>>(p);
}
```

```cpp
#include <hip/hip_runtime.h>
#include <hip/hip_bf16.h>
#include <cstdlib>
#pragma clang diagnostic ignored "-Winline-asm"

#define LAS __attribute__((address_space(3)))
#define GAS __attribute__((address_space(1)))
typedef unsigned short u16;
typedef __bf16 bf16x2 __attribute__((ext_vector_type(2)));
typedef __bf16 bf16x4 __attribute__((ext_vector_type(4)));
typedef __bf16 bf16x8 __attribute__((ext_vector_type(8)));
typedef __bf16 bf16x16 __attribute__((ext_vector_type(16)));
typedef short s16x4 __attribute__((ext_vector_type(4)));
typedef float f32x2 __attribute__((ext_vector_type(2)));
typedef float f32x4 __attribute__((ext_vector_type(4)));
typedef unsigned u32x2 __attribute__((ext_vector_type(2)));
typedef unsigned u32x4 __attribute__((ext_vector_type(4)));

constexpr int S_ = 2048, D_ = 1024, NH = 32, DH = 128, HD = 4096, NSEG = 64, FF = 3584;

constexpr unsigned MB = 1u << 20;
constexpr unsigned X_MEMP = 0;
constexpr unsigned X_OX = 4 * MB;
constexpr unsigned X_VM = 8 * MB;
constexpr unsigned F_LN = 12 * MB;
constexpr unsigned F_ST = F_LN + 32768;
constexpr unsigned F_CH = F_ST + 65536;
constexpr unsigned F_TL = F_CH + 4096;
constexpr unsigned X_CTL = F_TL + 8192;
constexpr unsigned X_TS = X_CTL + 4096;
constexpr unsigned X_BYTES = X_TS + 262144;
constexpr unsigned WS_MOD = 15 * MB;
constexpr unsigned WS_H = 16 * MB;
constexpr unsigned WS_Q = 20 * MB;
constexpr unsigned WS_K = 36 * MB;
constexpr unsigned WS_VT = 52 * MB;
constexpr unsigned WS_O = 68 * MB;
constexpr unsigned WS_X1 = 84 * MB;
constexpr unsigned WS_H2 = 88 * MB;
constexpr unsigned WS_G = 92 * MB;
constexpr unsigned WS_H1S = 96 * MB;
constexpr unsigned WS_X2 = 101 * MB;
constexpr unsigned WS_ST5 = 106 * MB;
constexpr unsigned WS_U = 108 * MB;
constexpr unsigned WS_KM = 124 * MB;
constexpr unsigned WS_P2 = 132 * MB;
constexpr unsigned WS_P8 = 164 * MB;
constexpr unsigned WS_END = 198 * MB;

__device__ __forceinline__ float bf2f(unsigned short b) { return __builtin_bit_cast(float, (unsigned)b << 16); }
__device__ __forceinline__ unsigned f2u(float f) { return __builtin_bit_cast(unsigned, f); }
__device__ __forceinline__ float u2f(unsigned u) { return __builtin_bit_cast(float, u); }
__device__ __forceinline__ float lo2f(unsigned w) { return __builtin_bit_cast(float, w << 16); }
__device__ __forceinline__ float hi2f(unsigned w) { return __builtin_bit_cast(float, w & 0xFFFF0000u); }
__device__ __forceinline__ unsigned pack2(float a, float b) { bf16x2 v; v[0] = (__bf16)a; v[1] = (__bf16)b; return __builtin_bit_cast(unsigned, v); }
__device__ __forceinline__ float rbf(float a) { return (float)(__bf16)a; }
__device__ __forceinline__ unsigned nz(unsigned w) { return max(w, 1u); }
__device__ __forceinline__ float gelu_tanh(float x) {
  const float z = 0.7978845608028654f * (x + 0.044715f * x * x * x);
  const float e = __builtin_amdgcn_exp2f(-2.885390081777927f * z);
  return x * __builtin_amdgcn_rcpf(1.0f + e);
}
template <int CTRL> __device__ __forceinline__ float dpp_add(float v) { return v + u2f(__builtin_amdgcn_update_dpp(0u, f2u(v), CTRL, 0xF, 0xF, true)); }
__device__ __forceinline__ float wave_sum(float v) {
  v = dpp_add<0xB1>(v); v = dpp_add<0x4E>(v); v = dpp_add<0x141>(v); v = dpp_add<0x140>(v);
  auto t = __builtin_amdgcn_permlane16_swap(f2u(v), f2u(v), false, false);
  v = u2f(t[0]) + u2f(t[1]);
  t = __builtin_amdgcn_permlane32_swap(f2u(v), f2u(v), false, false);
  return u2f(t[0]) + u2f(t[1]);
}
#define WAIT_V(n) asm volatile("s_waitcnt vmcnt(" #n ")" ::: "memory")
#define WAIT_L0() asm volatile("s_waitcnt lgkmcnt(0)" ::: "memory")
#define BAR() do { asm volatile("" ::: "memory"); __builtin_amdgcn_s_barrier(); asm volatile("" ::: "memory"); } while (0)
#define LBAR() do { WAIT_L0(); BAR(); } while (0)
template <int N> __device__ __forceinline__ void wait_v() { asm volatile("s_waitcnt vmcnt(%0)" :: "n"(N) : "memory"); }
__device__ __forceinline__ void dma16(__amdgpu_buffer_rsrc_t rs, unsigned voff, unsigned soff, unsigned ldsa) {
  asm volatile("s_mov_b32 m0, %0\n\ts_nop 3\n\tbuffer_load_dwordx4 %1, %2, %3 offen lds" :: "s"(ldsa), "v"(voff), "s"(rs), "s"(soff) : "m0", "memory");
}
__device__ __forceinline__ void dma16x(__amdgpu_buffer_rsrc_t rs, unsigned voff, unsigned soff, unsigned ldsa) {
  asm volatile("s_mov_b32 m0, %0\n\ts_nop 3\n\tbuffer_load_dwordx4 %1, %2, %3 offen sc1 lds" :: "s"(ldsa), "v"(voff), "s"(rs), "s"(soff) : "m0", "memory");
}
__device__ __forceinline__ u32x4 ldx16(__amdgpu_buffer_rsrc_t rs, unsigned voff, unsigned soff) { return __builtin_amdgcn_raw_buffer_load_b128(rs, voff, soff, 16); }
__device__ __forceinline__ u32x2 ldx8(__amdgpu_buffer_rsrc_t rs, unsigned voff, unsigned soff) { return __builtin_amdgcn_raw_buffer_load_b64(rs, voff, soff, 16); }
__device__ __forceinline__ unsigned ldx4(__amdgpu_buffer_rsrc_t rs, unsigned voff, unsigned soff) { return __builtin_amdgcn_raw_buffer_load_b32(rs, voff, soff, 16); }
__device__ __forceinline__ void stx16(__amdgpu_buffer_rsrc_t rs, unsigned voff, unsigned soff, u32x4 v) {
  __builtin_amdgcn_raw_buffer_store_b128(v, rs, voff, soff, 16);
  asm volatile("s_nop 1" :: "v"(v) : "memory");
}
__device__ __forceinline__ void stx8(__amdgpu_buffer_rsrc_t rs, unsigned voff, unsigned soff, u32x2 v) { __builtin_amdgcn_raw_buffer_store_b64(v, rs, voff, soff, 16); }
__device__ __forceinline__ void stx4(__amdgpu_buffer_rsrc_t rs, unsigned voff, unsigned soff, unsigned v) { __builtin_amdgcn_raw_buffer_store_b32(v, rs, voff, soff, 16); }
__device__ __forceinline__ void wait_ge(__amdgpu_buffer_rsrc_t rs, unsigned voff, unsigned soff, unsigned need, unsigned& cap) {
  unsigned t;
  asm volatile("s_nop 4\n\t"
               "1:\n\t"
               "buffer_load_dword %[t], %[vo], %[rs], %[so] offen sc1\n\t"
               "s_waitcnt vmcnt(0)\n\t"
               "v_cmp_gt_u32 vcc, %[need], %[t]\n\t"
               "s_cmp_eq_u64 vcc, 0\n\t"
               "s_cbranch_scc1 2f\n\t"
               "s_sleep 2\n\t"
               "s_sub_u32 %[cap], %[cap], 1\n\t"
               "s_cbranch_scc0 1b\n\t"
               "s_mov_b32 %[cap], 0\n\t"
               "2:"
               : [t] "=&v"(t), [cap] "+s"(cap) : [vo] "v"(voff), [rs] "s"(rs), [so] "s"(soff), [need] "s"(need) : "vcc", "scc", "memory");
}
__device__ __forceinline__ __amdgpu_buffer_rsrc_t mk_rs(const void* p, unsigned bytes) { return __builtin_amdgcn_make_buffer_rsrc((void*)p, 0, (int)bytes, 0x00020000); }
#define SCHED() __builtin_amdgcn_sched_barrier(0)
__device__ __forceinline__ unsigned now32() { unsigned long long t; asm volatile("s_memtime %0\n\ts_waitcnt lgkmcnt(0)" : "=s"(t) :: "memory"); return (unsigned)t; }
__device__ __forceinline__ unsigned rt32() { unsigned long long t; asm volatile("s_memrealtime %0\n\ts_waitcnt lgkmcnt(0)" : "=s"(t) :: "memory"); return (unsigned)t; }
__device__ __forceinline__ void wait_until(unsigned t) { while ((int)(rt32() - t) < 0) {} }
template <int X = 0> __device__ __forceinline__ LAS unsigned char* lad(unsigned v, unsigned u) {
  unsigned a;
  if (X == 0) asm volatile("v_add_u32 %0, %1, %2" : "=v"(a) : "s"(u), "v"(v));
  else asm volatile("v_xor_b32 %0, %3, %2\n\tv_add_u32 %0, %1, %0" : "=&v"(a) : "s"(u), "v"(v), "n"(X));
  return (LAS unsigned char*)(size_t)a;
}
__device__ __forceinline__ bf16x4 trread(LAS unsigned char* p) { return __builtin_amdgcn_ds_read_tr16_b64_v4bf16((LAS bf16x4*)p); }
__device__ __forceinline__ bf16x8 cat8(bf16x4 a, bf16x4 b) { return __builtin_shufflevector(a, b, 0, 1, 2, 3, 4, 5, 6, 7); }
__device__ __forceinline__ bf16x16 cat16(bf16x8 a, bf16x8 b) { return __builtin_shufflevector(a, b, 0, 1, 2, 3, 4, 5, 6, 7, 8, 9, 10, 11, 12, 13, 14, 15); }
__device__ __forceinline__ f32x4 mfma32(bf16x8 a, bf16x8 b, f32x4 c) { return __builtin_amdgcn_mfma_f32_16x16x32_bf16(a, b, c, 0, 0, 0); }
__device__ __forceinline__ f32x4 mfma16(u32x2 a, u32x2 b, f32x4 c) {
  return mfma32(__builtin_bit_cast(bf16x8, u32x4{a[0], a[1], 0u, 0u}), __builtin_bit_cast(bf16x8, u32x4{b[0], b[1], 0u, 0u}), c);
}
__device__ __forceinline__ f32x4 mfma16n(u32x2 a, u32x2 b, f32x4 c) { return __builtin_amdgcn_mfma_f32_16x16x16bf16_1k(__builtin_bit_cast(s16x4, a), __builtin_bit_cast(s16x4, b), c, 0, 0, 0); }
__device__ __forceinline__ f32x4 smf(bf16x8 a, bf16x16 b, f32x4 c, int idx) { return __builtin_amdgcn_smfmac_f32_16x16x64_bf16(a, b, c, idx, 0, 0); }

__device__ __forceinline__ void ln_row(float (&x)[16]) {
  float s = 0.f;
#pragma unroll
  for (int i = 0; i < 16; ++i) s += x[i];
  const float mean = wave_sum(s) * (1.0f / 1024.0f);
  float q = 0.f;
#pragma unroll
  for (int i = 0; i < 16; ++i) { const float d = x[i] - mean; q += d * d; }
  const float var = wave_sum(q) * (1.0f / 1024.0f);
  const float m = rbf(mean), isd = __builtin_amdgcn_rcpf(rbf(__builtin_amdgcn_sqrtf(rbf(rbf(var) + 1e-5f))));
#pragma unroll
  for (int i = 0; i < 16; ++i) x[i] = rbf(rbf(x[i] - m) * isd);
}
__device__ __forceinline__ void load_row16(const u16* rowp, int lane, float (&x)[16]) {
  const u32x4 a = *(const u32x4*)(rowp + lane * 8), b = *(const u32x4*)(rowp + 512 + lane * 8);
#pragma unroll
  for (int i = 0; i < 4; ++i) { x[2 * i] = lo2f(a[i]); x[2 * i + 1] = hi2f(a[i]); x[8 + 2 * i] = lo2f(b[i]); x[9 + 2 * i] = hi2f(b[i]); }
}
__device__ __forceinline__ void store_row16(u16* rowp, int lane, const float (&x)[16]) {
  u32x4 a, b;
#pragma unroll
  for (int i = 0; i < 4; ++i) { a[i] = pack2(x[2 * i], x[2 * i + 1]); b[i] = pack2(x[8 + 2 * i], x[9 + 2 * i]); }
  *(u32x4*)(rowp + lane * 8) = a; *(u32x4*)(rowp + 512 + lane * 8) = b;
}
__device__ __forceinline__ void modulate(float (&x)[16], const float* sc, const float* sh, int lane) {
#pragma unroll
  for (int hh = 0; hh < 2; ++hh)
#pragma unroll
    for (int j = 0; j < 2; ++j) {
      const f32x4 c = *(const f32x4*)(sc + hh * 512 + lane * 8 + j * 4), t = *(const f32x4*)(sh + hh * 512 + lane * 8 + j * 4);
#pragma unroll
      for (int i = 0; i < 4; ++i) { float& v = x[hh * 8 + j * 4 + i]; v = rbf(rbf(v * rbf(1.0f + c[i])) + t[i]); }
    }
}

__host__ __device__ __forceinline__ int mem_slot(int dl) {
  return ((dl >> 2) & 1) * 32 + (((dl >> 5) & 1) * 2 + ((dl >> 3) & 1)) * 8 + (((dl >> 4) & 1) * 2 + (dl & 1)) * 2 + ((dl >> 1) & 1);
}

__global__ void __launch_bounds__(512) k_prep(const u16* __restrict__ c, const u16* __restrict__ wmod, const u16* __restrict__ mem0, unsigned char* ws) {
  __shared__ float red[128][33];
  __shared__ float sc[1024];
  const int b = blockIdx.x, tid = threadIdx.x;
  {
    u32x4* z = (u32x4*)ws;
    const u32x4 zero = {0u, 0u, 0u, 0u};
    for (size_t i = (size_t)65536 / 16 + (size_t)b * 512 + tid; i < X_BYTES / 16; i += (size_t)256 * 512) z[i] = zero;
    if (b >= 192 && b < 208) {
      u32x4* y = (u32x4*)(ws + WS_H1S + ((b & 8) ? 64 * 65536 : 0));
      y[(b & 7) * 512 + tid] = zero;
    }
  }
  if (b == 255) {
    for (int d = tid; d < 1024; d += 512) {
      u32x4 v;
#pragma unroll
      for (int i = 0; i < 4; ++i) v[i] = nz((unsigned)mem0[(2 * i) * 1024 + d] | ((unsigned)mem0[(2 * i + 1) * 1024 + d] << 16));
      const u32x4 mz = {0x80008000u, 0x80008000u, 0x80008000u, 0x80008000u};
#pragma unroll
      for (int r = 0; r < 2; ++r) {
        *(u32x4*)(ws + X_MEMP + r * 32768 + (d >> 6) * 1024 + mem_slot(d & 63) * 16) = v;
        *(u32x4*)(ws + X_MEMP + r * 32768 + 16384 + d * 16) = mz;
      }
    }
  }
  if (b < 192) {
    for (int i = tid; i < 1024; i += 512) { const float v = bf2f(c[i]); sc[i] = rbf(v / (1.0f + __expf(-v))); }
    __syncthreads();
    const int kg = tid >> 2, c8 = tid & 3;
    float acc[8] = {0.f, 0.f, 0.f, 0.f, 0.f, 0.f, 0.f, 0.f};
    u32x4 w[8];
#pragma unroll
    for (int i = 0; i < 8; ++i) w[i] = *(const u32x4*)(wmod + (size_t)(kg + 128 * i) * 6144 + b * 32 + c8 * 8);
#pragma unroll
    for (int i = 0; i < 8; ++i) {
      const float s = sc[kg + 128 * i];
#pragma unroll
      for (int j = 0; j < 4; ++j) { acc[2 * j] += s * lo2f(w[i][j]); acc[2 * j + 1] += s * hi2f(w[i][j]); }
    }
#pragma unroll
    for (int j = 0; j < 8; ++j) red[kg][c8 * 8 + j] = acc[j];
    __syncthreads();
    if (tid < 256) {
      const int col = tid & 31, part = tid >> 5;
      float s = 0.f;
#pragma unroll
      for (int i = 0; i < 16; ++i) s += red[part * 16 + i][col];
      __syncthreads();
      red[part][col] = s;
    } else __syncthreads();
    __syncthreads();
    if (tid < 32) {
      float s = 0.f;
#pragma unroll
      for (int i = 0; i < 8; ++i) s += red[i][tid];
      ((float*)(ws + WS_MOD))[b * 32 + tid] = rbf(s);
    }
  }
}

#define WST(rs, wk, i)
struct MainP { const u16 *x, *Wq, *Wk, *Wv, *Wo, *w1, *w2, *w3, *w4; u16* out; unsigned char* ws; int d1, d2, d3, d4; };
constexpr unsigned SPIN_CAP = 1u << 18;

__device__ __forceinline__ float gmax(float v) {
  auto t = __builtin_amdgcn_permlane16_swap(f2u(v), f2u(v), false, false);
  v = fmaxf(u2f(t[0]), u2f(t[1]));
  t = __builtin_amdgcn_permlane32_swap(f2u(v), f2u(v), false, false);
  return fmaxf(u2f(t[0]), u2f(t[1]));
}
__device__ __forceinline__ float gsum(float v) {
  auto t = __builtin_amdgcn_permlane16_swap(f2u(v), f2u(v), false, false);
  v = u2f(t[0]) + u2f(t[1]);
  t = __builtin_amdgcn_permlane32_swap(f2u(v), f2u(v), false, false);
  return u2f(t[0]) + u2f(t[1]);
}
__device__ __forceinline__ u32x4 widen(f32x4 a, f32x4 b) {
  const auto r0 = __builtin_amdgcn_permlane16_swap(pack2(a[0], a[1]), pack2(b[0], b[1]), false, false);
  const auto r1 = __builtin_amdgcn_permlane16_swap(pack2(a[2], a[3]), pack2(b[2], b[3]), false, false);
  return u32x4{r0[0], r1[0], r0[1], r1[1]};
}
__device__ __forceinline__ void publish(__amdgpu_buffer_rsrc_t rs, unsigned word, unsigned val) {
  WAIT_V(0); BAR();
  if (threadIdx.x == 0) stx4(rs, 0, word, val);
}

template <bool VT>
__device__ __forceinline__ void proj_tile(LAS unsigned char* lds, __amdgpu_buffer_rsrc_t rs, __amdgpu_buffer_rsrc_t rsW, unsigned dst, int rb, int hd) {
  constexpr int BM = 128, BN = 128, WN = 2, WTM = 32, WTN = 64, MT = 2, NT = 4, NST = 4, PW = 4, STB = (BM + BN) * 128, BOFF = BM * 128;
  int tid = threadIdx.x; asm volatile("" : "+v"(tid));
  const int lane = tid & 63, w = __builtin_amdgcn_readfirstlane(tid >> 6);
  const int wm = w / WN, wn = w % WN, fr = lane & 15, g = lane >> 4;
  const unsigned lbase = (unsigned)(size_t)lds;
  const int ub = lane >> 3;
  const unsigned vA = (unsigned)(ub * D_ + (((lane & 7) ^ (ub & 7)) * 8)) * 2u;
  const unsigned vB = (unsigned)(((ub & 3) + 8 * (ub >> 2)) * HD + 16 * (((lane >> 1) & 3) ^ (ub >> 1)) + 8 * (lane & 1)) * 2u;
  const unsigned sA = WS_H + (unsigned)((rb * BM + 8 * w) * D_) * 2u;
  const unsigned sB = (unsigned)((32 * (w >> 2) + 16 * ((w >> 1) & 1) + 4 * (w & 1)) * HD + hd * BN) * 2u;
  auto stage = [&](int kt, int slot) {
    const unsigned d = lbase + slot * STB + w * 1024;
#pragma unroll
    for (int i = 0; i < 2; ++i) dma16x(rs, vA, sA + (unsigned)(i * 64 * D_ + kt * 64) * 2u, d + i * 8192);
#pragma unroll
    for (int i = 0; i < 2; ++i) dma16(rsW, vB, sB + (unsigned)(kt * 64 * HD + i * 64) * 2u, d + BOFF + i * 8192);
  };
  const int aoff = (wm * WTM + fr) * 128 + ((g ^ (fr & 7)) << 4);
  const int tu = 4 * (g & 1) + (fr >> 2);
  const int boff = BOFF + wn * 8192 + (g >> 1) * 2048 + tu * 128 + ((tu >> 1) << 5) + (fr & 3) * 8;
  f32x4 acc[MT][NT];
#pragma unroll
  for (int m = 0; m < MT; ++m)
#pragma unroll
    for (int n = 0; n < NT; ++n) acc[m][n] = f32x4{0.f, 0.f, 0.f, 0.f};
#pragma unroll
  for (int s = 0; s < NST - 1; ++s) stage(s, s);
  for (int kt = 0; kt < 16; ++kt) {
    if (kt < 14) wait_v<PW * 2>(); else if (kt == 14) wait_v<PW>(); else wait_v<0>();
    BAR();
    if (kt + NST - 1 < 16) stage(kt + NST - 1, (kt + NST - 1) & 3);
    LAS unsigned char* sb = lds + (kt & 3) * STB;
#pragma unroll
    for (int ks = 0; ks < 2; ++ks) {
      bf16x8 af[MT], bfr[NT];
#pragma unroll
      for (int n = 0; n < NT; ++n) { LAS unsigned char* q = sb + ((boff ^ (n << 5)) + ks * 4096); bfr[n] = cat8(trread(q), trread(q + 1024)); }
#pragma unroll
      for (int m = 0; m < MT; ++m) af[m] = *(const LAS bf16x8*)(sb + ((aoff ^ (ks * 64)) + m * 2048));
#pragma unroll
      for (int m = 0; m < MT; ++m)
#pragma unroll
        for (int n = 0; n < NT; ++n) acc[m][n] = VT ? mfma32(af[m], bfr[n], acc[m][n]) : mfma32(bfr[n], af[m], acc[m][n]);
    }
  }
  const unsigned ob = dst + (unsigned)((rb * 4 + wm) * NH + hd) * 8192;
  if (!VT) {
    const unsigned vo = (unsigned)((2 * (g & 1) + (g >> 1)) * 16 + fr) * 16;
#pragma unroll
    for (int m = 0; m < MT; ++m)
#pragma unroll
      for (int u = 0; u < 2; ++u) stx16(rs, vo, ob + (m * 4 + wn * 2 + u) * 1024, widen(acc[m][2 * u], acc[m][2 * u + 1]));
  } else {
#pragma unroll
    for (int n = 0; n < NT; ++n) {
      const f32x4 a = acc[0][n], b = acc[1][n];
      stx16(rs, lane * 16, ob + (wn * 4 + n) * 1024, u32x4{pack2(a[0], a[1]), pack2(a[2], a[3]), pack2(b[0], b[1]), pack2(b[2], b[3])});
    }
  }
}

constexpr int P_S2 = 0, P_S8 = 32, P_S7 = 64, P_S4 = 120, P_S5 = 136, P_S1 = 160, P_S3 = 288;
__device__ __forceinline__ void stage_attn(__amdgpu_buffer_rsrc_t rs, int wk, int grp, int kq, unsigned& cap) {
  const int tid = threadIdx.x, lane = tid & 63, w = __builtin_amdgcn_readfirstlane(tid >> 6), fr = lane & 15, g = lane >> 4;
  const int h = kq * 8 + w;
  const unsigned l16 = lane * 16;
  for (int seg = grp; seg < NSEG; seg += 4) {
    wait_ge(rs, (unsigned)(lane % 3) * 2048, F_TL + (unsigned)((seg >> 2) * 32 + h) * 4, 1u, cap);
    const unsigned ub = (unsigned)(seg * NH + h);
    f32x4 s0[2], s1[2], s2;
    s0[0] = s0[1] = s1[0] = s1[1] = s2 = f32x4{0.f, 0.f, 0.f, 0.f};
    bf16x8 q0[4], q1[4], vf[8];
#pragma unroll
    for (int ks = 0; ks < 4; ++ks) {
      q0[ks] = __builtin_bit_cast(bf16x8, ldx16(rs, l16, WS_Q + ub * 8192 + ks * 1024)); q1[ks] = __builtin_bit_cast(bf16x8, ldx16(rs, l16, WS_Q + ub * 8192 + 4096 + ks * 1024));
      const bf16x8 k0 = __builtin_bit_cast(bf16x8, ldx16(rs, l16, WS_K + ub * 8192 + ks * 1024)), k1 = __builtin_bit_cast(bf16x8, ldx16(rs, l16, WS_K + ub * 8192 + 4096 + ks * 1024));
      s1[0] = mfma32(k0, q0[ks], s1[0]); s1[1] = mfma32(k0, q1[ks], s1[1]); s2 = mfma32(k1, q1[ks], s2);
    }
    const unsigned ev = (unsigned)(fr >> 3) * 1024 + (unsigned)(g * 16 + 8 * ((fr >> 2) & 1) + (fr & 3)) * 16;
#pragma unroll
    for (int i = 0; i < 8; ++i) vf[i] = __builtin_bit_cast(bf16x8, ldx16(rs, ev, WS_VT + ub * 8192 + (i >> 1) * 2048 + (i & 1) * 64));
    wait_ge(rs, 0, F_CH + h * 128, (unsigned)seg + 1u, cap);
    u32x2 vmf[8];
    const unsigned em = (unsigned)(fr >> 3) * 256 + (unsigned)(g & 1) * 128 + (unsigned)(8 * ((fr >> 2) & 1) + (fr & 3)) * 8;
    for (;;) {
      asm volatile("" ::: "memory");
      unsigned mn = 0xFFFFFFFFu;
#pragma unroll
      for (int i = 0; i < 8; ++i) { vmf[i] = ldx8(rs, em, X_VM + ub * 2048 + (i >> 1) * 512 + (i & 1) * 32); }
#pragma unroll
      for (int ks = 0; ks < 4; ++ks) {
        const bf16x8 kmf = __builtin_bit_cast(bf16x8, ldx16(rs, l16, WS_KM + ub * 4096 + ks * 1024));
        if (ks == 0) { s0[0] = mfma32(kmf, q0[ks], f32x4{0.f, 0.f, 0.f, 0.f}); s0[1] = mfma32(kmf, q1[ks], f32x4{0.f, 0.f, 0.f, 0.f}); }
        else { s0[0] = mfma32(kmf, q0[ks], s0[0]); s0[1] = mfma32(kmf, q1[ks], s0[1]); }
      }
#pragma unroll
      for (int i = 0; i < 8; ++i) mn = min(mn, min(vmf[i][0], vmf[i][1]));
      if (__all(mn != 0u) || cap == 0u) break;
      cap = __builtin_amdgcn_readfirstlane(cap - 1u);
    }
    cap = __builtin_amdgcn_readfirstlane(cap);
    const float scale = 0.08838834764831845f, L2E = 1.4426950408889634f;
    u32x2 pm[2]; u32x4 pt[2];
#pragma unroll
    for (int t = 0; t < 2; ++t) {
      float a[4], b[4], c[4];
      float mx = -3.0e38f;
#pragma unroll
      for (int r = 0; r < 4; ++r) {
        const int j = 4 * g + r;
        a[r] = (g < 2) ? rbf(rbf(s0[t][r]) * scale) : -3.0e38f;
        b[r] = (t == 1 || j <= fr) ? rbf(rbf(s1[t][r]) * scale) : -3.0e38f;
        c[r] = (t == 1 && j <= fr) ? rbf(rbf(s2[r]) * scale) : -3.0e38f;
        mx = fmaxf(mx, fmaxf(a[r], fmaxf(b[r], c[r])));
      }
      mx = gmax(mx);
      float sum = 0.f;
#pragma unroll
      for (int r = 0; r < 4; ++r) {
        a[r] = __builtin_amdgcn_exp2f((a[r] - mx) * L2E); b[r] = __builtin_amdgcn_exp2f((b[r] - mx) * L2E); c[r] = __builtin_amdgcn_exp2f((c[r] - mx) * L2E);
        sum += a[r] + b[r] + c[r];
      }
      const float inv = __builtin_amdgcn_rcpf(gsum(sum));
      pm[t] = u32x2{pack2(a[0] * inv, a[1] * inv), pack2(a[2] * inv, a[3] * inv)};
      pt[t] = u32x4{pack2(b[0] * inv, b[1] * inv), pack2(b[2] * inv, b[3] * inv), pack2(c[0] * inv, c[1] * inv), pack2(c[2] * inv, c[3] * inv)};
    }
#pragma unroll
    for (int u = 0; u < 4; ++u) {
      f32x4 o[2][2];
#pragma unroll
      for (int bq = 0; bq < 2; ++bq)
#pragma unroll
        for (int t = 0; t < 2; ++t) {
          f32x4 z = {0.f, 0.f, 0.f, 0.f};
          z = mfma16(vmf[2 * u + bq], pm[t], z);
          o[bq][t] = mfma32(vf[2 * u + bq], __builtin_bit_cast(bf16x8, pt[t]), z);
        }
#pragma unroll
      for (int t = 0; t < 2; ++t)
        stx16(rs, l16, WS_O + (unsigned)seg * 262144 + (unsigned)((h * 4 + u) * 2 + t) * 1024,
              u32x4{pack2(o[0][t][0], o[0][t][1]), pack2(o[0][t][2], o[0][t][3]), pack2(o[1][t][0], o[1][t][1]), pack2(o[1][t][2], o[1][t][3])});
    }
    WAIT_V(0);
    if (lane == 0) stx4(rs, 0, F_ST + (unsigned)(P_S1 + grp * 32 + h) * 128, (unsigned)seg + 1u);
    WST(rs, wk, 16 + seg);
  }
}

enum { SG_WO = 0, SG_W1 = 1, SG_W2 = 2, SG_W3 = 3, SG_W4 = 4 };
constexpr int G_XCH = 131072, G_RDY = G_XCH + 24576;
template <int SG>
__device__ __forceinline__ void stage_gemm(LAS unsigned char* lds, __amdgpu_buffer_rsrc_t rs, const u16* Wg, int wk, int kq, int cb, unsigned& cap) {
  constexpr bool WIDE = (SG == SG_WO || SG == SG_W4);
  constexpr int KST = (SG == SG_W4) ? 28 : 32;
  constexpr int NP = WIDE ? 2 : 4, KSW = KST / NP;
  constexpr int CH = WIDE ? 4 : 2;
  constexpr int NCOL = WIDE ? 128 : 64;
  constexpr int LDW = (SG == SG_W3) ? FF : D_;
  constexpr int WROWS = (SG == SG_WO) ? HD : (SG == SG_W4 ? FF : D_);
  constexpr int NBLK = (SG == SG_WO || SG == SG_W1) ? 64 : 65;
  constexpr unsigned ABASE = SG == SG_WO ? WS_O : SG == SG_W1 ? WS_G : SG == SG_W2 ? WS_H1S : SG == SG_W3 ? WS_X2 : WS_U;
  constexpr unsigned ASTR = SG == SG_WO ? 262144u : SG == SG_W4 ? 229376u : 65536u;
  const unsigned myword = F_ST + (unsigned)((SG == SG_WO ? P_S2 + kq * 8 : SG == SG_W4 ? P_S8 + kq * 8 : SG == SG_W1 ? P_S4 : SG == SG_W2 ? P_S5 : P_S7) + cb) * 128u;
  const int tid = threadIdx.x, lane = tid & 63, w = __builtin_amdgcn_readfirstlane(tid >> 6), fr = lane & 15, g = lane >> 4;
  const int cg = WIDE ? (w & 3) : (w & 1), kp = WIDE ? (w >> 2) : (w >> 1);
  const unsigned lbase = (unsigned)(size_t)lds, l16 = lane * 16;
  const int col0 = cb * NCOL + cg * 32;
  bf16x8 Wf[2][KSW];
  {
    const __amdgpu_buffer_rsrc_t rsW = mk_rs(Wg, (unsigned)WROWS * LDW * 2u);
    const int ub = lane >> 3, tu = 4 * (g & 1) + (fr >> 2);
    const unsigned vB = (unsigned)(((ub & 3) + 8 * (ub >> 2)) * LDW + 16 * (((lane >> 1) & 3) ^ (ub >> 1)) + 8 * (lane & 1)) * 2u;
    const unsigned sB = (unsigned)((kq * KST * 32 + 32 * (w >> 2) + 16 * ((w >> 1) & 1) + 4 * (w & 1)) * LDW + cb * NCOL) * 2u;
    constexpr int TB = NCOL * 128;
    const int boff = kp * 4 * TB + (cg >> 1) * 8192 + (g >> 1) * 2048 + tu * 128 + (((tu >> 1) ^ (2 * (cg & 1))) << 5) + (fr & 3) * 8;
#pragma unroll
    for (int ps = 0; ps < (WIDE ? 2 : 1); ++ps) {
      BAR();
#pragma unroll
      for (int hh = 0; hh < NP; ++hh)
#pragma unroll
        for (int i = 0; i < 4; ++i)
          if (4 * ps + i < KSW / 2) {
            const unsigned so = sB + (unsigned)((hh * (KSW / 2) + 4 * ps + i) * 64 * LDW) * 2u, d = lbase + (hh * 4 + i) * TB + w * 1024;
            dma16(rsW, vB, so, d);
            if (WIDE) dma16(rsW, vB, so + 128, d + 8192);
          }
      WAIT_V(0); BAR();
#pragma unroll
      for (int i = 0; i < 4; ++i)
        if (4 * ps + i < KSW / 2) {
#pragma unroll
          for (int ks = 0; ks < 2; ++ks)
#pragma unroll
            for (int n = 0; n < 2; ++n) {
              LAS unsigned char* q = lds + ((boff ^ (n << 5)) + i * TB + ks * 4096);
              Wf[n][(4 * ps + i) * 2 + ks] = cat8(trread(q), trread(q + 1024));
            }
        }
      WAIT_L0();
    }
    BAR();
  }
  f32x4 cs[2];
  if (SG == SG_W3) {
    const u32x4 one4 = {0x3F803F80u, 0x3F803F80u, 0x3F803F80u, 0x3F803F80u};
    cs[0] = cs[1] = f32x4{0.f, 0.f, 0.f, 0.f};
#pragma unroll
    for (int k = 0; k < KSW; ++k) { cs[0] = mfma32(Wf[0][k], __builtin_bit_cast(bf16x8, one4), cs[0]); cs[1] = mfma32(Wf[1][k], __builtin_bit_cast(bf16x8, one4), cs[1]); }
    *(LAS f32x4*)(lds + w * 2048 + l16) = cs[0]; *(LAS f32x4*)(lds + w * 2048 + 1024 + l16) = cs[1];
    LBAR();
    cs[0] = cs[1] = f32x4{0.f, 0.f, 0.f, 0.f};
#pragma unroll
    for (int i = 0; i < 4; ++i) { cs[0] += *(const LAS f32x4*)(lds + (i * 2 + cg) * 2048 + l16); cs[1] += *(const LAS f32x4*)(lds + (i * 2 + cg) * 2048 + 1024 + l16); }
    LBAR();
  }
  auto pvo = [&](int b) -> unsigned {
    if (SG == SG_WO) return (unsigned)(P_S1 + (b & 3) * 32 + kq * 8 + (lane & 7)) * 128u;
    if (SG == SG_W1) return (unsigned)(P_S3 + (b & 3) * 32 + (lane & 31)) * 128u;
    if (SG == SG_W2) return (unsigned)(P_S4 + (lane & 15)) * 128u;
    if (SG == SG_W3) return (unsigned)(P_S5 + (lane & 15)) * 128u;
    return (unsigned)(P_S7 + kq * 14 + ((lane & 31) % 14)) * 128u;
  };
  auto need = [&](int b) -> unsigned { return (SG == SG_W2 && b == 64) ? 64u : (unsigned)b + 1u; };
  const unsigned vA = (SG == SG_W1) ? (unsigned)((lane >> 3) * 2048 + (((lane & 7) ^ (lane >> 3)) << 4)) : l16;
  auto piece = [&](int b, int i) {
    unsigned kb;
    if (WIDE) kb = (unsigned)((i & 1) * KSW * 2 + min((i >> 1) * 8 + w, KSW * 2 - 1));
    else kb = (unsigned)(((i & 1) * 2 + (w >> 2)) * 16 + (i >> 1) * 4 + (w & 3));
    const unsigned so = (SG == SG_W1) ? (kb & 3) * 16384u + (kb >> 2) * 128u : kb * 1024u;
    dma16x(rs, vA, ABASE + (unsigned)b * ASTR + (unsigned)kq * (KST * 2048) + so, lbase + (b & 1) * 65536 + kb * 1024);
  };
  auto fetch = [&](int b) {
#pragma unroll
    for (int i = 0; i < 8; ++i) piece(b, i);
  };
  constexpr int NS = (SG == SG_W1 || SG == SG_W3) ? 1 : 2;
  const int row = 16 * (kp & 1) + fr, co = 16 * (g & 1) + 8 * (g >> 1), c8 = col0 + co;
  const unsigned aoff = (SG == SG_W1) ? (unsigned)(fr * 128 + ((g ^ (fr & 7)) << 4)) : l16;
  WST(rs, wk, 10);
#define SEG(i)
  int mode = 0; bool ahead = false;
  for (int b = 0; b < NBLK; ++b) {
    if (mode == 2) { if (WIDE || w < 4) wait_v<NS>(); else wait_v<0>(); BAR(); }
    else if (mode == 0) {
      if (w == 0) wait_ge(rs, pvo(b), F_ST, need(b), cap);
      BAR();
      fetch(b);
    }
    SEG(0);
    f32x4 acc[2][2];
    acc[0][0] = acc[0][1] = acc[1][0] = acc[1][1] = f32x4{0.f, 0.f, 0.f, 0.f};
    LAS unsigned char* ab = lad(aoff, lbase + (b & 1) * 65536 + kp * (KSW * 2048));
    LAS unsigned char* ab1 = ab;
    if (SG == SG_W1) ab1 = lad<64>(aoff, lbase + (b & 1) * 65536 + kp * (KSW * 2048));
    bf16x8 af[2][CH][2];
    unsigned peek; u32x4 res, st[4];
#pragma unroll
    for (int c = 0; c < 5; ++c) {
      if (c < 4) {
        if (mode != 2) { if (c == 0) wait_v<6>(); else if (c == 1) wait_v<4>(); else if (c == 2) wait_v<2>(); else wait_v<0>(); BAR(); }
#pragma unroll
        for (int k = 0; k < CH; ++k) {
          const int kk = CH * c + k;
          if (kk < KSW) {
            if (SG == SG_W1) { LAS unsigned char* q = ((kk & 1) ? ab1 : ab) + (kk >> 1) * 4096; af[c & 1][k][0] = *(const LAS bf16x8*)(q); af[c & 1][k][1] = *(const LAS bf16x8*)(q + 2048); }
            else { af[c & 1][k][0] = *(const LAS bf16x8*)(ab + kk * 2048); af[c & 1][k][1] = *(const LAS bf16x8*)(ab + kk * 2048 + 1024); }
          }
        }
      }
      if (c == 3) {
        asm volatile("buffer_load_dword %0, %1, %2, %3 offen sc1" : "=v"(peek) : "v"(w == 0 ? (lane < 32 ? pvo(b + 1) : pvo(b + 2)) : 0xF0000000u), "s"(rs), "s"(F_ST) : "memory");
        if (SG == SG_W2) {
          const int t = 32 * b - 2 + row;
          asm volatile("buffer_load_dwordx4 %0, %1, %2, %3 offen sc1" : "=v"(res) : "v"((t >= 0 && t < S_ && w < 4) ? (unsigned)(t * D_ + c8) * 2u : 0xF0000000u), "s"(rs), "s"(WS_H2) : "memory");
        }
        if (SG == SG_W3) {
          const unsigned vo = w < 4 ? (unsigned)(g * 2048 + row * 16) : 0xF0000000u, so = WS_ST5 + (unsigned)b * 8192;
          asm volatile("buffer_load_dwordx4 %0, %4, %5, %6 offen sc1\n\tbuffer_load_dwordx4 %1, %4, %5, %6 offen offset:512 sc1\n\t"
                       "buffer_load_dwordx4 %2, %4, %5, %6 offen offset:1024 sc1\n\tbuffer_load_dwordx4 %3, %4, %5, %6 offen offset:1536 sc1"
                       : "=&v"(st[0]), "=&v"(st[1]), "=&v"(st[2]), "=&v"(st[3]) : "v"(vo), "s"(rs), "s"(so) : "memory");
        }
      }
      SCHED();
      if (c > 0) {
#pragma unroll
        for (int k = 0; k < CH; ++k) {
          const int kk = CH * (c - 1) + k;
          if (kk < KSW) {
            acc[0][0] = mfma32(Wf[0][kk], af[(c - 1) & 1][k][0], acc[0][0]); acc[0][1] = mfma32(Wf[1][kk], af[(c - 1) & 1][k][0], acc[0][1]);
            acc[1][0] = mfma32(Wf[0][kk], af[(c - 1) & 1][k][1], acc[1][0]); acc[1][1] = mfma32(Wf[1][kk], af[(c - 1) & 1][k][1], acc[1][1]);
          }
          SCHED();
          if (ahead && (mode == 2) == (c < 3)) {
            const int q = ((c - 1) & 1) * 4;
            if (WIDE) piece(b + 1, q + k); else { piece(b + 1, q + 2 * k); piece(b + 1, q + 2 * k + 1); }
          }
          SCHED();
        }
        SCHED();
      }
    }
    SEG(1);
    if (WIDE) {
      const unsigned so = (SG == SG_WO ? WS_P2 : WS_P8) + (unsigned)(b * 8 + kq * 2 + kp) * 65536u, vo = (unsigned)(fr * D_ + c8) * 2u;
      stx16(rs, vo, so, widen(acc[0][0], acc[0][1])); stx16(rs, vo, so + 32768, widen(acc[1][0], acc[1][1]));
      if (ahead && mode != 2) wait_v<8 + NS>(); else wait_v<NS>();
    } else {
#pragma unroll
      for (int mt = 0; mt < 2; ++mt)
        if (kp != mt) {
          LAS unsigned char* xw = lad(l16, lbase + G_XCH + (((kp > mt ? kp - 1 : kp) * 2 + mt) * 2 + cg) * 2048);
          *(LAS f32x4*)(xw) = acc[mt][0]; *(LAS f32x4*)(xw + 1024) = acc[mt][1];
        }
      if (ahead && mode != 2) wait_v<8>(); else wait_v<0>();
    }
    asm volatile("" : "+v"(peek));
    if (SG == SG_W2) asm volatile("" : "+v"(res));
    if (SG == SG_W3) asm volatile("" : "+v"(st[0]), "+v"(st[1]), "+v"(st[2]), "+v"(st[3]));
    if (w == 0) {
      const unsigned long long ok = __ballot(peek >= (lane < 32 ? need(b + 1) : need(b + 2)));
      const unsigned r = (((unsigned)ok == 0xFFFFFFFFu && b + 1 < NBLK) ? 1u : 0u) | (((unsigned)(ok >> 32) == 0xFFFFFFFFu && b + 2 < NBLK) ? 2u : 0u);
      if (lane == 0) *(LAS unsigned*)(lds + G_RDY) = r;
    }
    SEG(2);
    LBAR();
    SEG(3);
    if (mode == 2 && tid == 448) stx4(rs, 0, myword, (unsigned)b);
    const unsigned rdy = __builtin_amdgcn_readfirstlane(*(const LAS unsigned*)(lds + G_RDY));
    if (!WIDE && w < 4) {
      LAS unsigned char* xr = lad(l16, lbase + G_XCH + w * 2048);
      f32x4 y0 = kp ? acc[1][0] : acc[0][0], y1 = kp ? acc[1][1] : acc[0][1];
#pragma unroll
      for (int i = 0; i < 3; ++i) { y0 += *(const LAS f32x4*)(xr + i * 8192); y1 += *(const LAS f32x4*)(xr + i * 8192 + 1024); }
      const unsigned fo = (unsigned)((c8 >> 5) * 2048 + ((c8 >> 3) & 3) * 256);
      if (SG == SG_W1) {
        u32x4 v = widen(y0, y1);
#pragma unroll
        for (int i = 0; i < 4; ++i) v[i] = pack2(gelu_tanh(lo2f(v[i])), gelu_tanh(hi2f(v[i])));
        const int grp = cb >> 2, sh = (grp & 1) ? -(1 + (grp >> 1)) : (1 + (grp >> 1));
        const int rho = 32 * b + row + sh + 2;
        const unsigned vo = (rho >= 2 && rho < S_ + 2) ? (unsigned)(rho >> 5) * 65536u + fo + (unsigned)((rho >> 4) & 1) * 1024u + (unsigned)(rho & 15) * 16u : 0xF0000000u;
        stx16(rs, vo, WS_H1S, v);
      } else if (SG == SG_W2) {
        u32x4 v = widen(y0, y1);
        float z[8];
#pragma unroll
        for (int i = 0; i < 4; ++i) { v[i] = pack2(lo2f(res[i]) + lo2f(v[i]), hi2f(res[i]) + hi2f(v[i])); z[2 * i] = lo2f(v[i]); z[2 * i + 1] = hi2f(v[i]); }
        stx16(rs, fo + kp * 1024 + fr * 16, WS_X2 + (unsigned)b * 65536u, v);
        const float mw = gsum(((z[0] + z[1]) + (z[2] + z[3])) + ((z[4] + z[5]) + (z[6] + z[7]))) * (1.0f / 32.0f);
        float q = 0.f;
#pragma unroll
        for (int i = 0; i < 8; ++i) { const float d = z[i] - mw; q += d * d; }
        q = gsum(q);
        if (g == 0) stx8(rs, (unsigned)(cb * 512 + row * 16 + cg * 8), WS_ST5 + (unsigned)b * 8192, u32x2{f2u(mw), f2u(q)});
      } else {
        float ms = 0.f;
#pragma unroll
        for (int i = 0; i < 4; ++i) ms += u2f(st[i][0]) + u2f(st[i][2]);
        const float mean = gsum(ms) * (1.0f / 32.0f);
        float q = 0.f;
#pragma unroll
        for (int i = 0; i < 4; ++i) { const float d0 = u2f(st[i][0]) - mean, d1 = u2f(st[i][2]) - mean; q += u2f(st[i][1]) + u2f(st[i][3]) + 32.0f * (d0 * d0 + d1 * d1); }
        const float var = gsum(q) * (1.0f / 1024.0f);
        const float m = rbf(mean), isd = __builtin_amdgcn_rcpf(rbf(__builtin_amdgcn_sqrtf(rbf(rbf(var) + 1e-5f))));
        y0 = (y0 - m * cs[0]) * isd; y1 = (y1 - m * cs[1]) * isd;
        u32x4 v = widen(y0, y1);
#pragma unroll
        for (int i = 0; i < 4; ++i) v[i] = pack2(gelu_tanh(lo2f(v[i])), gelu_tanh(hi2f(v[i])));
        stx16(rs, fo + kp * 1024 + fr * 16, WS_U + (unsigned)b * 229376u, v);
      }
    }
    if (ahead) mode = 2;
    else {
      if (rdy & 1u) { fetch(b + 1); wait_v<8>(); mode = 1; } else { wait_v<0>(); mode = 0; }
      BAR();
      if (tid == 448) stx4(rs, 0, myword, (unsigned)b + 1u);
    }
    ahead = mode != 0 && (rdy & 2u);
    WST(rs, wk, 16 + b);
    SEG(4);
  }
}

__device__ __forceinline__ void wait32(__amdgpu_buffer_rsrc_t rs, int first, int mine, int lane, unsigned need, unsigned& cap) {
  wait_ge(rs, 0, F_ST + (unsigned)(first + mine) * 128u, need, cap);
  wait_ge(rs, (unsigned)(lane & 31) * 128u, F_ST + (unsigned)first * 128u, need, cap);
}
__device__ __forceinline__ void sum_partials(__amdgpu_buffer_rsrc_t rs, unsigned l16, unsigned so, float (&a)[16]) {
  u32x4 pp[8][2];
#pragma unroll
  for (int k = 0; k < 8; ++k) { pp[k][0] = ldx16(rs, l16, so + k * 65536u); pp[k][1] = ldx16(rs, l16, so + k * 65536u + 1024); }
#pragma unroll
  for (int i = 0; i < 8; ++i) {
    f32x2 t = {0.f, 0.f};
#pragma unroll
    for (int k = 0; k < 8; ++k) t += f32x2{lo2f(pp[k][i >> 2][i & 3]), hi2f(pp[k][i >> 2][i & 3])};
    a[2 * i] = rbf(t[0]); a[2 * i + 1] = rbf(t[1]);
  }
}
__device__ __forceinline__ void put_row(__amdgpu_buffer_rsrc_t rs, unsigned l16, unsigned so, const float (&v)[16]) {
  stx16(rs, l16, so, u32x4{pack2(v[0], v[1]), pack2(v[2], v[3]), pack2(v[4], v[5]), pack2(v[6], v[7])});
  stx16(rs, l16, so + 1024, u32x4{pack2(v[8], v[9]), pack2(v[10], v[11]), pack2(v[12], v[13]), pack2(v[14], v[15])});
}
__device__ __forceinline__ void load_vec16(const float* p, int lane, float (&v)[16]) {
#pragma unroll
  for (int i = 0; i < 4; ++i) { const f32x4 t = *(const f32x4*)(p + (i >> 1) * 512 + lane * 8 + (i & 1) * 4); v[4 * i] = t[0]; v[4 * i + 1] = t[1]; v[4 * i + 2] = t[2]; v[4 * i + 3] = t[3]; }
}

__device__ __forceinline__ void stage_mid(__amdgpu_buffer_rsrc_t rs, const u16* x, const float* mod, int wk, int cu, unsigned& cap) {
  const int tid = threadIdx.x, lane = tid & 63, w = __builtin_amdgcn_readfirstlane(tid >> 6);
  const int grp = w >> 1, rl = cu * 2 + (w & 1);
  const unsigned l16 = lane * 16;
  float ga[16], sc[16], sh[16];
  load_vec16(mod + 2048, lane, ga); load_vec16(mod + 4096, lane, sc); load_vec16(mod + 3072, lane, sh);
#pragma unroll
  for (int i = 0; i < 16; ++i) sc[i] = rbf(1.0f + sc[i]);
  for (int seg = grp; seg < NSEG; seg += 4) {
    const int row = seg * 32 + rl;
    float v[16], a[16];
    load_row16(x + (size_t)row * D_, lane, v);
    wait32(rs, P_S2, rl, lane, (unsigned)seg + 1u, cap);
    sum_partials(rs, l16, WS_P2 + (unsigned)seg * 524288u + (unsigned)rl * 2048, a);
#pragma unroll
    for (int i = 0; i < 16; ++i) v[i] = rbf(v[i] + rbf(ga[i] * a[i]));
    put_row(rs, l16, WS_X1 + (unsigned)row * 2048, v);
    ln_row(v);
#pragma unroll
    for (int i = 0; i < 16; ++i) v[i] = rbf(rbf(v[i] * sc[i]) + sh[i]);
    put_row(rs, l16, WS_H2 + (unsigned)row * 2048, v);
    ln_row(v);
    put_row(rs, l16, WS_G + (unsigned)row * 2048, v);
    WAIT_V(0);
    if (lane == 0) stx4(rs, 0, F_ST + (unsigned)(P_S3 + grp * 32 + rl) * 128, (unsigned)seg + 1u);
    WST(rs, wk, 16 + seg);
  }
}

__device__ __forceinline__ void stage_out(__amdgpu_buffer_rsrc_t rs, u16* out, const float* mod, int wk, int cu, unsigned& cap) {
  const int tid = threadIdx.x, lane = tid & 63, w = __builtin_amdgcn_readfirstlane(tid >> 6);
  const int grp = w >> 2, rl = cu * 4 + (w & 3);
  const unsigned l16 = lane * 16;
  float gb[16];
  load_vec16(mod + 5120, lane, gb);
  for (int b = grp; b < 65; b += 2) {
    const int t = 32 * b - 2 + rl;
    if (t < 0 || t >= S_) continue;
    wait32(rs, P_S8, rl, lane, (unsigned)b + 1u, cap);
    const unsigned xo = (unsigned)((lane >> 2) * 2048 + (lane & 3) * 256), xs = WS_X2 + (unsigned)b * 65536u + (unsigned)((rl >> 4) * 1024 + (rl & 15) * 16);
    const u32x4 x2a = ldx16(rs, xo, xs), x2b = ldx16(rs, xo, xs + 32768);
    const u32x4 x1a = ldx16(rs, l16, WS_X1 + (unsigned)t * 2048), x1b = ldx16(rs, l16, WS_X1 + (unsigned)t * 2048 + 1024);
    float a[16];
    sum_partials(rs, l16, WS_P8 + (unsigned)b * 524288u + (unsigned)rl * 2048, a);
#pragma unroll
    for (int i = 0; i < 4; ++i) {
      a[2 * i] = lo2f(x1a[i]) + rbf(gb[2 * i] * rbf(lo2f(x2a[i]) + a[2 * i])); a[2 * i + 1] = hi2f(x1a[i]) + rbf(gb[2 * i + 1] * rbf(hi2f(x2a[i]) + a[2 * i + 1]));
      a[8 + 2 * i] = lo2f(x1b[i]) + rbf(gb[8 + 2 * i] * rbf(lo2f(x2b[i]) + a[8 + 2 * i])); a[9 + 2 * i] = hi2f(x1b[i]) + rbf(gb[9 + 2 * i] * rbf(hi2f(x2b[i]) + a[9 + 2 * i]));
    }
    store_row16(out + (size_t)t * D_, lane, a);
    WST(rs, wk, 16 + b);
  }
}

__device__ __forceinline__ void worker(const MainP& p, LAS unsigned char* lds, int xw, int j) {
  const int tid = threadIdx.x, lane = tid & 63, w = __builtin_amdgcn_readfirstlane(tid >> 6);
  const __amdgpu_buffer_rsrc_t rs = mk_rs(p.ws, WS_END);
  const float* mod = (const float*)(p.ws + WS_MOD);
  unsigned cap = SPIN_CAP;
  const int wk = xw * 32 + j;
  WST(rs, wk, 0);
  {
    const int c = j * 6 + xw;
    for (int row = c * 8 + w; row < S_; row += 1536) {
      float v[16];
      load_row16(p.x + (size_t)row * D_, lane, v);
      ln_row(v);
      modulate(v, mod + 1024, mod, lane);
      stx16(rs, (unsigned)lane * 16, WS_H + (unsigned)row * 2048, u32x4{pack2(v[0], v[1]), pack2(v[2], v[3]), pack2(v[4], v[5]), pack2(v[6], v[7])});
      stx16(rs, (unsigned)lane * 16, WS_H + (unsigned)row * 2048 + 1024, u32x4{pack2(v[8], v[9]), pack2(v[10], v[11]), pack2(v[12], v[13]), pack2(v[14], v[15])});
    }
    publish(rs, F_LN + c * 128, 1u);
    WST(rs, wk, 1);
  }
  for (int r = 0; r < 8; ++r) {
    int rb, mat, hd;
    if (r < 5) { rb = 3 * r + wk % 3; mat = (wk / 3) & 1; hd = (wk / 3) >> 1; }
    else {
      const int i = (r - 5) * 192 + wk;
      if (i < 384) { rb = 6 * (r - 5) + wk % 6; mat = 2; hd = wk / 6; }
      else if (i < 512) { rb = 12 + (wk & 3); mat = 2; hd = wk >> 2; }
      else { rb = 15; mat = wk & 1; hd = (wk - 128) >> 1; }
    }
    if (w == 0) wait_ge(rs, (unsigned)((rb % 12) * 16 + (lane & 15)) * 128u, F_LN, 1u, cap);
    BAR();
    if (mat == 1) proj_tile<true>(lds, rs, mk_rs(p.Wv, D_ * HD * 2), WS_VT, rb, hd);
    else proj_tile<false>(lds, rs, mk_rs(mat == 0 ? p.Wk : p.Wq, D_ * HD * 2), mat == 0 ? WS_K : WS_Q, rb, hd);
    publish(rs, F_TL + (unsigned)(mat * 512 + rb * 32 + hd) * 4, 1u);
    WST(rs, wk, 2 + r);
  }
  if (xw == 0) stage_gemm<SG_WO>(lds, rs, p.Wo, wk, j >> 3, j & 7, cap);
  else if (xw == 1) stage_gemm<SG_W4>(lds, rs, p.w4, wk, j >> 3, j & 7, cap);
  else if (xw < 4) {
    if (j < 28) stage_gemm<SG_W3>(lds, rs, p.w3, wk, 0, (xw - 2) * 28 + j, cap);
    else stage_out(rs, p.out, mod, wk, (xw - 2) * 4 + j - 28, cap);
  } else if (xw == 4) {
    if (j < 16) stage_gemm<SG_W1>(lds, rs, p.w1, wk, 0, j, cap);
    else stage_gemm<SG_W2>(lds, rs, p.w2, wk, 0, j - 16, cap);
  } else {
    if (j < 16) stage_attn(rs, wk, j >> 2, j & 3, cap);
    else stage_mid(rs, p.x, mod, wk, j - 16, cap);
  }
  if (cap == 0u && lane == 0) stx4(rs, 0, X_CTL, 1u);
}

#define ST_DECL
#define ST0()
#define ST(i)
#define ST_FLUSH()
constexpr int R_W = 0, R_MEM = 131072, R_QK = R_MEM + 16384, R_KT = R_QK + 8192, R_RED = R_MEM + 16384, R_LDS = 163840;
__device__ __forceinline__ void bar_lo(int w) { asm volatile("s_cmp_ge_u32 %0, 4\n\ts_cbranch_scc1 1f\n\ts_barrier\n\t1:" :: "s"(w) : "scc", "memory"); }
__device__ __forceinline__ void bar_hi(int w) { asm volatile("s_cmp_lt_u32 %0, 4\n\ts_cbranch_scc1 1f\n\ts_setprio 3\n\ts_barrier\n\t1:" :: "s"(w) : "scc", "memory"); }
__device__ __forceinline__ void issue4(__amdgpu_buffer_rsrc_t rs, unsigned vo, unsigned s0, unsigned s1, unsigned s2, unsigned s3, u32x4& a0, u32x4& a1, u32x4& a2, u32x4& a3) {
  asm volatile("buffer_load_dwordx4 v[240:243], %[vo], %[rs], %[s0] offen sc1\n\tbuffer_load_dwordx4 v[244:247], %[vo], %[rs], %[s1] offen sc1\n\t"
               "buffer_load_dwordx4 v[248:251], %[vo], %[rs], %[s2] offen sc1\n\tbuffer_load_dwordx4 v[252:255], %[vo], %[rs], %[s3] offen sc1"
               : "=&{v[240:243]}"(a0), "=&{v[244:247]}"(a1), "=&{v[248:251]}"(a2), "=&{v[252:255]}"(a3)
               : [vo] "v"(vo), [rs] "s"(rs), [s0] "s"(s0), [s1] "s"(s1), [s2] "s"(s2), [s3] "s"(s3) : "memory");
}
#define ARRIVE(R0, R1, R2, R3, CNT) \
  asm volatile("s_waitcnt vmcnt(" #CNT ")\n\t" \
               "1:\n\t" \
               "v_min3_u32 %[t], v" #R0 ", v" #R1 ", v" #R2 "\n\t" \
               "v_min_u32 %[t], %[t], v" #R3 "\n\t" \
               "v_cmp_eq_u32 vcc, 0, %[t]\n\t" \
               "s_cbranch_vccz 2f\n\t" \
               "s_mov_b64 exec, vcc\n\t" \
               "buffer_load_dwordx4 v[" #R0 ":" #R3 "], %[vo], %[rs], %[so] offen sc1\n\t" \
               "s_mov_b64 exec, -1\n\t" \
               "s_waitcnt vmcnt(0)\n\t" \
               "s_sub_u32 %[cap], %[cap], 1\n\t" \
               "s_cbranch_scc0 1b\n\t" \
               "s_mov_b32 %[cap], 0\n\t" \
               "2:" \
               : "+{v[" #R0 ":" #R3 "]}"(a), [t] "=&v"(t), [cap] "+s"(cap) : [vo] "v"(vo), [rs] "s"(rs), [so] "s"(so) : "vcc", "scc", "memory")
template <int I> __device__ __forceinline__ void arrive(__amdgpu_buffer_rsrc_t rs, unsigned vo, unsigned so, u32x4& a, unsigned& cap) {
  unsigned t;
  if (I == 0) ARRIVE(240, 241, 242, 243, 3);
  else if (I == 1) ARRIVE(244, 245, 246, 247, 2);
  else if (I == 2) ARRIVE(248, 249, 250, 251, 1);
  else ARRIVE(252, 253, 254, 255, 0);
}
__device__ __forceinline__ void poll1_finish(__amdgpu_buffer_rsrc_t rs, unsigned vo, unsigned s0, u32x2& a, unsigned& cap) {
  unsigned t;
  asm volatile("1:\n\t"
               "v_min_u32 %[t], v254, v255\n\t"
               "v_cmp_eq_u32 vcc, 0, %[t]\n\t"
               "s_cbranch_vccz 2f\n\t"
               "buffer_load_dwordx2 v[254:255], %[vo], %[rs], %[s0] offen sc1\n\t"
               "s_waitcnt vmcnt(0)\n\t"
               "s_sub_u32 %[cap], %[cap], 1\n\t"
               "s_cbranch_scc0 1b\n\t"
               "s_mov_b32 %[cap], 0\n\t"
               "2:\n\ts_nop 1"
               : "+{v[254:255]}"(a), [t] "=&v"(t), [cap] "+s"(cap) : [vo] "v"(vo), [rs] "s"(rs), [s0] "s"(s0) : "vcc", "scc", "memory");
}
__device__ __forceinline__ u32x4 add_bf16x8(u32x4 a, u32x4 b) {
  u32x4 r;
#pragma unroll
  for (int i = 0; i < 4; ++i) { const f32x2 x = f32x2{lo2f(a[i]), hi2f(a[i])} + f32x2{lo2f(b[i]), hi2f(b[i])}; r[i] = pack2(x[0], x[1]); }
  return r;
}
__device__ __forceinline__ void gather_mem(__amdgpu_buffer_rsrc_t rs, unsigned lbase, int s, int half, int w, unsigned l16, unsigned& cap) {
  const unsigned so = X_MEMP + s * 65536 + half * 32768 + w * 2048;
  u32x4 a0, a1, b0, b1;
  issue4(rs, l16, so, so + 16384, so + 1024, so + 16384 + 1024, a0, b0, a1, b1);
  LAS unsigned char* d = lad(l16, lbase + R_MEM + w * 2048);
  arrive<0>(rs, l16, so, a0, cap); arrive<1>(rs, l16, so + 16384, b0, cap);
  *(LAS u32x4*)(d) = add_bf16x8(a0, b0);
  SCHED();
  arrive<2>(rs, l16, so + 1024, a1, cap); arrive<3>(rs, l16, so + 16384 + 1024, b1, cap);
  *(LAS u32x4*)(d + 1024) = add_bf16x8(a1, b1);
}
__device__ __forceinline__ bf16x8 mem_frag(LAS unsigned char* mp, int chunk) { return cat8(trread(mp + chunk * 1024), trread(mp + chunk * 1024 + 512)); }
__device__ __forceinline__ bf16x16 w_frag(LAS unsigned char* p) {
  return cat16(cat8(trread(p), trread(p + 2048)), cat8(trread(p + 4096), trread(p + 6144)));
}
__device__ __forceinline__ bf16x16 l_frag(LAS unsigned char* p, int i) { return cat16(*(const LAS bf16x8*)(p + i * 2048), *(const LAS bf16x8*)(p + i * 2048 + 1024)); }
__device__ __forceinline__ void make_resident(LAS unsigned char* wb, unsigned lo, unsigned l16) {
#pragma unroll
  for (int c = 0; c < 2; ++c) {
    bf16x16 f[4];
#pragma unroll
    for (int t = 0; t < 4; ++t) f[t] = w_frag(wb + c * 8192 + (lo ^ (t << 5)));
    WAIT_L0(); SCHED();
#pragma unroll
    for (int t = 0; t < 4; ++t) {
      *(LAS bf16x8*)(wb + (c * 4 + t) * 2048 + l16) = __builtin_shufflevector(f[t], f[t], 0, 1, 2, 3, 4, 5, 6, 7);
      *(LAS bf16x8*)(wb + (c * 4 + t) * 2048 + 1024 + l16) = __builtin_shufflevector(f[t], f[t], 8, 9, 10, 11, 12, 13, 14, 15);
    }
    WAIT_L0(); SCHED();
  }
}
__device__ __forceinline__ f32x4 fold(f32x4 a, f32x4 b) {
  f32x4 r;
#pragma unroll
  for (int i = 0; i < 4; ++i) {
    const auto t = __builtin_amdgcn_permlane32_swap(f2u(a[i]), f2u(b[i]), false, false);
    r[i] = u2f(t[0]) + u2f(t[1]);
  }
  return r;
}

__device__ __forceinline__ unsigned retime(__amdgpu_buffer_rsrc_t rs, unsigned l16, int s, unsigned tau, int margin, int slack) {
  unsigned vo;
  asm volatile("v_and_b32 %0, 0x1f0, %1\n\tv_lshlrev_b32 %0, 3, %0" : "=v"(vo) : "v"(l16));
  const unsigned ts = ldx4(rs, vo, X_TS + (unsigned)s * 4096);
  int late = ts != 0u ? (int)(ts + margin - tau) : 0;
#pragma unroll
  for (int o = 16; o >= 1; o >>= 1) late = max(late, __shfl_xor(late, o));
  late = __builtin_amdgcn_readfirstlane(late);
  return late > 0 ? tau + late + slack : tau;
}

__device__ __forceinline__ void role_a(const MainP& p, LAS unsigned char* lds, int half, int hl) {
  const int tid = threadIdx.x, lane = tid & 63, w = __builtin_amdgcn_readfirstlane(tid >> 6);
  const int h = half * 16 + hl, cg = w & 3, kh = w >> 2;
  const __amdgpu_buffer_rsrc_t rs = mk_rs(p.ws, WS_END);
  const unsigned lbase = (unsigned)(size_t)lds;
  unsigned l16 = lane * 16, l8 = lane * 8;
  asm volatile("" : "+v"(l16), "+v"(l8));
  const int sidx = (lane & 8) ? 0xEEEE : 0x4444;
  bf16x16 W[24];
  unsigned qoff;
  {
    const int fr = lane & 15, g = lane >> 4, tu = 4 * (g & 1) + (fr >> 2), ub = lane >> 3;
    const unsigned lo = (g >> 1) * 1024 + tu * 128 + ((tu >> 1) << 5) + (fr & 3) * 8;
    qoff = R_QK + (g >> 1) * 256 + (fr & 1) * 128 + (8 * (g & 1) + (fr >> 2)) * 8;
    const __amdgpu_buffer_rsrc_t rw = mk_rs(cg < 2 ? p.Wq : p.Wk, D_ * HD * 2);
    const unsigned vo = (unsigned)(ub * HD + 16 * (((lane >> 1) & 3) ^ (ub >> 1)) + 8 * (lane & 1)) * 2u;
    const unsigned so = (unsigned)(kh * 512 * HD + h * DH + (cg & 1) * 64) * 2u;
    auto stage = [&](int c) {
#pragma unroll
      for (int rho = 0; rho < 8; ++rho) dma16(rw, vo, so + (unsigned)((c * 64 + rho * 8) * HD) * 2u, lbase + R_W + w * 16384 + (c & 1) * 8192 + rho * 1024);
    };
    stage(0); stage(1);
#pragma unroll
    for (int c = 0; c < 6; ++c) {
      WAIT_V(8);
#pragma unroll
      for (int t = 0; t < 4; ++t) W[4 * c + t] = w_frag(lds + R_W + w * 16384 + (c & 1) * 8192 + (lo ^ (t << 5)));
      WAIT_L0(); SCHED();
      stage(c + 2);
    }
    WAIT_V(0);
    make_resident(lds + R_W + w * 16384, lo, l16);
  }
  unsigned cap = SPIN_CAP;
  wait_ge(rs, (l16 & 16) * 128, F_TL + h * 4, 1u, cap);
  dma16x(rs, l16, WS_K + h * 8192 + w * 1024, lbase + R_KT + w * 1024);
  unsigned tau = rt32() - 100000;
  const int cu_ = half * 32 + hl; (void)cu_;
  ST_DECL
  for (int s = 0; s < NSEG; ++s) {
    LAS unsigned char* lp = lad(l16, lbase + R_W + w * 16384);
    bf16x16 L[2];
    L[0] = l_frag(lp, 0); L[1] = l_frag(lp, 1);
    SCHED();
    if (w == 7) wait_until(tau);
    BAR();
    ST0();
    gather_mem(rs, lbase, s, half, (w + hl) & 7, l16, cap);
    ST(1);
    LBAR();
    ST(2);
    f32x4 c[4];
#pragma unroll
    for (int t = 0; t < 4; ++t) c[t] = f32x4{0.f, 0.f, 0.f, 0.f};
    LAS unsigned char* mp = lad(l8, lbase + R_MEM + kh * 8192);
    bf16x8 al, af[2];
    u32x4 vt0, vt1;
    u32x2 vmv;
    al = mem_frag(mp, 6); af[0] = mem_frag(mp, 0); af[1] = mem_frag(mp, 1);
#pragma unroll
    for (int i = 0; i < 8; ++i) {
      c[i & 3] = smf(al, L[i & 1], c[i & 3], sidx);
      SCHED();
      if (i == 3) al = mem_frag(mp, 7);
      if (i < 6) L[i & 1] = l_frag(lp, i + 2);
      if (i == 6) {
        const unsigned vo = WS_VT + (unsigned)(s * NH + h) * 8192 + cg * 2048;
        vt0 = ldx16(rs, l16, vo); vt1 = ldx16(rs, l16, vo + 1024);
      }
      SCHED();
#pragma unroll
      for (int r = 3 * i; r < 3 * i + 3; ++r) {
        c[(r + 1) & 3] = smf(af[(r >> 2) & 1], W[(r & ~3) + ((r + 1) & 3)], c[(r + 1) & 3], sidx);
        SCHED();
        if ((r & 3) == 3 && (r >> 2) + 2 < 6) af[(r >> 2) & 1] = mem_frag(mp, (r >> 2) + 2);
        SCHED();
      }
    }
    {
      const f32x4 f0 = fold(c[0], c[1]), f1 = fold(c[2], c[3]);
      *(LAS f32x4*)(lad(l16, lbase + R_QK + (cg * 2 + (kh ^ 1)) * 1024)) = kh ? f0 : f1;
      LBAR();
      const f32x4 f = (kh ? f1 : f0) + *(const LAS f32x4*)(lad(l16, lbase + R_QK + (cg * 2 + kh) * 1024));
      *(LAS u32x2*)(lad(l8, lbase + R_QK + (cg * 2 + kh) * 1024)) = u32x2{pack2(f[0], f[1]), pack2(f[2], f[3])};
    }
    ST(3);
    if (w < 4) {
      const unsigned vso = X_VM + (unsigned)(s * NH + h) * 2048 + w * 512;
      bf16x8 k1[4], k2[4];
      LAS unsigned char* kb = lad(l16, lbase + R_KT);
#pragma unroll
      for (int ks = 0; ks < 2; ++ks) { k1[ks] = *(const LAS bf16x8*)(kb + ks * 1024); k2[ks] = *(const LAS bf16x8*)(kb + 4096 + ks * 1024); }
      LBAR();
      ST(4);
      vmv = ldx8(rs, l8, vso);
      f32x4 t0 = {0.f, 0.f, 0.f, 0.f}, t1 = t0, t2 = t0;
      LAS unsigned char* qb = lad(qoff, lbase);
      bf16x8 qf[4], kf[4];
#pragma unroll
      for (int ks = 0; ks < 2; ++ks) {
        qf[ks] = cat8(trread(qb + ks * 1024), trread(qb + ks * 1024 + 32));
        kf[ks] = cat8(trread(qb + 4096 + ks * 1024), trread(qb + 4096 + ks * 1024 + 32));
      }
      SCHED();
#pragma unroll
      for (int ks = 0; ks < 4; ++ks) {
        t1 = mfma32(k1[ks], qf[ks], t1); t2 = mfma32(k2[ks], qf[ks], t2); t0 = mfma32(kf[ks], qf[ks], t0);
        SCHED();
        if (ks < 2) {
          qf[ks + 2] = cat8(trread(qb + (ks + 2) * 1024), trread(qb + (ks + 2) * 1024 + 32));
          k1[ks + 2] = *(const LAS bf16x8*)(kb + (ks + 2) * 1024); k2[ks + 2] = *(const LAS bf16x8*)(kb + 4096 + (ks + 2) * 1024);
          kf[ks + 2] = cat8(trread(qb + 4096 + (ks + 2) * 1024), trread(qb + 4096 + (ks + 2) * 1024 + 32));
        }
        SCHED();
      }
      ST(5);
      if (s + 1 < NSEG) {
        const float sl = 0.08838834764831845f * 1.4426950408889634f;
        float mx = fmaxf(fmaxf(fmaxf(t1[0], t1[1]), fmaxf(t1[2], t1[3])), fmaxf(fmaxf(t2[0], t2[1]), fmaxf(t2[2], t2[3])));
        mx = fmaxf(mx, fmaxf(fmaxf(t0[0], t0[1]), fmaxf(t0[2], t0[3])));
        const float nm = -gmax(mx) * sl;
#pragma unroll
        for (int r = 0; r < 4; ++r) { t1[r] = __builtin_amdgcn_exp2f(fmaf(t1[r], sl, nm)); t2[r] = __builtin_amdgcn_exp2f(fmaf(t2[r], sl, nm)); }
        const u32x4 pt = {pack2(t1[0], t1[1]), pack2(t1[2], t1[3]), pack2(t2[0], t2[1]), pack2(t2[2], t2[3])};
        f32x4 o0 = {0.f, 0.f, 0.f, 0.f}, o1 = o0;
        o0 = mfma32(__builtin_bit_cast(bf16x8, vt0), __builtin_bit_cast(bf16x8, pt), o0);
        o1 = mfma32(__builtin_bit_cast(bf16x8, vt1), __builtin_bit_cast(bf16x8, pt), o1);
#pragma unroll
        for (int r = 0; r < 4; ++r) t0[r] = __builtin_amdgcn_exp2f(fmaf(t0[r], sl, nm));
        const u32x2 pmv = {pack2(t0[0], t0[1]), pack2(t0[2], t0[3])};
        const u32x2 zz = {0u, 0u};
        const u32x2 pm0 = (l16 < 512) ? pmv : zz, pm1 = (l16 < 512) ? zz : pmv;
        const float ssum = ((t1[0] + t1[1]) + (t1[2] + t1[3])) + ((t2[0] + t2[1]) + (t2[2] + t2[3])) + 0.5f * ((t0[0] + t0[1]) + (t0[2] + t0[3]));
        const float inv = __builtin_amdgcn_rcpf(gsum(ssum));
        ST(6);
        poll1_finish(rs, l8, vso, vmv, cap);
        ST(7);
        o0 = mfma16n(vmv, pm0, o0);
        o1 = mfma16n(vmv, pm1, o1);
        if ((l16 & 128) == 0) {
          const u32x4 v = {nz(pack2(o0[0] * inv, o0[1] * inv)), nz(pack2(o0[2] * inv, o0[3] * inv)), nz(pack2(o1[0] * inv, o1[1] * inv)), nz(pack2(o1[2] * inv, o1[3] * inv))};
          stx16(rs, l16, X_OX + s * 65536 + half * 32768 + (hl * 2 + (w >> 1)) * 1024 + (w & 1) * 128, v);
        }
        ST(8);
      }
    } else {
      LBAR();
      LAS unsigned char* qb = lad(qoff, lbase + 4096 + (w - 4) * 1024);
      const bf16x8 kf = cat8(trread(qb), trread(qb + 32));
      stx16(rs, l16, WS_KM + (unsigned)(s * NH + h) * 4096 + (w - 4) * 1024, __builtin_bit_cast(u32x4, kf));
      WAIT_V(0);
    }
    LBAR();
    if (w == 4 && l16 == 0) stx4(rs, 0, F_CH + h * 128, (unsigned)s + 1u);
    ST(9);
    if (w == 7) tau = retime(rs, l16, s, tau, p.d3, p.d4) + p.d2;
    if (s + 1 < NSEG) {
      unsigned fo;
      asm volatile("v_and_b32 %0, 16, %1\n\tv_lshlrev_b32 %0, 7, %0" : "=v"(fo) : "v"(l16));
      if (((s + 1) & 3) == 0) wait_ge(rs, fo, F_TL + (unsigned)(((s + 1) >> 2) * 32 + h) * 4, 1u, cap);
      dma16x(rs, l16, WS_K + (unsigned)((s + 1) * NH + h) * 8192 + w * 1024, lbase + R_KT + w * 1024);
    }
  }
  ST_FLUSH();
  if (cap == 0u && l16 == 0) stx4(rs, 0, X_CTL, 1u);
}

__device__ __forceinline__ void role_vb(const MainP& p, LAS unsigned char* lds, int half, int jb) {
  const int tid = threadIdx.x, lane = tid & 63, w = __builtin_amdgcn_readfirstlane(tid >> 6);
  const int h = half * 16 + jb, wr = (w + jb) & 7;
  const __amdgpu_buffer_rsrc_t rs = mk_rs(p.ws, WS_END);
  const unsigned lbase = (unsigned)(size_t)lds;
  unsigned l16 = lane * 16, l8 = lane * 8;
  asm volatile("" : "+v"(l16), "+v"(l8));
  const int sidx = (lane & 8) ? 0xEEEE : 0x4444;
  bf16x16 WV[16], WB[8];
  unsigned roff;
  {
    const int fr = lane & 15, g = lane >> 4, tu = 4 * (g & 1) + (fr >> 2), ub = lane >> 3;
    const unsigned lo = (g >> 1) * 1024 + tu * 128 + ((tu >> 1) << 5) + (fr & 3) * 8;
    roff = R_RED + w * 2048 + mem_slot(16 * (g >> 1) + fr) * 32 + (g & 1) * 16;
    const unsigned vcol = (unsigned)(16 * (((lane >> 1) & 3) ^ (ub >> 1)) + 8 * (lane & 1)) * 2u;
    const __amdgpu_buffer_rsrc_t rv = mk_rs(p.Wv, D_ * HD * 2), ro = mk_rs(p.Wo, D_ * HD * 2);
    const unsigned vv = (unsigned)(ub * HD) * 2u + vcol, sv = (unsigned)(8 * w * HD + h * DH) * 2u;
    auto stage = [&](int c) {
      const unsigned d = lbase + R_W + (c & 7) * 16384 + w * 1024, sc = sv + (unsigned)c * 64 * HD * 2;
      dma16(rv, vv, sc, d); dma16(rv, vv, sc + 128, d + 8192);
    };
#pragma unroll
    for (int c = 0; c < 8; ++c) stage(c);
    const int vo = (w >> 2) * 8192 + (lo ^ ((w & 3) << 5));
#pragma unroll
    for (int c = 0; c < 16; ++c) {
      if (c < 8) WAIT_V(14); else wait_v<0>();
      BAR();
      WV[c] = w_frag(lds + R_W + (c & 7) * 16384 + vo);
      LBAR();
      if (c < 8) stage(c + 8);
    }
    const unsigned vw = (unsigned)((32 * ((ub >> 1) & 1) + 16 * (ub >> 2) + (ub & 1)) * D_) * 2u + vcol;
    const unsigned sw = (unsigned)((half * 2048 + wr * 256) * D_ + jb * 64) * 2u;
    auto stageo = [&](int c) {
      const unsigned d = lbase + R_W + w * 16384 + (c & 1) * 8192;
#pragma unroll
      for (int rho = 0; rho < 8; ++rho) {
        const int r = rho >> 1, hb = rho & 1;
        dma16(ro, vw, sw + (unsigned)((c * 64 + 8 * (r >> 1) + 4 * hb + 2 * (r & 1)) * D_) * 2u, d + rho * 1024);
      }
    };
    stageo(2); stageo(3);
    WAIT_V(0);
#pragma unroll
    for (int c = 0; c < 2; ++c)
#pragma unroll
      for (int t = 0; t < 4; ++t) WB[c * 4 + t] = w_frag(lds + R_W + w * 16384 + c * 8192 + (lo ^ (t << 5)));
    WAIT_L0(); SCHED();
    stageo(0); stageo(1);
    WAIT_V(0);
    make_resident(lds + R_W + w * 16384, lo, l16);
  }
  unsigned cap = SPIN_CAP, tau = rt32() - 100000;
  const int cu_ = half * 32 + 16 + jb; (void)cu_;
  ST_DECL
  for (int s = 0; s < NSEG; ++s) {
    if (w == 7) wait_until(tau);
    BAR();
    ST0();
    gather_mem(rs, lbase, s, half, wr, l16, cap);
    ST(1);
    LBAR();
    ST(2);
    {
      f32x4 c0 = {0.f, 0.f, 0.f, 0.f}, c1 = c0;
      LAS unsigned char* mp = lad(l8, lbase + R_MEM);
      bf16x8 af[8];
#pragma unroll
      for (int c = 0; c < 8; ++c) af[c] = mem_frag(mp, c);
      SCHED();
#pragma unroll
      for (int c = 0; c < 8; ++c) {
        if (c & 1) c1 = smf(af[c], WV[c], c1, sidx); else c0 = smf(af[c], WV[c], c0, sidx);
        SCHED();
        if (c >= 3) { af[c] = mem_frag(mp, 8 + c); if (c < 6) af[c - 3] = mem_frag(mp, 5 + c); }
        SCHED();
      }
#pragma unroll
      for (int c = 8; c < 16; ++c) { if (c & 1) c1 = smf(af[c - 8], WV[c], c1, sidx); else c0 = smf(af[c - 8], WV[c], c0, sidx); }
      SCHED();
      c0 += c1;
      const f32x4 f = fold(c0, c0);
      if (l16 < 512) stx8(rs, l8, X_VM + (unsigned)(s * NH + h) * 2048 + w * 256, u32x2{nz(pack2(f[0], f[1])), nz(pack2(f[2], f[3]))});
    }
    ST(3);
    if (w == 7) tau = retime(rs, l16, s, tau, p.d3, p.d4);
    if (s + 1 < NSEG) {
      LAS unsigned char* lp = lad(l16, lbase + R_W + w * 16384);
      bf16x16 L[2];
      L[0] = l_frag(lp, 0); L[1] = l_frag(lp, 1);
      SCHED();
      u32x4 a[4];
      const unsigned so = X_OX + s * 65536 + half * 32768 + wr * 4096;
      if (w == 7) wait_until(tau + p.d1);
      BAR();
      ST(4);
      issue4(rs, l16, so, so + 1024, so + 2048, so + 3072, a[0], a[1], a[2], a[3]);
      f32x4 c[4];
#pragma unroll
      for (int t = 0; t < 4; ++t) c[t] = f32x4{0.f, 0.f, 0.f, 0.f};
      arrive<0>(rs, l16, so, a[0], cap);
#pragma unroll
      for (int i = 0; i < 8; ++i) {
        if (i == 4) arrive<1>(rs, l16, so + 1024, a[1], cap);
        c[i & 3] = smf(__builtin_bit_cast(bf16x8, a[i >> 2]), L[i & 1], c[i & 3], sidx);
        SCHED();
        if (i + 2 < 8) L[i & 1] = l_frag(lp, i + 2);
        SCHED();
      }
      arrive<2>(rs, l16, so + 2048, a[2], cap);
#pragma unroll
      for (int t = 0; t < 4; ++t) c[t] = smf(__builtin_bit_cast(bf16x8, a[2]), WB[t], c[t], sidx);
      SCHED();
      arrive<3>(rs, l16, so + 3072, a[3], cap);
#pragma unroll
      for (int t = 0; t < 4; ++t) c[t] = smf(__builtin_bit_cast(bf16x8, a[3]), WB[4 + t], c[t], sidx);
      ST(5);
      LAS unsigned char* rw = lad(roff, lbase);
      *(LAS f32x4*)(rw) = fold(c[0], c[1]);
      *(LAS f32x4*)(rw + 512) = fold(c[2], c[3]);
      ST(6);
      LBAR();
      ST(7);
      if (w < 4) {
        LAS unsigned char* rp = lad(l16, lbase + R_RED + (w & 1) * 1024);
        f32x4 v[8];
#pragma unroll
        for (int i = 0; i < 8; ++i) v[i] = *(const LAS f32x4*)(rp + i * 2048);
        const f32x4 sum = ((v[0] + v[1]) + (v[2] + v[3])) + ((v[4] + v[5]) + (v[6] + v[7]));
        const u32x2 o = {nz(pack2(sum[0], sum[1])), nz(pack2(sum[2], sum[3]))};
        stx8(rs, l8, X_MEMP + (s + 1) * 65536 + (half ^ 1 ^ (w >> 1)) * 32768 + half * 16384 + jb * 1024 + (w & 1) * 512, o);
      }
      else if (w == 4) { const unsigned t = rt32() | 1u; if (l16 == 0) stx4(rs, 0, X_TS + (unsigned)(s + 1) * 4096 + (unsigned)h * 128, t); }
      ST(8);
    }
    tau += p.d2;
  }
  ST_FLUSH();
  if (cap == 0u && l16 == 0) stx4(rs, 0, X_CTL, 1u);
}

__global__ void __launch_bounds__(512, 2) k_main(MainP p) {
  extern __shared__ __attribute__((aligned(16))) unsigned char smem[];
  const int b = blockIdx.x, x = b & 7, j = b >> 3;
  if (x >= 2) worker(p, (LAS unsigned char*)smem, x - 2, j);
  else if (j < 16) role_a(p, (LAS unsigned char*)smem, x, j);
  else role_vb(p, (LAS unsigned char*)smem, x, j - 16);
}

extern "C" void kernel_launch(void* const* d_in, const int* in_sizes, int n_in, void* d_out, int out_size, void* d_ws, size_t ws_size,
                              hipStream_t stream) {
  unsigned char* ws = (unsigned char*)d_ws;
  k_prep<<<256, 512, 0, stream>>>((const u16*)d_in[12], (const u16*)d_in[13], (const u16*)d_in[1], ws);
  MainP p;
  p.x = (const u16*)d_in[0]; p.Wq = (const u16*)d_in[2]; p.Wk = (const u16*)d_in[3]; p.Wv = (const u16*)d_in[4]; p.Wo = (const u16*)d_in[5];
  p.w1 = (const u16*)d_in[8]; p.w2 = (const u16*)d_in[9]; p.w3 = (const u16*)d_in[10]; p.w4 = (const u16*)d_in[11];
  p.out = (u16*)d_out; p.ws = ws; p.d1 = 222; p.d2 = 344; p.d3 = 15; p.d4 = 200;
  (void)hipFuncSetAttribute((const void*)k_main, hipFuncAttributeMaxDynamicSharedMemorySize, R_LDS);
  k_main<<<256, 512, R_LDS, stream>>>(p);
}
```

```cpp
#include <hip/hip_runtime.h>
#include <hip/hip_bf16.h>
#include <cstdlib>
#pragma clang diagnostic ignored "-Winline-asm"

#define LAS __attribute__((address_space(3)))
#define GAS __attribute__((address_space(1)))
typedef unsigned short u16;
typedef __bf16 bf16x2 __attribute__((ext_vector_type(2)));
typedef __bf16 bf16x4 __attribute__((ext_vector_type(4)));
typedef __bf16 bf16x8 __attribute__((ext_vector_type(8)));
typedef __bf16 bf16x16 __attribute__((ext_vector_type(16)));
typedef short s16x4 __attribute__((ext_vector_type(4)));
typedef float f32x2 __attribute__((ext_vector_type(2)));
typedef float f32x4 __attribute__((ext_vector_type(4)));
typedef unsigned u32x2 __attribute__((ext_vector_type(2)));
typedef unsigned u32x4 __attribute__((ext_vector_type(4)));

constexpr int S_ = 2048, D_ = 1024, NH = 32, DH = 128, HD = 4096, NSEG = 64, FF = 3584;

constexpr unsigned MB = 1u << 20;
constexpr unsigned X_MEMP = 0;
constexpr unsigned X_OX = 4 * MB;
constexpr unsigned X_VM = 8 * MB;
constexpr unsigned F_LN = 12 * MB;
constexpr unsigned F_ST = F_LN + 32768;
constexpr unsigned F_CH = F_ST + 98304;
constexpr unsigned X_CTL = F_CH + 4096;
constexpr unsigned X_TS = X_CTL + 4096;
constexpr unsigned X_BYTES = X_TS + 65 * 8192;
constexpr unsigned WS_MOD = 15 * MB;
constexpr unsigned WS_H = 16 * MB;
constexpr unsigned WS_Q = 20 * MB;
constexpr unsigned WS_K = 36 * MB;
constexpr unsigned WS_VT = 52 * MB;
constexpr unsigned WS_O = 68 * MB;
constexpr unsigned WS_X1 = 84 * MB;
constexpr unsigned WS_H2 = 88 * MB;
constexpr unsigned WS_G = 92 * MB;
constexpr unsigned WS_H1S = 96 * MB;
constexpr unsigned WS_X2 = 101 * MB;
constexpr unsigned WS_ST5 = 106 * MB;
constexpr unsigned WS_U = 108 * MB;
constexpr unsigned WS_KM = 124 * MB;
constexpr unsigned WS_P2 = 132 * MB;
constexpr unsigned WS_P8 = 164 * MB;
constexpr unsigned WS_END = 198 * MB;

__device__ __forceinline__ float bf2f(unsigned short b) { return __builtin_bit_cast(float, (unsigned)b << 16); }
__device__ __forceinline__ unsigned f2u(float f) { return __builtin_bit_cast(unsigned, f); }
__device__ __forceinline__ float u2f(unsigned u) { return __builtin_bit_cast(float, u); }
__device__ __forceinline__ float lo2f(unsigned w) { return __builtin_bit_cast(float, w << 16); }
__device__ __forceinline__ float hi2f(unsigned w) { return __builtin_bit_cast(float, w & 0xFFFF0000u); }
__device__ __forceinline__ unsigned pack2(float a, float b) { bf16x2 v; v[0] = (__bf16)a; v[1] = (__bf16)b; return __builtin_bit_cast(unsigned, v); }
__device__ __forceinline__ float rbf(float a) { return (float)(__bf16)a; }
__device__ __forceinline__ unsigned nz(unsigned w) { return max(w, 1u); }
__device__ __forceinline__ float gelu_tanh(float x) {
  const float z = 0.7978845608028654f * (x + 0.044715f * x * x * x);
  const float e = __builtin_amdgcn_exp2f(-2.885390081777927f * z);
  return x * __builtin_amdgcn_rcpf(1.0f + e);
}
template <int CTRL> __device__ __forceinline__ float dpp_add(float v) { return v + u2f(__builtin_amdgcn_update_dpp(0u, f2u(v), CTRL, 0xF, 0xF, true)); }
__device__ __forceinline__ float wave_sum(float v) {
  v = dpp_add<0xB1>(v); v = dpp_add<0x4E>(v); v = dpp_add<0x141>(v); v = dpp_add<0x140>(v);
  auto t = __builtin_amdgcn_permlane16_swap(f2u(v), f2u(v), false, false);
  v = u2f(t[0]) + u2f(t[1]);
  t = __builtin_amdgcn_permlane32_swap(f2u(v), f2u(v), false, false);
  return u2f(t[0]) + u2f(t[1]);
}
#define WAIT_V(n) asm volatile("s_waitcnt vmcnt(" #n ")" ::: "memory")
#define WAIT_L0() asm volatile("s_waitcnt lgkmcnt(0)" ::: "memory")
#define BAR() do { asm volatile("" ::: "memory"); __builtin_amdgcn_s_barrier(); asm volatile("" ::: "memory"); } while (0)
#define LBAR() do { WAIT_L0(); BAR(); } while (0)
template <int N> __device__ __forceinline__ void wait_v() { asm volatile("s_waitcnt vmcnt(%0)" :: "n"(N) : "memory"); }
__device__ __forceinline__ void dma16(__amdgpu_buffer_rsrc_t rs, unsigned voff, unsigned soff, unsigned ldsa) {
  asm volatile("s_mov_b32 m0, %0\n\ts_nop 3\n\tbuffer_load_dwordx4 %1, %2, %3 offen lds" :: "s"(ldsa), "v"(voff), "s"(rs), "s"(soff) : "m0", "memory");
}
__device__ __forceinline__ void dma16x(__amdgpu_buffer_rsrc_t rs, unsigned voff, unsigned soff, unsigned ldsa) {
  asm volatile("s_mov_b32 m0, %0\n\ts_nop 3\n\tbuffer_load_dwordx4 %1, %2, %3 offen sc1 lds" :: "s"(ldsa), "v"(voff), "s"(rs), "s"(soff) : "m0", "memory");
}
__device__ __forceinline__ u32x4 ldx16(__amdgpu_buffer_rsrc_t rs, unsigned voff, unsigned soff) { return __builtin_amdgcn_raw_buffer_load_b128(rs, voff, soff, 16); }
__device__ __forceinline__ u32x2 ldx8(__amdgpu_buffer_rsrc_t rs, unsigned voff, unsigned soff) { return __builtin_amdgcn_raw_buffer_load_b64(rs, voff, soff, 16); }
__device__ __forceinline__ unsigned ldx4(__amdgpu_buffer_rsrc_t rs, unsigned voff, unsigned soff) { return __builtin_amdgcn_raw_buffer_load_b32(rs, voff, soff, 16); }
__device__ __forceinline__ void stx16(__amdgpu_buffer_rsrc_t rs, unsigned voff, unsigned soff, u32x4 v) {
  __builtin_amdgcn_raw_buffer_store_b128(v, rs, voff, soff, 16);
  asm volatile("s_nop 1" :: "v"(v) : "memory");
}
__device__ __forceinline__ void stx8(__amdgpu_buffer_rsrc_t rs, unsigned voff, unsigned soff, u32x2 v) { __builtin_amdgcn_raw_buffer_store_b64(v, rs, voff, soff, 16); }
__device__ __forceinline__ void stx4(__amdgpu_buffer_rsrc_t rs, unsigned voff, unsigned soff, unsigned v) { __builtin_amdgcn_raw_buffer_store_b32(v, rs, voff, soff, 16); }
__device__ __forceinline__ void wait_ge(__amdgpu_buffer_rsrc_t rs, unsigned voff, unsigned soff, unsigned need, unsigned& cap) {
  unsigned t;
  asm volatile("s_nop 4\n\t"
               "1:\n\t"
               "buffer_load_dword %[t], %[vo], %[rs], %[so] offen sc1\n\t"
               "s_waitcnt vmcnt(0)\n\t"
               "v_cmp_gt_u32 vcc, %[need], %[t]\n\t"
               "s_cmp_eq_u64 vcc, 0\n\t"
               "s_cbranch_scc1 2f\n\t"
               "s_sleep 2\n\t"
               "s_sub_u32 %[cap], %[cap], 1\n\t"
               "s_cbranch_scc0 1b\n\t"
               "s_mov_b32 %[cap], 0\n\t"
               "2:"
               : [t] "=&v"(t), [cap] "+s"(cap) : [vo] "v"(voff), [rs] "s"(rs), [so] "s"(soff), [need] "s"(need) : "vcc", "scc", "memory");
}
__device__ __forceinline__ __amdgpu_buffer_rsrc_t mk_rs(const void* p, unsigned bytes) { return __builtin_amdgcn_make_buffer_rsrc((void*)p, 0, (int)bytes, 0x00020000); }
#define SCHED() __builtin_amdgcn_sched_barrier(0)
__device__ __forceinline__ unsigned now32() { unsigned long long t; asm volatile("s_memtime %0\n\ts_waitcnt lgkmcnt(0)" : "=s"(t) :: "memory"); return (unsigned)t; }
__device__ __forceinline__ unsigned rt32() { unsigned long long t; asm volatile("s_memrealtime %0\n\ts_waitcnt lgkmcnt(0)" : "=s"(t) :: "memory"); return (unsigned)t; }
__device__ __forceinline__ void wait_until(unsigned t) { while ((int)(rt32() - t) < 0) {} }
template <int X = 0> __device__ __forceinline__ LAS unsigned char* lad(unsigned v, unsigned u) {
  unsigned a;
  if (X == 0) asm volatile("v_add_u32 %0, %1, %2" : "=v"(a) : "s"(u), "v"(v));
  else asm volatile("v_xor_b32 %0, %3, %2\n\tv_add_u32 %0, %1, %0" : "=&v"(a) : "s"(u), "v"(v), "n"(X));
  return (LAS unsigned char*)(size_t)a;
}
__device__ __forceinline__ bf16x4 trread(LAS unsigned char* p) { return __builtin_amdgcn_ds_read_tr16_b64_v4bf16((LAS bf16x4*)p); }
__device__ __forceinline__ bf16x8 cat8(bf16x4 a, bf16x4 b) { return __builtin_shufflevector(a, b, 0, 1, 2, 3, 4, 5, 6, 7); }
__device__ __forceinline__ bf16x16 cat16(bf16x8 a, bf16x8 b) { return __builtin_shufflevector(a, b, 0, 1, 2, 3, 4, 5, 6, 7, 8, 9, 10, 11, 12, 13, 14, 15); }
__device__ __forceinline__ f32x4 mfma32(bf16x8 a, bf16x8 b, f32x4 c) { return __builtin_amdgcn_mfma_f32_16x16x32_bf16(a, b, c, 0, 0, 0); }
__device__ __forceinline__ f32x4 mfma16(u32x2 a, u32x2 b, f32x4 c) {
  return mfma32(__builtin_bit_cast(bf16x8, u32x4{a[0], a[1], 0u, 0u}), __builtin_bit_cast(bf16x8, u32x4{b[0], b[1], 0u, 0u}), c);
}
__device__ __forceinline__ f32x4 mfma16n(u32x2 a, u32x2 b, f32x4 c) { return __builtin_amdgcn_mfma_f32_16x16x16bf16_1k(__builtin_bit_cast(s16x4, a), __builtin_bit_cast(s16x4, b), c, 0, 0, 0); }
__device__ __forceinline__ f32x4 smf(bf16x8 a, bf16x16 b, f32x4 c, int idx) { return __builtin_amdgcn_smfmac_f32_16x16x64_bf16(a, b, c, idx, 0, 0); }

__device__ __forceinline__ void ln_row(float (&x)[16]) {
  float s = 0.f;
#pragma unroll
  for (int i = 0; i < 16; ++i) s += x[i];
  const float mean = wave_sum(s) * (1.0f / 1024.0f);
  float q = 0.f;
#pragma unroll
  for (int i = 0; i < 16; ++i) { const float d = x[i] - mean; q += d * d; }
  const float var = wave_sum(q) * (1.0f / 1024.0f);
  const float m = rbf(mean), isd = __builtin_amdgcn_rcpf(rbf(__builtin_amdgcn_sqrtf(rbf(rbf(var) + 1e-5f))));
#pragma unroll
  for (int i = 0; i < 16; ++i) x[i] = rbf(rbf(x[i] - m) * isd);
}
__device__ __forceinline__ void load_row16(const u16* rowp, int lane, float (&x)[16]) {
  const u32x4 a = *(const u32x4*)(rowp + lane * 8), b = *(const u32x4*)(rowp + 512 + lane * 8);
#pragma unroll
  for (int i = 0; i < 4; ++i) { x[2 * i] = lo2f(a[i]); x[2 * i + 1] = hi2f(a[i]); x[8 + 2 * i] = lo2f(b[i]); x[9 + 2 * i] = hi2f(b[i]); }
}
__device__ __forceinline__ void store_row16(u16* rowp, int lane, const float (&x)[16]) {
  u32x4 a, b;
#pragma unroll
  for (int i = 0; i < 4; ++i) { a[i] = pack2(x[2 * i], x[2 * i + 1]); b[i] = pack2(x[8 + 2 * i], x[9 + 2 * i]); }
  *(u32x4*)(rowp + lane * 8) = a; *(u32x4*)(rowp + 512 + lane * 8) = b;
}
__device__ __forceinline__ void modulate(float (&x)[16], const float* sc, const float* sh, int lane) {
#pragma unroll
  for (int hh = 0; hh < 2; ++hh)
#pragma unroll
    for (int j = 0; j < 2; ++j) {
      const f32x4 c = *(const f32x4*)(sc + hh * 512 + lane * 8 + j * 4), t = *(const f32x4*)(sh + hh * 512 + lane * 8 + j * 4);
#pragma unroll
      for (int i = 0; i < 4; ++i) { float& v = x[hh * 8 + j * 4 + i]; v = rbf(rbf(v * rbf(1.0f + c[i])) + t[i]); }
    }
}

__host__ __device__ __forceinline__ int mem_slot(int dl) {
  return ((dl >> 2) & 1) * 32 + (((dl >> 5) & 1) * 2 + ((dl >> 3) & 1)) * 8 + (((dl >> 4) & 1) * 2 + (dl & 1)) * 2 + ((dl >> 1) & 1);
}

__global__ void __launch_bounds__(512) k_prep(const u16* __restrict__ c, const u16* __restrict__ wmod, const u16* __restrict__ mem0, unsigned char* ws) {
  __shared__ float red[128][33];
  __shared__ float sc[1024];
  const int b = blockIdx.x, tid = threadIdx.x;
  {
    u32x4* z = (u32x4*)ws;
    const u32x4 zero = {0u, 0u, 0u, 0u};
    for (size_t i = (size_t)65536 / 16 + (size_t)b * 512 + tid; i < X_BYTES / 16; i += (size_t)256 * 512) z[i] = zero;
    if (b >= 192 && b < 208) {
      u32x4* y = (u32x4*)(ws + WS_H1S + ((b & 8) ? 64 * 65536 : 0));
      y[(b & 7) * 512 + tid] = zero;
    }
  }
  if (b == 255) {
    for (int d = tid; d < 1024; d += 512) {
      u32x4 v;
#pragma unroll
      for (int i = 0; i < 4; ++i) v[i] = nz((unsigned)mem0[(2 * i) * 1024 + d] | ((unsigned)mem0[(2 * i + 1) * 1024 + d] << 16));
      const u32x4 mz = {0x80008000u, 0x80008000u, 0x80008000u, 0x80008000u};
#pragma unroll
      for (int r = 0; r < 2; ++r) {
        *(u32x4*)(ws + X_MEMP + r * 32768 + (d >> 6) * 1024 + mem_slot(d & 63) * 16) = v;
        *(u32x4*)(ws + X_MEMP + r * 32768 + 16384 + d * 16) = mz;
      }
    }
  }
  if (b < 192) {
    for (int i = tid; i < 1024; i += 512) { const float v = bf2f(c[i]); sc[i] = rbf(v / (1.0f + __expf(-v))); }
    __syncthreads();
    const int kg = tid >> 2, c8 = tid & 3;
    float acc[8] = {0.f, 0.f, 0.f, 0.f, 0.f, 0.f, 0.f, 0.f};
    u32x4 w[8];
#pragma unroll
    for (int i = 0; i < 8; ++i) w[i] = *(const u32x4*)(wmod + (size_t)(kg + 128 * i) * 6144 + b * 32 + c8 * 8);
#pragma unroll
    for (int i = 0; i < 8; ++i) {
      const float s = sc[kg + 128 * i];
#pragma unroll
      for (int j = 0; j < 4; ++j) { acc[2 * j] += s * lo2f(w[i][j]); acc[2 * j + 1] += s * hi2f(w[i][j]); }
    }
#pragma unroll
    for (int j = 0; j < 8; ++j) red[kg][c8 * 8 + j] = acc[j];
    __syncthreads();
    if (tid < 256) {
      const int col = tid & 31, part = tid >> 5;
      float s = 0.f;
#pragma unroll
      for (int i = 0; i < 16; ++i) s += red[part * 16 + i][col];
      __syncthreads();
      red[part][col] = s;
    } else __syncthreads();
    __syncthreads();
    if (tid < 32) {
      float s = 0.f;
#pragma unroll
      for (int i = 0; i < 8; ++i) s += red[i][tid];
      ((float*)(ws + WS_MOD))[b * 32 + tid] = rbf(s);
    }
  }
}

#define WST(rs, wk, i)
struct MainP { const u16 *x, *Wq, *Wk, *Wv, *Wo, *w1, *w2, *w3, *w4; u16* out; unsigned char* ws; int d1, d2, d3, d4; };
constexpr unsigned SPIN_CAP = 1u << 18;

__device__ __forceinline__ float gmax(float v) {
  auto t = __builtin_amdgcn_permlane16_swap(f2u(v), f2u(v), false, false);
  v = fmaxf(u2f(t[0]), u2f(t[1]));
  t = __builtin_amdgcn_permlane32_swap(f2u(v), f2u(v), false, false);
  return fmaxf(u2f(t[0]), u2f(t[1]));
}
__device__ __forceinline__ float gsum(float v) {
  auto t = __builtin_amdgcn_permlane16_swap(f2u(v), f2u(v), false, false);
  v = u2f(t[0]) + u2f(t[1]);
  t = __builtin_amdgcn_permlane32_swap(f2u(v), f2u(v), false, false);
  return u2f(t[0]) + u2f(t[1]);
}
__device__ __forceinline__ u32x4 widen(f32x4 a, f32x4 b) {
  const auto r0 = __builtin_amdgcn_permlane16_swap(pack2(a[0], a[1]), pack2(b[0], b[1]), false, false);
  const auto r1 = __builtin_amdgcn_permlane16_swap(pack2(a[2], a[3]), pack2(b[2], b[3]), false, false);
  return u32x4{r0[0], r1[0], r0[1], r1[1]};
}
__device__ __forceinline__ void publish(__amdgpu_buffer_rsrc_t rs, unsigned word, unsigned val) {
  WAIT_V(0); BAR();
  if (threadIdx.x == 0) stx4(rs, 0, word, val);
}

constexpr int P_S2 = 0, P_S8 = 32, P_S7 = 64, P_S4 = 120, P_S5 = 136, P_S1 = 160, P_S3 = 288, P_PJ = 416;
__device__ __forceinline__ void stage_attn(__amdgpu_buffer_rsrc_t rs, int wk, int grp, int kq, unsigned& cap) {
  const int tid = threadIdx.x, lane = tid & 63, w = __builtin_amdgcn_readfirstlane(tid >> 6), fr = lane & 15, g = lane >> 4;
  const int h = kq * 8 + w;
  const unsigned l16 = lane * 16;
  for (int seg = grp; seg < NSEG; seg += 4) {
    wait_ge(rs, (unsigned)(lane % 3) * 4096, F_ST + (unsigned)(P_PJ + (seg & 1) * 96 + h) * 128, (unsigned)seg + 1u, cap);
    const unsigned ub = (unsigned)(seg * NH + h);
    f32x4 s0[2], s1[2], s2;
    s0[0] = s0[1] = s1[0] = s1[1] = s2 = f32x4{0.f, 0.f, 0.f, 0.f};
    bf16x8 q0[4], q1[4], vf[8];
#pragma unroll
    for (int ks = 0; ks < 4; ++ks) {
      q0[ks] = __builtin_bit_cast(bf16x8, ldx16(rs, l16, WS_Q + ub * 8192 + ks * 1024)); q1[ks] = __builtin_bit_cast(bf16x8, ldx16(rs, l16, WS_Q + ub * 8192 + 4096 + ks * 1024));
      const bf16x8 k0 = __builtin_bit_cast(bf16x8, ldx16(rs, l16, WS_K + ub * 8192 + ks * 1024)), k1 = __builtin_bit_cast(bf16x8, ldx16(rs, l16, WS_K + ub * 8192 + 4096 + ks * 1024));
      s1[0] = mfma32(k0, q0[ks], s1[0]); s1[1] = mfma32(k0, q1[ks], s1[1]); s2 = mfma32(k1, q1[ks], s2);
    }
    const unsigned ev = (unsigned)(fr >> 3) * 1024 + (unsigned)(g * 16 + 8 * ((fr >> 2) & 1) + (fr & 3)) * 16;
#pragma unroll
    for (int i = 0; i < 8; ++i) vf[i] = __builtin_bit_cast(bf16x8, ldx16(rs, ev, WS_VT + ub * 8192 + (i >> 1) * 2048 + (i & 1) * 64));
    wait_ge(rs, 0, F_CH + h * 128, (unsigned)seg + 1u, cap);
    u32x2 vmf[8];
    const unsigned em = (unsigned)(fr >> 3) * 256 + (unsigned)(g & 1) * 128 + (unsigned)(8 * ((fr >> 2) & 1) + (fr & 3)) * 8;
    for (;;) {
      asm volatile("" ::: "memory");
      unsigned mn = 0xFFFFFFFFu;
#pragma unroll
      for (int i = 0; i < 8; ++i) { vmf[i] = ldx8(rs, em, X_VM + ub * 2048 + (i >> 1) * 512 + (i & 1) * 32); }
#pragma unroll
      for (int ks = 0; ks < 4; ++ks) {
        const bf16x8 kmf = __builtin_bit_cast(bf16x8, ldx16(rs, l16, WS_KM + ub * 4096 + ks * 1024));
        if (ks == 0) { s0[0] = mfma32(kmf, q0[ks], f32x4{0.f, 0.f, 0.f, 0.f}); s0[1] = mfma32(kmf, q1[ks], f32x4{0.f, 0.f, 0.f, 0.f}); }
        else { s0[0] = mfma32(kmf, q0[ks], s0[0]); s0[1] = mfma32(kmf, q1[ks], s0[1]); }
      }
#pragma unroll
      for (int i = 0; i < 8; ++i) mn = min(mn, min(vmf[i][0], vmf[i][1]));
      if (__all(mn != 0u) || cap == 0u) break;
      cap = __builtin_amdgcn_readfirstlane(cap - 1u);
    }
    cap = __builtin_amdgcn_readfirstlane(cap);
    const float scale = 0.08838834764831845f, L2E = 1.4426950408889634f;
    u32x2 pm[2]; u32x4 pt[2];
#pragma unroll
    for (int t = 0; t < 2; ++t) {
      float a[4], b[4], c[4];
      float mx = -3.0e38f;
#pragma unroll
      for (int r = 0; r < 4; ++r) {
        const int j = 4 * g + r;
        a[r] = (g < 2) ? rbf(rbf(s0[t][r]) * scale) : -3.0e38f;
        b[r] = (t == 1 || j <= fr) ? rbf(rbf(s1[t][r]) * scale) : -3.0e38f;
        c[r] = (t == 1 && j <= fr) ? rbf(rbf(s2[r]) * scale) : -3.0e38f;
        mx = fmaxf(mx, fmaxf(a[r], fmaxf(b[r], c[r])));
      }
      mx = gmax(mx);
      float sum = 0.f;
#pragma unroll
      for (int r = 0; r < 4; ++r) {
        a[r] = __builtin_amdgcn_exp2f((a[r] - mx) * L2E); b[r] = __builtin_amdgcn_exp2f((b[r] - mx) * L2E); c[r] = __builtin_amdgcn_exp2f((c[r] - mx) * L2E);
        sum += a[r] + b[r] + c[r];
      }
      const float inv = __builtin_amdgcn_rcpf(gsum(sum));
      pm[t] = u32x2{pack2(a[0] * inv, a[1] * inv), pack2(a[2] * inv, a[3] * inv)};
      pt[t] = u32x4{pack2(b[0] * inv, b[1] * inv), pack2(b[2] * inv, b[3] * inv), pack2(c[0] * inv, c[1] * inv), pack2(c[2] * inv, c[3] * inv)};
    }
#pragma unroll
    for (int u = 0; u < 4; ++u) {
      f32x4 o[2][2];
#pragma unroll
      for (int bq = 0; bq < 2; ++bq)
#pragma unroll
        for (int t = 0; t < 2; ++t) {
          f32x4 z = {0.f, 0.f, 0.f, 0.f};
          z = mfma16(vmf[2 * u + bq], pm[t], z);
          o[bq][t] = mfma32(vf[2 * u + bq], __builtin_bit_cast(bf16x8, pt[t]), z);
        }
#pragma unroll
      for (int t = 0; t < 2; ++t)
        stx16(rs, l16, WS_O + (unsigned)seg * 262144 + (unsigned)((h * 4 + u) * 2 + t) * 1024,
              u32x4{pack2(o[0][t][0], o[0][t][1]), pack2(o[0][t][2], o[0][t][3]), pack2(o[1][t][0], o[1][t][1]), pack2(o[1][t][2], o[1][t][3])});
    }
    WAIT_V(0);
    if (lane == 0) stx4(rs, 0, F_ST + (unsigned)(P_S1 + grp * 32 + h) * 128, (unsigned)seg + 1u);
    WST(rs, wk, 16 + seg);
  }
}

enum { SG_WO = 0, SG_W1 = 1, SG_W2 = 2, SG_W3 = 3, SG_W4 = 4, SG_QK = 5, SG_V = 6 };
#define PRA 1
#define PRB 2
#define PRC 0
constexpr int G_XCH = 131072, G_CTL = G_XCH + 16384;
template <int SG>
__device__ __forceinline__ void stage_gemm(LAS unsigned char* lds, __amdgpu_buffer_rsrc_t rs, const u16* Wg, int wk, int kq, int cb, unsigned dst, int pid, unsigned& cap) {
  constexpr bool PROJ = (SG == SG_QK || SG == SG_V), WIDE = (SG == SG_WO || SG == SG_W4 || PROJ), ROWM = (SG == SG_W1 || PROJ);
  constexpr int KSW = (SG == SG_W4) ? 14 : 16;
  constexpr int NM = WIDE ? 2 : 1, NCOL = WIDE ? 128 : 64;
  constexpr int LDW = PROJ ? HD : (SG == SG_W3) ? FF : D_;
  constexpr int WROWS = (SG == SG_WO) ? HD : (SG == SG_W4 ? FF : D_);
  constexpr int NBLK = PROJ ? 32 : (SG == SG_WO || SG == SG_W1) ? 64 : 65;
  constexpr unsigned ABASE = PROJ ? WS_H : SG == SG_WO ? WS_O : SG == SG_W1 ? WS_G : SG == SG_W2 ? WS_H1S : SG == SG_W3 ? WS_X2 : WS_U;
  constexpr unsigned ASTR = SG == SG_WO ? 262144u : SG == SG_W4 ? 229376u : 65536u;
  constexpr int NPC = KSW / 2;
  const unsigned myword = F_ST + (unsigned)pid * 128u;
  int tid = threadIdx.x; asm volatile("" : "+v"(tid));
  const int lane = tid & 63, w = __builtin_amdgcn_readfirstlane(tid >> 6), fr = lane & 15, g = lane >> 4;
  const int kh = w >> 2, wl = w & 3, cg = WIDE ? wl : (wl & 1), m0 = WIDE ? 0 : (wl >> 1);
  const int koff = PROJ ? 0 : kq;
  const unsigned lbase = (unsigned)(size_t)lds, l16 = lane * 16;
  const int col0 = cb * NCOL + cg * 32;
  bf16x8 Wf[2][KSW];
  {
    const __amdgpu_buffer_rsrc_t rsW = mk_rs(Wg, (unsigned)WROWS * LDW * 2u);
    const int ub = lane >> 3, tu = 4 * (g & 1) + (fr >> 2);
    const unsigned vB = (unsigned)(((ub & 3) + 8 * (ub >> 2)) * LDW + 16 * (((lane >> 1) & 3) ^ (ub >> 1)) + 8 * (lane & 1)) * 2u;
    const unsigned sB = (unsigned)((koff * KSW * 64 + 32 * (w >> 2) + 16 * ((w >> 1) & 1) + 4 * (w & 1)) * LDW + cb * NCOL) * 2u;
    constexpr int TB = NCOL * 128, TPP = WIDE ? 4 : 8;
    const int boff = kh * 65536 + (cg >> 1) * 8192 + (g >> 1) * 2048 + tu * 128 + (((tu >> 1) ^ (2 * (cg & 1))) << 5) + (fr & 3) * 8;
#pragma unroll
    for (int ps = 0; ps < (WIDE ? 2 : 1); ++ps) {
      BAR();
#pragma unroll
      for (int hh = 0; hh < 2; ++hh)
#pragma unroll
        for (int i = 0; i < TPP; ++i)
          if (TPP * ps + i < KSW / 2) {
            const unsigned so = sB + (unsigned)((hh * (KSW / 2) + TPP * ps + i) * 64 * LDW) * 2u, d = lbase + hh * 65536 + i * TB + w * 1024;
            dma16(rsW, vB, so, d);
            if (WIDE) dma16(rsW, vB, so + 128, d + 8192);
          }
      WAIT_V(0); BAR();
#pragma unroll
      for (int i = 0; i < TPP; ++i)
        if (TPP * ps + i < KSW / 2) {
#pragma unroll
          for (int ks = 0; ks < 2; ++ks)
#pragma unroll
            for (int n = 0; n < 2; ++n) {
              LAS unsigned char* q = lds + ((boff ^ (n << 5)) + i * TB + ks * 4096);
              Wf[n][(TPP * ps + i) * 2 + ks] = cat8(trread(q), trread(q + 1024));
            }
        }
      WAIT_L0();
    }
    if (tid < 8) *(LAS unsigned*)(lds + G_CTL + tid * 4) = 0u;
    LBAR();
  }
  f32x4 cs[2];
  if (SG == SG_W3) {
    const u32x4 one4 = {0x3F803F80u, 0x3F803F80u, 0x3F803F80u, 0x3F803F80u};
    cs[0] = cs[1] = f32x4{0.f, 0.f, 0.f, 0.f};
#pragma unroll
    for (int k = 0; k < KSW; ++k) { cs[0] = mfma32(Wf[0][k], __builtin_bit_cast(bf16x8, one4), cs[0]); cs[1] = mfma32(Wf[1][k], __builtin_bit_cast(bf16x8, one4), cs[1]); }
    *(LAS f32x4*)(lds + w * 2048 + l16) = cs[0]; *(LAS f32x4*)(lds + w * 2048 + 1024 + l16) = cs[1];
    LBAR();
    cs[0] += *(const LAS f32x4*)(lds + (w ^ 4) * 2048 + l16); cs[1] += *(const LAS f32x4*)(lds + (w ^ 4) * 2048 + 1024 + l16);
    LBAR();
  }
  auto blk = [&](int i) -> int { return PROJ ? 2 * i + kq : i; };
  const unsigned aoff = ROWM ? (unsigned)((m0 * 16 + fr) * 128 + ((g ^ (fr & 7)) << 4)) : l16 + m0 * 1024;
  const unsigned vA = ROWM ? (unsigned)((lane >> 3) * 2048 + (((lane & 7) ^ (lane >> 3)) << 4)) : l16;
  auto piece = [&](int hf, int q) {
    const int i = hf >> 1, hh = hf & 1;
    const unsigned so = ABASE + (unsigned)blk(i) * ASTR + (unsigned)koff * (KSW * 4096) + (ROWM ? wl * 16384 + hh * 1024 : (hh * KSW * 2 + wl) * 1024);
    dma16x(rs, vA, so + q * (ROWM ? 128 : 4096), lbase + (i & 1) * 65536 + hh * 32768 + wl * 1024 + q * 4096);
  };
  auto fetch = [&](int hf) {
#pragma unroll
    for (int q = 0; q < NPC; ++q) piece(hf, q);
  };
  auto products = [&](int i, f32x4 (&acc)[NM][2], auto&& early, auto&& handover) {
    const unsigned ub = lbase + (i & 1) * 65536 + kh * 32768;
    LAS unsigned char* ab = lad(aoff, ub);
    LAS unsigned char* ab1 = ROWM ? lad<64>(aoff, ub) : ab;
    constexpr int PD = WIDE ? 6 : 10;
    bf16x8 af[PD + 1][NM];
    auto rd = [&](int k) {
#pragma unroll
      for (int m = 0; m < NM; ++m)
        af[k % (PD + 1)][m] = ROWM ? *(const LAS bf16x8*)(((k & 1) ? ab1 : ab) + (k >> 1) * 4096 + m * 2048) : *(const LAS bf16x8*)(ab + k * 2048 + m * 1024);
    };
#pragma unroll
    for (int k = 0; k < PD; ++k) rd(k);
    __builtin_amdgcn_s_setprio(PRA);
    SCHED();
#pragma unroll
    for (int k = 0; k < KSW; ++k) {
      if (k + PD < KSW) rd(k + PD);
#pragma unroll
      for (int m = 0; m < NM; ++m)
#pragma unroll
        for (int n = 0; n < 2; ++n) acc[m][n] = SG == SG_V ? mfma32(af[k % (PD + 1)][m], Wf[n][k], acc[m][n]) : mfma32(Wf[n][k], af[k % (PD + 1)][m], acc[m][n]);
      SCHED();
      if (k == KSW - (WIDE ? 6 : 9)) { early(); SCHED(); }
      if (k == KSW - (WIDE ? 3 : 5)) { __builtin_amdgcn_s_setprio(PRB); handover(); SCHED(); }
    }
#pragma unroll
    for (int m = 0; m < NM; ++m) asm volatile("" : "+v"(acc[m][0]), "+v"(acc[m][1]));
    __builtin_amdgcn_s_setprio(PRC);
  };
  WST(rs, wk, PROJ ? 2 : 10);
#define SEG(x)
  if (kh == 0) {
    auto pvo = [&](int i) -> unsigned {
      const int b = blk(i);
      if (PROJ) return F_LN + (unsigned)((b % 48) * 4 + (lane & 3)) * 128u;
      if (SG == SG_WO) return F_ST + (unsigned)(P_S1 + (b & 3) * 32 + kq * 8 + (lane & 7)) * 128u;
      if (SG == SG_W1) return F_ST + (unsigned)(P_S3 + (b & 3) * 32 + (lane & 31)) * 128u;
      if (SG == SG_W2) return F_ST + (unsigned)(P_S4 + (lane & 15)) * 128u;
      if (SG == SG_W3) return F_ST + (unsigned)(P_S5 + (lane & 15)) * 128u;
      return F_ST + (unsigned)(P_S7 + kq * 14 + ((lane & 31) % 14)) * 128u;
    };
    auto need = [&](int i) -> unsigned { return PROJ ? 1u : (SG == SG_W2 && i == 64) ? 64u : (unsigned)i + 1u; };
    int R = 0, Rp = 0;
    for (int i = 0; i < NBLK; ++i) {
      if (R <= i) { wait_ge(rs, pvo(i), 0, need(i), cap); fetch(2 * i); fetch(2 * i + 1); }
      if (R <= i + 1) wait_v<NPC>(); else wait_v<2 * NPC>();
      BAR();
      SEG(1);
      f32x4 acc[NM][2];
#pragma unroll
      for (int m = 0; m < NM; ++m) acc[m][0] = acc[m][1] = f32x4{0.f, 0.f, 0.f, 0.f};
      unsigned peek;
      asm volatile("buffer_load_dword %0, %1, %2, 0 offen sc1" : "=v"(peek) : "v"(w == 0 ? (lane < 32 ? pvo(i + 1) : pvo(i + 2)) : 0xF0000000u), "s"(rs) : "memory");
      products(i, acc, []() {}, [&]() {
        SEG(4);
        WAIT_V(0);
        SEG(0);
        asm volatile("" : "+v"(peek));
        if (w == 0) {
          const unsigned long long ok = __ballot(peek >= (lane < 32 ? need(i + 1) : need(i + 2)));
          const bool r1 = (R >= i + 2 || (unsigned)ok == 0xFFFFFFFFu) && i + 1 < NBLK, r2 = r1 && (unsigned)(ok >> 32) == 0xFFFFFFFFu && i + 2 < NBLK;
          if (lane == 0) *(volatile LAS unsigned*)(lds + G_CTL + 4) = (unsigned)(i + 1 + (r1 ? 1 : 0) + (r2 ? 1 : 0));
        }
        LBAR();
      });
      SEG(2);
      const unsigned rv = *(const volatile LAS unsigned*)(lds + G_CTL + 4);
      LAS unsigned char* xw = lad(l16, lbase + G_XCH + wl * 4096);
#pragma unroll
      for (int m = 0; m < NM; ++m) { *(LAS f32x4*)(xw + m * 2048) = acc[m][0]; *(LAS f32x4*)(xw + m * 2048 + 1024) = acc[m][1]; }
      asm volatile("" ::: "memory");
      if (lane == 0) *(volatile LAS unsigned*)(lds + G_CTL + 16 + wl * 4) = (unsigned)i + 1u;
      Rp = R; R = __builtin_amdgcn_readfirstlane(rv);
      SEG(3);
      if (R >= i + 2 && Rp < i + 2) { fetch(2 * i + 2); fetch(2 * i + 3); }
      if (R >= i + 3) fetch(2 * i + 4);
      SEG(5);
    }
    BAR();
  } else {
    const int row = 16 * m0 + fr, c8 = col0 + 16 * (g & 1) + 8 * (g >> 1);
    bool pending = false;
    BAR();
    for (int i = 0; i < NBLK; ++i) {
      const int b = blk(i);
      BAR();
      SEG(0);
      const unsigned rv = *(const volatile LAS unsigned*)(lds + G_CTL + 4);
      u32x4 res, st[4];
      if (SG == SG_W2) { const int t = 32 * b - 2 + row; res = ldx16(rs, (t >= 0 && t < S_) ? (unsigned)(t * D_ + c8) * 2u : 0xF0000000u, WS_H2); }
      if (SG == SG_W3) {
#pragma unroll
        for (int q = 0; q < 4; ++q) st[q] = ldx16(rs, (unsigned)(g * 2048 + row * 16), WS_ST5 + (unsigned)b * 8192 + q * 512);
      }
      f32x4 acc[NM][2];
#pragma unroll
      for (int m = 0; m < NM; ++m) acc[m][0] = acc[m][1] = f32x4{0.f, 0.f, 0.f, 0.f};
      int R = 0;
      LAS unsigned char* xr = lad(l16, lbase + G_XCH + wl * 4096);
      f32x4 xa[NM][2];
      unsigned xf;
      auto take = [&]() {
        asm volatile("" ::: "memory");
        xf = *(const volatile LAS unsigned*)(lds + G_CTL + 16 + wl * 4);
        asm volatile("" ::: "memory");
#pragma unroll
        for (int m = 0; m < NM; ++m) { xa[m][0] = *(const LAS f32x4*)(xr + m * 2048); xa[m][1] = *(const LAS f32x4*)(xr + m * 2048 + 1024); }
        asm volatile("" ::: "memory");
      };
      products(i, acc, take, [&]() {
        SEG(5);
        R = __builtin_amdgcn_readfirstlane(rv);
        if (R >= i + 2) {
          if (SG == SG_W2) wait_v<1>(); else if (SG == SG_W3) wait_v<4>(); else wait_v<0>();
          LBAR();
          if (pending && tid == 256) stx4(rs, 0, myword, (unsigned)blk(i - 1) + 1u);
        }
      });
      const bool more = R >= i + 2;
      SEG(1);
      while (__builtin_amdgcn_readfirstlane(xf) <= (unsigned)i) take();
#pragma unroll
      for (int m = 0; m < NM; ++m) { acc[m][0] += xa[m][0]; acc[m][1] += xa[m][1]; }
      SEG(2);
      const unsigned fo = (unsigned)((c8 >> 5) * 2048 + ((c8 >> 3) & 3) * 256);
      if (SG == SG_WO || SG == SG_W4) {
        const unsigned so = dst + (unsigned)(b * 4 + kq) * 65536u, vo = (unsigned)(fr * D_ + c8) * 2u;
        stx16(rs, vo, so, widen(acc[0][0], acc[0][1])); stx16(rs, vo, so + 32768, widen(acc[NM - 1][0], acc[NM - 1][1]));
      } else if (SG == SG_QK) {
        const unsigned so = dst + (unsigned)(b * NH + cb) * 8192 + cg * 1024, vo = (unsigned)((2 * (g & 1) + (g >> 1)) * 16 + fr) * 16;
        stx16(rs, vo, so, widen(acc[0][0], acc[0][1])); stx16(rs, vo, so + 4096, widen(acc[NM - 1][0], acc[NM - 1][1]));
      } else if (SG == SG_V) {
#pragma unroll
        for (int n = 0; n < 2; ++n) {
          const f32x4 x = acc[0][n], y = acc[NM - 1][n];
          stx16(rs, l16, dst + (unsigned)(b * NH + cb) * 8192 + (cg * 2 + n) * 1024, u32x4{pack2(x[0], x[1]), pack2(x[2], x[3]), pack2(y[0], y[1]), pack2(y[2], y[3])});
        }
      } else if (SG == SG_W1) {
        u32x4 v = widen(acc[0][0], acc[0][1]);
#pragma unroll
        for (int q = 0; q < 4; ++q) v[q] = pack2(gelu_tanh(lo2f(v[q])), gelu_tanh(hi2f(v[q])));
        const int grp = cb >> 2, sh = (grp & 1) ? -(1 + (grp >> 1)) : (1 + (grp >> 1));
        const int rho = 32 * b + row + sh + 2;
        const unsigned vo = (rho >= 2 && rho < S_ + 2) ? (unsigned)(rho >> 5) * 65536u + fo + (unsigned)((rho >> 4) & 1) * 1024u + (unsigned)(rho & 15) * 16u : 0xF0000000u;
        stx16(rs, vo, WS_H1S, v);
      } else if (SG == SG_W2) {
        u32x4 v = widen(acc[0][0], acc[0][1]);
        float z[8];
#pragma unroll
        for (int q = 0; q < 4; ++q) { v[q] = pack2(lo2f(res[q]) + lo2f(v[q]), hi2f(res[q]) + hi2f(v[q])); z[2 * q] = lo2f(v[q]); z[2 * q + 1] = hi2f(v[q]); }
        stx16(rs, fo + m0 * 1024 + fr * 16, WS_X2 + (unsigned)b * 65536u, v);
        const float mw = gsum(((z[0] + z[1]) + (z[2] + z[3])) + ((z[4] + z[5]) + (z[6] + z[7]))) * (1.0f / 32.0f);
        float q2 = 0.f;
#pragma unroll
        for (int q = 0; q < 8; ++q) { const float d = z[q] - mw; q2 += d * d; }
        q2 = gsum(q2);
        if (g == 0) stx8(rs, (unsigned)(cb * 512 + row * 16 + cg * 8), WS_ST5 + (unsigned)b * 8192, u32x2{f2u(mw), f2u(q2)});
      } else {
        float ms = 0.f;
#pragma unroll
        for (int q = 0; q < 4; ++q) ms += u2f(st[q][0]) + u2f(st[q][2]);
        const float mean = gsum(ms) * (1.0f / 32.0f);
        float q2 = 0.f;
#pragma unroll
        for (int q = 0; q < 4; ++q) { const float d0 = u2f(st[q][0]) - mean, d1 = u2f(st[q][2]) - mean; q2 += u2f(st[q][1]) + u2f(st[q][3]) + 32.0f * (d0 * d0 + d1 * d1); }
        const float var = gsum(q2) * (1.0f / 1024.0f);
        const float m = rbf(mean), isd = __builtin_amdgcn_rcpf(rbf(__builtin_amdgcn_sqrtf(rbf(rbf(var) + 1e-5f))));
        u32x4 v = widen((acc[0][0] - m * cs[0]) * isd, (acc[0][1] - m * cs[1]) * isd);
#pragma unroll
        for (int q = 0; q < 4; ++q) v[q] = pack2(gelu_tanh(lo2f(v[q])), gelu_tanh(hi2f(v[q])));
        stx16(rs, fo + m0 * 1024 + fr * 16, WS_U + (unsigned)b * 229376u, v);
      }
      SEG(3);
      pending = more && i >= 2;
      if (!pending) {
        WAIT_V(0);
        if (lane == 0) {
          const unsigned c = __hip_atomic_fetch_add((LAS unsigned*)(lds + G_CTL), 1u, __ATOMIC_RELAXED, __HIP_MEMORY_SCOPE_WORKGROUP);
          if ((c & 3u) == 3u) stx4(rs, 0, myword, (unsigned)b + 1u);
        }
      }
      if (R >= i + 3) fetch(2 * i + 5);
      if (!PROJ) WST(rs, wk, 16 + b);
      SEG(4);
      if (!more) BAR();
    }
  }
}

__device__ __forceinline__ void wait32(__amdgpu_buffer_rsrc_t rs, int first, int mine, int lane, unsigned need, unsigned& cap) {
  wait_ge(rs, 0, F_ST + (unsigned)(first + mine) * 128u, need, cap);
  wait_ge(rs, (unsigned)(lane & 31) * 128u, F_ST + (unsigned)first * 128u, need, cap);
}
__device__ __forceinline__ void sum_partials(__amdgpu_buffer_rsrc_t rs, unsigned l16, unsigned so, float (&a)[16]) {
  u32x4 pp[4][2];
#pragma unroll
  for (int k = 0; k < 4; ++k) { pp[k][0] = ldx16(rs, l16, so + k * 65536u); pp[k][1] = ldx16(rs, l16, so + k * 65536u + 1024); }
#pragma unroll
  for (int i = 0; i < 8; ++i) {
    f32x2 t = {0.f, 0.f};
#pragma unroll
    for (int k = 0; k < 4; ++k) t += f32x2{lo2f(pp[k][i >> 2][i & 3]), hi2f(pp[k][i >> 2][i & 3])};
    a[2 * i] = rbf(t[0]); a[2 * i + 1] = rbf(t[1]);
  }
}
__device__ __forceinline__ void put_row(__amdgpu_buffer_rsrc_t rs, unsigned l16, unsigned so, const float (&v)[16]) {
  stx16(rs, l16, so, u32x4{pack2(v[0], v[1]), pack2(v[2], v[3]), pack2(v[4], v[5]), pack2(v[6], v[7])});
  stx16(rs, l16, so + 1024, u32x4{pack2(v[8], v[9]), pack2(v[10], v[11]), pack2(v[12], v[13]), pack2(v[14], v[15])});
}
__device__ __forceinline__ void load_vec16(const float* p, int lane, float (&v)[16]) {
#pragma unroll
  for (int i = 0; i < 4; ++i) { const f32x4 t = *(const f32x4*)(p + (i >> 1) * 512 + lane * 8 + (i & 1) * 4); v[4 * i] = t[0]; v[4 * i + 1] = t[1]; v[4 * i + 2] = t[2]; v[4 * i + 3] = t[3]; }
}

__device__ __forceinline__ void stage_mid(__amdgpu_buffer_rsrc_t rs, const u16* x, const float* mod, int wk, int cu, unsigned& cap) {
  const int tid = threadIdx.x, lane = tid & 63, w = __builtin_amdgcn_readfirstlane(tid >> 6);
  const int grp = w >> 1, rl = cu * 2 + (w & 1);
  const unsigned l16 = lane * 16;
  float ga[16], sc[16], sh[16];
  load_vec16(mod + 2048, lane, ga); load_vec16(mod + 4096, lane, sc); load_vec16(mod + 3072, lane, sh);
#pragma unroll
  for (int i = 0; i < 16; ++i) sc[i] = rbf(1.0f + sc[i]);
  for (int seg = grp; seg < NSEG; seg += 4) {
    const int row = seg * 32 + rl;
    float v[16], a[16];
    load_row16(x + (size_t)row * D_, lane, v);
    wait32(rs, P_S2, rl, lane, (unsigned)seg + 1u, cap);
    sum_partials(rs, l16, WS_P2 + (unsigned)seg * 262144u + (unsigned)rl * 2048, a);
#pragma unroll
    for (int i = 0; i < 16; ++i) v[i] = rbf(v[i] + rbf(ga[i] * a[i]));
    put_row(rs, l16, WS_X1 + (unsigned)row * 2048, v);
    ln_row(v);
#pragma unroll
    for (int i = 0; i < 16; ++i) v[i] = rbf(rbf(v[i] * sc[i]) + sh[i]);
    put_row(rs, l16, WS_H2 + (unsigned)row * 2048, v);
    ln_row(v);
    put_row(rs, l16, WS_G + (unsigned)row * 2048, v);
    WAIT_V(0);
    if (lane == 0) stx4(rs, 0, F_ST + (unsigned)(P_S3 + grp * 32 + rl) * 128, (unsigned)seg + 1u);
    WST(rs, wk, 16 + seg);
  }
}

__device__ __forceinline__ void stage_out(__amdgpu_buffer_rsrc_t rs, u16* out, const float* mod, int wk, int cu, unsigned& cap) {
  const int tid = threadIdx.x, lane = tid & 63, w = __builtin_amdgcn_readfirstlane(tid >> 6);
  const int grp = w >> 2, rl = cu * 4 + (w & 3);
  const unsigned l16 = lane * 16;
  float gb[16];
  load_vec16(mod + 5120, lane, gb);
  for (int b = grp; b < 65; b += 2) {
    const int t = 32 * b - 2 + rl;
    if (t < 0 || t >= S_) continue;
    wait32(rs, P_S8, rl, lane, (unsigned)b + 1u, cap);
    const unsigned xo = (unsigned)((lane >> 2) * 2048 + (lane & 3) * 256), xs = WS_X2 + (unsigned)b * 65536u + (unsigned)((rl >> 4) * 1024 + (rl & 15) * 16);
    const u32x4 x2a = ldx16(rs, xo, xs), x2b = ldx16(rs, xo, xs + 32768);
    const u32x4 x1a = ldx16(rs, l16, WS_X1 + (unsigned)t * 2048), x1b = ldx16(rs, l16, WS_X1 + (unsigned)t * 2048 + 1024);
    float a[16];
    sum_partials(rs, l16, WS_P8 + (unsigned)b * 262144u + (unsigned)rl * 2048, a);
#pragma unroll
    for (int i = 0; i < 4; ++i) {
      a[2 * i] = lo2f(x1a[i]) + rbf(gb[2 * i] * rbf(lo2f(x2a[i]) + a[2 * i])); a[2 * i + 1] = hi2f(x1a[i]) + rbf(gb[2 * i + 1] * rbf(hi2f(x2a[i]) + a[2 * i + 1]));
      a[8 + 2 * i] = lo2f(x1b[i]) + rbf(gb[8 + 2 * i] * rbf(lo2f(x2b[i]) + a[8 + 2 * i])); a[9 + 2 * i] = hi2f(x1b[i]) + rbf(gb[9 + 2 * i] * rbf(hi2f(x2b[i]) + a[9 + 2 * i]));
    }
    store_row16(out + (size_t)t * D_, lane, a);
    WST(rs, wk, 16 + b);
  }
}

__device__ __forceinline__ void worker(const MainP& p, LAS unsigned char* lds, int xw, int j) {
  const int tid = threadIdx.x, lane = tid & 63, w = __builtin_amdgcn_readfirstlane(tid >> 6);
  const __amdgpu_buffer_rsrc_t rs = mk_rs(p.ws, WS_END);
  const float* mod = (const float*)(p.ws + WS_MOD);
  unsigned cap = SPIN_CAP;
  const int wk = xw * 32 + j;
  WST(rs, wk, 0);
  {
    const int c = j * 6 + xw;
    for (int row = c * 8 + w; row < S_; row += 1536) {
      float v[16];
      load_row16(p.x + (size_t)row * D_, lane, v);
      ln_row(v);
      modulate(v, mod + 1024, mod, lane);
      stx16(rs, (unsigned)lane * 16, WS_H + (unsigned)row * 2048, u32x4{pack2(v[0], v[1]), pack2(v[2], v[3]), pack2(v[4], v[5]), pack2(v[6], v[7])});
      stx16(rs, (unsigned)lane * 16, WS_H + (unsigned)row * 2048 + 1024, u32x4{pack2(v[8], v[9]), pack2(v[10], v[11]), pack2(v[12], v[13]), pack2(v[14], v[15])});
    }
    publish(rs, F_LN + c * 128, 1u);
    WST(rs, wk, 1);
  }
  {
    const int mat = xw % 3, par = xw / 3, pid = P_PJ + xw * 32 + j;
    if (mat == 1) stage_gemm<SG_V>(lds, rs, p.Wv, wk, par, j, WS_VT, pid, cap);
    else stage_gemm<SG_QK>(lds, rs, mat == 0 ? p.Wk : p.Wq, wk, par, j, mat == 0 ? WS_K : WS_Q, pid, cap);
    WST(rs, wk, 3);
  }
  if (xw == 0) stage_gemm<SG_WO>(lds, rs, p.Wo, wk, j >> 3, j & 7, WS_P2, P_S2 + j, cap);
  else if (xw == 1) stage_gemm<SG_W4>(lds, rs, p.w4, wk, j >> 3, j & 7, WS_P8, P_S8 + j, cap);
  else if (xw < 4) {
    if (j < 28) stage_gemm<SG_W3>(lds, rs, p.w3, wk, 0, (xw - 2) * 28 + j, 0, P_S7 + (xw - 2) * 28 + j, cap);
    else stage_out(rs, p.out, mod, wk, (xw - 2) * 4 + j - 28, cap);
  } else if (xw == 4) {
    if (j < 16) stage_gemm<SG_W1>(lds, rs, p.w1, wk, 0, j, 0, P_S4 + j, cap);
    else stage_gemm<SG_W2>(lds, rs, p.w2, wk, 0, j - 16, 0, P_S5 + j - 16, cap);
  } else {
    if (j < 16) stage_attn(rs, wk, j >> 2, j & 3, cap);
    else stage_mid(rs, p.x, mod, wk, j - 16, cap);
  }
  if (cap == 0u && lane == 0) stx4(rs, 0, X_CTL, 1u);
}

#define ST_DECL
#define ST0()
#define ST(i)
#define ST_FLUSH()
constexpr int R_W = 0, R_MEM = 131072, R_QK = R_MEM + 16384, R_KT = R_QK + 8192, R_RED = R_MEM + 16384, R_LDS = 163840;
__device__ __forceinline__ void bar_lo(int w) { asm volatile("s_cmp_ge_u32 %0, 4\n\ts_cbranch_scc1 1f\n\ts_barrier\n\t1:" :: "s"(w) : "scc", "memory"); }
__device__ __forceinline__ void bar_hi(int w) { asm volatile("s_cmp_lt_u32 %0, 4\n\ts_cbranch_scc1 1f\n\ts_setprio 3\n\ts_barrier\n\t1:" :: "s"(w) : "scc", "memory"); }
__device__ __forceinline__ void issue4(__amdgpu_buffer_rsrc_t rs, unsigned vo, unsigned s0, unsigned s1, unsigned s2, unsigned s3, u32x4& a0, u32x4& a1, u32x4& a2, u32x4& a3) {
  asm volatile("buffer_load_dwordx4 v[240:243], %[vo], %[rs], %[s0] offen sc1\n\tbuffer_load_dwordx4 v[244:247], %[vo], %[rs], %[s1] offen sc1\n\t"
               "buffer_load_dwordx4 v[248:251], %[vo], %[rs], %[s2] offen sc1\n\tbuffer_load_dwordx4 v[252:255], %[vo], %[rs], %[s3] offen sc1"
               : "=&{v[240:243]}"(a0), "=&{v[244:247]}"(a1), "=&{v[248:251]}"(a2), "=&{v[252:255]}"(a3)
               : [vo] "v"(vo), [rs] "s"(rs), [s0] "s"(s0), [s1] "s"(s1), [s2] "s"(s2), [s3] "s"(s3) : "memory");
}
#define ARRIVE(R0, R1, R2, R3, CNT) \
  asm volatile("s_waitcnt vmcnt(" #CNT ")\n\t" \
               "1:\n\t" \
               "v_min3_u32 %[t], v" #R0 ", v" #R1 ", v" #R2 "\n\t" \
               "v_min_u32 %[t], %[t], v" #R3 "\n\t" \
               "v_cmp_eq_u32 vcc, 0, %[t]\n\t" \
               "s_cbranch_vccz 2f\n\t" \
               "s_mov_b64 exec, vcc\n\t" \
               "buffer_load_dwordx4 v[" #R0 ":" #R3 "], %[vo], %[rs], %[so] offen sc1\n\t" \
               "s_mov_b64 exec, -1\n\t" \
               "s_waitcnt vmcnt(0)\n\t" \
               "s_sub_u32 %[cap], %[cap], 1\n\t" \
               "s_cbranch_scc0 1b\n\t" \
               "s_mov_b32 %[cap], 0\n\t" \
               "2:" \
               : "+{v[" #R0 ":" #R3 "]}"(a), [t] "=&v"(t), [cap] "+s"(cap) : [vo] "v"(vo), [rs] "s"(rs), [so] "s"(so) : "vcc", "scc", "memory")
template <int I> __device__ __forceinline__ void arrive(__amdgpu_buffer_rsrc_t rs, unsigned vo, unsigned so, u32x4& a, unsigned& cap) {
  unsigned t;
  if (I == 0) ARRIVE(240, 241, 242, 243, 3);
  else if (I == 1) ARRIVE(244, 245, 246, 247, 2);
  else if (I == 2) ARRIVE(248, 249, 250, 251, 1);
  else ARRIVE(252, 253, 254, 255, 0);
}
__device__ __forceinline__ void poll1_finish(__amdgpu_buffer_rsrc_t rs, unsigned vo, unsigned s0, u32x2& a, unsigned& cap) {
  unsigned t;
  asm volatile("1:\n\t"
               "v_min_u32 %[t], v254, v255\n\t"
               "v_cmp_eq_u32 vcc, 0, %[t]\n\t"
               "s_cbranch_vccz 2f\n\t"
               "buffer_load_dwordx2 v[254:255], %[vo], %[rs], %[s0] offen sc1\n\t"
               "s_waitcnt vmcnt(0)\n\t"
               "s_sub_u32 %[cap], %[cap], 1\n\t"
               "s_cbranch_scc0 1b\n\t"
               "s_mov_b32 %[cap], 0\n\t"
               "2:\n\ts_nop 1"
               : "+{v[254:255]}"(a), [t] "=&v"(t), [cap] "+s"(cap) : [vo] "v"(vo), [rs] "s"(rs), [s0] "s"(s0) : "vcc", "scc", "memory");
}
__device__ __forceinline__ u32x4 add_bf16x8(u32x4 a, u32x4 b) {
  u32x4 r;
#pragma unroll
  for (int i = 0; i < 4; ++i) { const f32x2 x = f32x2{lo2f(a[i]), hi2f(a[i])} + f32x2{lo2f(b[i]), hi2f(b[i])}; r[i] = pack2(x[0], x[1]); }
  return r;
}
__device__ __forceinline__ void gather_mem(__amdgpu_buffer_rsrc_t rs, unsigned lbase, int s, int half, int w, unsigned l16, unsigned& cap) {
  const unsigned so = X_MEMP + s * 65536 + half * 32768 + w * 2048;
  u32x4 a0, a1, b0, b1;
  issue4(rs, l16, so, so + 16384, so + 1024, so + 16384 + 1024, a0, b0, a1, b1);
  LAS unsigned char* d = lad(l16, lbase + R_MEM + w * 2048);
  arrive<0>(rs, l16, so, a0, cap); arrive<1>(rs, l16, so + 16384, b0, cap);
  *(LAS u32x4*)(d) = add_bf16x8(a0, b0);
  SCHED();
  arrive<2>(rs, l16, so + 1024, a1, cap); arrive<3>(rs, l16, so + 16384 + 1024, b1, cap);
  *(LAS u32x4*)(d + 1024) = add_bf16x8(a1, b1);
}
__device__ __forceinline__ bf16x8 mem_frag(LAS unsigned char* mp, int chunk) { return cat8(trread(mp + chunk * 1024), trread(mp + chunk * 1024 + 512)); }
__device__ __forceinline__ bf16x16 w_frag(LAS unsigned char* p) {
  return cat16(cat8(trread(p), trread(p + 2048)), cat8(trread(p + 4096), trread(p + 6144)));
}
__device__ __forceinline__ bf16x16 l_frag(LAS unsigned char* p, int i) { return cat16(*(const LAS bf16x8*)(p + i * 2048), *(const LAS bf16x8*)(p + i * 2048 + 1024)); }
__device__ __forceinline__ void make_resident(LAS unsigned char* wb, unsigned lo, unsigned l16) {
#pragma unroll
  for (int c = 0; c < 2; ++c) {
    bf16x16 f[4];
#pragma unroll
    for (int t = 0; t < 4; ++t) f[t] = w_frag(wb + c * 8192 + (lo ^ (t << 5)));
    WAIT_L0(); SCHED();
#pragma unroll
    for (int t = 0; t < 4; ++t) {
      *(LAS bf16x8*)(wb + (c * 4 + t) * 2048 + l16) = __builtin_shufflevector(f[t], f[t], 0, 1, 2, 3, 4, 5, 6, 7);
      *(LAS bf16x8*)(wb + (c * 4 + t) * 2048 + 1024 + l16) = __builtin_shufflevector(f[t], f[t], 8, 9, 10, 11, 12, 13, 14, 15);
    }
    WAIT_L0(); SCHED();
  }
}
__device__ __forceinline__ f32x4 fold(f32x4 a, f32x4 b) {
  f32x4 r;
#pragma unroll
  for (int i = 0; i < 4; ++i) {
    const auto t = __builtin_amdgcn_permlane32_swap(f2u(a[i]), f2u(b[i]), false, false);
    r[i] = u2f(t[0]) + u2f(t[1]);
  }
  return r;
}

struct Sched { unsigned tau, prev; int ea, eb, d1; bool moved; };
__device__ __forceinline__ void retime(__amdgpu_buffer_rsrc_t rs, unsigned l16, int s, Sched& t, const MainP& p, unsigned& cap) {
  if (s > 0) {
    unsigned vo;
    asm volatile("v_lshlrev_b32 %0, 3, %1" : "=v"(vo) : "v"(l16));
    wait_ge(rs, vo, X_TS + (unsigned)s * 8192, 1u, cap);
    int v = (int)(ldx4(rs, vo, X_TS + (unsigned)s * 8192) - t.prev);
#pragma unroll
    for (int o = 16; o >= 1; o >>= 1) v = max(v, __shfl_xor(v, o));
    const int tb = __builtin_amdgcn_readlane(v, 0), ta = __builtin_amdgcn_readlane(v, 32);
    const int late = tb + p.d3 - (int)(t.tau - t.prev);
    if (s > 2) {
      const int up = s < 8 ? 160 : 16, dn = s < 8 ? 64 : 2;
      if (!t.moved) t.ea = max(t.ea - dn, min(ta * 8, t.ea + up));
      if (ta + p.d3 <= t.d1) t.eb = max(t.eb - dn, min((tb - t.d1) * 8, t.eb + up));
    }
    t.moved = late > 0;
    if (late > 0) t.tau += late + p.d4;
  }
  t.d1 = (t.ea >> 3) + p.d1;
}
__device__ __forceinline__ void advance(Sched& t, const MainP& p) { t.prev = t.tau; t.tau += t.d1 + (t.eb >> 3) + p.d2; }

__device__ __forceinline__ void role_a(const MainP& p, LAS unsigned char* lds, int half, int hl) {
  const int tid = threadIdx.x, lane = tid & 63, w = __builtin_amdgcn_readfirstlane(tid >> 6);
  const int h = half * 16 + hl, cg = w & 3, kh = w >> 2;
  const __amdgpu_buffer_rsrc_t rs = mk_rs(p.ws, WS_END);
  const unsigned lbase = (unsigned)(size_t)lds;
  unsigned l16 = lane * 16, l8 = lane * 8;
  asm volatile("" : "+v"(l16), "+v"(l8));
  const int sidx = (lane & 8) ? 0xEEEE : 0x4444;
  bf16x16 W[24];
  unsigned qoff;
  {
    const int fr = lane & 15, g = lane >> 4, tu = 4 * (g & 1) + (fr >> 2), ub = lane >> 3;
    const unsigned lo = (g >> 1) * 1024 + tu * 128 + ((tu >> 1) << 5) + (fr & 3) * 8;
    qoff = R_QK + ((fr >> 1) & 1) * 512 + (g >> 1) * 256 + (fr & 1) * 128 + (8 * (g & 1) + (fr >> 2)) * 8;
    const __amdgpu_buffer_rsrc_t rw = mk_rs(cg < 2 ? p.Wq : p.Wk, D_ * HD * 2);
    const unsigned vo = (unsigned)(ub * HD + 16 * (((lane >> 1) & 3) ^ (ub >> 1)) + 8 * (lane & 1)) * 2u;
    const unsigned so = (unsigned)(kh * 512 * HD + h * DH + (cg & 1) * 64) * 2u;
    auto stage = [&](int c) {
#pragma unroll
      for (int rho = 0; rho < 8; ++rho) dma16(rw, vo, so + (unsigned)((c * 64 + rho * 8) * HD) * 2u, lbase + R_W + w * 16384 + (c & 1) * 8192 + rho * 1024);
    };
    stage(0); stage(1);
#pragma unroll
    for (int c = 0; c < 6; ++c) {
      WAIT_V(8);
#pragma unroll
      for (int t = 0; t < 4; ++t) W[4 * c + t] = w_frag(lds + R_W + w * 16384 + (c & 1) * 8192 + (lo ^ (t << 5)));
      WAIT_L0(); SCHED();
      stage(c + 2);
    }
    WAIT_V(0);
    make_resident(lds + R_W + w * 16384, lo, l16);
  }
  unsigned cap = SPIN_CAP;
  wait_ge(rs, (l16 & 16) * 256, F_ST + (unsigned)(P_PJ + h) * 128, 1u, cap);
  dma16x(rs, l16, WS_K + h * 8192 + w * 1024, lbase + R_KT + w * 1024);
  Sched tt; tt.tau = rt32() - 100000; tt.prev = tt.tau; tt.ea = 240 * 8; tt.eb = 125 * 8; tt.d1 = 0; tt.moved = true;
  const int cu_ = half * 32 + hl; (void)cu_;
  ST_DECL
  for (int s = 0; s < NSEG; ++s) {
    LAS unsigned char* lp = lad(l16, lbase + R_W + w * 16384);
    bf16x16 L[2];
    L[0] = l_frag(lp, 0); L[1] = l_frag(lp, 1);
    SCHED();
    if (w == 7) wait_until(tt.tau);
    BAR();
    ST0();
    gather_mem(rs, lbase, s, half, (w + hl) & 7, l16, cap);
    ST(1);
    LBAR();
    ST(2);
    f32x4 c[4];
#pragma unroll
    for (int t = 0; t < 4; ++t) c[t] = f32x4{0.f, 0.f, 0.f, 0.f};
    LAS unsigned char* mp = lad(l8, lbase + R_MEM + kh * 8192);
    bf16x8 al, af[2];
    u32x4 vt0, vt1;
    u32x2 vmv;
    al = mem_frag(mp, 6); af[0] = mem_frag(mp, 0); af[1] = mem_frag(mp, 1);
#pragma unroll
    for (int i = 0; i < 8; ++i) {
      c[i & 3] = smf(al, L[i & 1], c[i & 3], sidx);
      SCHED();
      if (i == 3) al = mem_frag(mp, 7);
      if (i < 6) L[i & 1] = l_frag(lp, i + 2);
      if (i == 6) {
        const unsigned vo = WS_VT + (unsigned)(s * NH + h) * 8192 + cg * 2048;
        vt0 = ldx16(rs, l16, vo); vt1 = ldx16(rs, l16, vo + 1024);
      }
      SCHED();
#pragma unroll
      for (int r = 3 * i; r < 3 * i + 3; ++r) {
        c[(r + 1) & 3] = smf(af[(r >> 2) & 1], W[(r & ~3) + ((r + 1) & 3)], c[(r + 1) & 3], sidx);
        SCHED();
        if ((r & 3) == 3 && (r >> 2) + 2 < 6) af[(r >> 2) & 1] = mem_frag(mp, (r >> 2) + 2);
        SCHED();
      }
    }
    {
      LAS unsigned char* qw = lad(l8, lbase + R_QK + cg * 2048 + kh * 512);
      const f32x4 f0 = fold(c[0], c[1]);
      *(LAS u32x2*)(qw) = u32x2{pack2(f0[0], f0[1]), pack2(f0[2], f0[3])};
      const f32x4 f1 = fold(c[2], c[3]);
      *(LAS u32x2*)(qw + 1024) = u32x2{pack2(f1[0], f1[1]), pack2(f1[2], f1[3])};
    }
    ST(3);
    if (w < 4) {
      const unsigned vso = X_VM + (unsigned)(s * NH + h) * 2048 + w * 512;
      bf16x8 k1[4], k2[4];
      LAS unsigned char* kb = lad(l16, lbase + R_KT);
#pragma unroll
      for (int ks = 0; ks < 2; ++ks) { k1[ks] = *(const LAS bf16x8*)(kb + ks * 1024); k2[ks] = *(const LAS bf16x8*)(kb + 4096 + ks * 1024); }
      LBAR();
      ST(4);
      vmv = ldx8(rs, l8, vso);
      f32x4 t0 = {0.f, 0.f, 0.f, 0.f}, t1 = t0, t2 = t0;
      LAS unsigned char* qb = lad(qoff, lbase);
      bf16x8 qf[4], kf[4];
#pragma unroll
      for (int ks = 0; ks < 2; ++ks) {
        qf[ks] = cat8(trread(qb + ks * 1024), trread(qb + ks * 1024 + 32));
        kf[ks] = cat8(trread(qb + 4096 + ks * 1024), trread(qb + 4096 + ks * 1024 + 32));
      }
      SCHED();
#pragma unroll
      for (int ks = 0; ks < 4; ++ks) {
        t1 = mfma32(k1[ks], qf[ks], t1); t2 = mfma32(k2[ks], qf[ks], t2); t0 = mfma32(kf[ks], qf[ks], t0);
        SCHED();
        if (ks < 2) {
          qf[ks + 2] = cat8(trread(qb + (ks + 2) * 1024), trread(qb + (ks + 2) * 1024 + 32));
          k1[ks + 2] = *(const LAS bf16x8*)(kb + (ks + 2) * 1024); k2[ks + 2] = *(const LAS bf16x8*)(kb + 4096 + (ks + 2) * 1024);
          kf[ks + 2] = cat8(trread(qb + 4096 + (ks + 2) * 1024), trread(qb + 4096 + (ks + 2) * 1024 + 32));
        }
        SCHED();
      }
      ST(5);
      if (s + 1 < NSEG) {
        const float sl = 0.08838834764831845f * 1.4426950408889634f;
#pragma unroll
        for (int r = 0; r < 4; ++r) { t1[r] = dpp_add<0x128>(t1[r]); t2[r] = dpp_add<0x128>(t2[r]); }
        t0 = fold(t0, t0);
#pragma unroll
        for (int r = 0; r < 4; ++r) t0[r] = dpp_add<0x128>(t0[r]);
        float mx = fmaxf(fmaxf(fmaxf(t1[0], t1[1]), fmaxf(t1[2], t1[3])), fmaxf(fmaxf(t2[0], t2[1]), fmaxf(t2[2], t2[3])));
        mx = fmaxf(mx, fmaxf(fmaxf(t0[0], t0[1]), fmaxf(t0[2], t0[3])));
        const float nm = -gmax(mx) * sl;
#pragma unroll
        for (int r = 0; r < 4; ++r) { t1[r] = __builtin_amdgcn_exp2f(fmaf(t1[r], sl, nm)); t2[r] = __builtin_amdgcn_exp2f(fmaf(t2[r], sl, nm)); }
        const u32x4 pt = {pack2(t1[0], t1[1]), pack2(t1[2], t1[3]), pack2(t2[0], t2[1]), pack2(t2[2], t2[3])};
        f32x4 o0 = {0.f, 0.f, 0.f, 0.f}, o1 = o0;
        o0 = mfma32(__builtin_bit_cast(bf16x8, vt0), __builtin_bit_cast(bf16x8, pt), o0);
        o1 = mfma32(__builtin_bit_cast(bf16x8, vt1), __builtin_bit_cast(bf16x8, pt), o1);
#pragma unroll
        for (int r = 0; r < 4; ++r) t0[r] = __builtin_amdgcn_exp2f(fmaf(t0[r], sl, nm));
        const u32x2 pmv = {pack2(t0[0], t0[1]), pack2(t0[2], t0[3])};
        const u32x2 zz = {0u, 0u};
        const u32x2 pm0 = (l16 < 512) ? pmv : zz, pm1 = (l16 < 512) ? zz : pmv;
        const float ssum = ((t1[0] + t1[1]) + (t1[2] + t1[3])) + ((t2[0] + t2[1]) + (t2[2] + t2[3])) + 0.5f * ((t0[0] + t0[1]) + (t0[2] + t0[3]));
        const float inv = __builtin_amdgcn_rcpf(gsum(ssum));
        ST(6);
        poll1_finish(rs, l8, vso, vmv, cap);
        ST(7);
        o0 = mfma16n(vmv, pm0, o0);
        o1 = mfma16n(vmv, pm1, o1);
        if ((l16 & 128) == 0) {
          const u32x4 v = {nz(pack2(o0[0] * inv, o0[1] * inv)), nz(pack2(o0[2] * inv, o0[3] * inv)), nz(pack2(o1[0] * inv, o1[1] * inv)), nz(pack2(o1[2] * inv, o1[3] * inv))};
          stx16(rs, l16, X_OX + s * 65536 + half * 32768 + (hl * 2 + (w >> 1)) * 1024 + (w & 1) * 128, v);
        }
        if (w == 3) { const unsigned t = max(rt32(), 1u); if (l16 == 0) stx4(rs, 0, X_TS + (unsigned)(s + 1) * 8192 + (unsigned)(32 + h) * 128, t); }
        ST(8);
      }
    } else {
      LBAR();
      LAS unsigned char* qb = lad(qoff, lbase + 4096 + (w - 4) * 1024);
      u32x4 kf = __builtin_bit_cast(u32x4, cat8(trread(qb), trread(qb + 32)));
#pragma unroll
      for (int i = 0; i < 4; ++i) kf[i] = pack2(dpp_add<0x128>(lo2f(kf[i])), dpp_add<0x128>(hi2f(kf[i])));
      stx16(rs, l16, WS_KM + (unsigned)(s * NH + h) * 4096 + (w - 4) * 1024, kf);
      WAIT_V(0);
    }
    LBAR();
    if (w == 4 && l16 == 0) stx4(rs, 0, F_CH + h * 128, (unsigned)s + 1u);
    ST(9);
    if (w == 7) { retime(rs, l16, s, tt, p, cap); advance(tt, p); }
    if (s + 1 < NSEG) {
      unsigned fo;
      asm volatile("v_and_b32 %0, 16, %1\n\tv_lshlrev_b32 %0, 8, %0" : "=v"(fo) : "v"(l16));
      wait_ge(rs, fo, F_ST + (unsigned)(P_PJ + ((s + 1) & 1) * 96 + h) * 128, (unsigned)s + 2u, cap);
      dma16x(rs, l16, WS_K + (unsigned)((s + 1) * NH + h) * 8192 + w * 1024, lbase + R_KT + w * 1024);
    }
  }
  ST_FLUSH();
  if (cap == 0u && l16 == 0) stx4(rs, 0, X_CTL, 1u);
}

__device__ __forceinline__ void role_vb(const MainP& p, LAS unsigned char* lds, int half, int jb) {
  const int tid = threadIdx.x, lane = tid & 63, w = __builtin_amdgcn_readfirstlane(tid >> 6);
  const int h = half * 16 + jb, wr = (w + jb) & 7;
  const __amdgpu_buffer_rsrc_t rs = mk_rs(p.ws, WS_END);
  const unsigned lbase = (unsigned)(size_t)lds;
  unsigned l16 = lane * 16, l8 = lane * 8;
  asm volatile("" : "+v"(l16), "+v"(l8));
  const int sidx = (lane & 8) ? 0xEEEE : 0x4444;
  bf16x16 WV[16], WB[8];
  unsigned roff;
  {
    const int fr = lane & 15, g = lane >> 4, tu = 4 * (g & 1) + (fr >> 2), ub = lane >> 3;
    const unsigned lo = (g >> 1) * 1024 + tu * 128 + ((tu >> 1) << 5) + (fr & 3) * 8;
    roff = R_RED + w * 2048 + mem_slot(16 * (g >> 1) + fr) * 32 + (g & 1) * 16;
    const unsigned vcol = (unsigned)(16 * (((lane >> 1) & 3) ^ (ub >> 1)) + 8 * (lane & 1)) * 2u;
    const __amdgpu_buffer_rsrc_t rv = mk_rs(p.Wv, D_ * HD * 2), ro = mk_rs(p.Wo, D_ * HD * 2);
    const unsigned vv = (unsigned)(ub * HD) * 2u + vcol, sv = (unsigned)(8 * w * HD + h * DH) * 2u;
    auto stage = [&](int c) {
      const unsigned d = lbase + R_W + (c & 7) * 16384 + w * 1024, sc = sv + (unsigned)c * 64 * HD * 2;
      dma16(rv, vv, sc, d); dma16(rv, vv, sc + 128, d + 8192);
    };
#pragma unroll
    for (int c = 0; c < 8; ++c) stage(c);
    const int vo = (w >> 2) * 8192 + (lo ^ ((w & 3) << 5));
#pragma unroll
    for (int c = 0; c < 16; ++c) {
      if (c < 8) WAIT_V(14); else wait_v<0>();
      BAR();
      WV[c] = w_frag(lds + R_W + (c & 7) * 16384 + vo);
      LBAR();
      if (c < 8) stage(c + 8);
    }
    const unsigned vw = (unsigned)((32 * ((ub >> 1) & 1) + 16 * (ub >> 2) + (ub & 1)) * D_) * 2u + vcol;
    const unsigned sw = (unsigned)((half * 2048 + wr * 256) * D_ + jb * 64) * 2u;
    auto stageo = [&](int c) {
      const unsigned d = lbase + R_W + w * 16384 + (c & 1) * 8192;
#pragma unroll
      for (int rho = 0; rho < 8; ++rho) {
        const int r = rho >> 1, hb = rho & 1;
        dma16(ro, vw, sw + (unsigned)((c * 64 + 8 * (r >> 1) + 4 * hb + 2 * (r & 1)) * D_) * 2u, d + rho * 1024);
      }
    };
    stageo(2); stageo(3);
    WAIT_V(0);
#pragma unroll
    for (int c = 0; c < 2; ++c)
#pragma unroll
      for (int t = 0; t < 4; ++t) WB[c * 4 + t] = w_frag(lds + R_W + w * 16384 + c * 8192 + (lo ^ (t << 5)));
    WAIT_L0(); SCHED();
    stageo(0); stageo(1);
    WAIT_V(0);
    make_resident(lds + R_W + w * 16384, lo, l16);
  }
  unsigned cap = SPIN_CAP;
  Sched tt; tt.tau = rt32() - 100000; tt.prev = tt.tau; tt.ea = 240 * 8; tt.eb = 125 * 8; tt.d1 = 0; tt.moved = true;
  const int cu_ = half * 32 + 16 + jb; (void)cu_;
  ST_DECL
  for (int s = 0; s < NSEG; ++s) {
    if (w == 7) wait_until(tt.tau);
    BAR();
    ST0();
    gather_mem(rs, lbase, s, half, wr, l16, cap);
    ST(1);
    LBAR();
    ST(2);
    {
      f32x4 c0 = {0.f, 0.f, 0.f, 0.f}, c1 = c0;
      LAS unsigned char* mp = lad(l8, lbase + R_MEM);
      bf16x8 af[8];
#pragma unroll
      for (int c = 0; c < 8; ++c) af[c] = mem_frag(mp, c);
      SCHED();
#pragma unroll
      for (int c = 0; c < 8; ++c) {
        if (c & 1) c1 = smf(af[c], WV[c], c1, sidx); else c0 = smf(af[c], WV[c], c0, sidx);
        SCHED();
        if (c >= 3) { af[c] = mem_frag(mp, 8 + c); if (c < 6) af[c - 3] = mem_frag(mp, 5 + c); }
        SCHED();
      }
#pragma unroll
      for (int c = 8; c < 16; ++c) { if (c & 1) c1 = smf(af[c - 8], WV[c], c1, sidx); else c0 = smf(af[c - 8], WV[c], c0, sidx); }
      SCHED();
      c0 += c1;
      const f32x4 f = fold(c0, c0);
      if (l16 < 512) stx8(rs, l8, X_VM + (unsigned)(s * NH + h) * 2048 + w * 256, u32x2{nz(pack2(f[0], f[1])), nz(pack2(f[2], f[3]))});
    }
    ST(3);
    if (w == 7) retime(rs, l16, s, tt, p, cap);
    if (s + 1 < NSEG) {
      LAS unsigned char* lp = lad(l16, lbase + R_W + w * 16384);
      bf16x16 L[2];
      L[0] = l_frag(lp, 0); L[1] = l_frag(lp, 1);
      SCHED();
      u32x4 a[4];
      const unsigned so = X_OX + s * 65536 + half * 32768 + wr * 4096;
      if (w == 7) wait_until(tt.tau + tt.d1);
      BAR();
      ST(4);
      issue4(rs, l16, so, so + 1024, so + 2048, so + 3072, a[0], a[1], a[2], a[3]);
      f32x4 c[4];
#pragma unroll
      for (int t = 0; t < 4; ++t) c[t] = f32x4{0.f, 0.f, 0.f, 0.f};
      arrive<0>(rs, l16, so, a[0], cap);
#pragma unroll
      for (int i = 0; i < 8; ++i) {
        if (i == 4) arrive<1>(rs, l16, so + 1024, a[1], cap);
        c[i & 3] = smf(__builtin_bit_cast(bf16x8, a[i >> 2]), L[i & 1], c[i & 3], sidx);
        SCHED();
        if (i + 2 < 8) L[i & 1] = l_frag(lp, i + 2);
        SCHED();
      }
      arrive<2>(rs, l16, so + 2048, a[2], cap);
#pragma unroll
      for (int t = 0; t < 4; ++t) c[t] = smf(__builtin_bit_cast(bf16x8, a[2]), WB[t], c[t], sidx);
      SCHED();
      arrive<3>(rs, l16, so + 3072, a[3], cap);
#pragma unroll
      for (int t = 0; t < 4; ++t) c[t] = smf(__builtin_bit_cast(bf16x8, a[3]), WB[4 + t], c[t], sidx);
      ST(5);
      LAS unsigned char* rw = lad(roff, lbase);
      *(LAS f32x4*)(rw) = fold(c[0], c[1]);
      *(LAS f32x4*)(rw + 512) = fold(c[2], c[3]);
      ST(6);
      LBAR();
      ST(7);
      if (w < 4) {
        LAS unsigned char* rp = lad(l16, lbase + R_RED + (w & 1) * 1024);
        f32x4 v[8];
#pragma unroll
        for (int i = 0; i < 8; ++i) v[i] = *(const LAS f32x4*)(rp + i * 2048);
        const f32x4 sum = ((v[0] + v[1]) + (v[2] + v[3])) + ((v[4] + v[5]) + (v[6] + v[7]));
        const u32x2 o = {nz(pack2(sum[0], sum[1])), nz(pack2(sum[2], sum[3]))};
        stx8(rs, l8, X_MEMP + (s + 1) * 65536 + (half ^ 1 ^ (w >> 1)) * 32768 + half * 16384 + jb * 1024 + (w & 1) * 512, o);
      }
      if (w == 3) { const unsigned t = max(rt32(), 1u); if (l16 == 0) stx4(rs, 0, X_TS + (unsigned)(s + 1) * 8192 + (unsigned)h * 128, t); }
      ST(8);
    }
    advance(tt, p);
  }
  ST_FLUSH();
  if (cap == 0u && l16 == 0) stx4(rs, 0, X_CTL, 1u);
}

#if defined(__HIP_DEVICE_COMPILE__)
__attribute__((target("no-packed-fp32-ops")))
#endif
__global__ void __launch_bounds__(512, 2) k_main(MainP p) {
  extern __shared__ __attribute__((aligned(16))) unsigned char smem[];
  const int b = blockIdx.x, x = b & 7, j = b >> 3;
  if (x >= 2) worker(p, (LAS unsigned char*)smem, x - 2, j);
  else if (j < 16) role_a(p, (LAS unsigned char*)smem, x, j);
  else role_vb(p, (LAS unsigned char*)smem, x, j - 16);
}

extern "C" void kernel_launch(void* const* d_in, const int* in_sizes, int n_in, void* d_out, int out_size, void* d_ws, size_t ws_size,
                              hipStream_t stream) {
  unsigned char* ws = (unsigned char*)d_ws;
  k_prep<<<256, 512, 0, stream>>>((const u16*)d_in[12], (const u16*)d_in[13], (const u16*)d_in[1], ws);
  MainP p;
  p.x = (const u16*)d_in[0]; p.Wq = (const u16*)d_in[2]; p.Wk = (const u16*)d_in[3]; p.Wv = (const u16*)d_in[4]; p.Wo = (const u16*)d_in[5];
  p.w1 = (const u16*)d_in[8]; p.w2 = (const u16*)d_in[9]; p.w3 = (const u16*)d_in[10]; p.w4 = (const u16*)d_in[11];
  p.out = (u16*)d_out; p.ws = ws; p.d1 = 10; p.d2 = 10; p.d3 = 4; p.d4 = 200;
  (void)hipFuncSetAttribute((const void*)k_main, hipFuncAttributeMaxDynamicSharedMemorySize, R_LDS);
  k_main<<<256, 512, R_LDS, stream>>>(p);
}
```

```cpp
#include <hip/hip_runtime.h>
#include <hip/hip_bf16.h>
#include <cstdlib>
#pragma clang diagnostic ignored "-Winline-asm"

#define LAS __attribute__((address_space(3)))
#define GAS __attribute__((address_space(1)))
typedef unsigned short u16;
typedef __bf16 bf16x2 __attribute__((ext_vector_type(2)));
typedef __bf16 bf16x4 __attribute__((ext_vector_type(4)));
typedef __bf16 bf16x8 __attribute__((ext_vector_type(8)));
typedef __bf16 bf16x16 __attribute__((ext_vector_type(16)));
typedef short s16x4 __attribute__((ext_vector_type(4)));
typedef float f32x2 __attribute__((ext_vector_type(2)));
typedef float f32x4 __attribute__((ext_vector_type(4)));
typedef unsigned u32x2 __attribute__((ext_vector_type(2)));
typedef unsigned u32x4 __attribute__((ext_vector_type(4)));

constexpr int S_ = 2048, D_ = 1024, NH = 32, DH = 128, HD = 4096, NSEG = 64, FF = 3584;

constexpr unsigned MB = 1u << 20;
constexpr unsigned X_MEMP = 0;
constexpr unsigned X_OX = 4 * MB;
constexpr unsigned X_VM = 8 * MB;
constexpr unsigned F_LN = 12 * MB;
constexpr unsigned F_ST = F_LN + 32768;
constexpr unsigned F_CH = F_ST + 98304;
constexpr unsigned X_CTL = F_CH + 4096;
constexpr unsigned X_TS = X_CTL + 4096;
constexpr unsigned X_BYTES = X_TS + 65 * 8192;
constexpr unsigned WS_MOD = 15 * MB;
constexpr unsigned WS_H = 16 * MB;
constexpr unsigned WS_Q = 20 * MB;
constexpr unsigned WS_K = 36 * MB;
constexpr unsigned WS_VT = 52 * MB;
constexpr unsigned WS_O = 68 * MB;
constexpr unsigned WS_X1 = 84 * MB;
constexpr unsigned WS_H2 = 88 * MB;
constexpr unsigned WS_G = 92 * MB;
constexpr unsigned WS_H1S = 96 * MB;
constexpr unsigned WS_X2 = 101 * MB;
constexpr unsigned WS_ST5 = 106 * MB;
constexpr unsigned WS_U = 108 * MB;
constexpr unsigned WS_KM = 124 * MB;
constexpr unsigned WS_P2 = 132 * MB;
constexpr unsigned WS_P8 = 164 * MB;
constexpr unsigned WS_END = 198 * MB;

__device__ __forceinline__ float bf2f(unsigned short b) { return __builtin_bit_cast(float, (unsigned)b << 16); }
__device__ __forceinline__ unsigned f2u(float f) { return __builtin_bit_cast(unsigned, f); }
__device__ __forceinline__ float u2f(unsigned u) { return __builtin_bit_cast(float, u); }
__device__ __forceinline__ float lo2f(unsigned w) { return __builtin_bit_cast(float, w << 16); }
__device__ __forceinline__ float hi2f(unsigned w) { return __builtin_bit_cast(float, w & 0xFFFF0000u); }
__device__ __forceinline__ unsigned pack2(float a, float b) { bf16x2 v; v[0] = (__bf16)a; v[1] = (__bf16)b; return __builtin_bit_cast(unsigned, v); }
__device__ __forceinline__ float rbf(float a) { return (float)(__bf16)a; }
__device__ __forceinline__ unsigned nz(unsigned w) { return max(w, 1u); }
__device__ __forceinline__ float gelu_tanh(float x) {
  const float z = 0.7978845608028654f * (x + 0.044715f * x * x * x);
  const float e = __builtin_amdgcn_exp2f(-2.885390081777927f * z);
  return x * __builtin_amdgcn_rcpf(1.0f + e);
}
template <int CTRL> __device__ __forceinline__ float dpp_add(float v) { return v + u2f(__builtin_amdgcn_update_dpp(0u, f2u(v), CTRL, 0xF, 0xF, true)); }
__device__ __forceinline__ float wave_sum(float v) {
  v = dpp_add<0xB1>(v); v = dpp_add<0x4E>(v); v = dpp_add<0x141>(v); v = dpp_add<0x140>(v);
  auto t = __builtin_amdgcn_permlane16_swap(f2u(v), f2u(v), false, false);
  v = u2f(t[0]) + u2f(t[1]);
  t = __builtin_amdgcn_permlane32_swap(f2u(v), f2u(v), false, false);
  return u2f(t[0]) + u2f(t[1]);
}
#define WAIT_V(n) asm volatile("s_waitcnt vmcnt(" #n ")" ::: "memory")
#define WAIT_L0() asm volatile("s_waitcnt lgkmcnt(0)" ::: "memory")
#define BAR() do { asm volatile("" ::: "memory"); __builtin_amdgcn_s_barrier(); asm volatile("" ::: "memory"); } while (0)
#define LBAR() do { WAIT_L0(); BAR(); } while (0)
template <int N> __device__ __forceinline__ void wait_v() { asm volatile("s_waitcnt vmcnt(%0)" :: "n"(N) : "memory"); }
__device__ __forceinline__ void dma16(__amdgpu_buffer_rsrc_t rs, unsigned voff, unsigned soff, unsigned ldsa) {
  asm volatile("s_mov_b32 m0, %0\n\ts_nop 3\n\tbuffer_load_dwordx4 %1, %2, %3 offen lds" :: "s"(ldsa), "v"(voff), "s"(rs), "s"(soff) : "m0", "memory");
}
__device__ __forceinline__ void dma16x(__amdgpu_buffer_rsrc_t rs, unsigned voff, unsigned soff, unsigned ldsa) {
  asm volatile("s_mov_b32 m0, %0\n\ts_nop 3\n\tbuffer_load_dwordx4 %1, %2, %3 offen sc1 lds" :: "s"(ldsa), "v"(voff), "s"(rs), "s"(soff) : "m0", "memory");
}
__device__ __forceinline__ u32x4 ldx16(__amdgpu_buffer_rsrc_t rs, unsigned voff, unsigned soff) { return __builtin_amdgcn_raw_buffer_load_b128(rs, voff, soff, 16); }
__device__ __forceinline__ u32x2 ldx8(__amdgpu_buffer_rsrc_t rs, unsigned voff, unsigned soff) { return __builtin_amdgcn_raw_buffer_load_b64(rs, voff, soff, 16); }
__device__ __forceinline__ unsigned ldx4(__amdgpu_buffer_rsrc_t rs, unsigned voff, unsigned soff) { return __builtin_amdgcn_raw_buffer_load_b32(rs, voff, soff, 16); }
__device__ __forceinline__ void stx16(__amdgpu_buffer_rsrc_t rs, unsigned voff, unsigned soff, u32x4 v) {
  __builtin_amdgcn_raw_buffer_store_b128(v, rs, voff, soff, 16);
  asm volatile("s_nop 1" :: "v"(v) : "memory");
}
__device__ __forceinline__ void stx8(__amdgpu_buffer_rsrc_t rs, unsigned voff, unsigned soff, u32x2 v) { __builtin_amdgcn_raw_buffer_store_b64(v, rs, voff, soff, 16); }
__device__ __forceinline__ void stx4(__amdgpu_buffer_rsrc_t rs, unsigned voff, unsigned soff, unsigned v) { __builtin_amdgcn_raw_buffer_store_b32(v, rs, voff, soff, 16); }
__device__ __forceinline__ void wait_ge(__amdgpu_buffer_rsrc_t rs, unsigned voff, unsigned soff, unsigned need, unsigned& cap) {
  unsigned t;
  asm volatile("s_nop 4\n\t"
               "1:\n\t"
               "buffer_load_dword %[t], %[vo], %[rs], %[so] offen sc1\n\t"
               "s_waitcnt vmcnt(0)\n\t"
               "v_cmp_gt_u32 vcc, %[need], %[t]\n\t"
               "s_cmp_eq_u64 vcc, 0\n\t"
               "s_cbranch_scc1 2f\n\t"
               "s_sleep 2\n\t"
               "s_sub_u32 %[cap], %[cap], 1\n\t"
               "s_cbranch_scc0 1b\n\t"
               "s_mov_b32 %[cap], 0\n\t"
               "2:"
               : [t] "=&v"(t), [cap] "+s"(cap) : [vo] "v"(voff), [rs] "s"(rs), [so] "s"(soff), [need] "s"(need) : "vcc", "scc", "memory");
}
__device__ __forceinline__ __amdgpu_buffer_rsrc_t mk_rs(const void* p, unsigned bytes) { return __builtin_amdgcn_make_buffer_rsrc((void*)p, 0, (int)bytes, 0x00020000); }
#define SCHED() __builtin_amdgcn_sched_barrier(0)
__device__ __forceinline__ unsigned now32() { unsigned long long t; asm volatile("s_memtime %0\n\ts_waitcnt lgkmcnt(0)" : "=s"(t) :: "memory"); return (unsigned)t; }
__device__ __forceinline__ unsigned rt32() { unsigned long long t; asm volatile("s_memrealtime %0\n\ts_waitcnt lgkmcnt(0)" : "=s"(t) :: "memory"); return (unsigned)t; }
__device__ __forceinline__ void wait_until(unsigned t) { while ((int)(rt32() - t) < 0) {} }
template <int X = 0> __device__ __forceinline__ LAS unsigned char* lad(unsigned v, unsigned u) {
  unsigned a;
  if (X == 0) asm volatile("v_add_u32 %0, %1, %2" : "=v"(a) : "s"(u), "v"(v));
  else asm volatile("v_xor_b32 %0, %3, %2\n\tv_add_u32 %0, %1, %0" : "=&v"(a) : "s"(u), "v"(v), "n"(X));
  return (LAS unsigned char*)(size_t)a;
}
__device__ __forceinline__ bf16x4 trread(LAS unsigned char* p) { return __builtin_amdgcn_ds_read_tr16_b64_v4bf16((LAS bf16x4*)p); }
__device__ __forceinline__ bf16x8 cat8(bf16x4 a, bf16x4 b) { return __builtin_shufflevector(a, b, 0, 1, 2, 3, 4, 5, 6, 7); }
__device__ __forceinline__ bf16x16 cat16(bf16x8 a, bf16x8 b) { return __builtin_shufflevector(a, b, 0, 1, 2, 3, 4, 5, 6, 7, 8, 9, 10, 11, 12, 13, 14, 15); }
__device__ __forceinline__ f32x4 mfma32(bf16x8 a, bf16x8 b, f32x4 c) { return __builtin_amdgcn_mfma_f32_16x16x32_bf16(a, b, c, 0, 0, 0); }
__device__ __forceinline__ f32x4 mfma16(u32x2 a, u32x2 b, f32x4 c) {
  return mfma32(__builtin_bit_cast(bf16x8, u32x4{a[0], a[1], 0u, 0u}), __builtin_bit_cast(bf16x8, u32x4{b[0], b[1], 0u, 0u}), c);
}
__device__ __forceinline__ f32x4 mfma16n(u32x2 a, u32x2 b, f32x4 c) { return __builtin_amdgcn_mfma_f32_16x16x16bf16_1k(__builtin_bit_cast(s16x4, a), __builtin_bit_cast(s16x4, b), c, 0, 0, 0); }
__device__ __forceinline__ f32x4 smf(bf16x8 a, bf16x16 b, f32x4 c, int idx) { return __builtin_amdgcn_smfmac_f32_16x16x64_bf16(a, b, c, idx, 0, 0); }

__device__ __forceinline__ void ln_row(float (&x)[16]) {
  float s = 0.f;
#pragma unroll
  for (int i = 0; i < 16; ++i) s += x[i];
  const float mean = wave_sum(s) * (1.0f / 1024.0f);
  float q = 0.f;
#pragma unroll
  for (int i = 0; i < 16; ++i) { const float d = x[i] - mean; q += d * d; }
  const float var = wave_sum(q) * (1.0f / 1024.0f);
  const float m = rbf(mean), isd = __builtin_amdgcn_rcpf(rbf(__builtin_amdgcn_sqrtf(rbf(rbf(var) + 1e-5f))));
#pragma unroll
  for (int i = 0; i < 16; ++i) x[i] = rbf(rbf(x[i] - m) * isd);
}
__device__ __forceinline__ void load_row16(const u16* rowp, int lane, float (&x)[16]) {
  const u32x4 a = *(const u32x4*)(rowp + lane * 8), b = *(const u32x4*)(rowp + 512 + lane * 8);
#pragma unroll
  for (int i = 0; i < 4; ++i) { x[2 * i] = lo2f(a[i]); x[2 * i + 1] = hi2f(a[i]); x[8 + 2 * i] = lo2f(b[i]); x[9 + 2 * i] = hi2f(b[i]); }
}
__device__ __forceinline__ void store_row16(u16* rowp, int lane, const float (&x)[16]) {
  u32x4 a, b;
#pragma unroll
  for (int i = 0; i < 4; ++i) { a[i] = pack2(x[2 * i], x[2 * i + 1]); b[i] = pack2(x[8 + 2 * i], x[9 + 2 * i]); }
  *(u32x4*)(rowp + lane * 8) = a; *(u32x4*)(rowp + 512 + lane * 8) = b;
}
__device__ __forceinline__ void modulate(float (&x)[16], const float* sc, const float* sh, int lane) {
#pragma unroll
  for (int hh = 0; hh < 2; ++hh)
#pragma unroll
    for (int j = 0; j < 2; ++j) {
      const f32x4 c = *(const f32x4*)(sc + hh * 512 + lane * 8 + j * 4), t = *(const f32x4*)(sh + hh * 512 + lane * 8 + j * 4);
#pragma unroll
      for (int i = 0; i < 4; ++i) { float& v = x[hh * 8 + j * 4 + i]; v = rbf(rbf(v * rbf(1.0f + c[i])) + t[i]); }
    }
}

__host__ __device__ __forceinline__ int mem_slot(int dl) {
  return ((dl >> 2) & 1) * 32 + (((dl >> 5) & 1) * 2 + ((dl >> 3) & 1)) * 8 + (((dl >> 4) & 1) * 2 + (dl & 1)) * 2 + ((dl >> 1) & 1);
}

__global__ void __launch_bounds__(512) k_prep(const u16* __restrict__ c, const u16* __restrict__ wmod, const u16* __restrict__ mem0, unsigned char* ws) {
  __shared__ float red[128][33];
  __shared__ float sc[1024];
  const int b = blockIdx.x, tid = threadIdx.x;
  {
    u32x4* z = (u32x4*)ws;
    const u32x4 zero = {0u, 0u, 0u, 0u};
    for (size_t i = (size_t)65536 / 16 + (size_t)b * 512 + tid; i < X_BYTES / 16; i += (size_t)256 * 512) z[i] = zero;
    if (b >= 192 && b < 208) {
      u32x4* y = (u32x4*)(ws + WS_H1S + ((b & 8) ? 64 * 65536 : 0));
      y[(b & 7) * 512 + tid] = zero;
    }
  }
  if (b == 255) {
    for (int d = tid; d < 1024; d += 512) {
      u32x4 v;
#pragma unroll
      for (int i = 0; i < 4; ++i) v[i] = nz((unsigned)mem0[(2 * i) * 1024 + d] | ((unsigned)mem0[(2 * i + 1) * 1024 + d] << 16));
      const u32x4 mz = {0x80008000u, 0x80008000u, 0x80008000u, 0x80008000u};
#pragma unroll
      for (int r = 0; r < 2; ++r) {
        *(u32x4*)(ws + X_MEMP + r * 32768 + (d >> 6) * 1024 + mem_slot(d & 63) * 16) = v;
        *(u32x4*)(ws + X_MEMP + r * 32768 + 16384 + d * 16) = mz;
      }
    }
  }
  if (b < 192) {
    for (int i = tid; i < 1024; i += 512) { const float v = bf2f(c[i]); sc[i] = rbf(v / (1.0f + __expf(-v))); }
    __syncthreads();
    const int kg = tid >> 2, c8 = tid & 3;
    float acc[8] = {0.f, 0.f, 0.f, 0.f, 0.f, 0.f, 0.f, 0.f};
    u32x4 w[8];
#pragma unroll
    for (int i = 0; i < 8; ++i) w[i] = *(const u32x4*)(wmod + (size_t)(kg + 128 * i) * 6144 + b * 32 + c8 * 8);
#pragma unroll
    for (int i = 0; i < 8; ++i) {
      const float s = sc[kg + 128 * i];
#pragma unroll
      for (int j = 0; j < 4; ++j) { acc[2 * j] += s * lo2f(w[i][j]); acc[2 * j + 1] += s * hi2f(w[i][j]); }
    }
#pragma unroll
    for (int j = 0; j < 8; ++j) red[kg][c8 * 8 + j] = acc[j];
    __syncthreads();
    if (tid < 256) {
      const int col = tid & 31, part = tid >> 5;
      float s = 0.f;
#pragma unroll
      for (int i = 0; i < 16; ++i) s += red[part * 16 + i][col];
      __syncthreads();
      red[part][col] = s;
    } else __syncthreads();
    __syncthreads();
    if (tid < 32) {
      float s = 0.f;
#pragma unroll
      for (int i = 0; i < 8; ++i) s += red[i][tid];
      ((float*)(ws + WS_MOD))[b * 32 + tid] = rbf(s);
    }
  }
}

#define WST(rs, wk, i)
struct MainP { const u16 *x, *Wq, *Wk, *Wv, *Wo, *w1, *w2, *w3, *w4; u16* out; unsigned char* ws; int d1, d2, d3, d4; };
constexpr unsigned SPIN_CAP = 1u << 18;

__device__ __forceinline__ float gmax(float v) {
  auto t = __builtin_amdgcn_permlane16_swap(f2u(v), f2u(v), false, false);
  v = fmaxf(u2f(t[0]), u2f(t[1]));
  t = __builtin_amdgcn_permlane32_swap(f2u(v), f2u(v), false, false);
  return fmaxf(u2f(t[0]), u2f(t[1]));
}
__device__ __forceinline__ float gsum(float v) {
  auto t = __builtin_amdgcn_permlane16_swap(f2u(v), f2u(v), false, false);
  v = u2f(t[0]) + u2f(t[1]);
  t = __builtin_amdgcn_permlane32_swap(f2u(v), f2u(v), false, false);
  return u2f(t[0]) + u2f(t[1]);
}
__device__ __forceinline__ u32x4 widen(f32x4 a, f32x4 b) {
  const auto r0 = __builtin_amdgcn_permlane16_swap(pack2(a[0], a[1]), pack2(b[0], b[1]), false, false);
  const auto r1 = __builtin_amdgcn_permlane16_swap(pack2(a[2], a[3]), pack2(b[2], b[3]), false, false);
  return u32x4{r0[0], r1[0], r0[1], r1[1]};
}
__device__ __forceinline__ void publish(__amdgpu_buffer_rsrc_t rs, unsigned word, unsigned val) {
  WAIT_V(0); BAR();
  if (threadIdx.x == 0) stx4(rs, 0, word, val);
}

constexpr int P_S2 = 0, P_S8 = 32, P_S7 = 64, P_S4 = 120, P_S5 = 136, P_S1 = 160, P_S3 = 288, P_PJ = 416;
__device__ __forceinline__ void stage_attn(__amdgpu_buffer_rsrc_t rs, int wk, int grp, int kq, unsigned& cap) {
  const int tid = threadIdx.x, lane = tid & 63, w = __builtin_amdgcn_readfirstlane(tid >> 6), fr = lane & 15, g = lane >> 4;
  const int h = kq * 8 + w;
  const unsigned l16 = lane * 16;
  for (int seg = grp; seg < NSEG; seg += 4) {
    wait_ge(rs, (unsigned)(lane % 3) * 4096, F_ST + (unsigned)(P_PJ + (seg & 1) * 96 + h) * 128, (unsigned)seg + 1u, cap);
    const unsigned ub = (unsigned)(seg * NH + h);
    f32x4 s0[2], s1[2], s2;
    s0[0] = s0[1] = s1[0] = s1[1] = s2 = f32x4{0.f, 0.f, 0.f, 0.f};
    bf16x8 q0[4], q1[4], vf[8];
#pragma unroll
    for (int ks = 0; ks < 4; ++ks) {
      q0[ks] = __builtin_bit_cast(bf16x8, ldx16(rs, l16, WS_Q + ub * 8192 + ks * 1024)); q1[ks] = __builtin_bit_cast(bf16x8, ldx16(rs, l16, WS_Q + ub * 8192 + 4096 + ks * 1024));
      const bf16x8 k0 = __builtin_bit_cast(bf16x8, ldx16(rs, l16, WS_K + ub * 8192 + ks * 1024)), k1 = __builtin_bit_cast(bf16x8, ldx16(rs, l16, WS_K + ub * 8192 + 4096 + ks * 1024));
      s1[0] = mfma32(k0, q0[ks], s1[0]); s1[1] = mfma32(k0, q1[ks], s1[1]); s2 = mfma32(k1, q1[ks], s2);
    }
    const unsigned ev = (unsigned)(fr >> 3) * 1024 + (unsigned)(g * 16 + 8 * ((fr >> 2) & 1) + (fr & 3)) * 16;
#pragma unroll
    for (int i = 0; i < 8; ++i) vf[i] = __builtin_bit_cast(bf16x8, ldx16(rs, ev, WS_VT + ub * 8192 + (i >> 1) * 2048 + (i & 1) * 64));
    wait_ge(rs, 0, F_CH + h * 128, (unsigned)seg + 1u, cap);
    u32x2 vmf[8];
    const unsigned em = (unsigned)(fr >> 3) * 256 + (unsigned)(g & 1) * 64 + (unsigned)(8 * ((fr >> 2) & 1) + (fr & 3)) * 4;
    for (;;) {
      asm volatile("" ::: "memory");
      unsigned mn = 0xFFFFFFFFu;
#pragma unroll
      for (int i = 0; i < 8; ++i) { const unsigned so = X_VM + ub * 2048 + (i >> 1) * 512 + (i & 1) * 16; vmf[i] = u32x2{ldx4(rs, em, so), ldx4(rs, em, so + 128)}; }
#pragma unroll
      for (int ks = 0; ks < 4; ++ks) {
        const bf16x8 kmf = __builtin_bit_cast(bf16x8, ldx16(rs, l16, WS_KM + ub * 4096 + ks * 1024));
        if (ks == 0) { s0[0] = mfma32(kmf, q0[ks], f32x4{0.f, 0.f, 0.f, 0.f}); s0[1] = mfma32(kmf, q1[ks], f32x4{0.f, 0.f, 0.f, 0.f}); }
        else { s0[0] = mfma32(kmf, q0[ks], s0[0]); s0[1] = mfma32(kmf, q1[ks], s0[1]); }
      }
#pragma unroll
      for (int i = 0; i < 8; ++i) mn = min(mn, min(vmf[i][0], vmf[i][1]));
      if (__all(mn != 0u) || cap == 0u) break;
      cap = __builtin_amdgcn_readfirstlane(cap - 1u);
    }
    cap = __builtin_amdgcn_readfirstlane(cap);
    const float scale = 0.08838834764831845f, L2E = 1.4426950408889634f;
    u32x2 pm[2]; u32x4 pt[2];
#pragma unroll
    for (int t = 0; t < 2; ++t) {
      float a[4], b[4], c[4];
      float mx = -3.0e38f;
#pragma unroll
      for (int r = 0; r < 4; ++r) {
        const int j = 4 * g + r;
        a[r] = (g < 2) ? rbf(rbf(s0[t][r]) * scale) : -3.0e38f;
        b[r] = (t == 1 || j <= fr) ? rbf(rbf(s1[t][r]) * scale) : -3.0e38f;
        c[r] = (t == 1 && j <= fr) ? rbf(rbf(s2[r]) * scale) : -3.0e38f;
        mx = fmaxf(mx, fmaxf(a[r], fmaxf(b[r], c[r])));
      }
      mx = gmax(mx);
      float sum = 0.f;
#pragma unroll
      for (int r = 0; r < 4; ++r) {
        a[r] = __builtin_amdgcn_exp2f((a[r] - mx) * L2E); b[r] = __builtin_amdgcn_exp2f((b[r] - mx) * L2E); c[r] = __builtin_amdgcn_exp2f((c[r] - mx) * L2E);
        sum += a[r] + b[r] + c[r];
      }
      const float inv = __builtin_amdgcn_rcpf(gsum(sum));
      pm[t] = u32x2{pack2(a[0] * inv, a[1] * inv), pack2(a[2] * inv, a[3] * inv)};
      pt[t] = u32x4{pack2(b[0] * inv, b[1] * inv), pack2(b[2] * inv, b[3] * inv), pack2(c[0] * inv, c[1] * inv), pack2(c[2] * inv, c[3] * inv)};
    }
#pragma unroll
    for (int u = 0; u < 4; ++u) {
      f32x4 o[2][2];
#pragma unroll
      for (int bq = 0; bq < 2; ++bq)
#pragma unroll
        for (int t = 0; t < 2; ++t) {
          f32x4 z = {0.f, 0.f, 0.f, 0.f};
          z = mfma16(vmf[2 * u + bq], pm[t], z);
          o[bq][t] = mfma32(vf[2 * u + bq], __builtin_bit_cast(bf16x8, pt[t]), z);
        }
#pragma unroll
      for (int t = 0; t < 2; ++t)
        stx16(rs, l16, WS_O + (unsigned)seg * 262144 + (unsigned)((h * 4 + u) * 2 + t) * 1024,
              u32x4{pack2(o[0][t][0], o[0][t][1]), pack2(o[0][t][2], o[0][t][3]), pack2(o[1][t][0], o[1][t][1]), pack2(o[1][t][2], o[1][t][3])});
    }
    WAIT_V(0);
    if (lane == 0) stx4(rs, 0, F_ST + (unsigned)(P_S1 + grp * 32 + h) * 128, (unsigned)seg + 1u);
    WST(rs, wk, 16 + seg);
  }
}

enum { SG_WO = 0, SG_W1 = 1, SG_W2 = 2, SG_W3 = 3, SG_W4 = 4, SG_QK = 5, SG_V = 6 };
#define PRA 1
#define PRB 2
#define PRC 0
constexpr int G_XCH = 131072, G_CTL = G_XCH + 16384;
template <int SG>
__device__ __forceinline__ void stage_gemm(LAS unsigned char* lds, __amdgpu_buffer_rsrc_t rs, const u16* Wg, int wk, int kq, int cb, unsigned dst, int pid, unsigned& cap) {
  constexpr bool PROJ = (SG == SG_QK || SG == SG_V), WIDE = (SG == SG_WO || SG == SG_W4 || PROJ), ROWM = (SG == SG_W1 || PROJ);
  constexpr int KSW = (SG == SG_W4) ? 14 : 16;
  constexpr int NM = WIDE ? 2 : 1, NCOL = WIDE ? 128 : 64;
  constexpr int LDW = PROJ ? HD : (SG == SG_W3) ? FF : D_;
  constexpr int WROWS = (SG == SG_WO) ? HD : (SG == SG_W4 ? FF : D_);
  constexpr int NBLK = PROJ ? 32 : (SG == SG_WO || SG == SG_W1) ? 64 : 65;
  constexpr unsigned ABASE = PROJ ? WS_H : SG == SG_WO ? WS_O : SG == SG_W1 ? WS_G : SG == SG_W2 ? WS_H1S : SG == SG_W3 ? WS_X2 : WS_U;
  constexpr unsigned ASTR = SG == SG_WO ? 262144u : SG == SG_W4 ? 229376u : 65536u;
  constexpr int NPC = KSW / 2;
  const unsigned myword = F_ST + (unsigned)pid * 128u;
  int tid = threadIdx.x; asm volatile("" : "+v"(tid));
  const int lane = tid & 63, w = __builtin_amdgcn_readfirstlane(tid >> 6), fr = lane & 15, g = lane >> 4;
  const int kh = w >> 2, wl = w & 3, cg = WIDE ? wl : (wl & 1), m0 = WIDE ? 0 : (wl >> 1);
  const int koff = PROJ ? 0 : kq;
  const unsigned lbase = (unsigned)(size_t)lds, l16 = lane * 16;
  const int col0 = cb * NCOL + cg * 32;
  bf16x8 Wf[2][KSW];
  {
    const __amdgpu_buffer_rsrc_t rsW = mk_rs(Wg, (unsigned)WROWS * LDW * 2u);
    const int ub = lane >> 3, tu = 4 * (g & 1) + (fr >> 2);
    const unsigned vB = (unsigned)(((ub & 3) + 8 * (ub >> 2)) * LDW + 16 * (((lane >> 1) & 3) ^ (ub >> 1)) + 8 * (lane & 1)) * 2u;
    const unsigned sB = (unsigned)((koff * KSW * 64 + 32 * (w >> 2) + 16 * ((w >> 1) & 1) + 4 * (w & 1)) * LDW + cb * NCOL) * 2u;
    constexpr int TB = NCOL * 128, TPP = WIDE ? 4 : 8;
    const int boff = kh * 65536 + (cg >> 1) * 8192 + (g >> 1) * 2048 + tu * 128 + (((tu >> 1) ^ (2 * (cg & 1))) << 5) + (fr & 3) * 8;
#pragma unroll
    for (int ps = 0; ps < (WIDE ? 2 : 1); ++ps) {
      BAR();
#pragma unroll
      for (int hh = 0; hh < 2; ++hh)
#pragma unroll
        for (int i = 0; i < TPP; ++i)
          if (TPP * ps + i < KSW / 2) {
            const unsigned so = sB + (unsigned)((hh * (KSW / 2) + TPP * ps + i) * 64 * LDW) * 2u, d = lbase + hh * 65536 + i * TB + w * 1024;
            dma16(rsW, vB, so, d);
            if (WIDE) dma16(rsW, vB, so + 128, d + 8192);
          }
      WAIT_V(0); BAR();
#pragma unroll
      for (int i = 0; i < TPP; ++i)
        if (TPP * ps + i < KSW / 2) {
#pragma unroll
          for (int ks = 0; ks < 2; ++ks)
#pragma unroll
            for (int n = 0; n < 2; ++n) {
              LAS unsigned char* q = lds + ((boff ^ (n << 5)) + i * TB + ks * 4096);
              Wf[n][(TPP * ps + i) * 2 + ks] = cat8(trread(q), trread(q + 1024));
            }
        }
      WAIT_L0();
    }
    if (tid < 8) *(LAS unsigned*)(lds + G_CTL + tid * 4) = 0u;
    LBAR();
  }
  f32x4 cs[2];
  if (SG == SG_W3) {
    const u32x4 one4 = {0x3F803F80u, 0x3F803F80u, 0x3F803F80u, 0x3F803F80u};
    cs[0] = cs[1] = f32x4{0.f, 0.f, 0.f, 0.f};
#pragma unroll
    for (int k = 0; k < KSW; ++k) { cs[0] = mfma32(Wf[0][k], __builtin_bit_cast(bf16x8, one4), cs[0]); cs[1] = mfma32(Wf[1][k], __builtin_bit_cast(bf16x8, one4), cs[1]); }
    *(LAS f32x4*)(lds + w * 2048 + l16) = cs[0]; *(LAS f32x4*)(lds + w * 2048 + 1024 + l16) = cs[1];
    LBAR();
    cs[0] += *(const LAS f32x4*)(lds + (w ^ 4) * 2048 + l16); cs[1] += *(const LAS f32x4*)(lds + (w ^ 4) * 2048 + 1024 + l16);
    LBAR();
  }
  auto blk = [&](int i) -> int { return PROJ ? 2 * i + kq : i; };
  const unsigned aoff = ROWM ? (unsigned)((m0 * 16 + fr) * 128 + ((g ^ (fr & 7)) << 4)) : l16 + m0 * 1024;
  const unsigned vA = ROWM ? (unsigned)((lane >> 3) * 2048 + (((lane & 7) ^ (lane >> 3)) << 4)) : l16;
  auto piece = [&](int hf, int q) {
    const int i = hf >> 1, hh = hf & 1;
    const unsigned so = ABASE + (unsigned)blk(i) * ASTR + (unsigned)koff * (KSW * 4096) + (ROWM ? wl * 16384 + hh * 1024 : (hh * KSW * 2 + wl) * 1024);
    dma16x(rs, vA, so + q * (ROWM ? 128 : 4096), lbase + (i & 1) * 65536 + hh * 32768 + wl * 1024 + q * 4096);
  };
  auto fetch = [&](int hf) {
#pragma unroll
    for (int q = 0; q < NPC; ++q) piece(hf, q);
  };
  auto products = [&](int i, f32x4 (&acc)[NM][2], auto&& early, auto&& handover) {
    const unsigned ub = lbase + (i & 1) * 65536 + kh * 32768;
    LAS unsigned char* ab = lad(aoff, ub);
    LAS unsigned char* ab1 = ROWM ? lad<64>(aoff, ub) : ab;
    constexpr int PD = WIDE ? 6 : 10;
    bf16x8 af[PD + 1][NM];
    auto rd = [&](int k) {
#pragma unroll
      for (int m = 0; m < NM; ++m)
        af[k % (PD + 1)][m] = ROWM ? *(const LAS bf16x8*)(((k & 1) ? ab1 : ab) + (k >> 1) * 4096 + m * 2048) : *(const LAS bf16x8*)(ab + k * 2048 + m * 1024);
    };
#pragma unroll
    for (int k = 0; k < PD; ++k) rd(k);
    __builtin_amdgcn_s_setprio(PRA);
    SCHED();
#pragma unroll
    for (int k = 0; k < KSW; ++k) {
      if (k + PD < KSW) rd(k + PD);
#pragma unroll
      for (int m = 0; m < NM; ++m)
#pragma unroll
        for (int n = 0; n < 2; ++n) acc[m][n] = SG == SG_V ? mfma32(af[k % (PD + 1)][m], Wf[n][k], acc[m][n]) : mfma32(Wf[n][k], af[k % (PD + 1)][m], acc[m][n]);
      SCHED();
      if (k == KSW - (WIDE ? 6 : 9)) { early(); SCHED(); }
      if (k == KSW - (WIDE ? 3 : 5)) { __builtin_amdgcn_s_setprio(PRB); handover(); SCHED(); }
    }
#pragma unroll
    for (int m = 0; m < NM; ++m) asm volatile("" : "+v"(acc[m][0]), "+v"(acc[m][1]));
    __builtin_amdgcn_s_setprio(PRC);
  };
  WST(rs, wk, PROJ ? 2 : 10);
#define SEG(x)
  if (kh == 0) {
    auto pvo = [&](int i) -> unsigned {
      const int b = blk(i);
      if (PROJ) return F_LN + (unsigned)((b % 48) * 4 + (lane & 3)) * 128u;
      if (SG == SG_WO) return F_ST + (unsigned)(P_S1 + (b & 3) * 32 + kq * 8 + (lane & 7)) * 128u;
      if (SG == SG_W1) return F_ST + (unsigned)(P_S3 + (b & 3) * 32 + (lane & 31)) * 128u;
      if (SG == SG_W2) return F_ST + (unsigned)(P_S4 + (lane & 15)) * 128u;
      if (SG == SG_W3) return F_ST + (unsigned)(P_S5 + (lane & 15)) * 128u;
      return F_ST + (unsigned)(P_S7 + kq * 14 + ((lane & 31) % 14)) * 128u;
    };
    auto need = [&](int i) -> unsigned { return PROJ ? 1u : (SG == SG_W2 && i == 64) ? 64u : (unsigned)i + 1u; };
    int R = 0, Rp = 0;
    for (int i = 0; i < NBLK; ++i) {
      if (R <= i) { wait_ge(rs, pvo(i), 0, need(i), cap); fetch(2 * i); fetch(2 * i + 1); }
      if (R <= i + 1) wait_v<NPC>(); else wait_v<2 * NPC>();
      BAR();
      SEG(1);
      f32x4 acc[NM][2];
#pragma unroll
      for (int m = 0; m < NM; ++m) acc[m][0] = acc[m][1] = f32x4{0.f, 0.f, 0.f, 0.f};
      unsigned peek;
      asm volatile("buffer_load_dword %0, %1, %2, 0 offen sc1" : "=v"(peek) : "v"(w == 0 ? (lane < 32 ? pvo(i + 1) : pvo(i + 2)) : 0xF0000000u), "s"(rs) : "memory");
      products(i, acc, []() {}, [&]() {
        SEG(4);
        WAIT_V(0);
        SEG(0);
        asm volatile("" : "+v"(peek));
        if (w == 0) {
          const unsigned long long ok = __ballot(peek >= (lane < 32 ? need(i + 1) : need(i + 2)));
          const bool r1 = (R >= i + 2 || (unsigned)ok == 0xFFFFFFFFu) && i + 1 < NBLK, r2 = r1 && (unsigned)(ok >> 32) == 0xFFFFFFFFu && i + 2 < NBLK;
          if (lane == 0) *(volatile LAS unsigned*)(lds + G_CTL + 4) = (unsigned)(i + 1 + (r1 ? 1 : 0) + (r2 ? 1 : 0));
        }
        LBAR();
      });
      SEG(2);
      const unsigned rv = *(const volatile LAS unsigned*)(lds + G_CTL + 4);
      LAS unsigned char* xw = lad(l16, lbase + G_XCH + wl * 4096);
#pragma unroll
      for (int m = 0; m < NM; ++m) { *(LAS f32x4*)(xw + m * 2048) = acc[m][0]; *(LAS f32x4*)(xw + m * 2048 + 1024) = acc[m][1]; }
      asm volatile("" ::: "memory");
      if (lane == 0) *(volatile LAS unsigned*)(lds + G_CTL + 16 + wl * 4) = (unsigned)i + 1u;
      Rp = R; R = __builtin_amdgcn_readfirstlane(rv);
      SEG(3);
      if (R >= i + 2 && Rp < i + 2) { fetch(2 * i + 2); fetch(2 * i + 3); }
      if (R >= i + 3) fetch(2 * i + 4);
      SEG(5);
    }
    BAR();
  } else {
    const int row = 16 * m0 + fr, c8 = col0 + 16 * (g & 1) + 8 * (g >> 1);
    bool pending = false;
    BAR();
    for (int i = 0; i < NBLK; ++i) {
      const int b = blk(i);
      BAR();
      SEG(0);
      const unsigned rv = *(const volatile LAS unsigned*)(lds + G_CTL + 4);
      u32x4 res, st[4];
      if (SG == SG_W2) { const int t = 32 * b - 2 + row; res = ldx16(rs, (t >= 0 && t < S_) ? (unsigned)(t * D_ + c8) * 2u : 0xF0000000u, WS_H2); }
      if (SG == SG_W3) {
#pragma unroll
        for (int q = 0; q < 4; ++q) st[q] = ldx16(rs, (unsigned)(g * 2048 + row * 16), WS_ST5 + (unsigned)b * 8192 + q * 512);
      }
      f32x4 acc[NM][2];
#pragma unroll
      for (int m = 0; m < NM; ++m) acc[m][0] = acc[m][1] = f32x4{0.f, 0.f, 0.f, 0.f};
      int R = 0;
      LAS unsigned char* xr = lad(l16, lbase + G_XCH + wl * 4096);
      f32x4 xa[NM][2];
      unsigned xf;
      auto take = [&]() {
        asm volatile("" ::: "memory");
        xf = *(const volatile LAS unsigned*)(lds + G_CTL + 16 + wl * 4);
        asm volatile("" ::: "memory");
#pragma unroll
        for (int m = 0; m < NM; ++m) { xa[m][0] = *(const LAS f32x4*)(xr + m * 2048); xa[m][1] = *(const LAS f32x4*)(xr + m * 2048 + 1024); }
        asm volatile("" ::: "memory");
      };
      products(i, acc, take, [&]() {
        SEG(5);
        R = __builtin_amdgcn_readfirstlane(rv);
        if (R >= i + 2) {
          if (SG == SG_W2) wait_v<1>(); else if (SG == SG_W3) wait_v<4>(); else wait_v<0>();
          LBAR();
          if (pending && tid == 256) stx4(rs, 0, myword, (unsigned)blk(i - 1) + 1u);
        }
      });
      const bool more = R >= i + 2;
      SEG(1);
      while (__builtin_amdgcn_readfirstlane(xf) <= (unsigned)i) take();
#pragma unroll
      for (int m = 0; m < NM; ++m) { acc[m][0] += xa[m][0]; acc[m][1] += xa[m][1]; }
      SEG(2);
      const unsigned fo = (unsigned)((c8 >> 5) * 2048 + ((c8 >> 3) & 3) * 256);
      if (SG == SG_WO || SG == SG_W4) {
        const unsigned so = dst + (unsigned)(b * 4 + kq) * 65536u, vo = (unsigned)(fr * D_ + c8) * 2u;
        stx16(rs, vo, so, widen(acc[0][0], acc[0][1])); stx16(rs, vo, so + 32768, widen(acc[NM - 1][0], acc[NM - 1][1]));
      } else if (SG == SG_QK) {
        const unsigned so = dst + (unsigned)(b * NH + cb) * 8192 + cg * 1024, vo = (unsigned)((2 * (g & 1) + (g >> 1)) * 16 + fr) * 16;
        stx16(rs, vo, so, widen(acc[0][0], acc[0][1])); stx16(rs, vo, so + 4096, widen(acc[NM - 1][0], acc[NM - 1][1]));
      } else if (SG == SG_V) {
#pragma unroll
        for (int n = 0; n < 2; ++n) {
          const f32x4 x = acc[0][n], y = acc[NM - 1][n];
          stx16(rs, l16, dst + (unsigned)(b * NH + cb) * 8192 + (cg * 2 + n) * 1024, u32x4{pack2(x[0], x[1]), pack2(x[2], x[3]), pack2(y[0], y[1]), pack2(y[2], y[3])});
        }
      } else if (SG == SG_W1) {
        u32x4 v = widen(acc[0][0], acc[0][1]);
#pragma unroll
        for (int q = 0; q < 4; ++q) v[q] = pack2(gelu_tanh(lo2f(v[q])), gelu_tanh(hi2f(v[q])));
        const int grp = cb >> 2, sh = (grp & 1) ? -(1 + (grp >> 1)) : (1 + (grp >> 1));
        const int rho = 32 * b + row + sh + 2;
        const unsigned vo = (rho >= 2 && rho < S_ + 2) ? (unsigned)(rho >> 5) * 65536u + fo + (unsigned)((rho >> 4) & 1) * 1024u + (unsigned)(rho & 15) * 16u : 0xF0000000u;
        stx16(rs, vo, WS_H1S, v);
      } else if (SG == SG_W2) {
        u32x4 v = widen(acc[0][0], acc[0][1]);
        float z[8];
#pragma unroll
        for (int q = 0; q < 4; ++q) { v[q] = pack2(lo2f(res[q]) + lo2f(v[q]), hi2f(res[q]) + hi2f(v[q])); z[2 * q] = lo2f(v[q]); z[2 * q + 1] = hi2f(v[q]); }
        stx16(rs, fo + m0 * 1024 + fr * 16, WS_X2 + (unsigned)b * 65536u, v);
        const float mw = gsum(((z[0] + z[1]) + (z[2] + z[3])) + ((z[4] + z[5]) + (z[6] + z[7]))) * (1.0f / 32.0f);
        float q2 = 0.f;
#pragma unroll
        for (int q = 0; q < 8; ++q) { const float d = z[q] - mw; q2 += d * d; }
        q2 = gsum(q2);
        if (g == 0) stx8(rs, (unsigned)(cb * 512 + row * 16 + cg * 8), WS_ST5 + (unsigned)b * 8192, u32x2{f2u(mw), f2u(q2)});
      } else {
        float ms = 0.f;
#pragma unroll
        for (int q = 0; q < 4; ++q) ms += u2f(st[q][0]) + u2f(st[q][2]);
        const float mean = gsum(ms) * (1.0f / 32.0f);
        float q2 = 0.f;
#pragma unroll
        for (int q = 0; q < 4; ++q) { const float d0 = u2f(st[q][0]) - mean, d1 = u2f(st[q][2]) - mean; q2 += u2f(st[q][1]) + u2f(st[q][3]) + 32.0f * (d0 * d0 + d1 * d1); }
        const float var = gsum(q2) * (1.0f / 1024.0f);
        const float m = rbf(mean), isd = __builtin_amdgcn_rcpf(rbf(__builtin_amdgcn_sqrtf(rbf(rbf(var) + 1e-5f))));
        u32x4 v = widen((acc[0][0] - m * cs[0]) * isd, (acc[0][1] - m * cs[1]) * isd);
#pragma unroll
        for (int q = 0; q < 4; ++q) v[q] = pack2(gelu_tanh(lo2f(v[q])), gelu_tanh(hi2f(v[q])));
        stx16(rs, fo + m0 * 1024 + fr * 16, WS_U + (unsigned)b * 229376u, v);
      }
      SEG(3);
      pending = more && i >= 2;
      if (!pending) {
        WAIT_V(0);
        if (lane == 0) {
          const unsigned c = __hip_atomic_fetch_add((LAS unsigned*)(lds + G_CTL), 1u, __ATOMIC_RELAXED, __HIP_MEMORY_SCOPE_WORKGROUP);
          if ((c & 3u) == 3u) stx4(rs, 0, myword, (unsigned)b + 1u);
        }
      }
      if (R >= i + 3) fetch(2 * i + 5);
      if (!PROJ) WST(rs, wk, 16 + b);
      SEG(4);
      if (!more) BAR();
    }
  }
}

__device__ __forceinline__ void wait32(__amdgpu_buffer_rsrc_t rs, int first, int mine, int lane, unsigned need, unsigned& cap) {
  wait_ge(rs, 0, F_ST + (unsigned)(first + mine) * 128u, need, cap);
  wait_ge(rs, (unsigned)(lane & 31) * 128u, F_ST + (unsigned)first * 128u, need, cap);
}
__device__ __forceinline__ void sum_partials(__amdgpu_buffer_rsrc_t rs, unsigned l16, unsigned so, float (&a)[16]) {
  u32x4 pp[4][2];
#pragma unroll
  for (int k = 0; k < 4; ++k) { pp[k][0] = ldx16(rs, l16, so + k * 65536u); pp[k][1] = ldx16(rs, l16, so + k * 65536u + 1024); }
#pragma unroll
  for (int i = 0; i < 8; ++i) {
    f32x2 t = {0.f, 0.f};
#pragma unroll
    for (int k = 0; k < 4; ++k) t += f32x2{lo2f(pp[k][i >> 2][i & 3]), hi2f(pp[k][i >> 2][i & 3])};
    a[2 * i] = rbf(t[0]); a[2 * i + 1] = rbf(t[1]);
  }
}
__device__ __forceinline__ void put_row(__amdgpu_buffer_rsrc_t rs, unsigned l16, unsigned so, const float (&v)[16]) {
  stx16(rs, l16, so, u32x4{pack2(v[0], v[1]), pack2(v[2], v[3]), pack2(v[4], v[5]), pack2(v[6], v[7])});
  stx16(rs, l16, so + 1024, u32x4{pack2(v[8], v[9]), pack2(v[10], v[11]), pack2(v[12], v[13]), pack2(v[14], v[15])});
}
__device__ __forceinline__ void load_vec16(const float* p, int lane, float (&v)[16]) {
#pragma unroll
  for (int i = 0; i < 4; ++i) { const f32x4 t = *(const f32x4*)(p + (i >> 1) * 512 + lane * 8 + (i & 1) * 4); v[4 * i] = t[0]; v[4 * i + 1] = t[1]; v[4 * i + 2] = t[2]; v[4 * i + 3] = t[3]; }
}

__device__ __forceinline__ void stage_mid(__amdgpu_buffer_rsrc_t rs, const u16* x, const float* mod, int wk, int cu, unsigned& cap) {
  const int tid = threadIdx.x, lane = tid & 63, w = __builtin_amdgcn_readfirstlane(tid >> 6);
  const int grp = w >> 1, rl = cu * 2 + (w & 1);
  const unsigned l16 = lane * 16;
  float ga[16], sc[16], sh[16];
  load_vec16(mod + 2048, lane, ga); load_vec16(mod + 4096, lane, sc); load_vec16(mod + 3072, lane, sh);
#pragma unroll
  for (int i = 0; i < 16; ++i) sc[i] = rbf(1.0f + sc[i]);
  for (int seg = grp; seg < NSEG; seg += 4) {
    const int row = seg * 32 + rl;
    float v[16], a[16];
    load_row16(x + (size_t)row * D_, lane, v);
    wait32(rs, P_S2, rl, lane, (unsigned)seg + 1u, cap);
    sum_partials(rs, l16, WS_P2 + (unsigned)seg * 262144u + (unsigned)rl * 2048, a);
#pragma unroll
    for (int i = 0; i < 16; ++i) v[i] = rbf(v[i] + rbf(ga[i] * a[i]));
    put_row(rs, l16, WS_X1 + (unsigned)row * 2048, v);
    ln_row(v);
#pragma unroll
    for (int i = 0; i < 16; ++i) v[i] = rbf(rbf(v[i] * sc[i]) + sh[i]);
    put_row(rs, l16, WS_H2 + (unsigned)row * 2048, v);
    ln_row(v);
    put_row(rs, l16, WS_G + (unsigned)row * 2048, v);
    WAIT_V(0);
    if (lane == 0) stx4(rs, 0, F_ST + (unsigned)(P_S3 + grp * 32 + rl) * 128, (unsigned)seg + 1u);
    WST(rs, wk, 16 + seg);
  }
}

__device__ __forceinline__ void stage_out(__amdgpu_buffer_rsrc_t rs, u16* out, const float* mod, int wk, int cu, unsigned& cap) {
  const int tid = threadIdx.x, lane = tid & 63, w = __builtin_amdgcn_readfirstlane(tid >> 6);
  const int grp = w >> 2, rl = cu * 4 + (w & 3);
  const unsigned l16 = lane * 16;
  float gb[16];
  load_vec16(mod + 5120, lane, gb);
  for (int b = grp; b < 65; b += 2) {
    const int t = 32 * b - 2 + rl;
    if (t < 0 || t >= S_) continue;
    wait32(rs, P_S8, rl, lane, (unsigned)b + 1u, cap);
    const unsigned xo = (unsigned)((lane >> 2) * 2048 + (lane & 3) * 256), xs = WS_X2 + (unsigned)b * 65536u + (unsigned)((rl >> 4) * 1024 + (rl & 15) * 16);
    const u32x4 x2a = ldx16(rs, xo, xs), x2b = ldx16(rs, xo, xs + 32768);
    const u32x4 x1a = ldx16(rs, l16, WS_X1 + (unsigned)t * 2048), x1b = ldx16(rs, l16, WS_X1 + (unsigned)t * 2048 + 1024);
    float a[16];
    sum_partials(rs, l16, WS_P8 + (unsigned)b * 262144u + (unsigned)rl * 2048, a);
#pragma unroll
    for (int i = 0; i < 4; ++i) {
      a[2 * i] = lo2f(x1a[i]) + rbf(gb[2 * i] * rbf(lo2f(x2a[i]) + a[2 * i])); a[2 * i + 1] = hi2f(x1a[i]) + rbf(gb[2 * i + 1] * rbf(hi2f(x2a[i]) + a[2 * i + 1]));
      a[8 + 2 * i] = lo2f(x1b[i]) + rbf(gb[8 + 2 * i] * rbf(lo2f(x2b[i]) + a[8 + 2 * i])); a[9 + 2 * i] = hi2f(x1b[i]) + rbf(gb[9 + 2 * i] * rbf(hi2f(x2b[i]) + a[9 + 2 * i]));
    }
    store_row16(out + (size_t)t * D_, lane, a);
    WST(rs, wk, 16 + b);
  }
}

__device__ __forceinline__ void worker(const MainP& p, LAS unsigned char* lds, int xw, int j) {
  const int tid = threadIdx.x, lane = tid & 63, w = __builtin_amdgcn_readfirstlane(tid >> 6);
  const __amdgpu_buffer_rsrc_t rs = mk_rs(p.ws, WS_END);
  const float* mod = (const float*)(p.ws + WS_MOD);
  unsigned cap = SPIN_CAP;
  const int wk = xw * 32 + j;
  WST(rs, wk, 0);
  {
    const int c = j * 6 + xw;
    for (int row = c * 8 + w; row < S_; row += 1536) {
      float v[16];
      load_row16(p.x + (size_t)row * D_, lane, v);
      ln_row(v);
      modulate(v, mod + 1024, mod, lane);
      stx16(rs, (unsigned)lane * 16, WS_H + (unsigned)row * 2048, u32x4{pack2(v[0], v[1]), pack2(v[2], v[3]), pack2(v[4], v[5]), pack2(v[6], v[7])});
      stx16(rs, (unsigned)lane * 16, WS_H + (unsigned)row * 2048 + 1024, u32x4{pack2(v[8], v[9]), pack2(v[10], v[11]), pack2(v[12], v[13]), pack2(v[14], v[15])});
    }
    publish(rs, F_LN + c * 128, 1u);
    WST(rs, wk, 1);
  }
  {
    const int mat = xw % 3, par = xw / 3, pid = P_PJ + xw * 32 + j;
    if (mat == 1) stage_gemm<SG_V>(lds, rs, p.Wv, wk, par, j, WS_VT, pid, cap);
    else stage_gemm<SG_QK>(lds, rs, mat == 0 ? p.Wk : p.Wq, wk, par, j, mat == 0 ? WS_K : WS_Q, pid, cap);
    WST(rs, wk, 3);
  }
  if (xw == 0) stage_gemm<SG_WO>(lds, rs, p.Wo, wk, j >> 3, j & 7, WS_P2, P_S2 + j, cap);
  else if (xw == 1) stage_gemm<SG_W4>(lds, rs, p.w4, wk, j >> 3, j & 7, WS_P8, P_S8 + j, cap);
  else if (xw < 4) {
    if (j < 28) stage_gemm<SG_W3>(lds, rs, p.w3, wk, 0, (xw - 2) * 28 + j, 0, P_S7 + (xw - 2) * 28 + j, cap);
    else stage_out(rs, p.out, mod, wk, (xw - 2) * 4 + j - 28, cap);
  } else if (xw == 4) {
    if (j < 16) stage_gemm<SG_W1>(lds, rs, p.w1, wk, 0, j, 0, P_S4 + j, cap);
    else stage_gemm<SG_W2>(lds, rs, p.w2, wk, 0, j - 16, 0, P_S5 + j - 16, cap);
  } else {
    if (j < 16) stage_attn(rs, wk, j >> 2, j & 3, cap);
    else stage_mid(rs, p.x, mod, wk, j - 16, cap);
  }
  if (cap == 0u && lane == 0) stx4(rs, 0, X_CTL, 1u);
}

#define ST_DECL
#define ST0()
#define ST(i)
#define ST_FLUSH()
constexpr int R_W = 0, R_MEM = 131072, R_QK = R_MEM + 16384, R_KT = R_QK + 8192, R_RED = R_MEM + 16384, R_LDS = 163840;
__device__ __forceinline__ void bar_lo(int w) { asm volatile("s_cmp_ge_u32 %0, 4\n\ts_cbranch_scc1 1f\n\ts_barrier\n\t1:" :: "s"(w) : "scc", "memory"); }
__device__ __forceinline__ void bar_hi(int w) { asm volatile("s_cmp_lt_u32 %0, 4\n\ts_cbranch_scc1 1f\n\ts_setprio 3\n\ts_barrier\n\t1:" :: "s"(w) : "scc", "memory"); }
__device__ __forceinline__ void issue4(__amdgpu_buffer_rsrc_t rs, unsigned vo, unsigned s0, unsigned s1, unsigned s2, unsigned s3, u32x4& a0, u32x4& a1, u32x4& a2, u32x4& a3) {
  asm volatile("buffer_load_dwordx4 v[240:243], %[vo], %[rs], %[s0] offen sc1\n\tbuffer_load_dwordx4 v[244:247], %[vo], %[rs], %[s1] offen sc1\n\t"
               "buffer_load_dwordx4 v[248:251], %[vo], %[rs], %[s2] offen sc1\n\tbuffer_load_dwordx4 v[252:255], %[vo], %[rs], %[s3] offen sc1"
               : "=&{v[240:243]}"(a0), "=&{v[244:247]}"(a1), "=&{v[248:251]}"(a2), "=&{v[252:255]}"(a3)
               : [vo] "v"(vo), [rs] "s"(rs), [s0] "s"(s0), [s1] "s"(s1), [s2] "s"(s2), [s3] "s"(s3) : "memory");
}
#define ARRIVE(R0, R1, R2, R3, CNT) \
  asm volatile("s_waitcnt vmcnt(" #CNT ")\n\t" \
               "1:\n\t" \
               "v_min3_u32 %[t], v" #R0 ", v" #R1 ", v" #R2 "\n\t" \
               "v_min_u32 %[t], %[t], v" #R3 "\n\t" \
               "v_cmp_eq_u32 vcc, 0, %[t]\n\t" \
               "s_cbranch_vccz 2f\n\t" \
               "s_mov_b64 exec, vcc\n\t" \
               "buffer_load_dwordx4 v[" #R0 ":" #R3 "], %[vo], %[rs], %[so] offen sc1\n\t" \
               "s_mov_b64 exec, -1\n\t" \
               "s_waitcnt vmcnt(0)\n\t" \
               "s_sub_u32 %[cap], %[cap], 1\n\t" \
               "s_cbranch_scc0 1b\n\t" \
               "s_mov_b32 %[cap], 0\n\t" \
               "2:" \
               : "+{v[" #R0 ":" #R3 "]}"(a), [t] "=&v"(t), [cap] "+s"(cap) : [vo] "v"(vo), [rs] "s"(rs), [so] "s"(so) : "vcc", "scc", "memory")
template <int I> __device__ __forceinline__ void arrive(__amdgpu_buffer_rsrc_t rs, unsigned vo, unsigned so, u32x4& a, unsigned& cap) {
  unsigned t;
  if (I == 0) ARRIVE(240, 241, 242, 243, 3);
  else if (I == 1) ARRIVE(244, 245, 246, 247, 2);
  else if (I == 2) ARRIVE(248, 249, 250, 251, 1);
  else ARRIVE(252, 253, 254, 255, 0);
}
__device__ __forceinline__ void issue2(__amdgpu_buffer_rsrc_t rs, unsigned l8, unsigned so, int w, u32x2& a) {
  asm volatile("s_cmp_ge_u32 %[w], 4\n\ts_cbranch_scc1 1f\n\tv_lshrrev_b32 v255, 1, %[l8]\n\t"
               "buffer_load_dword v254, v255, %[rs], %[so] offen sc1\n\tbuffer_load_dword v255, v255, %[rs], %[so] offen offset:256 sc1\n\t1:"
               : "=&{v[254:255]}"(a) : [l8] "v"(l8), [rs] "s"(rs), [so] "s"(so), [w] "s"(w) : "scc", "memory");
}
__device__ __forceinline__ void arrive2(__amdgpu_buffer_rsrc_t rs, unsigned l8, unsigned so, u32x2& a, unsigned& cap) {
  unsigned t;
  asm volatile("s_waitcnt vmcnt(0)\n\t"
               "1:\n\t"
               "v_min_u32 %[t], v254, v255\n\t"
               "v_cmp_eq_u32 vcc, 0, %[t]\n\t"
               "s_cbranch_vccz 2f\n\t"
               "v_lshrrev_b32 %[t], 1, %[l8]\n\t"
               "buffer_load_dword v254, %[t], %[rs], %[so] offen sc1\n\t"
               "buffer_load_dword v255, %[t], %[rs], %[so] offen offset:256 sc1\n\t"
               "s_waitcnt vmcnt(0)\n\t"
               "s_sub_u32 %[cap], %[cap], 1\n\t"
               "s_cbranch_scc0 1b\n\t"
               "s_mov_b32 %[cap], 0\n\t"
               "2:"
               : "+{v[254:255]}"(a), [t] "=&v"(t), [cap] "+s"(cap) : [l8] "v"(l8), [rs] "s"(rs), [so] "s"(so) : "vcc", "scc", "memory");
}
__device__ __forceinline__ unsigned ror8(unsigned v) { return __builtin_amdgcn_update_dpp(0u, v, 0x128, 0xF, 0xF, true); }
__device__ __forceinline__ u32x4 add_bf16x8(u32x4 a, u32x4 b) {
  u32x4 r;
#pragma unroll
  for (int i = 0; i < 4; ++i) { r[i] = pack2(lo2f(a[i]) + lo2f(b[i]), hi2f(a[i]) + hi2f(b[i])); }
  return r;
}
__device__ __forceinline__ void gather_mem(__amdgpu_buffer_rsrc_t rs, unsigned lbase, int s, int half, int w, unsigned l16, unsigned& cap) {
  const unsigned so = X_MEMP + s * 65536 + half * 32768 + w * 2048;
  u32x4 a0, a1, b0, b1;
  issue4(rs, l16, so, so + 16384, so + 1024, so + 16384 + 1024, a0, b0, a1, b1);
  LAS unsigned char* d = lad(l16, lbase + R_MEM + w * 2048);
  arrive<0>(rs, l16, so, a0, cap); arrive<1>(rs, l16, so + 16384, b0, cap);
  *(LAS u32x4*)(d) = add_bf16x8(a0, b0);
  SCHED();
  arrive<2>(rs, l16, so + 1024, a1, cap); arrive<3>(rs, l16, so + 16384 + 1024, b1, cap);
  *(LAS u32x4*)(d + 1024) = add_bf16x8(a1, b1);
}
__device__ __forceinline__ bf16x8 mem_frag(LAS unsigned char* mp, int chunk) { return cat8(trread(mp + chunk * 1024), trread(mp + chunk * 1024 + 512)); }
__device__ __forceinline__ bf16x16 w_frag(LAS unsigned char* p) {
  return cat16(cat8(trread(p), trread(p + 2048)), cat8(trread(p + 4096), trread(p + 6144)));
}
__device__ __forceinline__ bf16x16 l_frag(LAS unsigned char* p, int i) { return cat16(*(const LAS bf16x8*)(p + i * 2048), *(const LAS bf16x8*)(p + i * 2048 + 1024)); }
__device__ __forceinline__ void make_resident(LAS unsigned char* wb, unsigned lo, unsigned l16) {
#pragma unroll
  for (int c = 0; c < 2; ++c) {
    bf16x16 f[4];
#pragma unroll
    for (int t = 0; t < 4; ++t) f[t] = w_frag(wb + c * 8192 + (lo ^ (t << 5)));
    WAIT_L0(); SCHED();
#pragma unroll
    for (int t = 0; t < 4; ++t) {
      *(LAS bf16x8*)(wb + (c * 4 + t) * 2048 + l16) = __builtin_shufflevector(f[t], f[t], 0, 1, 2, 3, 4, 5, 6, 7);
      *(LAS bf16x8*)(wb + (c * 4 + t) * 2048 + 1024 + l16) = __builtin_shufflevector(f[t], f[t], 8, 9, 10, 11, 12, 13, 14, 15);
    }
    WAIT_L0(); SCHED();
  }
}
__device__ __forceinline__ f32x4 fold(f32x4 a, f32x4 b) {
  f32x4 r;
#pragma unroll
  for (int i = 0; i < 4; ++i) {
    const auto t = __builtin_amdgcn_permlane32_swap(f2u(a[i]), f2u(b[i]), false, false);
    r[i] = u2f(t[0]) + u2f(t[1]);
  }
  return r;
}

struct Sched { unsigned tau, prev; int ea, eb, d1; bool moved; };
__device__ __forceinline__ void retime(__amdgpu_buffer_rsrc_t rs, unsigned l16, int s, Sched& t, const MainP& p, unsigned& cap) {
  if (s > 0) {
    unsigned vo;
    asm volatile("v_lshlrev_b32 %0, 3, %1" : "=v"(vo) : "v"(l16));
    wait_ge(rs, vo, X_TS + (unsigned)s * 8192, 1u, cap);
    int v = (int)(ldx4(rs, vo, X_TS + (unsigned)s * 8192) - t.prev);
#pragma unroll
    for (int o = 16; o >= 1; o >>= 1) v = max(v, __shfl_xor(v, o));
    const int tb = __builtin_amdgcn_readlane(v, 0), ta = __builtin_amdgcn_readlane(v, 32);
    const int late = tb + p.d3 - (int)(t.tau - t.prev);
    if (s > 2) {
      const int up = s < 8 ? 160 : 16, dn = s < 8 ? 64 : 2;
      if (!t.moved) t.ea = max(t.ea - dn, min(ta * 8, t.ea + up));
      if (ta + p.d3 <= t.d1) t.eb = max(t.eb - dn, min((tb - t.d1) * 8, t.eb + up));
    }
    t.moved = late > 0;
    if (late > 0) t.tau += late + p.d4;
  }
  t.d1 = (t.ea >> 3) + p.d1;
}
__device__ __forceinline__ void advance(Sched& t, const MainP& p) { t.prev = t.tau; t.tau += t.d1 + (t.eb >> 3) + p.d2; }

__device__ __forceinline__ void role_a(const MainP& p, LAS unsigned char* lds, int half, int hl) {
  const int tid = threadIdx.x, lane = tid & 63, w = __builtin_amdgcn_readfirstlane(tid >> 6);
  const int h = half * 16 + hl, cg = w & 3, kh = w >> 2;
  const __amdgpu_buffer_rsrc_t rs = mk_rs(p.ws, WS_END);
  const unsigned lbase = (unsigned)(size_t)lds;
  unsigned l16 = lane * 16, l8 = lane * 8;
  asm volatile("" : "+v"(l16), "+v"(l8));
  const int sidx = (lane & 8) ? 0xEEEE : 0x4444;
  bf16x16 W[24];
  unsigned qoff;
  {
    const int fr = lane & 15, g = lane >> 4, tu = 4 * (g & 1) + (fr >> 2), ub = lane >> 3;
    const unsigned lo = (g >> 1) * 1024 + tu * 128 + ((tu >> 1) << 5) + (fr & 3) * 8;
    qoff = R_QK + ((fr >> 1) & 1) * 512 + (g >> 1) * 256 + (fr & 1) * 128 + (8 * (g & 1) + (fr >> 2)) * 8;
    const __amdgpu_buffer_rsrc_t rw = mk_rs(cg < 2 ? p.Wq : p.Wk, D_ * HD * 2);
    const unsigned vo = (unsigned)(ub * HD + 16 * (((lane >> 1) & 3) ^ (ub >> 1)) + 8 * (lane & 1)) * 2u;
    const unsigned so = (unsigned)(kh * 512 * HD + h * DH + (cg & 1) * 64) * 2u;
    auto stage = [&](int c) {
#pragma unroll
      for (int rho = 0; rho < 8; ++rho) dma16(rw, vo, so + (unsigned)((c * 64 + rho * 8) * HD) * 2u, lbase + R_W + w * 16384 + (c & 1) * 8192 + rho * 1024);
    };
    stage(0); stage(1);
#pragma unroll
    for (int c = 0; c < 6; ++c) {
      WAIT_V(8);
#pragma unroll
      for (int t = 0; t < 4; ++t) W[4 * c + t] = w_frag(lds + R_W + w * 16384 + (c & 1) * 8192 + (lo ^ (t << 5)));
      WAIT_L0(); SCHED();
      stage(c + 2);
    }
    WAIT_V(0);
    make_resident(lds + R_W + w * 16384, lo, l16);
  }
  unsigned cap = SPIN_CAP;
  wait_ge(rs, (l16 & 16) * 256, F_ST + (unsigned)(P_PJ + h) * 128, 1u, cap);
  dma16x(rs, l16, WS_K + h * 8192 + w * 1024, lbase + R_KT + w * 1024);
  Sched tt; tt.tau = rt32() - 100000; tt.prev = tt.tau; tt.ea = 240 * 8; tt.eb = 125 * 8; tt.d1 = 0; tt.moved = true;
  const int cu_ = half * 32 + hl; (void)cu_;
  ST_DECL
  for (int s = 0; s < NSEG; ++s) {
    LAS unsigned char* lp = lad(l16, lbase + R_W + w * 16384);
    bf16x16 L[2];
    L[0] = l_frag(lp, 0); L[1] = l_frag(lp, 1);
    SCHED();
    if (w == 7) wait_until(tt.tau);
    BAR();
    ST0();
    gather_mem(rs, lbase, s, half, (w + hl) & 7, l16, cap);
    ST(1);
    LBAR();
    ST(2);
    f32x4 c[4];
#pragma unroll
    for (int t = 0; t < 4; ++t) c[t] = f32x4{0.f, 0.f, 0.f, 0.f};
    LAS unsigned char* mp = lad(l8, lbase + R_MEM + kh * 8192);
    bf16x8 al, af[2];
    u32x4 vt0, vt1;
    u32x2 vmv;
    const unsigned vso = X_VM + (unsigned)(s * NH + h) * 2048 + cg * 512;
    al = mem_frag(mp, 6); af[0] = mem_frag(mp, 0); af[1] = mem_frag(mp, 1);
#pragma unroll
    for (int i = 0; i < 8; ++i) {
      c[i & 3] = smf(al, L[i & 1], c[i & 3], sidx);
      SCHED();
      if (i == 3) al = mem_frag(mp, 7);
      if (i < 6) L[i & 1] = l_frag(lp, i + 2);
      if (i == 6) {
        const unsigned vo = WS_VT + (unsigned)(s * NH + h) * 8192 + cg * 2048;
        asm volatile("s_cmp_ge_u32 %5, 4\n\ts_cbranch_scc1 1f\n\t"
                     "buffer_load_dwordx4 %0, %2, %3, %4 offen sc1\n\tbuffer_load_dwordx4 %1, %2, %3, %4 offen offset:1024 sc1\n\t1:"
                     : "=&v"(vt0), "=&v"(vt1) : "v"(l16), "s"(rs), "s"(vo), "s"(w) : "scc", "memory");
      }
      SCHED();
#pragma unroll
      for (int r = 3 * i; r < 3 * i + 3; ++r) {
        c[(r + 1) & 3] = smf(af[(r >> 2) & 1], W[(r & ~3) + ((r + 1) & 3)], c[(r + 1) & 3], sidx);
        SCHED();
        if ((r & 3) == 3 && (r >> 2) + 2 < 6) af[(r >> 2) & 1] = mem_frag(mp, (r >> 2) + 2);
        SCHED();
      }
    }
    {
      LAS unsigned char* qw = lad(l8, lbase + R_QK + cg * 2048 + kh * 512);
      const f32x4 f0 = fold(c[0], c[1]);
      *(LAS u32x2*)(qw) = u32x2{pack2(f0[0], f0[1]), pack2(f0[2], f0[3])};
      const f32x4 f1 = fold(c[2], c[3]);
      *(LAS u32x2*)(qw + 1024) = u32x2{pack2(f1[0], f1[1]), pack2(f1[2], f1[3])};

    }
    ST(3);
    if (w < 4) {
      bf16x8 k1[4], k2[4];
      LAS unsigned char* kb = lad(l16, lbase + R_KT);
#pragma unroll
      for (int ks = 0; ks < 2; ++ks) { k1[ks] = *(const LAS bf16x8*)(kb + ks * 1024); k2[ks] = *(const LAS bf16x8*)(kb + 4096 + ks * 1024); }
      LBAR();
      ST(4);
      issue2(rs, l8, vso, w, vmv);
      f32x4 t0 = {0.f, 0.f, 0.f, 0.f}, t1 = t0, t2 = t0;
      LAS unsigned char* qb = lad(qoff, lbase);
      bf16x8 qf[4], kf[4];
#pragma unroll
      for (int ks = 0; ks < 2; ++ks) {
        qf[ks] = cat8(trread(qb + ks * 1024), trread(qb + ks * 1024 + 32));
        kf[ks] = cat8(trread(qb + 4096 + ks * 1024), trread(qb + 4096 + ks * 1024 + 32));
      }
      SCHED();
#pragma unroll
      for (int ks = 0; ks < 4; ++ks) {
        t1 = mfma32(k1[ks], qf[ks], t1); t2 = mfma32(k2[ks], qf[ks], t2); t0 = mfma32(kf[ks], qf[ks], t0);
        SCHED();
        if (ks < 2) {
          qf[ks + 2] = cat8(trread(qb + (ks + 2) * 1024), trread(qb + (ks + 2) * 1024 + 32));
          k1[ks + 2] = *(const LAS bf16x8*)(kb + (ks + 2) * 1024); k2[ks + 2] = *(const LAS bf16x8*)(kb + 4096 + (ks + 2) * 1024);
          kf[ks + 2] = cat8(trread(qb + 4096 + (ks + 2) * 1024), trread(qb + 4096 + (ks + 2) * 1024 + 32));
        }
        SCHED();
      }
      ST(5);
      {
        const float sl = 0.08838834764831845f * 1.4426950408889634f;
        float z[4] = {t1[0], t1[1], t1[2], t1[3]}, y = t0[0], mx;
        {
          float u1 = t0[1], u2 = t0[2], u3 = t0[3];
#define M8(Z, B) "v_add_f32_dpp " Z ", " Z ", " Z " row_ror:8 row_mask:0xf bank_mask:0x3\n\tv_add_f32_dpp " Z ", " B ", " B " row_ror:8 row_mask:0xf bank_mask:0xc\n\t"
          asm("s_nop 7\n\t"
              "v_permlane32_swap_b32 %4, %6\n\tv_permlane32_swap_b32 %5, %7\n\t"
              M8("%0", "%9") "v_add_f32 %4, %4, %6\n\tv_add_f32 %5, %5, %7\n\t" M8("%1", "%10") M8("%2", "%11") M8("%4", "%5") M8("%3", "%12")
              "v_max3_f32 %8, %0, %1, %2\n\tv_max3_f32 %8, %8, %4, %3\n\ts_nop 1\n\t"
              "v_max_f32_dpp %8, %8, %8 row_ror:8 row_mask:0xf bank_mask:0xf\n\tv_mov_b32 %5, %8\n\ts_nop 1\n\t"
              "v_permlane16_swap_b32 %8, %5\n\tv_max_f32 %8, %8, %5\n\tv_mov_b32 %5, %8\n\ts_nop 1\n\t"
              "v_permlane32_swap_b32 %8, %5\n\tv_max_f32 %8, %8, %5"
              : "+v"(z[0]), "+v"(z[1]), "+v"(z[2]), "+v"(z[3]), "+v"(y), "+v"(u1), "+v"(u2), "+v"(u3), "=&v"(mx) : "v"(t2[0]), "v"(t2[1]), "v"(t2[2]), "v"(t2[3]));
#undef M8
        }
        const float nm = -mx * sl;
#pragma unroll
        for (int r = 0; r < 4; ++r) z[r] = __builtin_amdgcn_exp2f(fmaf(z[r], sl, nm));
        const unsigned p0 = pack2(z[0], z[1]), p1 = pack2(z[2], z[3]);
        const u32x4 pt = {p0, p1, ror8(p0), ror8(p1)};
        SCHED();
        asm volatile("s_waitcnt vmcnt(2)" : "+v"(vt0), "+v"(vt1) :: "memory");
        f32x4 o0 = {0.f, 0.f, 0.f, 0.f}, o1 = o0;
        o0 = mfma32(__builtin_bit_cast(bf16x8, vt0), __builtin_bit_cast(bf16x8, pt), o0);
        o1 = mfma32(__builtin_bit_cast(bf16x8, vt1), __builtin_bit_cast(bf16x8, pt), o1);
        y = __builtin_amdgcn_exp2f(fmaf(y, sl, nm));
        const unsigned pm = pack2(y, u2f(ror8(f2u(y))));
        float inv = __builtin_amdgcn_rcpf(gsum(dpp_add<0x128>(((z[0] + z[1]) + (z[2] + z[3])) + y)));
        o0 *= inv; o1 *= inv;
        unsigned ov;
        asm volatile("v_and_b32 %0, 0x80, %4\n\tv_lshl_or_b32 %0, %0, 24, %4" : "=&v"(ov), "+v"(o0), "+v"(o1), "+v"(inv) : "v"(l16));
        ST(6);
        arrive2(rs, l8, vso, vmv, cap);
        ST(7);
        const f32x4 n0 = mfma16n(vmv, u32x2{pm, 0u}, f32x4{0.f, 0.f, 0.f, 0.f}), n1 = mfma16n(vmv, u32x2{0u, pm}, f32x4{0.f, 0.f, 0.f, 0.f});
        {
          const u32x4 v = {nz(pack2(fmaf(n0[0], inv, o0[0]), fmaf(n0[1], inv, o0[1]))), nz(pack2(fmaf(n0[2], inv, o0[2]), fmaf(n0[3], inv, o0[3]))),
                           nz(pack2(fmaf(n1[0], inv, o1[0]), fmaf(n1[1], inv, o1[1]))), nz(pack2(fmaf(n1[2], inv, o1[2]), fmaf(n1[3], inv, o1[3])))};
          stx16(rs, ov, X_OX + s * 65536 + half * 32768 + (hl * 2 + (w >> 1)) * 1024 + (w & 1) * 128, v);
        }
        if (w == 3) { const unsigned t = max(rt32(), 1u); if (l16 == 0) stx4(rs, 0, X_TS + (unsigned)(s + 1) * 8192 + (unsigned)(32 + h) * 128, t); }
        ST(8);
      }
    } else {
      LBAR();
      LAS unsigned char* qb = lad(qoff, lbase + 4096 + (w - 4) * 1024);
      u32x4 kf = __builtin_bit_cast(u32x4, cat8(trread(qb), trread(qb + 32)));
#pragma unroll
      for (int i = 0; i < 4; ++i) kf[i] = pack2(dpp_add<0x128>(lo2f(kf[i])), dpp_add<0x128>(hi2f(kf[i])));
      stx16(rs, l16, WS_KM + (unsigned)(s * NH + h) * 4096 + (w - 4) * 1024, kf);
      WAIT_V(0);
    }
    LBAR();
    if (w == 4 && l16 == 0) stx4(rs, 0, F_CH + h * 128, (unsigned)s + 1u);
    ST(9);
    if (w == 7) { retime(rs, l16, s, tt, p, cap); advance(tt, p); }
    if (s + 1 < NSEG) {
      unsigned fo;
      asm volatile("v_and_b32 %0, 16, %1\n\tv_lshlrev_b32 %0, 8, %0" : "=v"(fo) : "v"(l16));
      wait_ge(rs, fo, F_ST + (unsigned)(P_PJ + ((s + 1) & 1) * 96 + h) * 128, (unsigned)s + 2u, cap);
      dma16x(rs, l16, WS_K + (unsigned)((s + 1) * NH + h) * 8192 + w * 1024, lbase + R_KT + w * 1024);
    }
  }
  ST_FLUSH();
  if (cap == 0u && l16 == 0) stx4(rs, 0, X_CTL, 1u);
}

__device__ __forceinline__ void role_vb(const MainP& p, LAS unsigned char* lds, int half, int jb) {
  const int tid = threadIdx.x, lane = tid & 63, w = __builtin_amdgcn_readfirstlane(tid >> 6);
  const int h = half * 16 + jb, wr = (w + jb) & 7;
  const __amdgpu_buffer_rsrc_t rs = mk_rs(p.ws, WS_END);
  const unsigned lbase = (unsigned)(size_t)lds;
  unsigned l16 = lane * 16, l8 = lane * 8;
  asm volatile("" : "+v"(l16), "+v"(l8));
  const int sidx = (lane & 8) ? 0xEEEE : 0x4444;
  bf16x16 WV[16], WB[8];
  unsigned roff;
  {
    const int fr = lane & 15, g = lane >> 4, tu = 4 * (g & 1) + (fr >> 2), ub = lane >> 3;
    const unsigned lo = (g >> 1) * 1024 + tu * 128 + ((tu >> 1) << 5) + (fr & 3) * 8;
    roff = R_RED + w * 2048 + mem_slot(16 * (g >> 1) + fr) * 32 + (g & 1) * 16;
    const unsigned vcol = (unsigned)(16 * (((lane >> 1) & 3) ^ (ub >> 1)) + 8 * (lane & 1)) * 2u;
    const __amdgpu_buffer_rsrc_t rv = mk_rs(p.Wv, D_ * HD * 2), ro = mk_rs(p.Wo, D_ * HD * 2);
    const unsigned vv = (unsigned)(ub * HD) * 2u + vcol, sv = (unsigned)(8 * w * HD + h * DH) * 2u;
    auto stage = [&](int c) {
      const unsigned d = lbase + R_W + (c & 7) * 16384 + w * 1024, sc = sv + (unsigned)c * 64 * HD * 2;
      dma16(rv, vv, sc, d); dma16(rv, vv, sc + 128, d + 8192);
    };
#pragma unroll
    for (int c = 0; c < 8; ++c) stage(c);
    const int vo = (w >> 2) * 8192 + (lo ^ ((w & 3) << 5));
#pragma unroll
    for (int c = 0; c < 16; ++c) {
      if (c < 8) WAIT_V(14); else wait_v<0>();
      BAR();
      WV[c] = w_frag(lds + R_W + (c & 7) * 16384 + vo);
      LBAR();
      if (c < 8) stage(c + 8);
    }
    const unsigned vw = (unsigned)((32 * ((ub >> 1) & 1) + 16 * (ub >> 2) + (ub & 1)) * D_) * 2u + vcol;
    const unsigned sw = (unsigned)((half * 2048 + wr * 256) * D_ + jb * 64) * 2u;
    auto stageo = [&](int c) {
      const unsigned d = lbase + R_W + w * 16384 + (c & 1) * 8192;
#pragma unroll
      for (int rho = 0; rho < 8; ++rho) {
        const int r = rho >> 1, hb = rho & 1;
        dma16(ro, vw, sw + (unsigned)((c * 64 + 8 * (r >> 1) + 4 * hb + 2 * (r & 1)) * D_) * 2u, d + rho * 1024);
      }
    };
    stageo(2); stageo(3);
    WAIT_V(0);
#pragma unroll
    for (int c = 0; c < 2; ++c)
#pragma unroll
      for (int t = 0; t < 4; ++t) WB[c * 4 + t] = w_frag(lds + R_W + w * 16384 + c * 8192 + (lo ^ (t << 5)));
    WAIT_L0(); SCHED();
    stageo(0); stageo(1);
    WAIT_V(0);
    make_resident(lds + R_W + w * 16384, lo, l16);
  }
  unsigned cap = SPIN_CAP;
  Sched tt; tt.tau = rt32() - 100000; tt.prev = tt.tau; tt.ea = 240 * 8; tt.eb = 125 * 8; tt.d1 = 0; tt.moved = true;
  const int cu_ = half * 32 + 16 + jb; (void)cu_;
  ST_DECL
  for (int s = 0; s < NSEG; ++s) {
    if (w == 7) wait_until(tt.tau);
    BAR();
    ST0();
    gather_mem(rs, lbase, s, half, wr, l16, cap);
    ST(1);
    LBAR();
    ST(2);
    {
      f32x4 c0 = {0.f, 0.f, 0.f, 0.f}, c1 = c0;
      LAS unsigned char* mp = lad(l8, lbase + R_MEM);
      bf16x8 af[8];
#pragma unroll
      for (int c = 0; c < 8; ++c) af[c] = mem_frag(mp, c);
      SCHED();
#pragma unroll
      for (int c = 0; c < 8; ++c) {
        if (c & 1) c1 = smf(af[c], WV[c], c1, sidx); else c0 = smf(af[c], WV[c], c0, sidx);
        SCHED();
        if (c >= 3) { af[c] = mem_frag(mp, 8 + c); if (c < 6) af[c - 3] = mem_frag(mp, 5 + c); }
        SCHED();
      }
#pragma unroll
      for (int c = 8; c < 16; ++c) { if (c & 1) c1 = smf(af[c - 8], WV[c], c1, sidx); else c0 = smf(af[c - 8], WV[c], c0, sidx); }
      SCHED();
      c0 += c1;
      const f32x4 f = fold(c0, c0);
      unsigned l4; asm volatile("v_lshrrev_b32 %0, 1, %1" : "=v"(l4) : "v"(l8));
      stx4(rs, l4, X_VM + (unsigned)(s * NH + h) * 2048 + w * 256, nz(l16 < 512 ? pack2(f[0], f[1]) : pack2(f[2], f[3])));
    }
    ST(3);
    if (w == 7) retime(rs, l16, s, tt, p, cap);
    if (s + 1 < NSEG) {
      LAS unsigned char* lp = lad(l16, lbase + R_W + w * 16384);
      bf16x16 L[2];
      L[0] = l_frag(lp, 0); L[1] = l_frag(lp, 1);
      SCHED();
      u32x4 a[4];
      const unsigned so = X_OX + s * 65536 + half * 32768 + wr * 4096;
      if (w == 7) wait_until(tt.tau + tt.d1);
      BAR();
      ST(4);
      issue4(rs, l16, so, so + 1024, so + 2048, so + 3072, a[0], a[1], a[2], a[3]);
      f32x4 c[4];
#pragma unroll
      for (int t = 0; t < 4; ++t) c[t] = f32x4{0.f, 0.f, 0.f, 0.f};
      arrive<0>(rs, l16, so, a[0], cap);
#pragma unroll
      for (int i = 0; i < 8; ++i) {
        if (i == 4) arrive<1>(rs, l16, so + 1024, a[1], cap);
        c[i & 3] = smf(__builtin_bit_cast(bf16x8, a[i >> 2]), L[i & 1], c[i & 3], sidx);
        SCHED();
        if (i + 2 < 8) L[i & 1] = l_frag(lp, i + 2);
        SCHED();
      }
      arrive<2>(rs, l16, so + 2048, a[2], cap);
#pragma unroll
      for (int t = 0; t < 4; ++t) c[t] = smf(__builtin_bit_cast(bf16x8, a[2]), WB[t], c[t], sidx);
      SCHED();
      arrive<3>(rs, l16, so + 3072, a[3], cap);
#pragma unroll
      for (int t = 0; t < 4; ++t) c[t] = smf(__builtin_bit_cast(bf16x8, a[3]), WB[4 + t], c[t], sidx);
      ST(5);
      LAS unsigned char* rw = lad(roff, lbase);
      *(LAS f32x4*)(rw) = fold(c[0], c[1]);
      *(LAS f32x4*)(rw + 512) = fold(c[2], c[3]);
      ST(6);
      LBAR();
      ST(7);
      {
        LAS unsigned char* rp = lad(l8, lbase + R_RED + (w & 3) * 512);
        f32x2 v[8];
#pragma unroll
        for (int i = 0; i < 8; ++i) v[i] = *(const LAS f32x2*)(rp + i * 2048);
        unsigned l4; asm volatile("v_lshrrev_b32 %0, 1, %1" : "=v"(l4) : "v"(l8));
        const f32x2 sum = ((v[0] + v[1]) + (v[2] + v[3])) + ((v[4] + v[5]) + (v[6] + v[7]));
        stx4(rs, l4, X_MEMP + (s + 1) * 65536 + (half ^ 1 ^ (w >> 2)) * 32768 + half * 16384 + jb * 1024 + (w & 3) * 256, nz(pack2(sum[0], sum[1])));
      }
      if (w == 6) { const unsigned t = max(rt32(), 1u); if (l16 == 0) stx4(rs, 0, X_TS + (unsigned)(s + 1) * 8192 + (unsigned)h * 128, t); }
      ST(8);
    }
    advance(tt, p);
  }
  ST_FLUSH();
  if (cap == 0u && l16 == 0) stx4(rs, 0, X_CTL, 1u);
}

#if defined(__HIP_DEVICE_COMPILE__)
__attribute__((target("no-packed-fp32-ops")))
#endif
__global__ void __launch_bounds__(512, 2) k_main(MainP p) {
  extern __shared__ __attribute__((aligned(16))) unsigned char smem[];
  const int b = blockIdx.x, x = b & 7, j = b >> 3;
  if (x >= 2) worker(p, (LAS unsigned char*)smem, x - 2, j);
  else if (j < 16) role_a(p, (LAS unsigned char*)smem, x, j);
  else role_vb(p, (LAS unsigned char*)smem, x, j - 16);
}

extern "C" void kernel_launch(void* const* d_in, const int* in_sizes, int n_in, void* d_out, int out_size, void* d_ws, size_t ws_size,
                              hipStream_t stream) {
  unsigned char* ws = (unsigned char*)d_ws;
  k_prep<<<256, 512, 0, stream>>>((const u16*)d_in[12], (const u16*)d_in[13], (const u16*)d_in[1], ws);
  MainP p;
  p.x = (const u16*)d_in[0]; p.Wq = (const u16*)d_in[2]; p.Wk = (const u16*)d_in[3]; p.Wv = (const u16*)d_in[4]; p.Wo = (const u16*)d_in[5];
  p.w1 = (const u16*)d_in[8]; p.w2 = (const u16*)d_in[9]; p.w3 = (const u16*)d_in[10]; p.w4 = (const u16*)d_in[11];
  p.out = (u16*)d_out; p.ws = ws; p.d1 = 10; p.d2 = 10; p.d3 = 4; p.d4 = 200;
  (void)hipFuncSetAttribute((const void*)k_main, hipFuncAttributeMaxDynamicSharedMemorySize, R_LDS);
  k_main<<<256, 512, R_LDS, stream>>>(p);
}
```

```cpp
#include <hip/hip_runtime.h>
#include <hip/hip_bf16.h>
#include <cstdlib>
#pragma clang diagnostic ignored "-Winline-asm"

#define LAS __attribute__((address_space(3)))
#define GAS __attribute__((address_space(1)))
typedef unsigned short u16;
typedef __bf16 bf16x2 __attribute__((ext_vector_type(2)));
typedef __bf16 bf16x4 __attribute__((ext_vector_type(4)));
typedef __bf16 bf16x8 __attribute__((ext_vector_type(8)));
typedef __bf16 bf16x16 __attribute__((ext_vector_type(16)));
typedef short s16x4 __attribute__((ext_vector_type(4)));
typedef float f32x2 __attribute__((ext_vector_type(2)));
typedef float f32x4 __attribute__((ext_vector_type(4)));
typedef float f32x16 __attribute__((ext_vector_type(16)));
typedef unsigned u32x2 __attribute__((ext_vector_type(2)));
typedef unsigned u32x4 __attribute__((ext_vector_type(4)));

constexpr int S_ = 2048, D_ = 1024, NH = 32, DH = 128, HD = 4096, NSEG = 64, FF = 3584;

constexpr unsigned MB = 1u << 20;
constexpr unsigned X_MEMP = 0;
constexpr unsigned X_OX = 4 * MB;
constexpr unsigned X_VM = 8 * MB;
constexpr unsigned F_LN = 12 * MB;
constexpr unsigned F_ST = F_LN + 32768;
constexpr unsigned F_CH = F_ST + 98304;
constexpr unsigned X_CTL = F_CH + 4096;
constexpr unsigned X_TS = X_CTL + 4096;
constexpr unsigned X_BYTES = X_TS + 65 * 8192;
constexpr unsigned WS_MOD = 15 * MB;
constexpr unsigned WS_H = 16 * MB;
constexpr unsigned WS_Q = 20 * MB;
constexpr unsigned WS_K = 36 * MB;
constexpr unsigned WS_VT = 52 * MB;
constexpr unsigned WS_O = 68 * MB;
constexpr unsigned WS_X1 = 84 * MB;
constexpr unsigned WS_H2 = 88 * MB;
constexpr unsigned WS_G = 92 * MB;
constexpr unsigned WS_H1S = 96 * MB;
constexpr unsigned WS_X2 = 101 * MB;
constexpr unsigned WS_ST5 = 106 * MB;
constexpr unsigned WS_U = 108 * MB;
constexpr unsigned WS_KM = 124 * MB;
constexpr unsigned WS_P2 = 132 * MB;
constexpr unsigned WS_P8 = 164 * MB;
constexpr unsigned WS_END = 198 * MB;

__device__ __forceinline__ float bf2f(unsigned short b) { return __builtin_bit_cast(float, (unsigned)b << 16); }
__device__ __forceinline__ unsigned f2u(float f) { return __builtin_bit_cast(unsigned, f); }
__device__ __forceinline__ float u2f(unsigned u) { return __builtin_bit_cast(float, u); }
__device__ __forceinline__ float lo2f(unsigned w) { return __builtin_bit_cast(float, w << 16); }
__device__ __forceinline__ float hi2f(unsigned w) { return __builtin_bit_cast(float, w & 0xFFFF0000u); }
__device__ __forceinline__ unsigned pack2(float a, float b) { bf16x2 v; v[0] = (__bf16)a; v[1] = (__bf16)b; return __builtin_bit_cast(unsigned, v); }
__device__ __forceinline__ float rbf(float a) { return (float)(__bf16)a; }
__device__ __forceinline__ unsigned nz(unsigned w) { return max(w, 1u); }
__device__ __forceinline__ float gelu_tanh(float x) {
  const float z = 0.7978845608028654f * (x + 0.044715f * x * x * x);
  const float e = __builtin_amdgcn_exp2f(-2.885390081777927f * z);
  return x * __builtin_amdgcn_rcpf(1.0f + e);
}
template <int CTRL> __device__ __forceinline__ float dpp_add(float v) { return v + u2f(__builtin_amdgcn_update_dpp(0u, f2u(v), CTRL, 0xF, 0xF, true)); }
__device__ __forceinline__ float wave_sum(float v) {
  v = dpp_add<0xB1>(v); v = dpp_add<0x4E>(v); v = dpp_add<0x141>(v); v = dpp_add<0x140>(v);
  auto t = __builtin_amdgcn_permlane16_swap(f2u(v), f2u(v), false, false);
  v = u2f(t[0]) + u2f(t[1]);
  t = __builtin_amdgcn_permlane32_swap(f2u(v), f2u(v), false, false);
  return u2f(t[0]) + u2f(t[1]);
}
#define WAIT_V(n) asm volatile("s_waitcnt vmcnt(" #n ")" ::: "memory")
#define WAIT_L0() asm volatile("s_waitcnt lgkmcnt(0)" ::: "memory")
#define BAR() do { asm volatile("" ::: "memory"); __builtin_amdgcn_s_barrier(); asm volatile("" ::: "memory"); } while (0)
#define LBAR() do { WAIT_L0(); BAR(); } while (0)
template <int N> __device__ __forceinline__ void wait_v() { asm volatile("s_waitcnt vmcnt(%0)" :: "n"(N) : "memory"); }
__device__ __forceinline__ void dma16(__amdgpu_buffer_rsrc_t rs, unsigned voff, unsigned soff, unsigned ldsa) {
  asm volatile("s_mov_b32 m0, %0\n\ts_nop 3\n\tbuffer_load_dwordx4 %1, %2, %3 offen lds" :: "s"(ldsa), "v"(voff), "s"(rs), "s"(soff) : "m0", "memory");
}
__device__ __forceinline__ void dma16x(__amdgpu_buffer_rsrc_t rs, unsigned voff, unsigned soff, unsigned ldsa) {
  asm volatile("s_mov_b32 m0, %0\n\ts_nop 3\n\tbuffer_load_dwordx4 %1, %2, %3 offen sc1 lds" :: "s"(ldsa), "v"(voff), "s"(rs), "s"(soff) : "m0", "memory");
}
__device__ __forceinline__ u32x4 ldx16(__amdgpu_buffer_rsrc_t rs, unsigned voff, unsigned soff) { return __builtin_amdgcn_raw_buffer_load_b128(rs, voff, soff, 16); }
__device__ __forceinline__ u32x2 ldx8(__amdgpu_buffer_rsrc_t rs, unsigned voff, unsigned soff) { return __builtin_amdgcn_raw_buffer_load_b64(rs, voff, soff, 16); }
__device__ __forceinline__ unsigned ldx4(__amdgpu_buffer_rsrc_t rs, unsigned voff, unsigned soff) { return __builtin_amdgcn_raw_buffer_load_b32(rs, voff, soff, 16); }
__device__ __forceinline__ void stx16(__amdgpu_buffer_rsrc_t rs, unsigned voff, unsigned soff, u32x4 v) {
  __builtin_amdgcn_raw_buffer_store_b128(v, rs, voff, soff, 16);
  asm volatile("s_nop 1" :: "v"(v) : "memory");
}
__device__ __forceinline__ void stx8(__amdgpu_buffer_rsrc_t rs, unsigned voff, unsigned soff, u32x2 v) { __builtin_amdgcn_raw_buffer_store_b64(v, rs, voff, soff, 16); }
__device__ __forceinline__ void stx4(__amdgpu_buffer_rsrc_t rs, unsigned voff, unsigned soff, unsigned v) { __builtin_amdgcn_raw_buffer_store_b32(v, rs, voff, soff, 16); }
__device__ __forceinline__ void wait_ge(__amdgpu_buffer_rsrc_t rs, unsigned voff, unsigned soff, unsigned need, unsigned& cap) {
  unsigned t;
  asm volatile("s_nop 4\n\t"
               "1:\n\t"
               "buffer_load_dword %[t], %[vo], %[rs], %[so] offen sc1\n\t"
               "s_waitcnt vmcnt(0)\n\t"
               "v_cmp_gt_u32 vcc, %[need], %[t]\n\t"
               "s_cmp_eq_u64 vcc, 0\n\t"
               "s_cbranch_scc1 2f\n\t"
               "s_sleep 2\n\t"
               "s_sub_u32 %[cap], %[cap], 1\n\t"
               "s_cbranch_scc0 1b\n\t"
               "s_mov_b32 %[cap], 0\n\t"
               "2:"
               : [t] "=&v"(t), [cap] "+s"(cap) : [vo] "v"(voff), [rs] "s"(rs), [so] "s"(soff), [need] "s"(need) : "vcc", "scc", "memory");
}
__device__ __forceinline__ __amdgpu_buffer_rsrc_t mk_rs(const void* p, unsigned bytes) { return __builtin_amdgcn_make_buffer_rsrc((void*)p, 0, (int)bytes, 0x00020000); }
#define SCHED() __builtin_amdgcn_sched_barrier(0)
__device__ __forceinline__ unsigned now32() { unsigned long long t; asm volatile("s_memtime %0\n\ts_waitcnt lgkmcnt(0)" : "=s"(t) :: "memory"); return (unsigned)t; }
__device__ __forceinline__ unsigned rt32() { unsigned long long t; asm volatile("s_memrealtime %0\n\ts_waitcnt lgkmcnt(0)" : "=s"(t) :: "memory"); return (unsigned)t; }
__device__ __forceinline__ void wait_until(unsigned t) { while ((int)(rt32() - t) < 0) {} }
template <int X = 0> __device__ __forceinline__ LAS unsigned char* lad(unsigned v, unsigned u) {
  unsigned a;
  if (X == 0) asm volatile("v_add_u32 %0, %1, %2" : "=v"(a) : "s"(u), "v"(v));
  else asm volatile("v_xor_b32 %0, %3, %2\n\tv_add_u32 %0, %1, %0" : "=&v"(a) : "s"(u), "v"(v), "n"(X));
  return (LAS unsigned char*)(size_t)a;
}
__device__ __forceinline__ bf16x4 trread(LAS unsigned char* p) { return __builtin_amdgcn_ds_read_tr16_b64_v4bf16((LAS bf16x4*)p); }
__device__ __forceinline__ bf16x8 cat8(bf16x4 a, bf16x4 b) { return __builtin_shufflevector(a, b, 0, 1, 2, 3, 4, 5, 6, 7); }
__device__ __forceinline__ bf16x16 cat16(bf16x8 a, bf16x8 b) { return __builtin_shufflevector(a, b, 0, 1, 2, 3, 4, 5, 6, 7, 8, 9, 10, 11, 12, 13, 14, 15); }
__device__ __forceinline__ f32x4 mfma32(bf16x8 a, bf16x8 b, f32x4 c) { return __builtin_amdgcn_mfma_f32_16x16x32_bf16(a, b, c, 0, 0, 0); }
__device__ __forceinline__ f32x4 mfma16(u32x2 a, u32x2 b, f32x4 c) {
  return mfma32(__builtin_bit_cast(bf16x8, u32x4{a[0], a[1], 0u, 0u}), __builtin_bit_cast(bf16x8, u32x4{b[0], b[1], 0u, 0u}), c);
}
__device__ __forceinline__ f32x4 mfma16n(u32x2 a, u32x2 b, f32x4 c) { return __builtin_amdgcn_mfma_f32_16x16x16bf16_1k(__builtin_bit_cast(s16x4, a), __builtin_bit_cast(s16x4, b), c, 0, 0, 0); }
__device__ __forceinline__ f32x4 smf(bf16x8 a, bf16x16 b, f32x4 c, int idx) { return __builtin_amdgcn_smfmac_f32_16x16x64_bf16(a, b, c, idx, 0, 0); }

__device__ __forceinline__ void ln_row(float (&x)[16]) {
  float s = 0.f;
#pragma unroll
  for (int i = 0; i < 16; ++i) s += x[i];
  const float mean = wave_sum(s) * (1.0f / 1024.0f);
  float q = 0.f;
#pragma unroll
  for (int i = 0; i < 16; ++i) { const float d = x[i] - mean; q += d * d; }
  const float var = wave_sum(q) * (1.0f / 1024.0f);
  const float m = rbf(mean), isd = __builtin_amdgcn_rcpf(rbf(__builtin_amdgcn_sqrtf(rbf(rbf(var) + 1e-5f))));
#pragma unroll
  for (int i = 0; i < 16; ++i) x[i] = rbf(rbf(x[i] - m) * isd);
}
__device__ __forceinline__ void load_row16(const u16* rowp, int lane, float (&x)[16]) {
  const u32x4 a = *(const u32x4*)(rowp + lane * 8), b = *(const u32x4*)(rowp + 512 + lane * 8);
#pragma unroll
  for (int i = 0; i < 4; ++i) { x[2 * i] = lo2f(a[i]); x[2 * i + 1] = hi2f(a[i]); x[8 + 2 * i] = lo2f(b[i]); x[9 + 2 * i] = hi2f(b[i]); }
}
__device__ __forceinline__ void store_row16(u16* rowp, int lane, const float (&x)[16]) {
  u32x4 a, b;
#pragma unroll
  for (int i = 0; i < 4; ++i) { a[i] = pack2(x[2 * i], x[2 * i + 1]); b[i] = pack2(x[8 + 2 * i], x[9 + 2 * i]); }
  *(u32x4*)(rowp + lane * 8) = a; *(u32x4*)(rowp + 512 + lane * 8) = b;
}
__device__ __forceinline__ void modulate(float (&x)[16], const float* sc, const float* sh, int lane) {
#pragma unroll
  for (int hh = 0; hh < 2; ++hh)
#pragma unroll
    for (int j = 0; j < 2; ++j) {
      const f32x4 c = *(const f32x4*)(sc + hh * 512 + lane * 8 + j * 4), t = *(const f32x4*)(sh + hh * 512 + lane * 8 + j * 4);
#pragma unroll
      for (int i = 0; i < 4; ++i) { float& v = x[hh * 8 + j * 4 + i]; v = rbf(rbf(v * rbf(1.0f + c[i])) + t[i]); }
    }
}

__host__ __device__ __forceinline__ int mem_slot(int dl) {
  return ((dl >> 2) & 1) * 32 + (((dl >> 5) & 1) * 2 + ((dl >> 3) & 1)) * 8 + (((dl >> 4) & 1) * 2 + (dl & 1)) * 2 + ((dl >> 1) & 1);
}

__global__ void __launch_bounds__(512) k_prep(const u16* __restrict__ c, const u16* __restrict__ wmod, const u16* __restrict__ mem0, unsigned char* ws) {
  __shared__ float red[128][33];
  __shared__ float sc[1024];
  const int b = blockIdx.x, tid = threadIdx.x;
  {
    u32x4* z = (u32x4*)ws;
    const u32x4 zero = {0u, 0u, 0u, 0u};
    for (size_t i = (size_t)65536 / 16 + (size_t)b * 512 + tid; i < X_BYTES / 16; i += (size_t)256 * 512) z[i] = zero;
    if (b >= 192 && b < 208) {
      u32x4* y = (u32x4*)(ws + WS_H1S + ((b & 8) ? 64 * 65536 : 0));
      y[(b & 7) * 512 + tid] = zero;
    }
  }
  if (b == 255) {
    for (int d = tid; d < 1024; d += 512) {
      u32x4 v;
#pragma unroll
      for (int i = 0; i < 4; ++i) v[i] = nz((unsigned)mem0[(2 * i) * 1024 + d] | ((unsigned)mem0[(2 * i + 1) * 1024 + d] << 16));
      const u32x4 mz = {0x80008000u, 0x80008000u, 0x80008000u, 0x80008000u};
#pragma unroll
      for (int r = 0; r < 2; ++r) {
        *(u32x4*)(ws + X_MEMP + r * 32768 + (d >> 6) * 1024 + mem_slot(d & 63) * 16) = v;
        *(u32x4*)(ws + X_MEMP + r * 32768 + 16384 + d * 16) = mz;
      }
    }
  }
  if (b < 192) {
    for (int i = tid; i < 1024; i += 512) { const float v = bf2f(c[i]); sc[i] = rbf(v / (1.0f + __expf(-v))); }
    __syncthreads();
    const int kg = tid >> 2, c8 = tid & 3;
    float acc[8] = {0.f, 0.f, 0.f, 0.f, 0.f, 0.f, 0.f, 0.f};
    u32x4 w[8];
#pragma unroll
    for (int i = 0; i < 8; ++i) w[i] = *(const u32x4*)(wmod + (size_t)(kg + 128 * i) * 6144 + b * 32 + c8 * 8);
#pragma unroll
    for (int i = 0; i < 8; ++i) {
      const float s = sc[kg + 128 * i];
#pragma unroll
      for (int j = 0; j < 4; ++j) { acc[2 * j] += s * lo2f(w[i][j]); acc[2 * j + 1] += s * hi2f(w[i][j]); }
    }
#pragma unroll
    for (int j = 0; j < 8; ++j) red[kg][c8 * 8 + j] = acc[j];
    __syncthreads();
    if (tid < 256) {
      const int col = tid & 31, part = tid >> 5;
      float s = 0.f;
#pragma unroll
      for (int i = 0; i < 16; ++i) s += red[part * 16 + i][col];
      __syncthreads();
      red[part][col] = s;
    } else __syncthreads();
    __syncthreads();
    if (tid < 32) {
      float s = 0.f;
#pragma unroll
      for (int i = 0; i < 8; ++i) s += red[i][tid];
      ((float*)(ws + WS_MOD))[b * 32 + tid] = rbf(s);
    }
  }
}

#define WST(rs, wk, i)
struct MainP { const u16 *x, *Wq, *Wk, *Wv, *Wo, *w1, *w2, *w3, *w4; u16* out; unsigned char* ws; int d1, d2, d3, d4; };
constexpr unsigned SPIN_CAP = 1u << 18;

__device__ __forceinline__ float gmax(float v) {
  auto t = __builtin_amdgcn_permlane16_swap(f2u(v), f2u(v), false, false);
  v = fmaxf(u2f(t[0]), u2f(t[1]));
  t = __builtin_amdgcn_permlane32_swap(f2u(v), f2u(v), false, false);
  return fmaxf(u2f(t[0]), u2f(t[1]));
}
__device__ __forceinline__ float gsum(float v) {
  auto t = __builtin_amdgcn_permlane16_swap(f2u(v), f2u(v), false, false);
  v = u2f(t[0]) + u2f(t[1]);
  t = __builtin_amdgcn_permlane32_swap(f2u(v), f2u(v), false, false);
  return u2f(t[0]) + u2f(t[1]);
}
__device__ __forceinline__ void publish(__amdgpu_buffer_rsrc_t rs, unsigned word, unsigned val) {
  WAIT_V(0); BAR();
  if (threadIdx.x == 0) stx4(rs, 0, word, val);
}

constexpr int P_S2 = 0, P_S8 = 32, P_S7 = 64, P_S4 = 96, P_S5 = 104, P_S1 = 112, P_S3 = 144, P_PJ = 240;
constexpr int NLN = 160;

enum { SG_WO = 0, SG_W1 = 1, SG_W2 = 2, SG_W3 = 3, SG_W4 = 4, SG_K = 5, SG_V = 6, SG_Q = 7 };
constexpr int G_XCH = 131072, G_Q = G_XCH + 16384, G_CTL = G_Q + 8192;
__device__ __forceinline__ f32x16 mfma3232(bf16x8 a, bf16x8 b, f32x16 c) { return __builtin_amdgcn_mfma_f32_32x32x16_bf16(a, b, c, 0, 0, 0); }
__device__ __forceinline__ void widen32(const f32x16& a, u32x4 (&v)[2]) {
#pragma unroll
  for (int u = 0; u < 2; ++u) {
    const auto r0 = __builtin_amdgcn_permlane32_swap(pack2(a[8 * u], a[8 * u + 1]), pack2(a[8 * u + 4], a[8 * u + 5]), false, false);
    const auto r1 = __builtin_amdgcn_permlane32_swap(pack2(a[8 * u + 2], a[8 * u + 3]), pack2(a[8 * u + 6], a[8 * u + 7]), false, false);
    v[u] = u32x4{r0[0], r1[0], r0[1], r1[1]};
  }
}
__device__ __forceinline__ float hsum(float v) { const auto t = __builtin_amdgcn_permlane32_swap(f2u(v), f2u(v), false, false); return u2f(t[0]) + u2f(t[1]); }

__device__ __forceinline__ void attend(__amdgpu_buffer_rsrc_t rs, LAS unsigned char* qb, int seg, int h, int t, int eh, int lane, unsigned& cap) {
  const int fr = lane & 15, g = lane >> 4;
  const unsigned l16 = lane * 16, ub = (unsigned)(seg * NH + h);
  wait_ge(rs, (unsigned)(lane & 1) * 4096, F_ST + (unsigned)(P_PJ + (seg & 1) * 64 + h) * 128, (unsigned)seg + 1u, cap);
  f32x4 s0 = {0.f, 0.f, 0.f, 0.f}, s1 = s0, s2 = s0;
  bf16x8 q[4], vf[4];
#pragma unroll
  for (int ks = 0; ks < 4; ++ks) {
    q[ks] = *(const LAS bf16x8*)(qb + ks * 1024);
    const bf16x8 k0 = __builtin_bit_cast(bf16x8, ldx16(rs, l16, WS_K + ub * 8192 + ks * 1024)), k1 = __builtin_bit_cast(bf16x8, ldx16(rs, l16, WS_K + ub * 8192 + 4096 + ks * 1024));
    s1 = mfma32(k0, q[ks], s1); s2 = mfma32(k1, q[ks], s2);
  }
  const unsigned ev = (unsigned)(fr >> 3) * 1024 + (unsigned)(g * 16 + 8 * ((fr >> 2) & 1) + (fr & 3)) * 16;
#pragma unroll
  for (int i = 0; i < 4; ++i) vf[i] = __builtin_bit_cast(bf16x8, ldx16(rs, ev, WS_VT + ub * 8192 + (2 * eh + (i >> 1)) * 2048 + (i & 1) * 64));
  const float scale = 0.08838834764831845f, L2E = 1.4426950408889634f;
  float a[4], b[4], c[4], mt = -3.0e38f;
#pragma unroll
  for (int r = 0; r < 4; ++r) {
    const int j = 4 * g + r;
    b[r] = (t == 1 || j <= fr) ? rbf(rbf(s1[r]) * scale) : -3.0e38f;
    c[r] = (t == 1 && j <= fr) ? rbf(rbf(s2[r]) * scale) : -3.0e38f;
    mt = fmaxf(mt, fmaxf(b[r], c[r]));
  }
  wait_ge(rs, 0, F_CH + h * 128, (unsigned)seg + 1u, cap);
  u32x2 vmf[4];
  const unsigned em = (unsigned)(fr >> 3) * 256 + (unsigned)(g & 1) * 64 + (unsigned)(8 * ((fr >> 2) & 1) + (fr & 3)) * 4;
  for (;;) {
    asm volatile("" ::: "memory");
    unsigned mn = 0xFFFFFFFFu;
    bf16x8 kmf[4];
#pragma unroll
    for (int ks = 0; ks < 4; ++ks) kmf[ks] = __builtin_bit_cast(bf16x8, ldx16(rs, l16, WS_KM + ub * 4096 + ks * 1024));
#pragma unroll
    for (int i = 0; i < 4; ++i) { const unsigned so = X_VM + ub * 2048 + (2 * eh + (i >> 1)) * 512 + (i & 1) * 16; vmf[i] = u32x2{ldx4(rs, em, so), ldx4(rs, em, so + 128)}; }
    s0 = f32x4{0.f, 0.f, 0.f, 0.f};
#pragma unroll
    for (int ks = 0; ks < 4; ++ks) s0 = mfma32(kmf[ks], q[ks], s0);
#pragma unroll
    for (int i = 0; i < 4; ++i) mn = min(mn, min(vmf[i][0], vmf[i][1]));
    if (__all(mn != 0u) || cap == 0u) break;
    cap = __builtin_amdgcn_readfirstlane(cap - 1u);
  }
  cap = __builtin_amdgcn_readfirstlane(cap);
#pragma unroll
  for (int r = 0; r < 4; ++r) { a[r] = (g < 2) ? rbf(rbf(s0[r]) * scale) : -3.0e38f; mt = fmaxf(mt, a[r]); }
  const float mx = gmax(mt);
  float sum = 0.f;
#pragma unroll
  for (int r = 0; r < 4; ++r) {
    a[r] = __builtin_amdgcn_exp2f((a[r] - mx) * L2E); b[r] = __builtin_amdgcn_exp2f((b[r] - mx) * L2E); c[r] = __builtin_amdgcn_exp2f((c[r] - mx) * L2E);
    sum += a[r] + b[r] + c[r];
  }
  const float inv = __builtin_amdgcn_rcpf(gsum(sum));
  const u32x2 pm = {pack2(a[0] * inv, a[1] * inv), pack2(a[2] * inv, a[3] * inv)};
  const u32x4 pt = {pack2(b[0] * inv, b[1] * inv), pack2(b[2] * inv, b[3] * inv), pack2(c[0] * inv, c[1] * inv), pack2(c[2] * inv, c[3] * inv)};
  f32x4 o[4];
#pragma unroll
  for (int i = 0; i < 4; ++i) o[i] = mfma32(vf[i], __builtin_bit_cast(bf16x8, pt), f32x4{0.f, 0.f, 0.f, 0.f});
#pragma unroll
  for (int i = 0; i < 4; ++i) o[i] = mfma16(vmf[i], pm, o[i]);
#pragma unroll
  for (int u = 0; u < 2; ++u)
    stx16(rs, l16, WS_O + (unsigned)seg * 262144 + (unsigned)((h * 4 + 2 * eh + u) * 2 + t) * 1024,
          u32x4{pack2(o[2 * u][0], o[2 * u][1]), pack2(o[2 * u][2], o[2 * u][3]), pack2(o[2 * u + 1][0], o[2 * u + 1][1]), pack2(o[2 * u + 1][2], o[2 * u + 1][3])});
}

template <int SG>
__device__ __forceinline__ void stage_gemm(LAS unsigned char* lds, __amdgpu_buffer_rsrc_t rs, const u16* Wg, int wk, int kq, int cb, int pid, unsigned& cap) {
  constexpr bool KV = (SG == SG_K || SG == SG_V), PROJ = KV || SG == SG_Q, ROWM = (SG == SG_W1 || PROJ);
  constexpr int KS = (SG == SG_W4) ? 28 : 32;
  constexpr int LDW = PROJ ? HD : (SG == SG_W3) ? FF : D_;
  constexpr int WROWS = (SG == SG_WO) ? HD : (SG == SG_W4 ? FF : D_);
  constexpr int NBLK = KV ? 32 : (SG == SG_WO || SG == SG_W1 || SG == SG_Q) ? 64 : 65;
  constexpr unsigned ABASE = PROJ ? WS_H : SG == SG_WO ? WS_O : SG == SG_W1 ? WS_G : SG == SG_W2 ? WS_H1S : SG == SG_W3 ? WS_X2 : WS_U;
  constexpr unsigned ASTR = SG == SG_WO ? 262144u : SG == SG_W4 ? 229376u : 65536u;
  constexpr int NPC = KS / 4;
  const unsigned myword = F_ST + (unsigned)pid * 128u;
  int tid = threadIdx.x; asm volatile("" : "+v"(tid));
  const int lane = tid & 63, w = __builtin_amdgcn_readfirstlane(tid >> 6), row = lane & 31, hf = lane >> 5;
  const int kh = w >> 2, wl = w & 3;
  const int koff = PROJ ? 0 : kq;
  const unsigned lbase = (unsigned)(size_t)lds, l16 = lane * 16;
  const int col0 = cb * 128 + wl * 32;
  bf16x8 Wf[KS];
  {
    const __amdgpu_buffer_rsrc_t rsW = mk_rs(Wg, (unsigned)WROWS * LDW * 2u);
    const int ub = lane >> 3, tu = 4 * hf + ((lane & 15) >> 2);
    const unsigned vB = (unsigned)(((ub & 3) + 8 * (ub >> 2)) * LDW + 16 * (((lane >> 1) & 3) ^ (ub >> 1)) + 8 * (lane & 1)) * 2u;
    const unsigned sB = (unsigned)((koff * KS * 32 + 32 * (w >> 2) + 16 * ((w >> 1) & 1) + 4 * (w & 1)) * LDW + cb * 128) * 2u;
    const int boff = kh * 65536 + (wl >> 1) * 8192 + tu * 128 + (((tu >> 1) ^ (2 * (wl & 1) + ((lane >> 4) & 1))) << 5) + (lane & 3) * 8;
#pragma unroll
    for (int ps = 0; ps < 2; ++ps) {
      BAR();
#pragma unroll
      for (int hh = 0; hh < 2; ++hh)
#pragma unroll
        for (int i = 0; i < 4; ++i)
          if (4 * ps + i < NPC) {
            const unsigned so = sB + (unsigned)((hh * NPC + 4 * ps + i) * 64 * LDW) * 2u, d = lbase + hh * 65536 + i * 16384 + w * 1024;
            dma16(rsW, vB, so, d); dma16(rsW, vB, so + 128, d + 8192);
          }
      WAIT_V(0); BAR();
#pragma unroll
      for (int i = 0; i < 4; ++i)
        if (4 * ps + i < NPC) {
#pragma unroll
          for (int k = 0; k < 4; ++k) { LAS unsigned char* q = lds + (boff + i * 16384 + k * 2048); Wf[(4 * ps + i) * 4 + k] = cat8(trread(q), trread(q + 1024)); }
        }
      WAIT_L0();
    }
    if (tid < 8) *(LAS unsigned*)(lds + G_CTL + tid * 4) = 0u;
    LBAR();
  }
  const f32x16 zero16 = {0.f, 0.f, 0.f, 0.f, 0.f, 0.f, 0.f, 0.f, 0.f, 0.f, 0.f, 0.f, 0.f, 0.f, 0.f, 0.f};
  auto put16 = [&](LAS unsigned char* p, const f32x16& a) {
#pragma unroll
    for (int q = 0; q < 4; ++q) *(LAS f32x4*)(p + q * 1024) = f32x4{a[4 * q], a[4 * q + 1], a[4 * q + 2], a[4 * q + 3]};
  };
  if (SG == SG_W3) {
    const u32x4 one4 = {0x3F803F80u, 0x3F803F80u, 0x3F803F80u, 0x3F803F80u};
    f32x16 cs = zero16;
#pragma unroll
    for (int k = 0; k < KS; ++k) cs = mfma3232(Wf[k], __builtin_bit_cast(bf16x8, one4), cs);
    if (kh == 0) put16(lds + wl * 4096 + l16, cs);
    LBAR();
    if (kh == 1 && row == 0) {
#pragma unroll
      for (int q = 0; q < 4; ++q)
        *(LAS f32x4*)(lds + G_Q + wl * 128 + hf * 64 + q * 16) = *(const LAS f32x4*)(lds + wl * 4096 + q * 1024 + l16) + f32x4{cs[4 * q], cs[4 * q + 1], cs[4 * q + 2], cs[4 * q + 3]};
    }
    LBAR();
  }
  auto blk = [&](int i) -> int { return KV ? 2 * i + kq : i; };
  const unsigned aoff = ROWM ? (unsigned)(row * 128 + ((hf ^ ((row >> 1) & 7)) << 4)) : (unsigned)((row >> 4) * 1024 + hf * 256 + (row & 15) * 16);
  const unsigned vA = ROWM ? (unsigned)((lane >> 3) * 2048 + (((lane & 7) ^ ((4 * wl + (lane >> 4)) & 7)) << 4)) : l16;
  auto fetch = [&](int hb) {
    const int i = hb >> 1, hh = hb & 1;
    const unsigned so = ABASE + (unsigned)blk(i) * ASTR + (unsigned)koff * (KS * 2048) + (ROWM ? wl * 16384 + hh * 1024 : (hh * KS + wl) * 1024);
#pragma unroll
    for (int q = 0; q < NPC; ++q) dma16x(rs, vA, so + q * (ROWM ? 128 : 4096), lbase + (i & 1) * 65536 + hh * 32768 + wl * 1024 + q * 4096);
  };
  auto products = [&](int i, f32x16& acc, auto&& early, auto&& handover) {
    const unsigned ub = lbase + (i & 1) * 65536 + kh * 32768;
    LAS unsigned char* ab[4];
    ab[0] = lad(aoff, ub);
    if (ROWM) { ab[1] = lad<32>(aoff, ub); ab[2] = lad<64>(aoff, ub); ab[3] = lad<96>(aoff, ub); }
    constexpr int PD = 10;
    bf16x8 af[PD + 1];
    auto rd = [&](int k) { af[k % (PD + 1)] = ROWM ? *(const LAS bf16x8*)(ab[k & 3] + (k >> 2) * 4096) : *(const LAS bf16x8*)(ab[0] + (k >> 1) * 2048 + (k & 1) * 512); };
#pragma unroll
    for (int k = 0; k < PD; ++k) rd(k);
    SCHED();
#pragma unroll
    for (int k = 0; k < KS; ++k) {
      if (k + PD < KS) rd(k + PD);
      acc = SG == SG_V ? mfma3232(af[k % (PD + 1)], Wf[k], k ? acc : zero16) : mfma3232(Wf[k], af[k % (PD + 1)], k ? acc : zero16);
      SCHED();
      if (k == KS - 10) { early(); SCHED(); }
      if (k == KS - 5) { handover(); SCHED(); }
    }
    asm volatile("" : "+v"(acc));
  };
  WST(rs, wk, KV ? 2 : 10);
#define SEG(x)
  if (kh == 0) {
    auto pvo = [&](int i) -> unsigned {
      const int b = blk(i);
      if (PROJ) return F_LN + (unsigned)((b % (NLN / 4)) * 4 + (lane & 3)) * 128u;
      if (SG == SG_WO) return F_ST + (unsigned)(P_S1 + kq * 8 + (lane & 7)) * 128u;
      if (SG == SG_W1) return F_ST + (unsigned)(P_S3 + (b % 3) * 32 + (lane & 31)) * 128u;
      if (SG == SG_W2) return F_ST + (unsigned)(P_S4 + (lane & 7)) * 128u;
      if (SG == SG_W3) return F_ST + (unsigned)(P_S5 + (lane & 7)) * 128u;
      return F_ST + (unsigned)(P_S7 + kq * 7 + min(lane & 7, 6)) * 128u;
    };
    auto need = [&](int i) -> unsigned { return PROJ ? 1u : (SG == SG_W2 && i == 64) ? 64u : (unsigned)i + 1u; };
    int R = 0, Rp = 0;
    for (int i = 0; i < NBLK; ++i) {
      if (R <= i) { wait_ge(rs, pvo(i), 0, need(i), cap); fetch(2 * i); fetch(2 * i + 1); }
      const unsigned pk = w == 0 ? (lane < 32 ? pvo(i + 1) : pvo(i + 2)) : 0xF0000000u;
      if (R <= i + 1) wait_v<NPC>(); else wait_v<2 * NPC>();
      BAR();
      SEG(1);
      f32x16 acc;
      unsigned peek;
      asm volatile("buffer_load_dword %0, %1, %2, 0 offen sc1" : "=v"(peek) : "v"(pk), "s"(rs) : "memory");
      products(i, acc, []() {}, [&]() {
        SEG(4);
        WAIT_V(0);
        SEG(0);
        asm volatile("" : "+v"(peek));
        if (w == 0) {
          const unsigned long long ok = __ballot(peek >= (lane < 32 ? need(i + 1) : need(i + 2)));
          const bool r1 = (R >= i + 2 || (unsigned)ok == 0xFFFFFFFFu) && i + 1 < NBLK, r2 = r1 && (unsigned)(ok >> 32) == 0xFFFFFFFFu && i + 2 < NBLK;
          if (lane == 0) *(volatile LAS unsigned*)(lds + G_CTL + 4) = (unsigned)(i + 1 + (r1 ? 1 : 0) + (r2 ? 1 : 0));
        }
        LBAR();
      });
      SEG(2);
      const unsigned rv = *(const volatile LAS unsigned*)(lds + G_CTL + 4);
      put16(lad(l16, lbase + G_XCH + wl * 4096), acc);
      asm volatile("" ::: "memory");
      if (lane == 0) *(volatile LAS unsigned*)(lds + G_CTL + 16 + wl * 4) = (unsigned)i + 1u;
      Rp = R; R = __builtin_amdgcn_readfirstlane(rv);
      SEG(3);
      if (R >= i + 2 && Rp < i + 2) { fetch(2 * i + 2); fetch(2 * i + 3); }
      if (R >= i + 3) fetch(2 * i + 4);
      SEG(5);
    }
    BAR();
  } else {
    const int c8 = col0 + 8 * hf;
    bool pending = false;
    BAR();
    for (int i = 0; i < NBLK; ++i) {
      const int b = blk(i);
      BAR();
      SEG(0);
      const unsigned rv = *(const volatile LAS unsigned*)(lds + G_CTL + 4);
      u32x4 res[2], st[8];
      if (SG == SG_W2) {
        const int t = 32 * b - 2 + row;
        const unsigned vo = (t >= 0 && t < S_) ? (unsigned)(t * D_ + c8) * 2u : 0xF0000000u;
        res[0] = ldx16(rs, vo, WS_H2); res[1] = ldx16(rs, vo, WS_H2 + 32);
      }
      f32x16 acc;
      int R = 0;
      LAS unsigned char* xr = lad(l16, lbase + G_XCH + wl * 4096);
      f32x4 xa[4];
      unsigned xf;
      auto take = [&]() {
        asm volatile("" ::: "memory");
        xf = *(const volatile LAS unsigned*)(lds + G_CTL + 16 + wl * 4);
        asm volatile("" ::: "memory");
#pragma unroll
        for (int q = 0; q < 4; ++q) xa[q] = *(const LAS f32x4*)(xr + q * 1024);
        asm volatile("" ::: "memory");
      };
      products(i, acc, [&]() {
        if (SG == SG_W3) {
#pragma unroll
          for (int q = 0; q < 8; ++q) st[q] = ldx16(rs, l16, WS_ST5 + (unsigned)b * 8192 + q * 1024);
        }
        take();
      }, [&]() {
        SEG(5);
        R = __builtin_amdgcn_readfirstlane(rv);
        if (R >= i + 2) {
          if (SG == SG_W2) wait_v<2>(); else if (SG == SG_W3) wait_v<8>(); else wait_v<0>();
          LBAR();
          if (pending && tid == 256) stx4(rs, 0, myword, (unsigned)blk(i - 1) + 1u);
        }
      });
      const bool more = R >= i + 2;
      SEG(1);
      while (__builtin_amdgcn_readfirstlane(xf) <= (unsigned)i) take();
#pragma unroll
      for (int q = 0; q < 16; ++q) acc[q] += xa[q >> 2][q & 3];
      SEG(2);
      if (SG == SG_Q && R >= i + 3) fetch(2 * i + 5);
      const unsigned fo = (unsigned)((col0 >> 5) * 2048 + hf * 256), ro = (unsigned)((row >> 4) * 1024 + (row & 15) * 16);
      u32x4 v[2];
      if (SG == SG_W3) {
        float ms = 0.f;
#pragma unroll
        for (int q = 0; q < 8; ++q) ms += u2f(st[q][0]) + u2f(st[q][2]);
        const float mean = hsum(ms) * (1.0f / 32.0f);
        float q2 = 0.f;
#pragma unroll
        for (int q = 0; q < 8; ++q) { const float d0 = u2f(st[q][0]) - mean, d1 = u2f(st[q][2]) - mean; q2 += u2f(st[q][1]) + u2f(st[q][3]) + 32.0f * (d0 * d0 + d1 * d1); }
        const float var = hsum(q2) * (1.0f / 1024.0f);
        const float m = rbf(mean), isd = __builtin_amdgcn_rcpf(rbf(__builtin_amdgcn_sqrtf(rbf(rbf(var) + 1e-5f))));
#pragma unroll
        for (int q = 0; q < 4; ++q) {
          const f32x4 cs = *(const LAS f32x4*)(lds + G_Q + wl * 128 + hf * 64 + q * 16);
#pragma unroll
          for (int e = 0; e < 4; ++e) acc[4 * q + e] = (acc[4 * q + e] - m * cs[e]) * isd;
        }
      }
      if (SG != SG_V) widen32(acc, v);
      if (SG == SG_W1 || SG == SG_W3) {
#pragma unroll
        for (int q = 0; q < 8; ++q) v[q >> 2][q & 3] = pack2(gelu_tanh(lo2f(v[q >> 2][q & 3])), gelu_tanh(hi2f(v[q >> 2][q & 3])));
      }
      if (SG == SG_WO || SG == SG_W4) {
        const unsigned so = (SG == SG_WO ? WS_P2 : WS_P8) + (unsigned)(b * 4 + kq) * 65536u, vo = (unsigned)(row * D_ + c8) * 2u;
        stx16(rs, vo, so, v[0]); stx16(rs, vo, so + 32, v[1]);
      } else if (SG == SG_K) {
        const unsigned so = WS_K + (unsigned)(b * NH + cb) * 8192 + wl * 1024, vo = (unsigned)((row >> 4) * 4096 + hf * 256 + (row & 15) * 16);
        stx16(rs, vo, so, v[0]); stx16(rs, vo, so + 512, v[1]);
      } else if (SG == SG_V) {
        const unsigned so = WS_VT + (unsigned)(b * NH + cb) * 8192 + wl * 2048, vo = ro + hf * 256;
#pragma unroll
        for (int u = 0; u < 2; ++u)
          stx16(rs, vo, so + u * 512, u32x4{pack2(acc[4 * u], acc[4 * u + 1]), pack2(acc[4 * u + 2], acc[4 * u + 3]), pack2(acc[8 + 4 * u], acc[9 + 4 * u]), pack2(acc[10 + 4 * u], acc[11 + 4 * u])});
      } else if (SG == SG_Q) {
        LAS unsigned char* qw = lds + G_Q + wl * 1024 + (row >> 4) * 4096 + hf * 256 + (row & 15) * 16;
        *(LAS u32x4*)(qw) = v[0]; *(LAS u32x4*)(qw + 512) = v[1];
        asm volatile("" ::: "memory");
        if (lane == 0) __hip_atomic_fetch_add((LAS unsigned*)(lds + G_CTL + 8), 1u, __ATOMIC_RELAXED, __HIP_MEMORY_SCOPE_WORKGROUP);
        while (__builtin_amdgcn_readfirstlane(*(const volatile LAS unsigned*)(lds + G_CTL + 8)) < 4u * (unsigned)i + 4u) {}
        asm volatile("" ::: "memory");
        attend(rs, lds + G_Q + (wl & 1) * 4096 + l16, b, cb, wl & 1, wl >> 1, lane, cap);
      } else if (SG == SG_W1) {
        const int grp = cb >> 1, sh = (grp & 1) ? -(1 + (grp >> 1)) : (1 + (grp >> 1));
        const int rho = 32 * b + row + sh + 2;
        const unsigned vo = (rho >= 2 && rho < S_ + 2) ? (unsigned)(rho >> 5) * 65536u + fo + (unsigned)((rho >> 4) & 1) * 1024u + (unsigned)(rho & 15) * 16u : 0xF0000000u;
        stx16(rs, vo, WS_H1S, v[0]); stx16(rs, vo, WS_H1S + 512, v[1]);
      } else if (SG == SG_W2) {
        float z[16], zs = 0.f;
#pragma unroll
        for (int q = 0; q < 8; ++q) {
          const unsigned x = pack2(lo2f(res[q >> 2][q & 3]) + lo2f(v[q >> 2][q & 3]), hi2f(res[q >> 2][q & 3]) + hi2f(v[q >> 2][q & 3]));
          v[q >> 2][q & 3] = x; z[2 * q] = lo2f(x); z[2 * q + 1] = hi2f(x); zs += z[2 * q] + z[2 * q + 1];
        }
        stx16(rs, fo + ro, WS_X2 + (unsigned)b * 65536u, v[0]); stx16(rs, fo + ro, WS_X2 + (unsigned)b * 65536u + 512, v[1]);
        const float mw = hsum(zs) * (1.0f / 32.0f);
        float q2 = 0.f;
#pragma unroll
        for (int q = 0; q < 16; ++q) { const float d = z[q] - mw; q2 += d * d; }
        q2 = hsum(q2);
        if (hf == 0) stx8(rs, (unsigned)((cb * 2 + (wl >> 1)) * 512 + row * 16 + (wl & 1) * 8), WS_ST5 + (unsigned)b * 8192, u32x2{f2u(mw), f2u(q2)});
      } else {
        stx16(rs, fo + ro, WS_U + (unsigned)b * 229376u, v[0]); stx16(rs, fo + ro, WS_U + (unsigned)b * 229376u + 512, v[1]);
      }
      SEG(3);
      pending = more && i >= 2 && SG != SG_Q;
      if (!pending) {
        WAIT_V(0);
        if (lane == 0) {
          const unsigned c = __hip_atomic_fetch_add((LAS unsigned*)(lds + G_CTL), 1u, __ATOMIC_RELAXED, __HIP_MEMORY_SCOPE_WORKGROUP);
          if ((c & 3u) == 3u) stx4(rs, 0, myword, (unsigned)b + 1u);
        }
      }
      if (SG != SG_Q && R >= i + 3) fetch(2 * i + 5);
      if (!KV) WST(rs, wk, 16 + b);
      SEG(4);
      if (!more) BAR();
    }
  }
}

__device__ __forceinline__ void wait32(__amdgpu_buffer_rsrc_t rs, int first, int mine, int lane, unsigned need, unsigned& cap) {
  wait_ge(rs, 0, F_ST + (unsigned)(first + mine) * 128u, need, cap);
  wait_ge(rs, (unsigned)(lane & 31) * 128u, F_ST + (unsigned)first * 128u, need, cap);
}
__device__ __forceinline__ void sum_partials(__amdgpu_buffer_rsrc_t rs, unsigned l16, unsigned so, float (&a)[16]) {
  u32x4 pp[4][2];
#pragma unroll
  for (int k = 0; k < 4; ++k) { pp[k][0] = ldx16(rs, l16, so + k * 65536u); pp[k][1] = ldx16(rs, l16, so + k * 65536u + 1024); }
#pragma unroll
  for (int i = 0; i < 8; ++i) {
    f32x2 t = {0.f, 0.f};
#pragma unroll
    for (int k = 0; k < 4; ++k) t += f32x2{lo2f(pp[k][i >> 2][i & 3]), hi2f(pp[k][i >> 2][i & 3])};
    a[2 * i] = rbf(t[0]); a[2 * i + 1] = rbf(t[1]);
  }
}
__device__ __forceinline__ void put_row(__amdgpu_buffer_rsrc_t rs, unsigned l16, unsigned so, const float (&v)[16]) {
  stx16(rs, l16, so, u32x4{pack2(v[0], v[1]), pack2(v[2], v[3]), pack2(v[4], v[5]), pack2(v[6], v[7])});
  stx16(rs, l16, so + 1024, u32x4{pack2(v[8], v[9]), pack2(v[10], v[11]), pack2(v[12], v[13]), pack2(v[14], v[15])});
}
__device__ __forceinline__ void load_vec16(const float* p, int lane, float (&v)[16]) {
#pragma unroll
  for (int i = 0; i < 4; ++i) { const f32x4 t = *(const f32x4*)(p + (i >> 1) * 512 + lane * 8 + (i & 1) * 4); v[4 * i] = t[0]; v[4 * i + 1] = t[1]; v[4 * i + 2] = t[2]; v[4 * i + 3] = t[3]; }
}

__device__ __forceinline__ void stage_mid(__amdgpu_buffer_rsrc_t rs, const u16* x, const float* mod, int wk, int cu, unsigned& cap) {
  const int tid = threadIdx.x, lane = tid & 63, w = __builtin_amdgcn_readfirstlane(tid >> 6);
  const int id = w * 12 + cu, grp = id >> 5, rl = id & 31;
  const unsigned l16 = lane * 16;
  float ga[16], sc[16], sh[16];
  load_vec16(mod + 2048, lane, ga); load_vec16(mod + 4096, lane, sc); load_vec16(mod + 3072, lane, sh);
#pragma unroll
  for (int i = 0; i < 16; ++i) sc[i] = rbf(1.0f + sc[i]);
  for (int seg = grp; seg < NSEG; seg += 3) {
    const int row = seg * 32 + rl;
    float v[16], a[16];
    load_row16(x + (size_t)row * D_, lane, v);
    wait32(rs, P_S2, rl, lane, (unsigned)seg + 1u, cap);
    sum_partials(rs, l16, WS_P2 + (unsigned)seg * 262144u + (unsigned)rl * 2048, a);
#pragma unroll
    for (int i = 0; i < 16; ++i) v[i] = rbf(v[i] + rbf(ga[i] * a[i]));
    put_row(rs, l16, WS_X1 + (unsigned)row * 2048, v);
    ln_row(v);
#pragma unroll
    for (int i = 0; i < 16; ++i) v[i] = rbf(rbf(v[i] * sc[i]) + sh[i]);
    put_row(rs, l16, WS_H2 + (unsigned)row * 2048, v);
    ln_row(v);
    put_row(rs, l16, WS_G + (unsigned)row * 2048, v);
    WAIT_V(0);
    if (lane == 0) stx4(rs, 0, F_ST + (unsigned)(P_S3 + grp * 32 + rl) * 128, (unsigned)seg + 1u);
    WST(rs, wk, 16 + seg);
  }
}

__device__ __forceinline__ void stage_out(__amdgpu_buffer_rsrc_t rs, u16* out, const float* mod, int wk, int cu, unsigned& cap) {
  const int tid = threadIdx.x, lane = tid & 63, w = __builtin_amdgcn_readfirstlane(tid >> 6);
  const int grp = w >> 2, rl = cu * 4 + (w & 3);
  const unsigned l16 = lane * 16;
  float gb[16];
  load_vec16(mod + 5120, lane, gb);
  for (int b = grp; b < 65; b += 2) {
    const int t = 32 * b - 2 + rl;
    if (t < 0 || t >= S_) continue;
    wait32(rs, P_S8, rl, lane, (unsigned)b + 1u, cap);
    const unsigned xo = (unsigned)((lane >> 2) * 2048 + (lane & 3) * 256), xs = WS_X2 + (unsigned)b * 65536u + (unsigned)((rl >> 4) * 1024 + (rl & 15) * 16);
    const u32x4 x2a = ldx16(rs, xo, xs), x2b = ldx16(rs, xo, xs + 32768);
    const u32x4 x1a = ldx16(rs, l16, WS_X1 + (unsigned)t * 2048), x1b = ldx16(rs, l16, WS_X1 + (unsigned)t * 2048 + 1024);
    float a[16];
    sum_partials(rs, l16, WS_P8 + (unsigned)b * 262144u + (unsigned)rl * 2048, a);
#pragma unroll
    for (int i = 0; i < 4; ++i) {
      a[2 * i] = lo2f(x1a[i]) + rbf(gb[2 * i] * rbf(lo2f(x2a[i]) + a[2 * i])); a[2 * i + 1] = hi2f(x1a[i]) + rbf(gb[2 * i + 1] * rbf(hi2f(x2a[i]) + a[2 * i + 1]));
      a[8 + 2 * i] = lo2f(x1b[i]) + rbf(gb[8 + 2 * i] * rbf(lo2f(x2b[i]) + a[8 + 2 * i])); a[9 + 2 * i] = hi2f(x1b[i]) + rbf(gb[9 + 2 * i] * rbf(hi2f(x2b[i]) + a[9 + 2 * i]));
    }
    store_row16(out + (size_t)t * D_, lane, a);
    WST(rs, wk, 16 + b);
  }
}

__device__ __forceinline__ void worker(const MainP& p, LAS unsigned char* lds, int xw, int j) {
  const int tid = threadIdx.x, lane = tid & 63, w = __builtin_amdgcn_readfirstlane(tid >> 6);
  const __amdgpu_buffer_rsrc_t rs = mk_rs(p.ws, WS_END);
  const float* mod = (const float*)(p.ws + WS_MOD);
  unsigned cap = SPIN_CAP;
  const int wk = xw * 32 + j;
  WST(rs, wk, 0);
  {
    const int c = j * 5 + xw;
    for (int row = c * 8 + w; row < S_; row += 8 * NLN) {
      float v[16];
      load_row16(p.x + (size_t)row * D_, lane, v);
      ln_row(v);
      modulate(v, mod + 1024, mod, lane);
      put_row(rs, (unsigned)lane * 16, WS_H + (unsigned)row * 2048, v);
    }
    publish(rs, F_LN + c * 128, 1u);
    WST(rs, wk, 1);
  }
  if (xw == 4) stage_gemm<SG_Q>(lds, rs, p.Wq, wk, 0, j, P_S1 + j, cap);
  else {
    if (xw & 1) stage_gemm<SG_V>(lds, rs, p.Wv, wk, xw >> 1, j, P_PJ + xw * 32 + j, cap);
    else stage_gemm<SG_K>(lds, rs, p.Wk, wk, xw >> 1, j, P_PJ + xw * 32 + j, cap);
    WST(rs, wk, 3);
    if (xw == 0) stage_gemm<SG_WO>(lds, rs, p.Wo, wk, j >> 3, j & 7, P_S2 + j, cap);
    else if (xw == 1) stage_gemm<SG_W4>(lds, rs, p.w4, wk, j >> 3, j & 7, P_S8 + j, cap);
    else if (j >= 28) stage_out(rs, p.out, mod, wk, (xw - 2) * 4 + j - 28, cap);
    else if (xw == 2) stage_gemm<SG_W3>(lds, rs, p.w3, wk, 0, j, P_S7 + j, cap);
    else if (j < 8) stage_gemm<SG_W1>(lds, rs, p.w1, wk, 0, j, P_S4 + j, cap);
    else if (j < 16) stage_gemm<SG_W2>(lds, rs, p.w2, wk, 0, j - 8, P_S5 + j - 8, cap);
    else stage_mid(rs, p.x, mod, wk, j - 16, cap);
  }
  if (cap == 0u && lane == 0) stx4(rs, 0, X_CTL, 1u);
}

#define ST_DECL
#define ST0()
#define ST(i)
#define ST_FLUSH()
constexpr int R_W = 0, R_MEM = 131072, R_QK = R_MEM + 16384, R_KT = R_QK + 8192, R_RED = R_MEM + 16384, R_LDS = 163840;
__device__ __forceinline__ void bar_lo(int w) { asm volatile("s_cmp_ge_u32 %0, 4\n\ts_cbranch_scc1 1f\n\ts_barrier\n\t1:" :: "s"(w) : "scc", "memory"); }
__device__ __forceinline__ void bar_hi(int w) { asm volatile("s_cmp_lt_u32 %0, 4\n\ts_cbranch_scc1 1f\n\ts_setprio 3\n\ts_barrier\n\t1:" :: "s"(w) : "scc", "memory"); }
__device__ __forceinline__ void issue4(__amdgpu_buffer_rsrc_t rs, unsigned vo, unsigned s0, unsigned s1, unsigned s2, unsigned s3, u32x4& a0, u32x4& a1, u32x4& a2, u32x4& a3) {
  asm volatile("buffer_load_dwordx4 v[240:243], %[vo], %[rs], %[s0] offen sc1\n\tbuffer_load_dwordx4 v[244:247], %[vo], %[rs], %[s1] offen sc1\n\t"
               "buffer_load_dwordx4 v[248:251], %[vo], %[rs], %[s2] offen sc1\n\tbuffer_load_dwordx4 v[252:255], %[vo], %[rs], %[s3] offen sc1"
               : "=&{v[240:243]}"(a0), "=&{v[244:247]}"(a1), "=&{v[248:251]}"(a2), "=&{v[252:255]}"(a3)
               : [vo] "v"(vo), [rs] "s"(rs), [s0] "s"(s0), [s1] "s"(s1), [s2] "s"(s2), [s3] "s"(s3) : "memory");
}
#define ARRIVE(R0, R1, R2, R3, CNT) \
  asm volatile("s_waitcnt vmcnt(" #CNT ")\n\t" \
               "1:\n\t" \
               "v_min3_u32 %[t], v" #R0 ", v" #R1 ", v" #R2 "\n\t" \
               "v_min_u32 %[t], %[t], v" #R3 "\n\t" \
               "v_cmp_eq_u32 vcc, 0, %[t]\n\t" \
               "s_cbranch_vccz 2f\n\t" \
               "s_mov_b64 exec, vcc\n\t" \
               "buffer_load_dwordx4 v[" #R0 ":" #R3 "], %[vo], %[rs], %[so] offen sc1\n\t" \
               "s_mov_b64 exec, -1\n\t" \
               "s_waitcnt vmcnt(0)\n\t" \
               "s_sub_u32 %[cap], %[cap], 1\n\t" \
               "s_cbranch_scc0 1b\n\t" \
               "s_mov_b32 %[cap], 0\n\t" \
               "2:" \
               : "+{v[" #R0 ":" #R3 "]}"(a), [t] "=&v"(t), [cap] "+s"(cap) : [vo] "v"(vo), [rs] "s"(rs), [so] "s"(so) : "vcc", "scc", "memory")
template <int I> __device__ __forceinline__ void arrive(__amdgpu_buffer_rsrc_t rs, unsigned vo, unsigned so, u32x4& a, unsigned& cap) {
  unsigned t;
  if (I == 0) ARRIVE(240, 241, 242, 243, 3);
  else if (I == 1) ARRIVE(244, 245, 246, 247, 2);
  else if (I == 2) ARRIVE(248, 249, 250, 251, 1);
  else ARRIVE(252, 253, 254, 255, 0);
}
__device__ __forceinline__ void issue2(__amdgpu_buffer_rsrc_t rs, unsigned l8, unsigned so, int w, u32x2& a) {
  asm volatile("s_cmp_ge_u32 %[w], 4\n\ts_cbranch_scc1 1f\n\tv_lshrrev_b32 v255, 1, %[l8]\n\t"
               "buffer_load_dword v254, v255, %[rs], %[so] offen sc1\n\tbuffer_load_dword v255, v255, %[rs], %[so] offen offset:256 sc1\n\t1:"
               : "=&{v[254:255]}"(a) : [l8] "v"(l8), [rs] "s"(rs), [so] "s"(so), [w] "s"(w) : "scc", "memory");
}
__device__ __forceinline__ void arrive2(__amdgpu_buffer_rsrc_t rs, unsigned l8, unsigned so, u32x2& a, unsigned& cap) {
  unsigned t;
  asm volatile("s_waitcnt vmcnt(0)\n\t"
               "1:\n\t"
               "v_min_u32 %[t], v254, v255\n\t"
               "v_cmp_eq_u32 vcc, 0, %[t]\n\t"
               "s_cbranch_vccz 2f\n\t"
               "v_lshrrev_b32 %[t], 1, %[l8]\n\t"
               "buffer_load_dword v254, %[t], %[rs], %[so] offen sc1\n\t"
               "buffer_load_dword v255, %[t], %[rs], %[so] offen offset:256 sc1\n\t"
               "s_waitcnt vmcnt(0)\n\t"
               "s_sub_u32 %[cap], %[cap], 1\n\t"
               "s_cbranch_scc0 1b\n\t"
               "s_mov_b32 %[cap], 0\n\t"
               "2:"
               : "+{v[254:255]}"(a), [t] "=&v"(t), [cap] "+s"(cap) : [l8] "v"(l8), [rs] "s"(rs), [so] "s"(so) : "vcc", "scc", "memory");
}
__device__ __forceinline__ unsigned ror8(unsigned v) { return __builtin_amdgcn_update_dpp(0u, v, 0x128, 0xF, 0xF, true); }
__device__ __forceinline__ u32x4 add_bf16x8(u32x4 a, u32x4 b) {
  u32x4 r;
#pragma unroll
  for (int i = 0; i < 4; ++i) { r[i] = pack2(lo2f(a[i]) + lo2f(b[i]), hi2f(a[i]) + hi2f(b[i])); }
  return r;
}
__device__ __forceinline__ void gather_mem(__amdgpu_buffer_rsrc_t rs, unsigned lbase, int s, int half, int w, unsigned l16, unsigned& cap) {
  const unsigned so = X_MEMP + s * 65536 + half * 32768 + w * 2048;
  u32x4 a0, a1, b0, b1;
  issue4(rs, l16, so, so + 16384, so + 1024, so + 16384 + 1024, a0, b0, a1, b1);
  LAS unsigned char* d = lad(l16, lbase + R_MEM + w * 2048);
  arrive<0>(rs, l16, so, a0, cap); arrive<1>(rs, l16, so + 16384, b0, cap);
  *(LAS u32x4*)(d) = add_bf16x8(a0, b0);
  SCHED();
  arrive<2>(rs, l16, so + 1024, a1, cap); arrive<3>(rs, l16, so + 16384 + 1024, b1, cap);
  *(LAS u32x4*)(d + 1024) = add_bf16x8(a1, b1);
}
__device__ __forceinline__ bf16x8 mem_frag(LAS unsigned char* mp, int chunk) { return cat8(trread(mp + chunk * 1024), trread(mp + chunk * 1024 + 512)); }
__device__ __forceinline__ bf16x16 w_frag(LAS unsigned char* p) {
  return cat16(cat8(trread(p), trread(p + 2048)), cat8(trread(p + 4096), trread(p + 6144)));
}
__device__ __forceinline__ bf16x16 l_frag(LAS unsigned char* p, int i) { return cat16(*(const LAS bf16x8*)(p + i * 2048), *(const LAS bf16x8*)(p + i * 2048 + 1024)); }
__device__ __forceinline__ void make_resident(LAS unsigned char* wb, unsigned lo, unsigned l16) {
#pragma unroll
  for (int c = 0; c < 2; ++c) {
    bf16x16 f[4];
#pragma unroll
    for (int t = 0; t < 4; ++t) f[t] = w_frag(wb + c * 8192 + (lo ^ (t << 5)));
    WAIT_L0(); SCHED();
#pragma unroll
    for (int t = 0; t < 4; ++t) {
      *(LAS bf16x8*)(wb + (c * 4 + t) * 2048 + l16) = __builtin_shufflevector(f[t], f[t], 0, 1, 2, 3, 4, 5, 6, 7);
      *(LAS bf16x8*)(wb + (c * 4 + t) * 2048 + 1024 + l16) = __builtin_shufflevector(f[t], f[t], 8, 9, 10, 11, 12, 13, 14, 15);
    }
    WAIT_L0(); SCHED();
  }
}
__device__ __forceinline__ f32x4 fold(f32x4 a, f32x4 b) {
  f32x4 r;
#pragma unroll
  for (int i = 0; i < 4; ++i) {
    const auto t = __builtin_amdgcn_permlane32_swap(f2u(a[i]), f2u(b[i]), false, false);
    r[i] = u2f(t[0]) + u2f(t[1]);
  }
  return r;
}

struct Sched { unsigned tau, prev; int ea, eb, d1; bool moved; };
__device__ __forceinline__ void retime(__amdgpu_buffer_rsrc_t rs, unsigned l16, int s, Sched& t, const MainP& p, unsigned& cap) {
  if (s > 0) {
    unsigned vo;
    asm volatile("v_lshlrev_b32 %0, 3, %1" : "=v"(vo) : "v"(l16));
    wait_ge(rs, vo, X_TS + (unsigned)s * 8192, 1u, cap);
    int v = (int)(ldx4(rs, vo, X_TS + (unsigned)s * 8192) - t.prev);
#pragma unroll
    for (int o = 16; o >= 1; o >>= 1) v = max(v, __shfl_xor(v, o));
    const int tb = __builtin_amdgcn_readlane(v, 0), ta = __builtin_amdgcn_readlane(v, 32);
    const int late = tb + p.d3 - (int)(t.tau - t.prev);
    if (s > 2) {
      const int up = s < 8 ? 160 : 16, dn = s < 8 ? 64 : 2;
      if (!t.moved) t.ea = max(t.ea - dn, min(ta * 8, t.ea + up));
      if (ta + p.d3 <= t.d1) t.eb = max(t.eb - dn, min((tb - t.d1) * 8, t.eb + up));
    }
    t.moved = late > 0;
    if (late > 0) t.tau += late + p.d4;
  }
  t.d1 = (t.ea >> 3) + p.d1;
}
__device__ __forceinline__ void advance(Sched& t, const MainP& p) { t.prev = t.tau; t.tau += t.d1 + (t.eb >> 3) + p.d2; }

__device__ __forceinline__ void role_a(const MainP& p, LAS unsigned char* lds, int half, int hl) {
  const int tid = threadIdx.x, lane = tid & 63, w = __builtin_amdgcn_readfirstlane(tid >> 6);
  const int h = half * 16 + hl, cg = w & 3, kh = w >> 2;
  const __amdgpu_buffer_rsrc_t rs = mk_rs(p.ws, WS_END);
  const unsigned lbase = (unsigned)(size_t)lds;
  unsigned l16 = lane * 16, l8 = lane * 8;
  asm volatile("" : "+v"(l16), "+v"(l8));
  const int sidx = (lane & 8) ? 0xEEEE : 0x4444;
  bf16x16 W[24];
  unsigned qoff;
  {
    const int fr = lane & 15, g = lane >> 4, tu = 4 * (g & 1) + (fr >> 2), ub = lane >> 3;
    const unsigned lo = (g >> 1) * 1024 + tu * 128 + ((tu >> 1) << 5) + (fr & 3) * 8;
    qoff = R_QK + ((fr >> 1) & 1) * 512 + (g >> 1) * 256 + (fr & 1) * 128 + (8 * (g & 1) + (fr >> 2)) * 8;
    const __amdgpu_buffer_rsrc_t rw = mk_rs(cg < 2 ? p.Wq : p.Wk, D_ * HD * 2);
    const unsigned vo = (unsigned)(ub * HD + 16 * (((lane >> 1) & 3) ^ (ub >> 1)) + 8 * (lane & 1)) * 2u;
    const unsigned so = (unsigned)(kh * 512 * HD + h * DH + (cg & 1) * 64) * 2u;
    auto stage = [&](int c) {
#pragma unroll
      for (int rho = 0; rho < 8; ++rho) dma16(rw, vo, so + (unsigned)((c * 64 + rho * 8) * HD) * 2u, lbase + R_W + w * 16384 + (c & 1) * 8192 + rho * 1024);
    };
    stage(0); stage(1);
#pragma unroll
    for (int c = 0; c < 6; ++c) {
      WAIT_V(8);
#pragma unroll
      for (int t = 0; t < 4; ++t) W[4 * c + t] = w_frag(lds + R_W + w * 16384 + (c & 1) * 8192 + (lo ^ (t << 5)));
      WAIT_L0(); SCHED();
      stage(c + 2);
    }
    WAIT_V(0);
    make_resident(lds + R_W + w * 16384, lo, l16);
  }
  unsigned cap = SPIN_CAP;
  wait_ge(rs, (l16 & 16) * 256, F_ST + (unsigned)(P_PJ + h) * 128, 1u, cap);
  dma16x(rs, l16, WS_K + h * 8192 + w * 1024, lbase + R_KT + w * 1024);
  Sched tt; tt.tau = rt32() - 100000; tt.prev = tt.tau; tt.ea = 240 * 8; tt.eb = 125 * 8; tt.d1 = 0; tt.moved = true;
  const int cu_ = half * 32 + hl; (void)cu_;
  ST_DECL
  for (int s = 0; s < NSEG; ++s) {
    LAS unsigned char* lp = lad(l16, lbase + R_W + w * 16384);
    bf16x16 L[2];
    L[0] = l_frag(lp, 0); L[1] = l_frag(lp, 1);
    SCHED();
    if (w == 7) wait_until(tt.tau);
    BAR();
    ST0();
    gather_mem(rs, lbase, s, half, (w + hl) & 7, l16, cap);
    ST(1);
    LBAR();
    ST(2);
    f32x4 c[4];
#pragma unroll
    for (int t = 0; t < 4; ++t) c[t] = f32x4{0.f, 0.f, 0.f, 0.f};
    LAS unsigned char* mp = lad(l8, lbase + R_MEM + kh * 8192);
    bf16x8 al, af[2];
    u32x4 vt0, vt1;
    u32x2 vmv;
    const unsigned vso = X_VM + (unsigned)(s * NH + h) * 2048 + cg * 512;
    al = mem_frag(mp, 6); af[0] = mem_frag(mp, 0); af[1] = mem_frag(mp, 1);
#pragma unroll
    for (int i = 0; i < 8; ++i) {
      c[i & 3] = smf(al, L[i & 1], c[i & 3], sidx);
      SCHED();
      if (i == 3) al = mem_frag(mp, 7);
      if (i < 6) L[i & 1] = l_frag(lp, i + 2);
      if (i == 6) {
        const unsigned vo = WS_VT + (unsigned)(s * NH + h) * 8192 + cg * 2048;
        asm volatile("s_cmp_ge_u32 %5, 4\n\ts_cbranch_scc1 1f\n\t"
                     "buffer_load_dwordx4 %0, %2, %3, %4 offen sc1\n\tbuffer_load_dwordx4 %1, %2, %3, %4 offen offset:1024 sc1\n\t1:"
                     : "=&v"(vt0), "=&v"(vt1) : "v"(l16), "s"(rs), "s"(vo), "s"(w) : "scc", "memory");
      }
      SCHED();
#pragma unroll
      for (int r = 3 * i; r < 3 * i + 3; ++r) {
        c[(r + 1) & 3] = smf(af[(r >> 2) & 1], W[(r & ~3) + ((r + 1) & 3)], c[(r + 1) & 3], sidx);
        SCHED();
        if ((r & 3) == 3 && (r >> 2) + 2 < 6) af[(r >> 2) & 1] = mem_frag(mp, (r >> 2) + 2);
        SCHED();
      }
    }
    {
      LAS unsigned char* qw = lad(l8, lbase + R_QK + cg * 2048 + kh * 512);
      const f32x4 f0 = fold(c[0], c[1]);
      *(LAS u32x2*)(qw) = u32x2{pack2(f0[0], f0[1]), pack2(f0[2], f0[3])};
      const f32x4 f1 = fold(c[2], c[3]);
      *(LAS u32x2*)(qw + 1024) = u32x2{pack2(f1[0], f1[1]), pack2(f1[2], f1[3])};

    }
    ST(3);
    if (w < 4) {
      bf16x8 k1[4], k2[4];
      LAS unsigned char* kb = lad(l16, lbase + R_KT);
#pragma unroll
      for (int ks = 0; ks < 2; ++ks) { k1[ks] = *(const LAS bf16x8*)(kb + ks * 1024); k2[ks] = *(const LAS bf16x8*)(kb + 4096 + ks * 1024); }
      LBAR();
      ST(4);
      issue2(rs, l8, vso, w, vmv);
      f32x4 t0 = {0.f, 0.f, 0.f, 0.f}, t1 = t0, t2 = t0;
      LAS unsigned char* qb = lad(qoff, lbase);
      bf16x8 qf[4], kf[4];
#pragma unroll
      for (int ks = 0; ks < 2; ++ks) {
        qf[ks] = cat8(trread(qb + ks * 1024), trread(qb + ks * 1024 + 32));
        kf[ks] = cat8(trread(qb + 4096 + ks * 1024), trread(qb + 4096 + ks * 1024 + 32));
      }
      SCHED();
#pragma unroll
      for (int ks = 0; ks < 4; ++ks) {
        t1 = mfma32(k1[ks], qf[ks], t1); t2 = mfma32(k2[ks], qf[ks], t2); t0 = mfma32(kf[ks], qf[ks], t0);
        SCHED();
        if (ks < 2) {
          qf[ks + 2] = cat8(trread(qb + (ks + 2) * 1024), trread(qb + (ks + 2) * 1024 + 32));
          k1[ks + 2] = *(const LAS bf16x8*)(kb + (ks + 2) * 1024); k2[ks + 2] = *(const LAS bf16x8*)(kb + 4096 + (ks + 2) * 1024);
          kf[ks + 2] = cat8(trread(qb + 4096 + (ks + 2) * 1024), trread(qb + 4096 + (ks + 2) * 1024 + 32));
        }
        SCHED();
      }
      ST(5);
      {
        const float sl = 0.08838834764831845f * 1.4426950408889634f;
        float z[4] = {t1[0], t1[1], t1[2], t1[3]}, y = t0[0], mx;
        {
          float u1 = t0[1], u2 = t0[2], u3 = t0[3];
#define M8(Z, B) "v_add_f32_dpp " Z ", " Z ", " Z " row_ror:8 row_mask:0xf bank_mask:0x3\n\tv_add_f32_dpp " Z ", " B ", " B " row_ror:8 row_mask:0xf bank_mask:0xc\n\t"
          asm("s_nop 7\n\t"
              "v_permlane32_swap_b32 %4, %6\n\tv_permlane32_swap_b32 %5, %7\n\t"
              M8("%0", "%9") "v_add_f32 %4, %4, %6\n\tv_add_f32 %5, %5, %7\n\t" M8("%1", "%10") M8("%2", "%11") M8("%4", "%5") M8("%3", "%12")
              "v_max3_f32 %8, %0, %1, %2\n\tv_max3_f32 %8, %8, %4, %3\n\ts_nop 1\n\t"
              "v_max_f32_dpp %8, %8, %8 row_ror:8 row_mask:0xf bank_mask:0xf\n\tv_mov_b32 %5, %8\n\ts_nop 1\n\t"
              "v_permlane16_swap_b32 %8, %5\n\tv_max_f32 %8, %8, %5\n\tv_mov_b32 %5, %8\n\ts_nop 1\n\t"
              "v_permlane32_swap_b32 %8, %5\n\tv_max_f32 %8, %8, %5"
              : "+v"(z[0]), "+v"(z[1]), "+v"(z[2]), "+v"(z[3]), "+v"(y), "+v"(u1), "+v"(u2), "+v"(u3), "=&v"(mx) : "v"(t2[0]), "v"(t2[1]), "v"(t2[2]), "v"(t2[3]));
#undef M8
        }
        const float nm = -mx * sl;
#pragma unroll
        for (int r = 0; r < 4; ++r) z[r] = __builtin_amdgcn_exp2f(fmaf(z[r], sl, nm));
        const unsigned p0 = pack2(z[0], z[1]), p1 = pack2(z[2], z[3]);
        const u32x4 pt = {p0, p1, ror8(p0), ror8(p1)};
        SCHED();
        asm volatile("s_waitcnt vmcnt(2)" : "+v"(vt0), "+v"(vt1) :: "memory");
        f32x4 o0 = {0.f, 0.f, 0.f, 0.f}, o1 = o0;
        o0 = mfma32(__builtin_bit_cast(bf16x8, vt0), __builtin_bit_cast(bf16x8, pt), o0);
        o1 = mfma32(__builtin_bit_cast(bf16x8, vt1), __builtin_bit_cast(bf16x8, pt), o1);
        y = __builtin_amdgcn_exp2f(fmaf(y, sl, nm));
        const unsigned pm = pack2(y, u2f(ror8(f2u(y))));
        float inv = __builtin_amdgcn_rcpf(gsum(dpp_add<0x128>(((z[0] + z[1]) + (z[2] + z[3])) + y)));
        o0 *= inv; o1 *= inv;
        unsigned ov;
        asm volatile("v_and_b32 %0, 0x80, %4\n\tv_lshl_or_b32 %0, %0, 24, %4" : "=&v"(ov), "+v"(o0), "+v"(o1), "+v"(inv) : "v"(l16));
        ST(6);
        arrive2(rs, l8, vso, vmv, cap);
        ST(7);
        const f32x4 n0 = mfma16n(vmv, u32x2{pm, 0u}, f32x4{0.f, 0.f, 0.f, 0.f}), n1 = mfma16n(vmv, u32x2{0u, pm}, f32x4{0.f, 0.f, 0.f, 0.f});
        {
          const u32x4 v = {nz(pack2(fmaf(n0[0], inv, o0[0]), fmaf(n0[1], inv, o0[1]))), nz(pack2(fmaf(n0[2], inv, o0[2]), fmaf(n0[3], inv, o0[3]))),
                           nz(pack2(fmaf(n1[0], inv, o1[0]), fmaf(n1[1], inv, o1[1]))), nz(pack2(fmaf(n1[2], inv, o1[2]), fmaf(n1[3], inv, o1[3])))};
          stx16(rs, ov, X_OX + s * 65536 + half * 32768 + (hl * 2 + (w >> 1)) * 1024 + (w & 1) * 128, v);
        }
        if (w == 3) { const unsigned t = max(rt32(), 1u); if (l16 == 0) stx4(rs, 0, X_TS + (unsigned)(s + 1) * 8192 + (unsigned)(32 + h) * 128, t); }
        ST(8);
      }
    } else {
      LBAR();
      LAS unsigned char* qb = lad(qoff, lbase + 4096 + (w - 4) * 1024);
      u32x4 kf = __builtin_bit_cast(u32x4, cat8(trread(qb), trread(qb + 32)));
#pragma unroll
      for (int i = 0; i < 4; ++i) kf[i] = pack2(dpp_add<0x128>(lo2f(kf[i])), dpp_add<0x128>(hi2f(kf[i])));
      stx16(rs, l16, WS_KM + (unsigned)(s * NH + h) * 4096 + (w - 4) * 1024, kf);
      WAIT_V(0);
    }
    LBAR();
    if (w == 4 && l16 == 0) stx4(rs, 0, F_CH + h * 128, (unsigned)s + 1u);
    ST(9);
    if (w == 7) { retime(rs, l16, s, tt, p, cap); advance(tt, p); }
    if (s + 1 < NSEG) {
      unsigned fo;
      asm volatile("v_and_b32 %0, 16, %1\n\tv_lshlrev_b32 %0, 8, %0" : "=v"(fo) : "v"(l16));
      wait_ge(rs, fo, F_ST + (unsigned)(P_PJ + ((s + 1) & 1) * 64 + h) * 128, (unsigned)s + 2u, cap);
      dma16x(rs, l16, WS_K + (unsigned)((s + 1) * NH + h) * 8192 + w * 1024, lbase + R_KT + w * 1024);
    }
  }
  ST_FLUSH();
  if (cap == 0u && l16 == 0) stx4(rs, 0, X_CTL, 1u);
}

__device__ __forceinline__ void role_vb(const MainP& p, LAS unsigned char* lds, int half, int jb) {
  const int tid = threadIdx.x, lane = tid & 63, w = __builtin_amdgcn_readfirstlane(tid >> 6);
  const int h = half * 16 + jb, wr = (w + jb) & 7;
  const __amdgpu_buffer_rsrc_t rs = mk_rs(p.ws, WS_END);
  const unsigned lbase = (unsigned)(size_t)lds;
  unsigned l16 = lane * 16, l8 = lane * 8;
  asm volatile("" : "+v"(l16), "+v"(l8));
  const int sidx = (lane & 8) ? 0xEEEE : 0x4444;
  bf16x16 WV[16], WB[8];
  unsigned roff;
  {
    const int fr = lane & 15, g = lane >> 4, tu = 4 * (g & 1) + (fr >> 2), ub = lane >> 3;
    const unsigned lo = (g >> 1) * 1024 + tu * 128 + ((tu >> 1) << 5) + (fr & 3) * 8;
    roff = R_RED + w * 2048 + mem_slot(16 * (g >> 1) + fr) * 32 + (g & 1) * 16;
    const unsigned vcol = (unsigned)(16 * (((lane >> 1) & 3) ^ (ub >> 1)) + 8 * (lane & 1)) * 2u;
    const __amdgpu_buffer_rsrc_t rv = mk_rs(p.Wv, D_ * HD * 2), ro = mk_rs(p.Wo, D_ * HD * 2);
    const unsigned vv = (unsigned)(ub * HD) * 2u + vcol, sv = (unsigned)(8 * w * HD + h * DH) * 2u;
    auto stage = [&](int c) {
      const unsigned d = lbase + R_W + (c & 7) * 16384 + w * 1024, sc = sv + (unsigned)c * 64 * HD * 2;
      dma16(rv, vv, sc, d); dma16(rv, vv, sc + 128, d + 8192);
    };
#pragma unroll
    for (int c = 0; c < 8; ++c) stage(c);
    const int vo = (w >> 2) * 8192 + (lo ^ ((w & 3) << 5));
#pragma unroll
    for (int c = 0; c < 16; ++c) {
      if (c < 8) WAIT_V(14); else wait_v<0>();
      BAR();
      WV[c] = w_frag(lds + R_W + (c & 7) * 16384 + vo);
      LBAR();
      if (c < 8) stage(c + 8);
    }
    const unsigned vw = (unsigned)((32 * ((ub >> 1) & 1) + 16 * (ub >> 2) + (ub & 1)) * D_) * 2u + vcol;
    const unsigned sw = (unsigned)((half * 2048 + wr * 256) * D_ + jb * 64) * 2u;
    auto stageo = [&](int c) {
      const unsigned d = lbase + R_W + w * 16384 + (c & 1) * 8192;
#pragma unroll
      for (int rho = 0; rho < 8; ++rho) {
        const int r = rho >> 1, hb = rho & 1;
        dma16(ro, vw, sw + (unsigned)((c * 64 + 8 * (r >> 1) + 4 * hb + 2 * (r & 1)) * D_) * 2u, d + rho * 1024);
      }
    };
    stageo(2); stageo(3);
    WAIT_V(0);
#pragma unroll
    for (int c = 0; c < 2; ++c)
#pragma unroll
      for (int t = 0; t < 4; ++t) WB[c * 4 + t] = w_frag(lds + R_W + w * 16384 + c * 8192 + (lo ^ (t << 5)));
    WAIT_L0(); SCHED();
    stageo(0); stageo(1);
    WAIT_V(0);
    make_resident(lds + R_W + w * 16384, lo, l16);
  }
  unsigned cap = SPIN_CAP;
  Sched tt; tt.tau = rt32() - 100000; tt.prev = tt.tau; tt.ea = 240 * 8; tt.eb = 125 * 8; tt.d1 = 0; tt.moved = true;
  const int cu_ = half * 32 + 16 + jb; (void)cu_;
  ST_DECL
  for (int s = 0; s < NSEG; ++s) {
    if (w == 7) wait_until(tt.tau);
    BAR();
    ST0();
    gather_mem(rs, lbase, s, half, wr, l16, cap);
    ST(1);
    LBAR();
    ST(2);
    {
      f32x4 c0 = {0.f, 0.f, 0.f, 0.f}, c1 = c0;
      LAS unsigned char* mp = lad(l8, lbase + R_MEM);
      bf16x8 af[8];
#pragma unroll
      for (int c = 0; c < 8; ++c) af[c] = mem_frag(mp, c);
      SCHED();
#pragma unroll
      for (int c = 0; c < 8; ++c) {
        if (c & 1) c1 = smf(af[c], WV[c], c1, sidx); else c0 = smf(af[c], WV[c], c0, sidx);
        SCHED();
        if (c >= 3) { af[c] = mem_frag(mp, 8 + c); if (c < 6) af[c - 3] = mem_frag(mp, 5 + c); }
        SCHED();
      }
#pragma unroll
      for (int c = 8; c < 16; ++c) { if (c & 1) c1 = smf(af[c - 8], WV[c], c1, sidx); else c0 = smf(af[c - 8], WV[c], c0, sidx); }
      SCHED();
      c0 += c1;
      const f32x4 f = fold(c0, c0);
      unsigned l4; asm volatile("v_lshrrev_b32 %0, 1, %1" : "=v"(l4) : "v"(l8));
      stx4(rs, l4, X_VM + (unsigned)(s * NH + h) * 2048 + w * 256, nz(l16 < 512 ? pack2(f[0], f[1]) : pack2(f[2], f[3])));
    }
    ST(3);
    if (w == 7) retime(rs, l16, s, tt, p, cap);
    if (s + 1 < NSEG) {
      LAS unsigned char* lp = lad(l16, lbase + R_W + w * 16384);
      bf16x16 L[2];
      L[0] = l_frag(lp, 0); L[1] = l_frag(lp, 1);
      SCHED();
      u32x4 a[4];
      const unsigned so = X_OX + s * 65536 + half * 32768 + wr * 4096;
      if (w == 7) wait_until(tt.tau + tt.d1);
      BAR();
      ST(4);
      issue4(rs, l16, so, so + 1024, so + 2048, so + 3072, a[0], a[1], a[2], a[3]);
      f32x4 c[4];
#pragma unroll
      for (int t = 0; t < 4; ++t) c[t] = f32x4{0.f, 0.f, 0.f, 0.f};
      arrive<0>(rs, l16, so, a[0], cap);
#pragma unroll
      for (int i = 0; i < 8; ++i) {
        if (i == 4) arrive<1>(rs, l16, so + 1024, a[1], cap);
        c[i & 3] = smf(__builtin_bit_cast(bf16x8, a[i >> 2]), L[i & 1], c[i & 3], sidx);
        SCHED();
        if (i + 2 < 8) L[i & 1] = l_frag(lp, i + 2);
        SCHED();
      }
      arrive<2>(rs, l16, so + 2048, a[2], cap);
#pragma unroll
      for (int t = 0; t < 4; ++t) c[t] = smf(__builtin_bit_cast(bf16x8, a[2]), WB[t], c[t], sidx);
      SCHED();
      arrive<3>(rs, l16, so + 3072, a[3], cap);
#pragma unroll
      for (int t = 0; t < 4; ++t) c[t] = smf(__builtin_bit_cast(bf16x8, a[3]), WB[4 + t], c[t], sidx);
      ST(5);
      LAS unsigned char* rw = lad(roff, lbase);
      *(LAS f32x4*)(rw) = fold(c[0], c[1]);
      *(LAS f32x4*)(rw + 512) = fold(c[2], c[3]);
      ST(6);
      LBAR();
      ST(7);
      {
        LAS unsigned char* rp = lad(l8, lbase + R_RED + (w & 3) * 512);
        f32x2 v[8];
#pragma unroll
        for (int i = 0; i < 8; ++i) v[i] = *(const LAS f32x2*)(rp + i * 2048);
        unsigned l4; asm volatile("v_lshrrev_b32 %0, 1, %1" : "=v"(l4) : "v"(l8));
        const f32x2 sum = ((v[0] + v[1]) + (v[2] + v[3])) + ((v[4] + v[5]) + (v[6] + v[7]));
        stx4(rs, l4, X_MEMP + (s + 1) * 65536 + (half ^ 1 ^ (w >> 2)) * 32768 + half * 16384 + jb * 1024 + (w & 3) * 256, nz(pack2(sum[0], sum[1])));
      }
      if (w == 6) { const unsigned t = max(rt32(), 1u); if (l16 == 0) stx4(rs, 0, X_TS + (unsigned)(s + 1) * 8192 + (unsigned)h * 128, t); }
      ST(8);
    }
    advance(tt, p);
  }
  ST_FLUSH();
  if (cap == 0u && l16 == 0) stx4(rs, 0, X_CTL, 1u);
}

#if defined(__HIP_DEVICE_COMPILE__)
__attribute__((target("no-packed-fp32-ops")))
#endif
__global__ void __launch_bounds__(512, 2) k_main(MainP p) {
  extern __shared__ __attribute__((aligned(16))) unsigned char smem[];
  const int b = blockIdx.x, x = b & 7, j = b >> 3;
  if (x >= 3) worker(p, (LAS unsigned char*)smem, x - 3, j);
  else if (x == 2) {}
  else if (j < 16) role_a(p, (LAS unsigned char*)smem, x, j);
  else role_vb(p, (LAS unsigned char*)smem, x, j - 16);
}

extern "C" void kernel_launch(void* const* d_in, const int* in_sizes, int n_in, void* d_out, int out_size, void* d_ws, size_t ws_size,
                              hipStream_t stream) {
  unsigned char* ws = (unsigned char*)d_ws;
  k_prep<<<256, 512, 0, stream>>>((const u16*)d_in[12], (const u16*)d_in[13], (const u16*)d_in[1], ws);
  MainP p;
  p.x = (const u16*)d_in[0]; p.Wq = (const u16*)d_in[2]; p.Wk = (const u16*)d_in[3]; p.Wv = (const u16*)d_in[4]; p.Wo = (const u16*)d_in[5];
  p.w1 = (const u16*)d_in[8]; p.w2 = (const u16*)d_in[9]; p.w3 = (const u16*)d_in[10]; p.w4 = (const u16*)d_in[11];
  p.out = (u16*)d_out; p.ws = ws; p.d1 = 10; p.d2 = 10; p.d3 = 4; p.d4 = 200;
  (void)hipFuncSetAttribute((const void*)k_main, hipFuncAttributeMaxDynamicSharedMemorySize, R_LDS);
  k_main<<<256, 512, R_LDS, stream>>>(p);
}
```

```cpp
#include <hip/hip_runtime.h>
#include <hip/hip_bf16.h>
#include <cstdlib>
#pragma clang diagnostic ignored "-Winline-asm"

#define LAS __attribute__((address_space(3)))
#define GAS __attribute__((address_space(1)))
typedef unsigned short u16;
typedef __bf16 bf16x2 __attribute__((ext_vector_type(2)));
typedef __bf16 bf16x4 __attribute__((ext_vector_type(4)));
typedef __bf16 bf16x8 __attribute__((ext_vector_type(8)));
typedef __bf16 bf16x16 __attribute__((ext_vector_type(16)));
typedef short s16x4 __attribute__((ext_vector_type(4)));
typedef float f32x2 __attribute__((ext_vector_type(2)));
typedef float f32x4 __attribute__((ext_vector_type(4)));
typedef float f32x16 __attribute__((ext_vector_type(16)));
typedef unsigned u32x2 __attribute__((ext_vector_type(2)));
typedef unsigned u32x4 __attribute__((ext_vector_type(4)));

constexpr int S_ = 2048, D_ = 1024, NH = 32, DH = 128, HD = 4096, NSEG = 64, FF = 3584;

constexpr unsigned MB = 1u << 20;
constexpr unsigned X_MEM = 0;
constexpr unsigned X_OX = 1 * MB;
constexpr unsigned X_VM = 5 * MB;
constexpr unsigned F_LN = 13 * MB;
constexpr unsigned F_ST = F_LN + 32768;
constexpr unsigned F_CH = F_ST + 98304;
constexpr unsigned X_CTL = F_CH + 4096;
constexpr unsigned X_TS = X_CTL + 4096;
constexpr unsigned X_BYTES = X_TS + 65 * 12288;
constexpr unsigned WS_MOD = 15 * MB;
constexpr unsigned WS_H = 16 * MB;
constexpr unsigned WS_Q = 20 * MB;
constexpr unsigned WS_K = 36 * MB;
constexpr unsigned WS_VT = 52 * MB;
constexpr unsigned WS_O = 68 * MB;
constexpr unsigned WS_X1 = 84 * MB;
constexpr unsigned WS_H2 = 88 * MB;
constexpr unsigned WS_G = 92 * MB;
constexpr unsigned WS_H1S = 96 * MB;
constexpr unsigned WS_X2 = 101 * MB;
constexpr unsigned WS_ST5 = 106 * MB;
constexpr unsigned WS_U = 108 * MB;
constexpr unsigned WS_KM = 124 * MB;
constexpr unsigned WS_P2 = 132 * MB;
constexpr unsigned WS_P8 = 164 * MB;
constexpr unsigned WS_END = 198 * MB;

__device__ __forceinline__ float bf2f(unsigned short b) { return __builtin_bit_cast(float, (unsigned)b << 16); }
__device__ __forceinline__ unsigned f2u(float f) { return __builtin_bit_cast(unsigned, f); }
__device__ __forceinline__ float u2f(unsigned u) { return __builtin_bit_cast(float, u); }
__device__ __forceinline__ float lo2f(unsigned w) { return __builtin_bit_cast(float, w << 16); }
__device__ __forceinline__ float hi2f(unsigned w) { return __builtin_bit_cast(float, w & 0xFFFF0000u); }
__device__ __forceinline__ unsigned pack2(float a, float b) { bf16x2 v; v[0] = (__bf16)a; v[1] = (__bf16)b; return __builtin_bit_cast(unsigned, v); }
__device__ __forceinline__ float rbf(float a) { return (float)(__bf16)a; }
__device__ __forceinline__ unsigned nz(unsigned w) { return max(w, 1u); }
__device__ __forceinline__ float gelu_tanh(float x) {
  const float z = 0.7978845608028654f * (x + 0.044715f * x * x * x);
  const float e = __builtin_amdgcn_exp2f(-2.885390081777927f * z);
  return x * __builtin_amdgcn_rcpf(1.0f + e);
}
template <int CTRL> __device__ __forceinline__ float dpp_add(float v) { return v + u2f(__builtin_amdgcn_update_dpp(0u, f2u(v), CTRL, 0xF, 0xF, true)); }
__device__ __forceinline__ float wave_sum(float v) {
  v = dpp_add<0xB1>(v); v = dpp_add<0x4E>(v); v = dpp_add<0x141>(v); v = dpp_add<0x140>(v);
  auto t = __builtin_amdgcn_permlane16_swap(f2u(v), f2u(v), false, false);
  v = u2f(t[0]) + u2f(t[1]);
  t = __builtin_amdgcn_permlane32_swap(f2u(v), f2u(v), false, false);
  return u2f(t[0]) + u2f(t[1]);
}
#define WAIT_V(n) asm volatile("s_waitcnt vmcnt(" #n ")" ::: "memory")
#define WAIT_L0() asm volatile("s_waitcnt lgkmcnt(0)" ::: "memory")
#define BAR() do { asm volatile("" ::: "memory"); __builtin_amdgcn_s_barrier(); asm volatile("" ::: "memory"); } while (0)
#define LBAR() do { WAIT_L0(); BAR(); } while (0)
template <int N> __device__ __forceinline__ void wait_v() { asm volatile("s_waitcnt vmcnt(%0)" :: "n"(N) : "memory"); }
__device__ __forceinline__ void dma16(__amdgpu_buffer_rsrc_t rs, unsigned voff, unsigned soff, unsigned ldsa) {
  asm volatile("s_mov_b32 m0, %0\n\ts_nop 3\n\tbuffer_load_dwordx4 %1, %2, %3 offen lds" :: "s"(ldsa), "v"(voff), "s"(rs), "s"(soff) : "m0", "memory");
}
__device__ __forceinline__ void dma16x(__amdgpu_buffer_rsrc_t rs, unsigned voff, unsigned soff, unsigned ldsa) {
  asm volatile("s_mov_b32 m0, %0\n\ts_nop 3\n\tbuffer_load_dwordx4 %1, %2, %3 offen sc1 lds" :: "s"(ldsa), "v"(voff), "s"(rs), "s"(soff) : "m0", "memory");
}
__device__ __forceinline__ u32x4 ldx16(__amdgpu_buffer_rsrc_t rs, unsigned voff, unsigned soff) { return __builtin_amdgcn_raw_buffer_load_b128(rs, voff, soff, 16); }
__device__ __forceinline__ u32x2 ldx8(__amdgpu_buffer_rsrc_t rs, unsigned voff, unsigned soff) { return __builtin_amdgcn_raw_buffer_load_b64(rs, voff, soff, 16); }
__device__ __forceinline__ unsigned ldx4(__amdgpu_buffer_rsrc_t rs, unsigned voff, unsigned soff) { return __builtin_amdgcn_raw_buffer_load_b32(rs, voff, soff, 16); }
__device__ __forceinline__ void stx16(__amdgpu_buffer_rsrc_t rs, unsigned voff, unsigned soff, u32x4 v) {
  __builtin_amdgcn_raw_buffer_store_b128(v, rs, voff, soff, 16);
  asm volatile("s_nop 1" :: "v"(v) : "memory");
}
__device__ __forceinline__ void stx8(__amdgpu_buffer_rsrc_t rs, unsigned voff, unsigned soff, u32x2 v) { __builtin_amdgcn_raw_buffer_store_b64(v, rs, voff, soff, 16); }
__device__ __forceinline__ void stx4(__amdgpu_buffer_rsrc_t rs, unsigned voff, unsigned soff, unsigned v) { __builtin_amdgcn_raw_buffer_store_b32(v, rs, voff, soff, 16); }
__device__ __forceinline__ void wait_ge(__amdgpu_buffer_rsrc_t rs, unsigned voff, unsigned soff, unsigned need, unsigned& cap) {
  unsigned t;
  asm volatile("s_nop 4\n\t"
               "1:\n\t"
               "buffer_load_dword %[t], %[vo], %[rs], %[so] offen sc1\n\t"
               "s_waitcnt vmcnt(0)\n\t"
               "v_cmp_gt_u32 vcc, %[need], %[t]\n\t"
               "s_cmp_eq_u64 vcc, 0\n\t"
               "s_cbranch_scc1 2f\n\t"
               "s_sleep 2\n\t"
               "s_sub_u32 %[cap], %[cap], 1\n\t"
               "s_cbranch_scc0 1b\n\t"
               "s_mov_b32 %[cap], 0\n\t"
               "2:"
               : [t] "=&v"(t), [cap] "+s"(cap) : [vo] "v"(voff), [rs] "s"(rs), [so] "s"(soff), [need] "s"(need) : "vcc", "scc", "memory");
}
__device__ __forceinline__ void wait_seen(__amdgpu_buffer_rsrc_t rs, unsigned voff, unsigned soff, unsigned need, unsigned& seen, unsigned& cap) {
  if (seen >= need) return;
  unsigned t;
  asm volatile("s_nop 4\n\t"
               "1:\n\t"
               "buffer_load_dword %[t], %[vo], %[rs], %[so] offen sc1\n\t"
               "s_waitcnt vmcnt(0)\n\t"
               "v_cmp_gt_u32 vcc, %[need], %[t]\n\t"
               "s_cbranch_vccz 2f\n\t"
               "s_sleep 1\n\t"
               "s_sub_u32 %[cap], %[cap], 1\n\t"
               "s_cbranch_scc0 1b\n\t"
               "s_mov_b32 %[cap], 0\n\t"
               "2:"
               : [t] "=&v"(t), [cap] "+s"(cap) : [vo] "v"(voff), [rs] "s"(rs), [so] "s"(soff), [need] "s"(need) : "vcc", "scc", "memory");
  seen = min((unsigned)__builtin_amdgcn_readlane(t, 0), (unsigned)__builtin_amdgcn_readlane(t, 1));
}
__device__ __forceinline__ __amdgpu_buffer_rsrc_t mk_rs(const void* p, unsigned bytes) { return __builtin_amdgcn_make_buffer_rsrc((void*)p, 0, (int)bytes, 0x00020000); }
#define SCHED() __builtin_amdgcn_sched_barrier(0)
__device__ __forceinline__ unsigned now32() { unsigned long long t; asm volatile("s_memtime %0\n\ts_waitcnt lgkmcnt(0)" : "=s"(t) :: "memory"); return (unsigned)t; }
__device__ __forceinline__ unsigned rt32() { unsigned long long t; asm volatile("s_memrealtime %0\n\ts_waitcnt lgkmcnt(0)" : "=s"(t) :: "memory"); return (unsigned)t; }
__device__ __forceinline__ void wait_until(unsigned t) { while ((int)(rt32() - t) < 0) {} }
template <int X = 0> __device__ __forceinline__ LAS unsigned char* lad(unsigned v, unsigned u) {
  unsigned a;
  if (X == 0) asm volatile("v_add_u32 %0, %1, %2" : "=v"(a) : "s"(u), "v"(v));
  else asm volatile("v_xor_b32 %0, %3, %2\n\tv_add_u32 %0, %1, %0" : "=&v"(a) : "s"(u), "v"(v), "n"(X));
  return (LAS unsigned char*)(size_t)a;
}
__device__ __forceinline__ bf16x4 trread(LAS unsigned char* p) { return __builtin_amdgcn_ds_read_tr16_b64_v4bf16((LAS bf16x4*)p); }
__device__ __forceinline__ bf16x8 cat8(bf16x4 a, bf16x4 b) { return __builtin_shufflevector(a, b, 0, 1, 2, 3, 4, 5, 6, 7); }
__device__ __forceinline__ bf16x16 cat16(bf16x8 a, bf16x8 b) { return __builtin_shufflevector(a, b, 0, 1, 2, 3, 4, 5, 6, 7, 8, 9, 10, 11, 12, 13, 14, 15); }
__device__ __forceinline__ f32x4 mfma32(bf16x8 a, bf16x8 b, f32x4 c) { return __builtin_amdgcn_mfma_f32_16x16x32_bf16(a, b, c, 0, 0, 0); }
__device__ __forceinline__ f32x4 mfma16(u32x2 a, u32x2 b, f32x4 c) {
  return mfma32(__builtin_bit_cast(bf16x8, u32x4{a[0], a[1], 0u, 0u}), __builtin_bit_cast(bf16x8, u32x4{b[0], b[1], 0u, 0u}), c);
}
__device__ __forceinline__ f32x4 mfma16n(u32x2 a, u32x2 b, f32x4 c) { return __builtin_amdgcn_mfma_f32_16x16x16bf16_1k(__builtin_bit_cast(s16x4, a), __builtin_bit_cast(s16x4, b), c, 0, 0, 0); }
__device__ __forceinline__ f32x4 smf(bf16x8 a, bf16x16 b, f32x4 c, int idx) { return __builtin_amdgcn_smfmac_f32_16x16x64_bf16(a, b, c, idx, 0, 0); }

__device__ __forceinline__ void ln_row(float (&x)[16]) {
  float s = 0.f;
#pragma unroll
  for (int i = 0; i < 16; ++i) s += x[i];
  const float mean = wave_sum(s) * (1.0f / 1024.0f);
  float q = 0.f;
#pragma unroll
  for (int i = 0; i < 16; ++i) { const float d = x[i] - mean; q += d * d; }
  const float var = wave_sum(q) * (1.0f / 1024.0f);
  const float m = rbf(mean), isd = __builtin_amdgcn_rcpf(rbf(__builtin_amdgcn_sqrtf(rbf(rbf(var) + 1e-5f))));
#pragma unroll
  for (int i = 0; i < 16; ++i) x[i] = rbf(rbf(x[i] - m) * isd);
}
__device__ __forceinline__ void load_row16(const u16* rowp, int lane, float (&x)[16]) {
  const u32x4 a = *(const u32x4*)(rowp + lane * 8), b = *(const u32x4*)(rowp + 512 + lane * 8);
#pragma unroll
  for (int i = 0; i < 4; ++i) { x[2 * i] = lo2f(a[i]); x[2 * i + 1] = hi2f(a[i]); x[8 + 2 * i] = lo2f(b[i]); x[9 + 2 * i] = hi2f(b[i]); }
}
__device__ __forceinline__ void store_row16(u16* rowp, int lane, const float (&x)[16]) {
  u32x4 a, b;
#pragma unroll
  for (int i = 0; i < 4; ++i) { a[i] = pack2(x[2 * i], x[2 * i + 1]); b[i] = pack2(x[8 + 2 * i], x[9 + 2 * i]); }
  *(u32x4*)(rowp + lane * 8) = a; *(u32x4*)(rowp + 512 + lane * 8) = b;
}
__device__ __forceinline__ void modulate(float (&x)[16], const float* sc, const float* sh, int lane) {
#pragma unroll
  for (int hh = 0; hh < 2; ++hh)
#pragma unroll
    for (int j = 0; j < 2; ++j) {
      const f32x4 c = *(const f32x4*)(sc + hh * 512 + lane * 8 + j * 4), t = *(const f32x4*)(sh + hh * 512 + lane * 8 + j * 4);
#pragma unroll
      for (int i = 0; i < 4; ++i) { float& v = x[hh * 8 + j * 4 + i]; v = rbf(rbf(v * rbf(1.0f + c[i])) + t[i]); }
    }
}

__host__ __device__ __forceinline__ int mem_slot(int dl) {
  return ((dl >> 2) & 1) * 32 + (((dl >> 5) & 1) * 2 + ((dl >> 3) & 1)) * 8 + (((dl >> 4) & 1) * 2 + (dl & 1)) * 2 + ((dl >> 1) & 1);
}

__global__ void __launch_bounds__(512) k_prep(const u16* __restrict__ c, const u16* __restrict__ wmod, const u16* __restrict__ mem0, unsigned char* ws) {
  __shared__ float red[128][33];
  __shared__ float sc[1024];
  const int b = blockIdx.x, tid = threadIdx.x;
  {
    u32x4* z = (u32x4*)ws;
    const u32x4 zero = {0u, 0u, 0u, 0u};
    for (size_t i = (size_t)16384 / 16 + (size_t)b * 512 + tid; i < X_BYTES / 16; i += (size_t)256 * 512) z[i] = zero;
    if (b >= 192 && b < 208) {
      u32x4* y = (u32x4*)(ws + WS_H1S + ((b & 8) ? 64 * 65536 : 0));
      y[(b & 7) * 512 + tid] = zero;
    }
  }
  if (b == 255) {
    for (int d = tid; d < 1024; d += 512) {
      u32x4 v;
#pragma unroll
      for (int i = 0; i < 4; ++i) v[i] = nz((unsigned)mem0[(2 * i) * 1024 + d] | ((unsigned)mem0[(2 * i + 1) * 1024 + d] << 16));
      *(u32x2*)(ws + X_MEM + (d >> 6) * 1024 + mem_slot(d & 63) * 8) = u32x2{v[0], v[1]};
      *(u32x2*)(ws + X_MEM + (d >> 6) * 1024 + 512 + (mem_slot(d & 63) ^ 16) * 8) = u32x2{v[2], v[3]};
    }
  }
  if (b < 192) {
    for (int i = tid; i < 1024; i += 512) { const float v = bf2f(c[i]); sc[i] = rbf(v / (1.0f + __expf(-v))); }
    __syncthreads();
    const int kg = tid >> 2, c8 = tid & 3;
    float acc[8] = {0.f, 0.f, 0.f, 0.f, 0.f, 0.f, 0.f, 0.f};
    u32x4 w[8];
#pragma unroll
    for (int i = 0; i < 8; ++i) w[i] = *(const u32x4*)(wmod + (size_t)(kg + 128 * i) * 6144 + b * 32 + c8 * 8);
#pragma unroll
    for (int i = 0; i < 8; ++i) {
      const float s = sc[kg + 128 * i];
#pragma unroll
      for (int j = 0; j < 4; ++j) { acc[2 * j] += s * lo2f(w[i][j]); acc[2 * j + 1] += s * hi2f(w[i][j]); }
    }
#pragma unroll
    for (int j = 0; j < 8; ++j) red[kg][c8 * 8 + j] = acc[j];
    __syncthreads();
    if (tid < 256) {
      const int col = tid & 31, part = tid >> 5;
      float s = 0.f;
#pragma unroll
      for (int i = 0; i < 16; ++i) s += red[part * 16 + i][col];
      __syncthreads();
      red[part][col] = s;
    } else __syncthreads();
    __syncthreads();
    if (tid < 32) {
      float s = 0.f;
#pragma unroll
      for (int i = 0; i < 8; ++i) s += red[i][tid];
      ((float*)(ws + WS_MOD))[b * 32 + tid] = rbf(s);
    }
  }
}

#define WST(rs, wk, i)
struct MainP { const u16 *x, *Wq, *Wk, *Wv, *Wo, *w1, *w2, *w3, *w4; u16* out; unsigned char* ws; int d1, d2, d3, d4; };
constexpr unsigned SPIN_CAP = 1u << 18;

__device__ __forceinline__ float gmax(float v) {
  auto t = __builtin_amdgcn_permlane16_swap(f2u(v), f2u(v), false, false);
  v = fmaxf(u2f(t[0]), u2f(t[1]));
  t = __builtin_amdgcn_permlane32_swap(f2u(v), f2u(v), false, false);
  return fmaxf(u2f(t[0]), u2f(t[1]));
}
__device__ __forceinline__ float gsum(float v) {
  auto t = __builtin_amdgcn_permlane16_swap(f2u(v), f2u(v), false, false);
  v = u2f(t[0]) + u2f(t[1]);
  t = __builtin_amdgcn_permlane32_swap(f2u(v), f2u(v), false, false);
  return u2f(t[0]) + u2f(t[1]);
}
__device__ __forceinline__ void publish(__amdgpu_buffer_rsrc_t rs, unsigned word, unsigned val) {
  WAIT_V(0); BAR();
  if (threadIdx.x == 0) stx4(rs, 0, word, val);
}

constexpr int P_S2 = 0, P_S8 = 32, P_S7 = 64, P_S4 = 96, P_S5 = 104, P_S1 = 112, P_S3 = 144, P_PJ = 240;
constexpr int NLN = 160;

enum { SG_WO = 0, SG_W1 = 1, SG_W2 = 2, SG_W3 = 3, SG_W4 = 4, SG_K = 5, SG_V = 6, SG_Q = 7 };
constexpr int G_XCH = 131072, G_K = G_XCH + 16384, G_CTL = G_K + 8192;
__device__ __forceinline__ f32x16 mfma3232(bf16x8 a, bf16x8 b, f32x16 c) { return __builtin_amdgcn_mfma_f32_32x32x16_bf16(a, b, c, 0, 0, 0); }
__device__ __forceinline__ void widen32(const f32x16& a, u32x4 (&v)[2]) {
#pragma unroll
  for (int u = 0; u < 2; ++u) {
    const auto r0 = __builtin_amdgcn_permlane32_swap(pack2(a[8 * u], a[8 * u + 1]), pack2(a[8 * u + 4], a[8 * u + 5]), false, false);
    const auto r1 = __builtin_amdgcn_permlane32_swap(pack2(a[8 * u + 2], a[8 * u + 3]), pack2(a[8 * u + 6], a[8 * u + 7]), false, false);
    v[u] = u32x4{r0[0], r1[0], r0[1], r1[1]};
  }
}
__device__ __forceinline__ float hsum(float v) { const auto t = __builtin_amdgcn_permlane32_swap(f2u(v), f2u(v), false, false); return u2f(t[0]) + u2f(t[1]); }

__device__ __forceinline__ void attend(__amdgpu_buffer_rsrc_t rs, LAS unsigned char* qb, LAS unsigned char* kb, int seg, int h, int t, int eh, int lane, const u32x4 (&vv)[4],
                                       unsigned& chs, unsigned& cap) {
  const int fr = lane & 15, g = lane >> 4;
  const unsigned l16 = lane * 16, ub = (unsigned)(seg * NH + h);
  f32x4 s0 = {0.f, 0.f, 0.f, 0.f}, s1 = s0, s2 = s0;
  bf16x8 q[4];
#pragma unroll
  for (int ks = 0; ks < 4; ++ks) {
    q[ks] = *(const LAS bf16x8*)(qb + ks * 4096);
    s1 = mfma32(*(const LAS bf16x8*)(kb + ks * 1024), q[ks], s1); s2 = mfma32(*(const LAS bf16x8*)(kb + 4096 + ks * 1024), q[ks], s2);
  }
  const float scale = 0.08838834764831845f, L2E = 1.4426950408889634f;
  float a[4], b[4], c[4], mt = -3.0e38f, lt = 0.f;
#pragma unroll
  for (int r = 0; r < 4; ++r) {
    const int j = 4 * g + r;
    b[r] = (t == 1 || j <= fr) ? rbf(rbf(s1[r]) * scale) : -3.0e38f;
    c[r] = (t == 1 && j <= fr) ? rbf(rbf(s2[r]) * scale) : -3.0e38f;
    mt = fmaxf(mt, fmaxf(b[r], c[r]));
  }
  mt = gmax(mt);
#pragma unroll
  for (int r = 0; r < 4; ++r) { b[r] = __builtin_amdgcn_exp2f((b[r] - mt) * L2E); c[r] = __builtin_amdgcn_exp2f((c[r] - mt) * L2E); lt += b[r] + c[r]; }
  const u32x4 pt = {pack2(b[0], b[1]), pack2(b[2], b[3]), pack2(c[0], c[1]), pack2(c[2], c[3])};
  f32x4 o[4];
#pragma unroll
  for (int i = 0; i < 4; ++i) o[i] = mfma32(__builtin_bit_cast(bf16x8, vv[i]), __builtin_bit_cast(bf16x8, pt), f32x4{0.f, 0.f, 0.f, 0.f});
  lt = gsum(lt);
  const unsigned em = (unsigned)(fr >> 3) * 512 + (unsigned)g * 128 + (unsigned)(8 * ((fr >> 2) & 1) + (fr & 3)) * 8;
  asm volatile("" : "+v"(o[0]), "+v"(o[1]), "+v"(o[2]), "+v"(o[3]), "+v"(lt));
  wait_seen(rs, 0, F_CH + h * 128, (unsigned)seg + 1u, chs, cap);
  u32x2 vmf[4];
  for (;;) {
    asm volatile("" ::: "memory");
    unsigned mn = 0xFFFFFFFFu;
    bf16x8 kmf[4];
#pragma unroll
    for (int ks = 0; ks < 4; ++ks) kmf[ks] = __builtin_bit_cast(bf16x8, ldx16(rs, l16, WS_KM + ub * 4096 + ks * 1024));
#pragma unroll
    for (int i = 0; i < 4; ++i) vmf[i] = ldx8(rs, em, X_VM + ub * 4096 + (2 * eh + (i >> 1)) * 1024 + (i & 1) * 32);
    s0 = f32x4{0.f, 0.f, 0.f, 0.f};
#pragma unroll
    for (int ks = 0; ks < 4; ++ks) s0 = mfma32(kmf[ks], q[ks], s0);
#pragma unroll
    for (int i = 0; i < 4; ++i) mn = min(mn, min(vmf[i][0], vmf[i][1]));
    if (__all(mn != 0u) || cap == 0u) break;
    cap = __builtin_amdgcn_readfirstlane(cap - 1u);
  }
  cap = __builtin_amdgcn_readfirstlane(cap);
#pragma unroll
  for (int r = 0; r < 4; ++r) a[r] = rbf(rbf(s0[r]) * scale);
  const float mx = fmaxf(mt, gmax(fmaxf(fmaxf(a[0], a[1]), fmaxf(a[2], a[3]))));
#pragma unroll
  for (int r = 0; r < 4; ++r) a[r] = __builtin_amdgcn_exp2f((a[r] - mx) * L2E);
  const u32x2 pm = {pack2(a[0], a[1]), pack2(a[2], a[3])};
  f32x4 om[4];
#pragma unroll
  for (int i = 0; i < 4; ++i) om[i] = mfma16n(vmf[i], pm, f32x4{0.f, 0.f, 0.f, 0.f});
  const float al = __builtin_amdgcn_exp2f((mt - mx) * L2E);
  const float inv = __builtin_amdgcn_rcpf(fmaf(al, lt, 0.5f * gsum((a[0] + a[1]) + (a[2] + a[3])))), ai = al * inv;
#pragma unroll
  for (int i = 0; i < 4; ++i) o[i] = o[i] * ai + om[i] * inv;
#pragma unroll
  for (int u = 0; u < 2; ++u)
    stx16(rs, l16, WS_O + (unsigned)seg * 262144 + (unsigned)((h * 4 + 2 * eh + u) * 2 + t) * 1024,
          u32x4{pack2(o[2 * u][0], o[2 * u][1]), pack2(o[2 * u][2], o[2 * u][3]), pack2(o[2 * u + 1][0], o[2 * u + 1][1]), pack2(o[2 * u + 1][2], o[2 * u + 1][3])});
}

template <int SG>
__device__ __forceinline__ void stage_gemm(LAS unsigned char* lds, __amdgpu_buffer_rsrc_t rs, const u16* Wg, int wk, int kq, int cb, int pid, unsigned& cap) {
  constexpr bool KV = (SG == SG_K || SG == SG_V), PROJ = KV || SG == SG_Q, ROWM = (SG == SG_W1 || PROJ);
  constexpr int KS = (SG == SG_W4) ? 28 : 32;
  constexpr int LDW = PROJ ? HD : (SG == SG_W3) ? FF : D_;
  constexpr int WROWS = (SG == SG_WO) ? HD : (SG == SG_W4 ? FF : D_);
  constexpr int NBLK = KV ? 32 : (SG == SG_WO || SG == SG_W1 || SG == SG_Q) ? 64 : 65;
  constexpr unsigned ABASE = PROJ ? WS_H : SG == SG_WO ? WS_O : SG == SG_W1 ? WS_G : SG == SG_W2 ? WS_H1S : SG == SG_W3 ? WS_X2 : WS_U;
  constexpr unsigned ASTR = SG == SG_WO ? 262144u : SG == SG_W4 ? 229376u : 65536u;
  constexpr int NPC = KS / 4;
  const unsigned myword = F_ST + (unsigned)pid * 128u;
  int tid = threadIdx.x; asm volatile("" : "+v"(tid));
  const int lane = tid & 63, w = __builtin_amdgcn_readfirstlane(tid >> 6), row = lane & 31, hf = lane >> 5;
  const int kh = w >> 2, wl = w & 3;
  const int koff = PROJ ? 0 : kq;
  const unsigned lbase = (unsigned)(size_t)lds, l16 = lane * 16;
  const int col0 = cb * 128 + wl * 32;
  bf16x8 Wf[KS];
  {
    const __amdgpu_buffer_rsrc_t rsW = mk_rs(Wg, (unsigned)WROWS * LDW * 2u);
    const int ub = lane >> 3, tu = 4 * hf + ((lane & 15) >> 2);
    const unsigned vB = (unsigned)(((ub & 3) + 8 * (ub >> 2)) * LDW + 16 * (((lane >> 1) & 3) ^ (ub >> 1)) + 8 * (lane & 1)) * 2u;
    const unsigned sB = (unsigned)((koff * KS * 32 + 32 * (w >> 2) + 16 * ((w >> 1) & 1) + 4 * (w & 1)) * LDW + cb * 128) * 2u;
    const int boff = kh * 65536 + (wl >> 1) * 8192 + tu * 128 + (((tu >> 1) ^ (2 * (wl & 1) + ((lane >> 4) & 1))) << 5) + (lane & 3) * 8;
#pragma unroll
    for (int ps = 0; ps < 2; ++ps) {
      BAR();
#pragma unroll
      for (int hh = 0; hh < 2; ++hh)
#pragma unroll
        for (int i = 0; i < 4; ++i)
          if (4 * ps + i < NPC) {
            const unsigned so = sB + (unsigned)((hh * NPC + 4 * ps + i) * 64 * LDW) * 2u, d = lbase + hh * 65536 + i * 16384 + w * 1024;
            dma16(rsW, vB, so, d); dma16(rsW, vB, so + 128, d + 8192);
          }
      WAIT_V(0); BAR();
#pragma unroll
      for (int i = 0; i < 4; ++i)
        if (4 * ps + i < NPC) {
#pragma unroll
          for (int k = 0; k < 4; ++k) { LAS unsigned char* q = lds + (boff + i * 16384 + k * 2048); Wf[(4 * ps + i) * 4 + k] = cat8(trread(q), trread(q + 1024)); }
        }
      WAIT_L0();
    }
    if (tid < 8) *(LAS unsigned*)(lds + G_CTL + tid * 4) = 0u;
    LBAR();
  }
  const f32x16 zero16 = {0.f, 0.f, 0.f, 0.f, 0.f, 0.f, 0.f, 0.f, 0.f, 0.f, 0.f, 0.f, 0.f, 0.f, 0.f, 0.f};
  auto put16 = [&](LAS unsigned char* p, const f32x16& a) {
#pragma unroll
    for (int q = 0; q < 4; ++q) *(LAS f32x4*)(p + q * 1024) = f32x4{a[4 * q], a[4 * q + 1], a[4 * q + 2], a[4 * q + 3]};
  };
  if (SG == SG_W3) {
    const u32x4 one4 = {0x3F803F80u, 0x3F803F80u, 0x3F803F80u, 0x3F803F80u};
    f32x16 cs = zero16;
#pragma unroll
    for (int k = 0; k < KS; ++k) cs = mfma3232(Wf[k], __builtin_bit_cast(bf16x8, one4), cs);
    if (kh == 0) put16(lds + wl * 4096 + l16, cs);
    LBAR();
    if (kh == 1 && row == 0) {
#pragma unroll
      for (int q = 0; q < 4; ++q)
        *(LAS f32x4*)(lds + G_K + wl * 128 + hf * 64 + q * 16) = *(const LAS f32x4*)(lds + wl * 4096 + q * 1024 + l16) + f32x4{cs[4 * q], cs[4 * q + 1], cs[4 * q + 2], cs[4 * q + 3]};
    }
    LBAR();
  }
  auto blk = [&](int i) -> int { return KV ? 2 * i + kq : i; };
  const unsigned aoff = ROWM ? (unsigned)(row * 128 + ((hf ^ ((row >> 1) & 7)) << 4)) : (unsigned)((row >> 4) * 1024 + hf * 256 + (row & 15) * 16);
  const unsigned vA = ROWM ? (unsigned)((lane >> 3) * 2048 + (((lane & 7) ^ ((4 * wl + (lane >> 4)) & 7)) << 4)) : l16;
  auto fetch = [&](int hb) {
    const int i = hb >> 1, hh = hb & 1;
    const unsigned so = ABASE + (unsigned)blk(i) * ASTR + (unsigned)koff * (KS * 2048) + (ROWM ? wl * 16384 + hh * 1024 : (hh * KS + wl) * 1024);
#pragma unroll
    for (int q = 0; q < NPC; ++q) dma16x(rs, vA, so + q * (ROWM ? 128 : 4096), lbase + (i & 1) * 65536 + hh * 32768 + wl * 1024 + q * 4096);
  };
  auto products = [&](int i, f32x16& acc, auto&& early, auto&& handover) {
    const unsigned ub = lbase + (i & 1) * 65536 + kh * 32768;
    LAS unsigned char* ab[4];
    ab[0] = lad(aoff, ub);
    if (ROWM) { ab[1] = lad<32>(aoff, ub); ab[2] = lad<64>(aoff, ub); ab[3] = lad<96>(aoff, ub); }
    constexpr int PD = 10;
    bf16x8 af[PD + 1];
    auto rd = [&](int k) { af[k % (PD + 1)] = ROWM ? *(const LAS bf16x8*)(ab[k & 3] + (k >> 2) * 4096) : *(const LAS bf16x8*)(ab[0] + (k >> 1) * 2048 + (k & 1) * 512); };
#pragma unroll
    for (int k = 0; k < PD; ++k) rd(k);
    SCHED();
#pragma unroll
    for (int k = 0; k < KS; ++k) {
      if (k + PD < KS) rd(k + PD);
      acc = SG == SG_V ? mfma3232(af[k % (PD + 1)], Wf[k], k ? acc : zero16) : mfma3232(Wf[k], af[k % (PD + 1)], k ? acc : zero16);
      SCHED();
      if (k == KS - 10) { early(); SCHED(); }
      if (k == KS - 5) { handover(); SCHED(); }
    }
    asm volatile("" : "+v"(acc));
  };
  WST(rs, wk, KV ? 2 : 10);
#define SEG(x)
  if (kh == 0) {
    auto pvo = [&](int i) -> unsigned {
      const int b = blk(i);
      if (PROJ) return F_LN + (unsigned)((b % (NLN / 4)) * 4 + (lane & 3)) * 128u;
      if (SG == SG_WO) return F_ST + (unsigned)(P_S1 + kq * 8 + (lane & 7)) * 128u;
      if (SG == SG_W1) return F_ST + (unsigned)(P_S3 + (b % 3) * 32 + (lane & 31)) * 128u;
      if (SG == SG_W2) return F_ST + (unsigned)(P_S4 + (lane & 7)) * 128u;
      if (SG == SG_W3) return F_ST + (unsigned)(P_S5 + (lane & 7)) * 128u;
      return F_ST + (unsigned)(P_S7 + kq * 7 + min(lane & 7, 6)) * 128u;
    };
    auto need = [&](int i) -> unsigned { return PROJ ? 1u : (SG == SG_W2 && i == 64) ? 64u : (unsigned)i + 1u; };
    int R = 0, Rp = 0;
    for (int i = 0; i < NBLK; ++i) {
      if (R <= i) { wait_ge(rs, pvo(i), 0, need(i), cap); fetch(2 * i); fetch(2 * i + 1); }
      const unsigned pk = w == 0 ? (lane < 32 ? pvo(i + 1) : pvo(i + 2)) : 0xF0000000u;
      if (R <= i + 1) wait_v<NPC>(); else wait_v<2 * NPC>();
      BAR();
      SEG(1);
      f32x16 acc;
      unsigned peek;
      asm volatile("buffer_load_dword %0, %1, %2, 0 offen sc1" : "=v"(peek) : "v"(pk), "s"(rs) : "memory");
      products(i, acc, []() {}, [&]() {
        SEG(4);
        WAIT_V(0);
        SEG(0);
        asm volatile("" : "+v"(peek));
        if (w == 0) {
          const unsigned long long ok = __ballot(peek >= (lane < 32 ? need(i + 1) : need(i + 2)));
          const bool r1 = (R >= i + 2 || (unsigned)ok == 0xFFFFFFFFu) && i + 1 < NBLK, r2 = r1 && (unsigned)(ok >> 32) == 0xFFFFFFFFu && i + 2 < NBLK;
          if (lane == 0) *(volatile LAS unsigned*)(lds + G_CTL + 4) = (unsigned)(i + 1 + (r1 ? 1 : 0) + (r2 ? 1 : 0));
        }
        LBAR();
      });
      SEG(2);
      const unsigned rv = *(const volatile LAS unsigned*)(lds + G_CTL + 4);
      put16(lad(l16, lbase + G_XCH + wl * 4096), acc);
      asm volatile("" ::: "memory");
      if (lane == 0) *(volatile LAS unsigned*)(lds + G_CTL + 16 + wl * 4) = (unsigned)i + 1u;
      Rp = R; R = __builtin_amdgcn_readfirstlane(rv);
      SEG(3);
      if (R >= i + 2 && Rp < i + 2) { fetch(2 * i + 2); fetch(2 * i + 3); }
      if (R >= i + 3) fetch(2 * i + 4);
      SEG(5);
    }
    BAR();
  } else {
    const int c8 = col0 + 8 * hf;
    bool pending = false;
    unsigned kvs[2] = {0u, 0u}, chs = 0u;
    BAR();
    for (int i = 0; i < NBLK; ++i) {
      const int b = blk(i);
      if (SG == SG_Q) {
        if (i & 1) wait_seen(rs, (unsigned)(lane & 1) * 4096, F_ST + (unsigned)(P_PJ + 64 + cb) * 128, (unsigned)i + 1u, kvs[1], cap);
        else wait_seen(rs, (unsigned)(lane & 1) * 4096, F_ST + (unsigned)(P_PJ + cb) * 128, (unsigned)i + 1u, kvs[0], cap);
      }
      BAR();
      SEG(0);
      const unsigned rv = *(const volatile LAS unsigned*)(lds + G_CTL + 4);
      u32x4 res[2], st[8], vv[4];
      if (SG == SG_W2) {
        const int t = 32 * b - 2 + row;
        const unsigned vo = (t >= 0 && t < S_) ? (unsigned)(t * D_ + c8) * 2u : 0xF0000000u;
        res[0] = ldx16(rs, vo, WS_H2); res[1] = ldx16(rs, vo, WS_H2 + 32);
      }
      if (SG == SG_Q) {
        const unsigned ub = (unsigned)(b * NH + cb) * 8192;
        dma16x(rs, l16, WS_K + ub + wl * 2048, lbase + G_K + wl * 2048); dma16x(rs, l16, WS_K + ub + wl * 2048 + 1024, lbase + G_K + wl * 2048 + 1024);
        const unsigned ev = (unsigned)((lane >> 3) & 1) * 1024 + (unsigned)((lane >> 4) * 16 + 8 * ((lane >> 2) & 1) + (lane & 3)) * 16;
#pragma unroll
        for (int q = 0; q < 4; ++q) vv[q] = ldx16(rs, ev, WS_VT + ub + (2 * (wl >> 1) + (q >> 1)) * 2048 + (q & 1) * 64);
      }
      f32x16 acc;
      int R = 0;
      LAS unsigned char* xr = lad(l16, lbase + G_XCH + wl * 4096);
      f32x4 xa[4];
      unsigned xf;
      auto take = [&]() {
        asm volatile("" ::: "memory");
        xf = *(const volatile LAS unsigned*)(lds + G_CTL + 16 + wl * 4);
        asm volatile("" ::: "memory");
#pragma unroll
        for (int q = 0; q < 4; ++q) xa[q] = *(const LAS f32x4*)(xr + q * 1024);
        asm volatile("" ::: "memory");
      };
      products(i, acc, [&]() {
        if (SG == SG_W3) {
#pragma unroll
          for (int q = 0; q < 8; ++q) st[q] = ldx16(rs, l16, WS_ST5 + (unsigned)b * 8192 + q * 1024);
        }
        take();
      }, [&]() {
        SEG(5);
        R = __builtin_amdgcn_readfirstlane(rv);
        if (R >= i + 2) {
          if (SG == SG_W2) wait_v<2>(); else if (SG == SG_W3) wait_v<8>(); else wait_v<0>();
          LBAR();
          if (pending && tid == 256) stx4(rs, 0, myword, (unsigned)blk(i - 1) + 1u);
        }
      });
      const bool more = R >= i + 2;
      SEG(1);
      while (__builtin_amdgcn_readfirstlane(xf) <= (unsigned)i) take();
#pragma unroll
      for (int q = 0; q < 16; ++q) acc[q] += xa[q >> 2][q & 3];
      SEG(2);
      const unsigned fo = (unsigned)((col0 >> 5) * 2048 + hf * 256), ro = (unsigned)((row >> 4) * 1024 + (row & 15) * 16);
      u32x4 v[2];
      if (SG == SG_W3) {
        float ms = 0.f;
#pragma unroll
        for (int q = 0; q < 8; ++q) ms += u2f(st[q][0]) + u2f(st[q][2]);
        const float mean = hsum(ms) * (1.0f / 32.0f);
        float q2 = 0.f;
#pragma unroll
        for (int q = 0; q < 8; ++q) { const float d0 = u2f(st[q][0]) - mean, d1 = u2f(st[q][2]) - mean; q2 += u2f(st[q][1]) + u2f(st[q][3]) + 32.0f * (d0 * d0 + d1 * d1); }
        const float var = hsum(q2) * (1.0f / 1024.0f);
        const float m = rbf(mean), isd = __builtin_amdgcn_rcpf(rbf(__builtin_amdgcn_sqrtf(rbf(rbf(var) + 1e-5f))));
#pragma unroll
        for (int q = 0; q < 4; ++q) {
          const f32x4 cs = *(const LAS f32x4*)(lds + G_K + wl * 128 + hf * 64 + q * 16);
#pragma unroll
          for (int e = 0; e < 4; ++e) acc[4 * q + e] = (acc[4 * q + e] - m * cs[e]) * isd;
        }
      }
      if (SG != SG_V) widen32(acc, v);
      if (SG == SG_W1 || SG == SG_W3) {
#pragma unroll
        for (int q = 0; q < 8; ++q) v[q >> 2][q & 3] = pack2(gelu_tanh(lo2f(v[q >> 2][q & 3])), gelu_tanh(hi2f(v[q >> 2][q & 3])));
      }
      if (SG == SG_WO || SG == SG_W4) {
        const unsigned so = (SG == SG_WO ? WS_P2 : WS_P8) + (unsigned)(b * 4 + kq) * 65536u, vo = (unsigned)(row * D_ + c8) * 2u;
        stx16(rs, vo, so, v[0]); stx16(rs, vo, so + 32, v[1]);
      } else if (SG == SG_K) {
        const unsigned so = WS_K + (unsigned)(b * NH + cb) * 8192 + wl * 1024, vo = (unsigned)((row >> 4) * 4096 + hf * 256 + (row & 15) * 16);
        stx16(rs, vo, so, v[0]); stx16(rs, vo, so + 512, v[1]);
      } else if (SG == SG_V) {
        const unsigned so = WS_VT + (unsigned)(b * NH + cb) * 8192 + wl * 2048, vo = ro + hf * 256;
#pragma unroll
        for (int u = 0; u < 2; ++u)
          stx16(rs, vo, so + u * 512, u32x4{pack2(acc[4 * u], acc[4 * u + 1]), pack2(acc[4 * u + 2], acc[4 * u + 3]), pack2(acc[8 + 4 * u], acc[9 + 4 * u]), pack2(acc[10 + 4 * u], acc[11 + 4 * u])});
      } else if (SG == SG_Q) {
        LAS unsigned char* qw = lds + G_XCH + wl * 4096 + (row >> 4) * 1024 + hf * 256 + (row & 15) * 16;
        *(LAS u32x4*)(qw) = v[0]; *(LAS u32x4*)(qw + 512) = v[1];
        WAIT_V(0);
        if (lane == 0) __hip_atomic_fetch_add((LAS unsigned*)(lds + G_CTL + 8), 1u, __ATOMIC_RELAXED, __HIP_MEMORY_SCOPE_WORKGROUP);
        while (__builtin_amdgcn_readfirstlane(*(const volatile LAS unsigned*)(lds + G_CTL + 8)) < 4u * (unsigned)i + 4u) {}
        asm volatile("" ::: "memory");
        if (R >= i + 3) fetch(2 * i + 5);
        attend(rs, lds + G_XCH + (wl & 1) * 1024 + l16, lds + G_K + l16, b, cb, wl & 1, wl >> 1, lane, vv, chs, cap);
      } else if (SG == SG_W1) {
        const int grp = cb >> 1, sh = (grp & 1) ? -(1 + (grp >> 1)) : (1 + (grp >> 1));
        const int rho = 32 * b + row + sh + 2;
        const unsigned vo = (rho >= 2 && rho < S_ + 2) ? (unsigned)(rho >> 5) * 65536u + fo + (unsigned)((rho >> 4) & 1) * 1024u + (unsigned)(rho & 15) * 16u : 0xF0000000u;
        stx16(rs, vo, WS_H1S, v[0]); stx16(rs, vo, WS_H1S + 512, v[1]);
      } else if (SG == SG_W2) {
        float z[16], zs = 0.f;
#pragma unroll
        for (int q = 0; q < 8; ++q) {
          const unsigned x = pack2(lo2f(res[q >> 2][q & 3]) + lo2f(v[q >> 2][q & 3]), hi2f(res[q >> 2][q & 3]) + hi2f(v[q >> 2][q & 3]));
          v[q >> 2][q & 3] = x; z[2 * q] = lo2f(x); z[2 * q + 1] = hi2f(x); zs += z[2 * q] + z[2 * q + 1];
        }
        stx16(rs, fo + ro, WS_X2 + (unsigned)b * 65536u, v[0]); stx16(rs, fo + ro, WS_X2 + (unsigned)b * 65536u + 512, v[1]);
        const float mw = hsum(zs) * (1.0f / 32.0f);
        float q2 = 0.f;
#pragma unroll
        for (int q = 0; q < 16; ++q) { const float d = z[q] - mw; q2 += d * d; }
        q2 = hsum(q2);
        if (hf == 0) stx8(rs, (unsigned)((cb * 2 + (wl >> 1)) * 512 + row * 16 + (wl & 1) * 8), WS_ST5 + (unsigned)b * 8192, u32x2{f2u(mw), f2u(q2)});
      } else {
        stx16(rs, fo + ro, WS_U + (unsigned)b * 229376u, v[0]); stx16(rs, fo + ro, WS_U + (unsigned)b * 229376u + 512, v[1]);
      }
      SEG(3);
      pending = more && i >= 2 && SG != SG_Q;
      if (!pending) {
        WAIT_V(0);
        if (lane == 0) {
          const unsigned c = __hip_atomic_fetch_add((LAS unsigned*)(lds + G_CTL), 1u, __ATOMIC_RELAXED, __HIP_MEMORY_SCOPE_WORKGROUP);
          if ((c & 3u) == 3u) stx4(rs, 0, myword, (unsigned)b + 1u);
        }
      }
      if (SG != SG_Q && R >= i + 3) fetch(2 * i + 5);
      if (!KV) WST(rs, wk, 16 + b);
      SEG(4);
      if (!more) BAR();
    }
  }
}

__device__ __forceinline__ void wait32(__amdgpu_buffer_rsrc_t rs, int first, int mine, int lane, unsigned need, unsigned& cap) {
  wait_ge(rs, 0, F_ST + (unsigned)(first + mine) * 128u, need, cap);
  wait_ge(rs, (unsigned)(lane & 31) * 128u, F_ST + (unsigned)first * 128u, need, cap);
}
__device__ __forceinline__ void sum_partials(__amdgpu_buffer_rsrc_t rs, unsigned l16, unsigned so, float (&a)[16]) {
  u32x4 pp[4][2];
#pragma unroll
  for (int k = 0; k < 4; ++k) { pp[k][0] = ldx16(rs, l16, so + k * 65536u); pp[k][1] = ldx16(rs, l16, so + k * 65536u + 1024); }
#pragma unroll
  for (int i = 0; i < 8; ++i) {
    f32x2 t = {0.f, 0.f};
#pragma unroll
    for (int k = 0; k < 4; ++k) t += f32x2{lo2f(pp[k][i >> 2][i & 3]), hi2f(pp[k][i >> 2][i & 3])};
    a[2 * i] = rbf(t[0]); a[2 * i + 1] = rbf(t[1]);
  }
}
__device__ __forceinline__ void put_row(__amdgpu_buffer_rsrc_t rs, unsigned l16, unsigned so, const float (&v)[16]) {
  stx16(rs, l16, so, u32x4{pack2(v[0], v[1]), pack2(v[2], v[3]), pack2(v[4], v[5]), pack2(v[6], v[7])});
  stx16(rs, l16, so + 1024, u32x4{pack2(v[8], v[9]), pack2(v[10], v[11]), pack2(v[12], v[13]), pack2(v[14], v[15])});
}
__device__ __forceinline__ void load_vec16(const float* p, int lane, float (&v)[16]) {
#pragma unroll
  for (int i = 0; i < 4; ++i) { const f32x4 t = *(const f32x4*)(p + (i >> 1) * 512 + lane * 8 + (i & 1) * 4); v[4 * i] = t[0]; v[4 * i + 1] = t[1]; v[4 * i + 2] = t[2]; v[4 * i + 3] = t[3]; }
}

__device__ __forceinline__ void stage_mid(__amdgpu_buffer_rsrc_t rs, const u16* x, const float* mod, int wk, int cu, unsigned& cap) {
  const int tid = threadIdx.x, lane = tid & 63, w = __builtin_amdgcn_readfirstlane(tid >> 6);
  const int id = w * 12 + cu, grp = id >> 5, rl = id & 31;
  const unsigned l16 = lane * 16;
  float ga[16], sc[16], sh[16];
  load_vec16(mod + 2048, lane, ga); load_vec16(mod + 4096, lane, sc); load_vec16(mod + 3072, lane, sh);
#pragma unroll
  for (int i = 0; i < 16; ++i) sc[i] = rbf(1.0f + sc[i]);
  for (int seg = grp; seg < NSEG; seg += 3) {
    const int row = seg * 32 + rl;
    float v[16], a[16];
    load_row16(x + (size_t)row * D_, lane, v);
    wait32(rs, P_S2, rl, lane, (unsigned)seg + 1u, cap);
    sum_partials(rs, l16, WS_P2 + (unsigned)seg * 262144u + (unsigned)rl * 2048, a);
#pragma unroll
    for (int i = 0; i < 16; ++i) v[i] = rbf(v[i] + rbf(ga[i] * a[i]));
    put_row(rs, l16, WS_X1 + (unsigned)row * 2048, v);
    ln_row(v);
#pragma unroll
    for (int i = 0; i < 16; ++i) v[i] = rbf(rbf(v[i] * sc[i]) + sh[i]);
    put_row(rs, l16, WS_H2 + (unsigned)row * 2048, v);
    ln_row(v);
    put_row(rs, l16, WS_G + (unsigned)row * 2048, v);
    WAIT_V(0);
    if (lane == 0) stx4(rs, 0, F_ST + (unsigned)(P_S3 + grp * 32 + rl) * 128, (unsigned)seg + 1u);
    WST(rs, wk, 16 + seg);
  }
}

__device__ __forceinline__ void stage_out(__amdgpu_buffer_rsrc_t rs, u16* out, const float* mod, int wk, int cu, unsigned& cap) {
  const int tid = threadIdx.x, lane = tid & 63, w = __builtin_amdgcn_readfirstlane(tid >> 6);
  const int grp = w >> 2, rl = cu * 4 + (w & 3);
  const unsigned l16 = lane * 16;
  float gb[16];
  load_vec16(mod + 5120, lane, gb);
  for (int b = grp; b < 65; b += 2) {
    const int t = 32 * b - 2 + rl;
    if (t < 0 || t >= S_) continue;
    wait32(rs, P_S8, rl, lane, (unsigned)b + 1u, cap);
    const unsigned xo = (unsigned)((lane >> 2) * 2048 + (lane & 3) * 256), xs = WS_X2 + (unsigned)b * 65536u + (unsigned)((rl >> 4) * 1024 + (rl & 15) * 16);
    const u32x4 x2a = ldx16(rs, xo, xs), x2b = ldx16(rs, xo, xs + 32768);
    const u32x4 x1a = ldx16(rs, l16, WS_X1 + (unsigned)t * 2048), x1b = ldx16(rs, l16, WS_X1 + (unsigned)t * 2048 + 1024);
    float a[16];
    sum_partials(rs, l16, WS_P8 + (unsigned)b * 262144u + (unsigned)rl * 2048, a);
#pragma unroll
    for (int i = 0; i < 4; ++i) {
      a[2 * i] = lo2f(x1a[i]) + rbf(gb[2 * i] * rbf(lo2f(x2a[i]) + a[2 * i])); a[2 * i + 1] = hi2f(x1a[i]) + rbf(gb[2 * i + 1] * rbf(hi2f(x2a[i]) + a[2 * i + 1]));
      a[8 + 2 * i] = lo2f(x1b[i]) + rbf(gb[8 + 2 * i] * rbf(lo2f(x2b[i]) + a[8 + 2 * i])); a[9 + 2 * i] = hi2f(x1b[i]) + rbf(gb[9 + 2 * i] * rbf(hi2f(x2b[i]) + a[9 + 2 * i]));
    }
    store_row16(out + (size_t)t * D_, lane, a);
    WST(rs, wk, 16 + b);
  }
}

__device__ __forceinline__ void worker(const MainP& p, LAS unsigned char* lds, int xw, int j) {
  const int tid = threadIdx.x, lane = tid & 63, w = __builtin_amdgcn_readfirstlane(tid >> 6);
  const __amdgpu_buffer_rsrc_t rs = mk_rs(p.ws, WS_END);
  const float* mod = (const float*)(p.ws + WS_MOD);
  unsigned cap = SPIN_CAP;
  const int wk = xw * 32 + j;
  WST(rs, wk, 0);
  {
    const int c = j * 5 + xw;
    for (int row = c * 8 + w; row < S_; row += 8 * NLN) {
      float v[16];
      load_row16(p.x + (size_t)row * D_, lane, v);
      ln_row(v);
      modulate(v, mod + 1024, mod, lane);
      put_row(rs, (unsigned)lane * 16, WS_H + (unsigned)row * 2048, v);
    }
    publish(rs, F_LN + c * 128, 1u);
    WST(rs, wk, 1);
  }
  if (xw == 4) stage_gemm<SG_Q>(lds, rs, p.Wq, wk, 0, j, P_S1 + j, cap);
  else {
    if (xw & 1) stage_gemm<SG_V>(lds, rs, p.Wv, wk, xw >> 1, j, P_PJ + xw * 32 + j, cap);
    else stage_gemm<SG_K>(lds, rs, p.Wk, wk, xw >> 1, j, P_PJ + xw * 32 + j, cap);
    WST(rs, wk, 3);
    if (xw == 0) stage_gemm<SG_WO>(lds, rs, p.Wo, wk, j >> 3, j & 7, P_S2 + j, cap);
    else if (xw == 1) stage_gemm<SG_W4>(lds, rs, p.w4, wk, j >> 3, j & 7, P_S8 + j, cap);
    else if (j >= 28) stage_out(rs, p.out, mod, wk, (xw - 2) * 4 + j - 28, cap);
    else if (xw == 2) stage_gemm<SG_W3>(lds, rs, p.w3, wk, 0, j, P_S7 + j, cap);
    else if (j < 8) stage_gemm<SG_W1>(lds, rs, p.w1, wk, 0, j, P_S4 + j, cap);
    else if (j < 16) stage_gemm<SG_W2>(lds, rs, p.w2, wk, 0, j - 8, P_S5 + j - 8, cap);
    else stage_mid(rs, p.x, mod, wk, j - 16, cap);
  }
  if (cap == 0u && lane == 0) stx4(rs, 0, X_CTL, 1u);
}

#define ST_DECL
#define ST0()
#define ST(i)
#define ST_FLUSH()
constexpr int R_W = 0, R_MEM = 131072, R_QK = R_MEM + 16384, R_KT = R_QK + 8192, R_RED = R_MEM + 16384, R_LDS = 163840;
__device__ __forceinline__ void bar_lo(int w) { asm volatile("s_cmp_ge_u32 %0, 4\n\ts_cbranch_scc1 1f\n\ts_barrier\n\t1:" :: "s"(w) : "scc", "memory"); }
__device__ __forceinline__ void bar_hi(int w) { asm volatile("s_cmp_lt_u32 %0, 4\n\ts_cbranch_scc1 1f\n\ts_setprio 3\n\ts_barrier\n\t1:" :: "s"(w) : "scc", "memory"); }
__device__ __forceinline__ void issue4(__amdgpu_buffer_rsrc_t rs, unsigned vo, unsigned s0, unsigned s1, unsigned s2, unsigned s3, u32x4& a0, u32x4& a1, u32x4& a2, u32x4& a3) {
  asm volatile("buffer_load_dwordx4 v[240:243], %[vo], %[rs], %[s0] offen sc1\n\tbuffer_load_dwordx4 v[244:247], %[vo], %[rs], %[s1] offen sc1\n\t"
               "buffer_load_dwordx4 v[248:251], %[vo], %[rs], %[s2] offen sc1\n\tbuffer_load_dwordx4 v[252:255], %[vo], %[rs], %[s3] offen sc1"
               : "=&{v[240:243]}"(a0), "=&{v[244:247]}"(a1), "=&{v[248:251]}"(a2), "=&{v[252:255]}"(a3)
               : [vo] "v"(vo), [rs] "s"(rs), [s0] "s"(s0), [s1] "s"(s1), [s2] "s"(s2), [s3] "s"(s3) : "memory");
}
#define ARRIVE(R0, R1, R2, R3, CNT) \
  asm volatile("s_waitcnt vmcnt(" #CNT ")\n\t" \
               "1:\n\t" \
               "v_min3_u32 %[t], v" #R0 ", v" #R1 ", v" #R2 "\n\t" \
               "v_min_u32 %[t], %[t], v" #R3 "\n\t" \
               "v_cmp_eq_u32 vcc, 0, %[t]\n\t" \
               "s_cbranch_vccz 2f\n\t" \
               "s_mov_b64 exec, vcc\n\t" \
               "buffer_load_dwordx4 v[" #R0 ":" #R3 "], %[vo], %[rs], %[so] offen sc1\n\t" \
               "s_mov_b64 exec, -1\n\t" \
               "s_waitcnt vmcnt(0)\n\t" \
               "s_sub_u32 %[cap], %[cap], 1\n\t" \
               "s_cbranch_scc0 1b\n\t" \
               "s_mov_b32 %[cap], 0\n\t" \
               "2:" \
               : "+{v[" #R0 ":" #R3 "]}"(a), [t] "=&v"(t), [cap] "+s"(cap) : [vo] "v"(vo), [rs] "s"(rs), [so] "s"(so) : "vcc", "scc", "memory")
template <int I> __device__ __forceinline__ void arrive(__amdgpu_buffer_rsrc_t rs, unsigned vo, unsigned so, u32x4& a, unsigned& cap) {
  unsigned t;
  if (I == 0) ARRIVE(240, 241, 242, 243, 3);
  else if (I == 1) ARRIVE(244, 245, 246, 247, 2);
  else if (I == 2) ARRIVE(248, 249, 250, 251, 1);
  else ARRIVE(252, 253, 254, 255, 0);
}
__device__ __forceinline__ void issue2(__amdgpu_buffer_rsrc_t rs, unsigned l8, unsigned so, int w, u32x4& a) {
  asm volatile("s_cmp_ge_u32 %[w], 4\n\ts_cbranch_scc1 1f\n\t"
               "buffer_load_dwordx2 v[252:253], %[l8], %[rs], %[so] offen sc1\n\tbuffer_load_dwordx2 v[254:255], %[l8], %[rs], %[so] offen offset:512 sc1\n\t1:"
               : "=&{v[252:255]}"(a) : [l8] "v"(l8), [rs] "s"(rs), [so] "s"(so), [w] "s"(w) : "scc", "memory");
}
__device__ __forceinline__ void arrive2(__amdgpu_buffer_rsrc_t rs, unsigned l8, unsigned so, u32x4& a, unsigned& cap) {
  unsigned t;
  asm volatile("s_waitcnt vmcnt(0)\n\t"
               "1:\n\t"
               "v_min3_u32 %[t], v252, v253, v254\n\t"
               "v_min_u32 %[t], %[t], v255\n\t"
               "v_cmp_eq_u32 vcc, 0, %[t]\n\t"
               "s_cbranch_vccz 2f\n\t"
               "buffer_load_dwordx2 v[252:253], %[l8], %[rs], %[so] offen sc1\n\t"
               "buffer_load_dwordx2 v[254:255], %[l8], %[rs], %[so] offen offset:512 sc1\n\t"
               "s_waitcnt vmcnt(0)\n\t"
               "s_sub_u32 %[cap], %[cap], 1\n\t"
               "s_cbranch_scc0 1b\n\t"
               "s_mov_b32 %[cap], 0\n\t"
               "2:"
               : "+{v[252:255]}"(a), [t] "=&v"(t), [cap] "+s"(cap) : [l8] "v"(l8), [rs] "s"(rs), [so] "s"(so) : "vcc", "scc", "memory");
}
__device__ __forceinline__ unsigned ror8(unsigned v) { return __builtin_amdgcn_update_dpp(0u, v, 0x128, 0xF, 0xF, true); }
__device__ __forceinline__ void gather_mem(__amdgpu_buffer_rsrc_t rs, unsigned lbase, int s, int w, unsigned l16, unsigned& cap) {
  const unsigned so = X_MEM + s * 16384 + w * 2048;
  u32x4 a0, a1;
  asm volatile("buffer_load_dwordx4 v[248:251], %[vo], %[rs], %[so] offen sc1\n\tbuffer_load_dwordx4 v[252:255], %[vo], %[rs], %[so] offen offset:1024 sc1"
               : "=&{v[248:251]}"(a0), "=&{v[252:255]}"(a1) : [vo] "v"(l16), [rs] "s"(rs), [so] "s"(so) : "memory");
  LAS unsigned char* d = lad(l16, lbase + R_MEM + w * 2048);
  arrive<2>(rs, l16, so, a0, cap);
  *(LAS u32x4*)(d) = a0;
  arrive<3>(rs, l16, so + 1024, a1, cap);
  *(LAS u32x4*)(d + 1024) = a1;
}
__device__ __forceinline__ unsigned mem_off(int lane) { return (unsigned)((lane & 1) * 512 + (((lane >> 1) ^ ((lane & 1) * 16)) * 8)); }
__device__ __forceinline__ bf16x8 mem_frag(LAS unsigned char* mp, int chunk) { return cat8(trread(mp + chunk * 1024), trread(mp + chunk * 1024 + 256)); }
__device__ __forceinline__ bf16x16 w_frag(LAS unsigned char* p) {
  return cat16(cat8(trread(p), trread(p + 2048)), cat8(trread(p + 4096), trread(p + 6144)));
}
__device__ __forceinline__ bf16x16 l_frag(LAS unsigned char* p, int i) { return cat16(*(const LAS bf16x8*)(p + i * 2048), *(const LAS bf16x8*)(p + i * 2048 + 1024)); }
__device__ __forceinline__ void make_resident(LAS unsigned char* wb, unsigned lo, unsigned l16) {
#pragma unroll
  for (int c = 0; c < 2; ++c) {
    bf16x16 f[4];
#pragma unroll
    for (int t = 0; t < 4; ++t) f[t] = w_frag(wb + c * 8192 + (lo ^ (t << 5)));
    WAIT_L0(); SCHED();
#pragma unroll
    for (int t = 0; t < 4; ++t) {
      *(LAS bf16x8*)(wb + (c * 4 + t) * 2048 + l16) = __builtin_shufflevector(f[t], f[t], 0, 1, 2, 3, 4, 5, 6, 7);
      *(LAS bf16x8*)(wb + (c * 4 + t) * 2048 + 1024 + l16) = __builtin_shufflevector(f[t], f[t], 8, 9, 10, 11, 12, 13, 14, 15);
    }
    WAIT_L0(); SCHED();
  }
}
__device__ __forceinline__ f32x4 fold(f32x4 a, f32x4 b) {
  f32x4 r;
#pragma unroll
  for (int i = 0; i < 4; ++i) {
    const auto t = __builtin_amdgcn_permlane32_swap(f2u(a[i]), f2u(b[i]), false, false);
    r[i] = u2f(t[0]) + u2f(t[1]);
  }
  return r;
}

struct Sched { unsigned tau, prev; int ea, eb, d1; bool moved; };
__device__ __forceinline__ void retime(__amdgpu_buffer_rsrc_t rs, unsigned l16, int s, Sched& t, const MainP& p, unsigned& cap) {
  if (s > 0) {
    unsigned vo;
    asm volatile("v_lshlrev_b32 %0, 3, %1" : "=v"(vo) : "v"(l16));
    unsigned va;
    asm volatile("v_and_b32 %0, 0xF80, %1" : "=v"(va) : "v"(vo));
    wait_ge(rs, vo, X_TS + (unsigned)s * 12288, 1u, cap);
    wait_ge(rs, va, X_TS + (unsigned)s * 12288 + 8192, 1u, cap);
    int vb = (int)(ldx4(rs, vo, X_TS + (unsigned)s * 12288) - t.prev), vc = (int)(ldx4(rs, va, X_TS + (unsigned)s * 12288 + 8192) - t.prev);
#pragma unroll
    for (int o = 32; o >= 1; o >>= 1) { vb = max(vb, __shfl_xor(vb, o)); vc = max(vc, __shfl_xor(vc, o)); }
    const int tb = __builtin_amdgcn_readfirstlane(vb), ta = __builtin_amdgcn_readfirstlane(vc);
    const int late = tb + p.d3 - (int)(t.tau - t.prev);
    if (s > 2) {
      const int up = s < 8 ? 160 : 16, dn = s < 8 ? 64 : 2;
      if (!t.moved) t.ea = max(t.ea - dn, min(ta * 8, t.ea + up));
      if (ta + p.d3 <= t.d1) t.eb = max(t.eb - dn, min((tb - t.d1) * 8, t.eb + up));
    }
    t.moved = late > 0;
    if (late > 0) t.tau += late + p.d4;
  }
  t.d1 = (t.ea >> 3) + p.d1;
}
__device__ __forceinline__ void advance(Sched& t, const MainP& p) { t.prev = t.tau; t.tau += t.d1 + (t.eb >> 3) + p.d2; }

__device__ __forceinline__ void role_a(const MainP& p, LAS unsigned char* lds, int h) {
  const int tid = threadIdx.x, lane = tid & 63, w = __builtin_amdgcn_readfirstlane(tid >> 6);
  const int cg = w & 3, kh = w >> 2;
  const __amdgpu_buffer_rsrc_t rs = mk_rs(p.ws, WS_END);
  const unsigned lbase = (unsigned)(size_t)lds;
  unsigned l16 = lane * 16, l8 = lane * 8;
  asm volatile("" : "+v"(l16), "+v"(l8));
  const int sidx = (lane & 8) ? 0xEEEE : 0x4444;
  bf16x16 W[24];
  unsigned qoff;
  {
    const int fr = lane & 15, g = lane >> 4, tu = 4 * (g & 1) + (fr >> 2), ub = lane >> 3;
    const unsigned lo = (g >> 1) * 1024 + tu * 128 + ((tu >> 1) << 5) + (fr & 3) * 8;
    qoff = R_QK + ((fr >> 1) & 1) * 512 + (g >> 1) * 256 + (fr & 1) * 128 + (8 * (g & 1) + (fr >> 2)) * 8;
    const __amdgpu_buffer_rsrc_t rw = mk_rs(cg < 2 ? p.Wq : p.Wk, D_ * HD * 2);
    const unsigned vo = (unsigned)(ub * HD + 16 * (((lane >> 1) & 3) ^ (ub >> 1)) + 8 * (lane & 1)) * 2u;
    const unsigned so = (unsigned)(kh * 512 * HD + h * DH + (cg & 1) * 64) * 2u;
    auto stage = [&](int c) {
#pragma unroll
      for (int rho = 0; rho < 8; ++rho) dma16(rw, vo, so + (unsigned)((c * 64 + rho * 8) * HD) * 2u, lbase + R_W + w * 16384 + (c & 1) * 8192 + rho * 1024);
    };
    stage(0); stage(1);
#pragma unroll
    for (int c = 0; c < 6; ++c) {
      WAIT_V(8);
#pragma unroll
      for (int t = 0; t < 4; ++t) W[4 * c + t] = w_frag(lds + R_W + w * 16384 + (c & 1) * 8192 + (lo ^ (t << 5)));
      WAIT_L0(); SCHED();
      stage(c + 2);
    }
    WAIT_V(0);
    make_resident(lds + R_W + w * 16384, lo, l16);
  }
  unsigned cap = SPIN_CAP;
  wait_ge(rs, (l16 & 16) * 256, F_ST + (unsigned)(P_PJ + h) * 128, 1u, cap);
  dma16x(rs, l16, WS_K + h * 8192 + w * 1024, lbase + R_KT + w * 1024);
  Sched tt; tt.tau = rt32() - 100000; tt.prev = tt.tau; tt.ea = 240 * 8; tt.eb = 125 * 8; tt.d1 = 0; tt.moved = true;
  const int cu_ = h; (void)cu_;
  ST_DECL
  for (int s = 0; s < NSEG; ++s) {
    LAS unsigned char* lp = lad(l16, lbase + R_W + w * 16384);
    bf16x16 L[2];
    L[0] = l_frag(lp, 0); L[1] = l_frag(lp, 1);
    unsigned lz = l8;
    asm volatile("" : "+v"(lz));
    LAS unsigned char* mp = lad(mem_off((int)(lz >> 3)), lbase + R_MEM + kh * 8192);
    SCHED();
    if (w == 7) wait_until(tt.tau);
    BAR();
    ST0();
    gather_mem(rs, lbase, s, (w + h) & 7, l16, cap);
    ST(1);
    LBAR();
    ST(2);
    f32x4 c[4];
#pragma unroll
    for (int t = 0; t < 4; ++t) c[t] = f32x4{0.f, 0.f, 0.f, 0.f};
    bf16x8 al, af[2];
    u32x4 vt0, vt1;
    u32x4 vmv;
    const unsigned vso = X_VM + (unsigned)(s * NH + h) * 4096 + cg * 1024;
    al = mem_frag(mp, 6); af[0] = mem_frag(mp, 0); af[1] = mem_frag(mp, 1);
#pragma unroll
    for (int i = 0; i < 8; ++i) {
      c[i & 3] = smf(al, L[i & 1], c[i & 3], sidx);
      SCHED();
      if (i == 3) al = mem_frag(mp, 7);
      if (i < 6) L[i & 1] = l_frag(lp, i + 2);
      if (i == 6) {
        const unsigned vo = WS_VT + (unsigned)(s * NH + h) * 8192 + cg * 2048;
        asm volatile("s_cmp_ge_u32 %5, 4\n\ts_cbranch_scc1 1f\n\t"
                     "buffer_load_dwordx4 %0, %2, %3, %4 offen sc1\n\tbuffer_load_dwordx4 %1, %2, %3, %4 offen offset:1024 sc1\n\t1:"
                     : "=&v"(vt0), "=&v"(vt1) : "v"(l16), "s"(rs), "s"(vo), "s"(w) : "scc", "memory");
      }
      SCHED();
#pragma unroll
      for (int r = 3 * i; r < 3 * i + 3; ++r) {
        c[(r + 1) & 3] = smf(af[(r >> 2) & 1], W[(r & ~3) + ((r + 1) & 3)], c[(r + 1) & 3], sidx);
        SCHED();
        if ((r & 3) == 3 && (r >> 2) + 2 < 6) af[(r >> 2) & 1] = mem_frag(mp, (r >> 2) + 2);
        SCHED();
      }
    }
    {
      LAS unsigned char* qw = lad(l8, lbase + R_QK + cg * 2048 + kh * 512);
      const f32x4 f0 = fold(c[0], c[1]);
      *(LAS u32x2*)(qw) = u32x2{pack2(f0[0], f0[1]), pack2(f0[2], f0[3])};
      const f32x4 f1 = fold(c[2], c[3]);
      *(LAS u32x2*)(qw + 1024) = u32x2{pack2(f1[0], f1[1]), pack2(f1[2], f1[3])};

    }
    ST(3);
    if (w < 4) {
      bf16x8 k1[4], k2[4];
      LAS unsigned char* kb = lad(l16, lbase + R_KT);
#pragma unroll
      for (int ks = 0; ks < 2; ++ks) { k1[ks] = *(const LAS bf16x8*)(kb + ks * 1024); k2[ks] = *(const LAS bf16x8*)(kb + 4096 + ks * 1024); }
      LBAR();
      ST(4);
      issue2(rs, l8, vso, w, vmv);
      f32x4 t0 = {0.f, 0.f, 0.f, 0.f}, t1 = t0, t2 = t0;
      LAS unsigned char* qb = lad(qoff, lbase);
      bf16x8 qf[4], kf[4];
#pragma unroll
      for (int ks = 0; ks < 2; ++ks) {
        qf[ks] = cat8(trread(qb + ks * 1024), trread(qb + ks * 1024 + 32));
        kf[ks] = cat8(trread(qb + 4096 + ks * 1024), trread(qb + 4096 + ks * 1024 + 32));
      }
      SCHED();
#pragma unroll
      for (int ks = 0; ks < 4; ++ks) {
        t1 = mfma32(k1[ks], qf[ks], t1); t2 = mfma32(k2[ks], qf[ks], t2); t0 = mfma32(kf[ks], qf[ks], t0);
        SCHED();
        if (ks < 2) {
          qf[ks + 2] = cat8(trread(qb + (ks + 2) * 1024), trread(qb + (ks + 2) * 1024 + 32));
          k1[ks + 2] = *(const LAS bf16x8*)(kb + (ks + 2) * 1024); k2[ks + 2] = *(const LAS bf16x8*)(kb + 4096 + (ks + 2) * 1024);
          kf[ks + 2] = cat8(trread(qb + 4096 + (ks + 2) * 1024), trread(qb + 4096 + (ks + 2) * 1024 + 32));
        }
        SCHED();
      }
      ST(5);
      {
        const float sl = 0.08838834764831845f * 1.4426950408889634f;
        float z[4] = {t1[0], t1[1], t1[2], t1[3]}, y = t0[0], mx;
        {
          float u1 = t0[1], u2 = t0[2], u3 = t0[3];
#define M8(Z, B) "v_add_f32_dpp " Z ", " Z ", " Z " row_ror:8 row_mask:0xf bank_mask:0x3\n\tv_add_f32_dpp " Z ", " B ", " B " row_ror:8 row_mask:0xf bank_mask:0xc\n\t"
          asm("s_nop 7\n\t"
              "v_permlane32_swap_b32 %4, %6\n\tv_permlane32_swap_b32 %5, %7\n\t"
              M8("%0", "%9") "v_add_f32 %4, %4, %6\n\tv_add_f32 %5, %5, %7\n\t" M8("%1", "%10") M8("%2", "%11") M8("%4", "%5") M8("%3", "%12")
              "v_max3_f32 %8, %0, %1, %2\n\tv_max3_f32 %8, %8, %4, %3\n\ts_nop 1\n\t"
              "v_max_f32_dpp %8, %8, %8 row_ror:8 row_mask:0xf bank_mask:0xf\n\tv_mov_b32 %5, %8\n\ts_nop 1\n\t"
              "v_permlane16_swap_b32 %8, %5\n\tv_max_f32 %8, %8, %5\n\tv_mov_b32 %5, %8\n\ts_nop 1\n\t"
              "v_permlane32_swap_b32 %8, %5\n\tv_max_f32 %8, %8, %5"
              : "+v"(z[0]), "+v"(z[1]), "+v"(z[2]), "+v"(z[3]), "+v"(y), "+v"(u1), "+v"(u2), "+v"(u3), "=&v"(mx) : "v"(t2[0]), "v"(t2[1]), "v"(t2[2]), "v"(t2[3]));
#undef M8
        }
        const float nm = -mx * sl;
#pragma unroll
        for (int r = 0; r < 4; ++r) z[r] = __builtin_amdgcn_exp2f(fmaf(z[r], sl, nm));
        const unsigned p0 = pack2(z[0], z[1]), p1 = pack2(z[2], z[3]);
        const u32x4 pt = {p0, p1, ror8(p0), ror8(p1)};
        SCHED();
        asm volatile("s_waitcnt vmcnt(2)" : "+v"(vt0), "+v"(vt1) :: "memory");
        f32x4 o0 = {0.f, 0.f, 0.f, 0.f}, o1 = o0;
        o0 = mfma32(__builtin_bit_cast(bf16x8, vt0), __builtin_bit_cast(bf16x8, pt), o0);
        o1 = mfma32(__builtin_bit_cast(bf16x8, vt1), __builtin_bit_cast(bf16x8, pt), o1);
        y = __builtin_amdgcn_exp2f(fmaf(y, sl, nm));
        const unsigned pm1 = pack2(y, u2f(ror8(f2u(y))));
        const auto pms = __builtin_amdgcn_permlane32_swap(pm1, pm1, false, false);
        const u32x2 pm = {pms[0], pms[1]};
        float inv = __builtin_amdgcn_rcpf(gsum(dpp_add<0x128>(((z[0] + z[1]) + (z[2] + z[3])) + y)));
        o0 *= inv; o1 *= inv;
        unsigned ov;
        unsigned ot;
        asm volatile("v_and_b32 %0, 0x30, %5\n\tv_lshrrev_b32 %1, 2, %5\n\tv_and_b32 %1, 0xC0, %1\n\tv_or_b32 %0, %0, %1\n\t"
                     "v_and_b32 %1, 0x40, %5\n\tv_lshl_or_b32 %0, %1, 9, %0\n\tv_and_b32 %1, 0x80, %5\n\tv_lshl_or_b32 %0, %1, 24, %0"
                     : "=&v"(ov), "=&v"(ot), "+v"(o0), "+v"(o1), "+v"(inv) : "v"(l16));
        ST(6);
        arrive2(rs, l8, vso, vmv, cap);
        ST(7);
        const f32x4 n0 = mfma16n(u32x2{vmv[0], vmv[1]}, pm, f32x4{0.f, 0.f, 0.f, 0.f}), n1 = mfma16n(u32x2{vmv[2], vmv[3]}, pm, f32x4{0.f, 0.f, 0.f, 0.f});
        {
          const u32x4 v = {nz(pack2(fmaf(n0[0], inv, o0[0]), fmaf(n0[1], inv, o0[1]))), nz(pack2(fmaf(n0[2], inv, o0[2]), fmaf(n0[3], inv, o0[3]))),
                           nz(pack2(fmaf(n1[0], inv, o1[0]), fmaf(n1[1], inv, o1[1]))), nz(pack2(fmaf(n1[2], inv, o1[2]), fmaf(n1[3], inv, o1[3])))};
          stx16(rs, ov, X_OX + s * 65536 + h * 1024 + w * 256, v);
        }
        if (w == 3) { const unsigned t = max(rt32(), 1u); if (l16 == 0) stx4(rs, 0, X_TS + (unsigned)(s + 1) * 12288 + (unsigned)(64 + h) * 128, t); }
        ST(8);
      }
    } else {
      LBAR();
      LAS unsigned char* qb = lad(qoff, lbase + 4096 + (w - 4) * 1024);
      u32x4 kf = __builtin_bit_cast(u32x4, cat8(trread(qb), trread(qb + 32)));
#pragma unroll
      for (int i = 0; i < 4; ++i) kf[i] = pack2(dpp_add<0x128>(lo2f(kf[i])), dpp_add<0x128>(hi2f(kf[i])));
      stx16(rs, l16, WS_KM + (unsigned)(s * NH + h) * 4096 + (w - 4) * 1024, kf);
      WAIT_V(0);
    }
    LBAR();
    if (w == 4 && l16 == 0) stx4(rs, 0, F_CH + h * 128, (unsigned)s + 1u);
    ST(9);
    if (w == 7) { retime(rs, l16, s, tt, p, cap); advance(tt, p); }
    if (s + 1 < NSEG) {
      unsigned fo;
      asm volatile("v_and_b32 %0, 16, %1\n\tv_lshlrev_b32 %0, 8, %0" : "=v"(fo) : "v"(l16));
      wait_ge(rs, fo, F_ST + (unsigned)(P_PJ + ((s + 1) & 1) * 64 + h) * 128, (unsigned)s + 2u, cap);
      dma16x(rs, l16, WS_K + (unsigned)((s + 1) * NH + h) * 8192 + w * 1024, lbase + R_KT + w * 1024);
    }
  }
  ST_FLUSH();
  if (cap == 0u && l16 == 0) stx4(rs, 0, X_CTL, 1u);
}

__device__ __forceinline__ void role_vb(const MainP& p, LAS unsigned char* lds, int rg, int cb) {
  const int tid = threadIdx.x, lane = tid & 63, w = __builtin_amdgcn_readfirstlane(tid >> 6);
  const int kh = w >> 2, wr = (w + cb) & 7;
  const __amdgpu_buffer_rsrc_t rs = mk_rs(p.ws, WS_END);
  const unsigned lbase = (unsigned)(size_t)lds;
  unsigned l16 = lane * 16, l8 = lane * 8, moff = mem_off(lane), roff, soff = (unsigned)((lane >> 5) * 256 + (lane & 31) * 4);
  const int sidx = (lane & 8) ? 0xEEEE : 0x4444;
  bf16x16 WV[8], WB[16];
  {
    const int fr = lane & 15, g = lane >> 4, tu = 4 * (g & 1) + (fr >> 2), ub = lane >> 3;
    { const int sl = mem_slot(16 * (g >> 1) + fr); roff = R_RED + w * 512 + ((sl >> 5) * 32 + (sl & 15) * 2 + (g & 1)) * 8; }
    const __amdgpu_buffer_rsrc_t rv = mk_rs(p.Wv, D_ * HD * 2), ro = mk_rs(p.Wo, D_ * HD * 2);
    const unsigned vv = (unsigned)(ub * HD + 16 * (((lane >> 1) & 3) ^ (ub >> 1)) + 8 * (lane & 1)) * 2u, sv = (unsigned)(8 * w * HD + cb * DH + rg * 64) * 2u;
#pragma unroll
    for (int c = 0; c < 16; ++c) dma16(rv, vv, sv + (unsigned)c * 64 * HD * 2, lbase + R_W + c * 8192 + w * 1024);
    WAIT_V(0); BAR();
    const unsigned lo = (g >> 1) * 1024 + tu * 128 + (((tu >> 1) ^ (w & 3)) << 5) + (fr & 3) * 8;
#pragma unroll
    for (int c = 0; c < 8; ++c) WV[c] = w_frag(lds + R_W + (kh * 8 + c) * 8192 + lo);
    LBAR();
    const int u = (lane >> 2) & 7;
    const unsigned vw = (unsigned)((16 * (u >> 2) + 8 * ((u >> 1) & 1) + 4 * (lane >> 5) + (u & 1)) * D_ + (lane & 3) * 8) * 2u;
    const unsigned sw = (unsigned)(wr * 512 * D_ + cb * 32) * 2u;
    const unsigned bo = (g >> 1) * 1024 + tu * 64 + (fr & 3) * 8;
#pragma unroll
    for (int ps = 0; ps < 2; ++ps) {
#pragma unroll
      for (int q = 0; q < 16; ++q) {
        const int r = (q >> 1) & 3, hb = q & 1;
        dma16(ro, vw, sw + (unsigned)(((ps * 2 + (q >> 3)) * 128 + 64 * (r >> 1) + 32 * hb + 2 * (r & 1)) * D_) * 2u, lbase + R_W + w * 16384 + q * 1024);
      }
      WAIT_V(0);
#pragma unroll
      for (int f = 0; f < 8; ++f) WB[ps * 8 + f] = w_frag(lds + R_W + w * 16384 + (f >> 2) * 8192 + ((f >> 1) & 1) * 512 + (f & 1) * 32 + bo);
      WAIT_L0(); SCHED();
    }
  }
  asm volatile("" : "+v"(l16), "+v"(l8), "+v"(moff), "+v"(roff), "+v"(soff));
  unsigned cap = SPIN_CAP;
  Sched tt; tt.tau = rt32() - 100000; tt.prev = tt.tau; tt.ea = 240 * 8; tt.eb = 125 * 8; tt.d1 = 0; tt.moved = true;
  const int cu_ = 32 + (rg * 32 + cb) / 2; (void)cu_;
  ST_DECL
  for (int s = 0; s < NSEG; ++s) {
    if (w == 7) wait_until(tt.tau);
    BAR();
    ST0();
    gather_mem(rs, lbase, s, wr, l16, cap);
    ST(1);
    LBAR();
    ST(2);
    {
      f32x4 c0 = {0.f, 0.f, 0.f, 0.f};
      LAS unsigned char* mp = lad(moff, lbase + R_MEM + kh * 8192);
      bf16x8 af[8];
#pragma unroll
      for (int c = 0; c < 8; ++c) af[c] = mem_frag(mp, c);
      SCHED();
#pragma unroll
      for (int c = 0; c < 8; ++c) c0 = smf(af[c], WV[c], c0, sidx);
      const f32x4 f = fold(c0, c0);
      if (l16 < 512) stx8(rs, l8, X_VM + (unsigned)(s * NH + cb) * 4096 + (rg * 4 + (w & 3)) * 512 + kh * 256, u32x2{nz(pack2(f[0], f[1])), nz(pack2(f[2], f[3]))});
    }
    ST(3);
    if (w == 7) retime(rs, l16, s, tt, p, cap);
    if (s + 1 < NSEG) {
      u32x4 a[4];
      const unsigned so = X_OX + s * 65536 + rg * 32768 + wr * 4096;
      if (w == 7) wait_until(tt.tau + tt.d1);
      BAR();
      ST(4);
      issue4(rs, l16, so, so + 1024, so + 2048, so + 3072, a[0], a[1], a[2], a[3]);
      f32x4 c[4];
#pragma unroll
      for (int t = 0; t < 4; ++t) c[t] = f32x4{0.f, 0.f, 0.f, 0.f};
#pragma unroll
      for (int i = 0; i < 4; ++i) {
        if (i == 0) arrive<0>(rs, l16, so, a[0], cap); else if (i == 1) arrive<1>(rs, l16, so + 1024, a[1], cap);
        else if (i == 2) arrive<2>(rs, l16, so + 2048, a[2], cap); else arrive<3>(rs, l16, so + 3072, a[3], cap);
#pragma unroll
        for (int t = 0; t < 4; ++t) c[t] = smf(__builtin_bit_cast(bf16x8, a[i]), WB[i * 4 + t], c[t], sidx);
        SCHED();
      }
      ST(5);
      {
        const bool odd = (l16 & 256) != 0;
        const f32x4 f = fold(odd ? c[2] : c[0], odd ? c[3] : c[1]);
        const auto e0 = __builtin_amdgcn_permlane16_swap(f2u(f[0]), f2u(f[2]), false, false), e1 = __builtin_amdgcn_permlane16_swap(f2u(f[1]), f2u(f[3]), false, false);
        *(LAS f32x2*)(lad(roff, lbase)) = f32x2{u2f(e0[0]) + u2f(e0[1]), u2f(e1[0]) + u2f(e1[1])};
      }
      ST(6);
      LBAR();
      ST(7);
      if (w == 0) {
        LAS unsigned char* rp = lad(l8, lbase + R_RED);
        f32x2 v[8];
#pragma unroll
        for (int i = 0; i < 8; ++i) v[i] = *(const LAS f32x2*)(rp + i * 512);
        const f32x2 sum = ((v[0] + v[1]) + (v[2] + v[3])) + ((v[4] + v[5]) + (v[6] + v[7]));
        stx4(rs, soff, X_MEM + (s + 1) * 16384 + (cb >> 1) * 1024 + rg * 512 + ((cb ^ rg) & 1) * 128, nz(pack2(sum[0], sum[1])));
        const unsigned t = max(rt32(), 1u);
        if (l16 == 0) stx4(rs, 0, X_TS + (unsigned)(s + 1) * 12288 + (unsigned)(rg * 32 + cb) * 128, t);
      }
      ST(8);
    }
    advance(tt, p);
  }
  ST_FLUSH();
  if (cap == 0u && l16 == 0) stx4(rs, 0, X_CTL, 1u);
}

#if defined(__HIP_DEVICE_COMPILE__)
__attribute__((target("no-packed-fp32-ops")))
#endif
__global__ void __launch_bounds__(512, 2) k_main(MainP p) {
  extern __shared__ __attribute__((aligned(16))) unsigned char smem[];
  const int b = blockIdx.x, x = b & 7, j = b >> 3;
  if (x >= 3) worker(p, (LAS unsigned char*)smem, x - 3, j);
  else if (x == 0) role_a(p, (LAS unsigned char*)smem, j);
  else role_vb(p, (LAS unsigned char*)smem, x - 1, j);
}

extern "C" void kernel_launch(void* const* d_in, const int* in_sizes, int n_in, void* d_out, int out_size, void* d_ws, size_t ws_size,
                              hipStream_t stream) {
  unsigned char* ws = (unsigned char*)d_ws;
  k_prep<<<256, 512, 0, stream>>>((const u16*)d_in[12], (const u16*)d_in[13], (const u16*)d_in[1], ws);
  MainP p;
  p.x = (const u16*)d_in[0]; p.Wq = (const u16*)d_in[2]; p.Wk = (const u16*)d_in[3]; p.Wv = (const u16*)d_in[4]; p.Wo = (const u16*)d_in[5];
  p.w1 = (const u16*)d_in[8]; p.w2 = (const u16*)d_in[9]; p.w3 = (const u16*)d_in[10]; p.w4 = (const u16*)d_in[11];
  p.out = (u16*)d_out; p.ws = ws; p.d1 = 10; p.d2 = 10; p.d3 = 4; p.d4 = 200;
  (void)hipFuncSetAttribute((const void*)k_main, hipFuncAttributeMaxDynamicSharedMemorySize, R_LDS);
  k_main<<<256, 512, R_LDS, stream>>>(p);
}
```

```cpp
#include <hip/hip_runtime.h>
#include <hip/hip_bf16.h>
#include <cstdlib>
#pragma clang diagnostic ignored "-Winline-asm"

#define LAS __attribute__((address_space(3)))
#define GAS __attribute__((address_space(1)))
typedef unsigned short u16;
typedef __bf16 bf16x2 __attribute__((ext_vector_type(2)));
typedef __bf16 bf16x4 __attribute__((ext_vector_type(4)));
typedef __bf16 bf16x8 __attribute__((ext_vector_type(8)));
typedef __bf16 bf16x16 __attribute__((ext_vector_type(16)));
typedef short s16x4 __attribute__((ext_vector_type(4)));
typedef float f32x2 __attribute__((ext_vector_type(2)));
typedef float f32x4 __attribute__((ext_vector_type(4)));
typedef float f32x16 __attribute__((ext_vector_type(16)));
typedef unsigned u32x2 __attribute__((ext_vector_type(2)));
typedef unsigned u32x4 __attribute__((ext_vector_type(4)));

constexpr int S_ = 2048, D_ = 1024, NH = 32, DH = 128, HD = 4096, NSEG = 64, FF = 3584;

constexpr unsigned MB = 1u << 20;
constexpr unsigned X_MEM = 0;
constexpr unsigned X_OX = 1 * MB;
constexpr unsigned X_VM = 5 * MB;
constexpr unsigned F_LN = 13 * MB;
constexpr unsigned F_ST = F_LN + 32768;
constexpr unsigned F_CH = F_ST + 98304;
constexpr unsigned X_CTL = F_CH + 4096;
constexpr unsigned X_TS = X_CTL + 4096;
constexpr unsigned X_BYTES = X_TS + 65 * 12288;
constexpr unsigned WS_MOD = 15 * MB;
constexpr unsigned WS_H = 16 * MB;
constexpr unsigned WS_Q = 20 * MB;
constexpr unsigned WS_K = 36 * MB;
constexpr unsigned WS_VT = 52 * MB;
constexpr unsigned WS_O = 68 * MB;
constexpr unsigned WS_X1 = 84 * MB;
constexpr unsigned WS_H2 = 88 * MB;
constexpr unsigned WS_G = 92 * MB;
constexpr unsigned WS_H1S = 96 * MB;
constexpr unsigned WS_X2 = 101 * MB;
constexpr unsigned WS_ST5 = 106 * MB;
constexpr unsigned WS_U = 108 * MB;
constexpr unsigned WS_KM = 124 * MB;
constexpr unsigned WS_P2 = 132 * MB;
constexpr unsigned WS_P8 = 164 * MB;
constexpr unsigned WS_END = 198 * MB;

__device__ __forceinline__ float bf2f(unsigned short b) { return __builtin_bit_cast(float, (unsigned)b << 16); }
__device__ __forceinline__ unsigned f2u(float f) { return __builtin_bit_cast(unsigned, f); }
__device__ __forceinline__ float u2f(unsigned u) { return __builtin_bit_cast(float, u); }
__device__ __forceinline__ float lo2f(unsigned w) { return __builtin_bit_cast(float, w << 16); }
__device__ __forceinline__ float hi2f(unsigned w) { return __builtin_bit_cast(float, w & 0xFFFF0000u); }
__device__ __forceinline__ unsigned pack2(float a, float b) { bf16x2 v; v[0] = (__bf16)a; v[1] = (__bf16)b; return __builtin_bit_cast(unsigned, v); }
__device__ __forceinline__ float rbf(float a) { return (float)(__bf16)a; }
__device__ __forceinline__ unsigned nz(unsigned w) { return max(w, 1u); }
__device__ __forceinline__ float gelu_tanh(float x) {
  const float e = __builtin_amdgcn_exp2f(x * fmaf(x * x, -0.1029432397f, -2.302208198f));
  return x * __builtin_amdgcn_rcpf(1.0f + e);
}
template <int CTRL> __device__ __forceinline__ float dpp_add(float v) { return v + u2f(__builtin_amdgcn_update_dpp(0u, f2u(v), CTRL, 0xF, 0xF, true)); }
__device__ __forceinline__ float wave_sum(float v) {
  v = dpp_add<0xB1>(v); v = dpp_add<0x4E>(v); v = dpp_add<0x141>(v); v = dpp_add<0x140>(v);
  auto t = __builtin_amdgcn_permlane16_swap(f2u(v), f2u(v), false, false);
  v = u2f(t[0]) + u2f(t[1]);
  t = __builtin_amdgcn_permlane32_swap(f2u(v), f2u(v), false, false);
  return u2f(t[0]) + u2f(t[1]);
}
#define WAIT_V(n) asm volatile("s_waitcnt vmcnt(" #n ")" ::: "memory")
#define WAIT_L0() asm volatile("s_waitcnt lgkmcnt(0)" ::: "memory")
#define BAR() do { asm volatile("" ::: "memory"); __builtin_amdgcn_s_barrier(); asm volatile("" ::: "memory"); } while (0)
#define LBAR() do { WAIT_L0(); BAR(); } while (0)
template <int N> __device__ __forceinline__ void wait_v() { asm volatile("s_waitcnt vmcnt(%0)" :: "n"(N) : "memory"); }
__device__ __forceinline__ void dma16(__amdgpu_buffer_rsrc_t rs, unsigned voff, unsigned soff, unsigned ldsa) {
  asm volatile("s_mov_b32 m0, %0\n\ts_nop 3\n\tbuffer_load_dwordx4 %1, %2, %3 offen lds" :: "s"(ldsa), "v"(voff), "s"(rs), "s"(soff) : "m0", "memory");
}
__device__ __forceinline__ void dma16x(__amdgpu_buffer_rsrc_t rs, unsigned voff, unsigned soff, unsigned ldsa) {
  asm volatile("s_mov_b32 m0, %0\n\ts_nop 3\n\tbuffer_load_dwordx4 %1, %2, %3 offen sc1 lds" :: "s"(ldsa), "v"(voff), "s"(rs), "s"(soff) : "m0", "memory");
}
__device__ __forceinline__ u32x4 ldx16(__amdgpu_buffer_rsrc_t rs, unsigned voff, unsigned soff) { return __builtin_amdgcn_raw_buffer_load_b128(rs, voff, soff, 16); }
__device__ __forceinline__ u32x2 ldx8(__amdgpu_buffer_rsrc_t rs, unsigned voff, unsigned soff) { return __builtin_amdgcn_raw_buffer_load_b64(rs, voff, soff, 16); }
__device__ __forceinline__ unsigned ldx4(__amdgpu_buffer_rsrc_t rs, unsigned voff, unsigned soff) { return __builtin_amdgcn_raw_buffer_load_b32(rs, voff, soff, 16); }
__device__ __forceinline__ void stx16(__amdgpu_buffer_rsrc_t rs, unsigned voff, unsigned soff, u32x4 v) {
  __builtin_amdgcn_raw_buffer_store_b128(v, rs, voff, soff, 16);
  asm volatile("s_nop 1" :: "v"(v) : "memory");
}
__device__ __forceinline__ void stx8(__amdgpu_buffer_rsrc_t rs, unsigned voff, unsigned soff, u32x2 v) { __builtin_amdgcn_raw_buffer_store_b64(v, rs, voff, soff, 16); }
__device__ __forceinline__ void stx4(__amdgpu_buffer_rsrc_t rs, unsigned voff, unsigned soff, unsigned v) { __builtin_amdgcn_raw_buffer_store_b32(v, rs, voff, soff, 16); }
__device__ __forceinline__ void wait_ge(__amdgpu_buffer_rsrc_t rs, unsigned voff, unsigned soff, unsigned need, unsigned& cap) {
  unsigned t;
  asm volatile("s_nop 4\n\t"
               "1:\n\t"
               "buffer_load_dword %[t], %[vo], %[rs], %[so] offen sc1\n\t"
               "s_waitcnt vmcnt(0)\n\t"
               "v_cmp_gt_u32 vcc, %[need], %[t]\n\t"
               "s_cmp_eq_u64 vcc, 0\n\t"
               "s_cbranch_scc1 2f\n\t"
               "s_sleep 2\n\t"
               "s_sub_u32 %[cap], %[cap], 1\n\t"
               "s_cbranch_scc0 1b\n\t"
               "s_mov_b32 %[cap], 0\n\t"
               "2:"
               : [t] "=&v"(t), [cap] "+s"(cap) : [vo] "v"(voff), [rs] "s"(rs), [so] "s"(soff), [need] "s"(need) : "vcc", "scc", "memory");
}
__device__ __forceinline__ void wait_seen(__amdgpu_buffer_rsrc_t rs, unsigned voff, unsigned soff, unsigned need, unsigned& seen, unsigned& cap) {
  if (seen >= need) return;
  unsigned t;
  asm volatile("s_nop 4\n\t"
               "1:\n\t"
               "buffer_load_dword %[t], %[vo], %[rs], %[so] offen sc1\n\t"
               "s_waitcnt vmcnt(0)\n\t"
               "v_cmp_gt_u32 vcc, %[need], %[t]\n\t"
               "s_cbranch_vccz 2f\n\t"
               "s_sleep 1\n\t"
               "s_sub_u32 %[cap], %[cap], 1\n\t"
               "s_cbranch_scc0 1b\n\t"
               "s_mov_b32 %[cap], 0\n\t"
               "2:"
               : [t] "=&v"(t), [cap] "+s"(cap) : [vo] "v"(voff), [rs] "s"(rs), [so] "s"(soff), [need] "s"(need) : "vcc", "scc", "memory");
  seen = min((unsigned)__builtin_amdgcn_readlane(t, 0), (unsigned)__builtin_amdgcn_readlane(t, 1));
}
__device__ __forceinline__ __amdgpu_buffer_rsrc_t mk_rs(const void* p, unsigned bytes) { return __builtin_amdgcn_make_buffer_rsrc((void*)p, 0, (int)bytes, 0x00020000); }
#define SCHED() __builtin_amdgcn_sched_barrier(0)
__device__ __forceinline__ unsigned now32() { unsigned long long t; asm volatile("s_memtime %0\n\ts_waitcnt lgkmcnt(0)" : "=s"(t) :: "memory"); return (unsigned)t; }
__device__ __forceinline__ unsigned rt32() { unsigned long long t; asm volatile("s_memrealtime %0\n\ts_waitcnt lgkmcnt(0)" : "=s"(t) :: "memory"); return (unsigned)t; }
__device__ __forceinline__ void wait_until(unsigned t) { while ((int)(rt32() - t) < 0) {} }
template <int X = 0> __device__ __forceinline__ LAS unsigned char* lad(unsigned v, unsigned u) {
  unsigned a;
  if (X == 0) asm volatile("v_add_u32 %0, %1, %2" : "=v"(a) : "s"(u), "v"(v));
  else asm volatile("v_xor_b32 %0, %3, %2\n\tv_add_u32 %0, %1, %0" : "=&v"(a) : "s"(u), "v"(v), "n"(X));
  return (LAS unsigned char*)(size_t)a;
}
__device__ __forceinline__ bf16x4 trread(LAS unsigned char* p) { return __builtin_amdgcn_ds_read_tr16_b64_v4bf16((LAS bf16x4*)p); }
__device__ __forceinline__ bf16x8 cat8(bf16x4 a, bf16x4 b) { return __builtin_shufflevector(a, b, 0, 1, 2, 3, 4, 5, 6, 7); }
__device__ __forceinline__ bf16x16 cat16(bf16x8 a, bf16x8 b) { return __builtin_shufflevector(a, b, 0, 1, 2, 3, 4, 5, 6, 7, 8, 9, 10, 11, 12, 13, 14, 15); }
__device__ __forceinline__ f32x4 mfma32(bf16x8 a, bf16x8 b, f32x4 c) { return __builtin_amdgcn_mfma_f32_16x16x32_bf16(a, b, c, 0, 0, 0); }
__device__ __forceinline__ f32x4 mfma16(u32x2 a, u32x2 b, f32x4 c) {
  return mfma32(__builtin_bit_cast(bf16x8, u32x4{a[0], a[1], 0u, 0u}), __builtin_bit_cast(bf16x8, u32x4{b[0], b[1], 0u, 0u}), c);
}
__device__ __forceinline__ f32x4 mfma16n(u32x2 a, u32x2 b, f32x4 c) { return __builtin_amdgcn_mfma_f32_16x16x16bf16_1k(__builtin_bit_cast(s16x4, a), __builtin_bit_cast(s16x4, b), c, 0, 0, 0); }
__device__ __forceinline__ f32x4 smf(bf16x8 a, bf16x16 b, f32x4 c, int idx) { return __builtin_amdgcn_smfmac_f32_16x16x64_bf16(a, b, c, idx, 0, 0); }

__device__ __forceinline__ void ln_row(float (&x)[16]) {
  float s = 0.f;
#pragma unroll
  for (int i = 0; i < 16; ++i) s += x[i];
  const float mean = wave_sum(s) * (1.0f / 1024.0f);
  float q = 0.f;
#pragma unroll
  for (int i = 0; i < 16; ++i) { const float d = x[i] - mean; q += d * d; }
  const float var = wave_sum(q) * (1.0f / 1024.0f);
  const float m = rbf(mean), isd = __builtin_amdgcn_rcpf(rbf(__builtin_amdgcn_sqrtf(rbf(rbf(var) + 1e-5f))));
#pragma unroll
  for (int i = 0; i < 16; ++i) x[i] = rbf(rbf(x[i] - m) * isd);
}
__device__ __forceinline__ void load_row16(const u16* rowp, int lane, float (&x)[16]) {
  const u32x4 a = *(const u32x4*)(rowp + lane * 8), b = *(const u32x4*)(rowp + 512 + lane * 8);
#pragma unroll
  for (int i = 0; i < 4; ++i) { x[2 * i] = lo2f(a[i]); x[2 * i + 1] = hi2f(a[i]); x[8 + 2 * i] = lo2f(b[i]); x[9 + 2 * i] = hi2f(b[i]); }
}
__device__ __forceinline__ void store_row16(u16* rowp, int lane, const float (&x)[16]) {
  u32x4 a, b;
#pragma unroll
  for (int i = 0; i < 4; ++i) { a[i] = pack2(x[2 * i], x[2 * i + 1]); b[i] = pack2(x[8 + 2 * i], x[9 + 2 * i]); }
  *(u32x4*)(rowp + lane * 8) = a; *(u32x4*)(rowp + 512 + lane * 8) = b;
}
__device__ __forceinline__ void modulate(float (&x)[16], const float* sc, const float* sh, int lane) {
#pragma unroll
  for (int hh = 0; hh < 2; ++hh)
#pragma unroll
    for (int j = 0; j < 2; ++j) {
      const f32x4 c = *(const f32x4*)(sc + hh * 512 + lane * 8 + j * 4), t = *(const f32x4*)(sh + hh * 512 + lane * 8 + j * 4);
#pragma unroll
      for (int i = 0; i < 4; ++i) { float& v = x[hh * 8 + j * 4 + i]; v = rbf(rbf(v * rbf(1.0f + c[i])) + t[i]); }
    }
}

__host__ __device__ __forceinline__ int mem_slot(int dl) {
  return ((dl >> 2) & 1) * 32 + (((dl >> 5) & 1) * 2 + ((dl >> 3) & 1)) * 8 + (((dl >> 4) & 1) * 2 + (dl & 1)) * 2 + ((dl >> 1) & 1);
}

__global__ void __launch_bounds__(512) k_prep(const u16* __restrict__ c, const u16* __restrict__ wmod, const u16* __restrict__ mem0, unsigned char* ws) {
  __shared__ float red[128][33];
  __shared__ float sc[1024];
  const int b = blockIdx.x, tid = threadIdx.x;
  {
    u32x4* z = (u32x4*)ws;
    const u32x4 zero = {0u, 0u, 0u, 0u};
    for (size_t i = (size_t)16384 / 16 + (size_t)b * 512 + tid; i < X_BYTES / 16; i += (size_t)256 * 512) z[i] = zero;
    if (b >= 192 && b < 208) {
      u32x4* y = (u32x4*)(ws + WS_H1S + ((b & 8) ? 64 * 65536 : 0));
      y[(b & 7) * 512 + tid] = zero;
    }
  }
  if (b == 255) {
    for (int d = tid; d < 1024; d += 512) {
      u32x4 v;
#pragma unroll
      for (int i = 0; i < 4; ++i) v[i] = nz((unsigned)mem0[(2 * i) * 1024 + d] | ((unsigned)mem0[(2 * i + 1) * 1024 + d] << 16));
      *(u32x2*)(ws + X_MEM + (d >> 6) * 1024 + mem_slot(d & 63) * 8) = u32x2{v[0], v[1]};
      *(u32x2*)(ws + X_MEM + (d >> 6) * 1024 + 512 + (mem_slot(d & 63) ^ 16) * 8) = u32x2{v[2], v[3]};
    }
  }
  if (b < 192) {
    for (int i = tid; i < 1024; i += 512) { const float v = bf2f(c[i]); sc[i] = rbf(v / (1.0f + __expf(-v))); }
    __syncthreads();
    const int kg = tid >> 2, c8 = tid & 3;
    float acc[8] = {0.f, 0.f, 0.f, 0.f, 0.f, 0.f, 0.f, 0.f};
    u32x4 w[8];
#pragma unroll
    for (int i = 0; i < 8; ++i) w[i] = *(const u32x4*)(wmod + (size_t)(kg + 128 * i) * 6144 + b * 32 + c8 * 8);
#pragma unroll
    for (int i = 0; i < 8; ++i) {
      const float s = sc[kg + 128 * i];
#pragma unroll
      for (int j = 0; j < 4; ++j) { acc[2 * j] += s * lo2f(w[i][j]); acc[2 * j + 1] += s * hi2f(w[i][j]); }
    }
#pragma unroll
    for (int j = 0; j < 8; ++j) red[kg][c8 * 8 + j] = acc[j];
    __syncthreads();
    if (tid < 256) {
      const int col = tid & 31, part = tid >> 5;
      float s = 0.f;
#pragma unroll
      for (int i = 0; i < 16; ++i) s += red[part * 16 + i][col];
      __syncthreads();
      red[part][col] = s;
    } else __syncthreads();
    __syncthreads();
    if (tid < 32) {
      float s = 0.f;
#pragma unroll
      for (int i = 0; i < 8; ++i) s += red[i][tid];
      ((float*)(ws + WS_MOD))[b * 32 + tid] = rbf(s);
    }
  }
}

#define WST(rs, wk, i)
struct MainP { const u16 *x, *Wq, *Wk, *Wv, *Wo, *w1, *w2, *w3, *w4; u16* out; unsigned char* ws; int d1, d2, d3, d4; };
constexpr unsigned SPIN_CAP = 1u << 18;

__device__ __forceinline__ float gmax(float v) {
  auto t = __builtin_amdgcn_permlane16_swap(f2u(v), f2u(v), false, false);
  v = fmaxf(u2f(t[0]), u2f(t[1]));
  t = __builtin_amdgcn_permlane32_swap(f2u(v), f2u(v), false, false);
  return fmaxf(u2f(t[0]), u2f(t[1]));
}
__device__ __forceinline__ float gsum(float v) {
  auto t = __builtin_amdgcn_permlane16_swap(f2u(v), f2u(v), false, false);
  v = u2f(t[0]) + u2f(t[1]);
  t = __builtin_amdgcn_permlane32_swap(f2u(v), f2u(v), false, false);
  return u2f(t[0]) + u2f(t[1]);
}
__device__ __forceinline__ void publish(__amdgpu_buffer_rsrc_t rs, unsigned word, unsigned val) {
  WAIT_V(0); BAR();
  if (threadIdx.x == 0) stx4(rs, 0, word, val);
}

constexpr int P_S2 = 0, P_S8 = 32, P_S7 = 64, P_S4 = 96, P_S5 = 104, P_S1 = 112, P_S3 = 144, P_PJ = 240;
constexpr int NLN = 160;

enum { SG_WO = 0, SG_W1 = 1, SG_W2 = 2, SG_W3 = 3, SG_W4 = 4, SG_K = 5, SG_V = 6, SG_Q = 7 };
constexpr int G_XCH = 131072, G_K = G_XCH + 16384, G_CTL = G_K + 8192;
__device__ __forceinline__ f32x16 mfma3232(bf16x8 a, bf16x8 b, f32x16 c) { return __builtin_amdgcn_mfma_f32_32x32x16_bf16(a, b, c, 0, 0, 0); }
__device__ __forceinline__ void widen32(const f32x16& a, u32x4 (&v)[2]) {
#pragma unroll
  for (int u = 0; u < 2; ++u) {
    const auto r0 = __builtin_amdgcn_permlane32_swap(pack2(a[8 * u], a[8 * u + 1]), pack2(a[8 * u + 4], a[8 * u + 5]), false, false);
    const auto r1 = __builtin_amdgcn_permlane32_swap(pack2(a[8 * u + 2], a[8 * u + 3]), pack2(a[8 * u + 6], a[8 * u + 7]), false, false);
    v[u] = u32x4{r0[0], r1[0], r0[1], r1[1]};
  }
}
__device__ __forceinline__ float hsum(float v) { const auto t = __builtin_amdgcn_permlane32_swap(f2u(v), f2u(v), false, false); return u2f(t[0]) + u2f(t[1]); }

__device__ __forceinline__ void attend(__amdgpu_buffer_rsrc_t rs, LAS unsigned char* qb, LAS unsigned char* kb, int seg, int h, int t, int eh, int lane, const u32x4 (&vv)[4],
                                       unsigned& chs, unsigned& cap) {
  const int fr = lane & 15, g = lane >> 4;
  const unsigned l16 = lane * 16, ub = (unsigned)(seg * NH + h);
  f32x4 s0 = {0.f, 0.f, 0.f, 0.f}, s1 = s0, s2 = s0;
  bf16x8 q[4];
#pragma unroll
  for (int ks = 0; ks < 4; ++ks) {
    q[ks] = *(const LAS bf16x8*)(qb + ks * 4096);
    s1 = mfma32(*(const LAS bf16x8*)(kb + ks * 1024), q[ks], s1); s2 = mfma32(*(const LAS bf16x8*)(kb + 4096 + ks * 1024), q[ks], s2);
  }
  const float scale = 0.08838834764831845f, L2E = 1.4426950408889634f;
  float a[4], b[4], c[4], mt = -3.0e38f, lt = 0.f;
#pragma unroll
  for (int r = 0; r < 4; ++r) {
    const int j = 4 * g + r;
    b[r] = (t == 1 || j <= fr) ? rbf(rbf(s1[r]) * scale) : -3.0e38f;
    c[r] = (t == 1 && j <= fr) ? rbf(rbf(s2[r]) * scale) : -3.0e38f;
    mt = fmaxf(mt, fmaxf(b[r], c[r]));
  }
  mt = gmax(mt);
#pragma unroll
  for (int r = 0; r < 4; ++r) { b[r] = __builtin_amdgcn_exp2f((b[r] - mt) * L2E); c[r] = __builtin_amdgcn_exp2f((c[r] - mt) * L2E); lt += b[r] + c[r]; }
  const u32x4 pt = {pack2(b[0], b[1]), pack2(b[2], b[3]), pack2(c[0], c[1]), pack2(c[2], c[3])};
  f32x4 o[4];
#pragma unroll
  for (int i = 0; i < 4; ++i) o[i] = mfma32(__builtin_bit_cast(bf16x8, vv[i]), __builtin_bit_cast(bf16x8, pt), f32x4{0.f, 0.f, 0.f, 0.f});
  lt = gsum(lt);
  const unsigned em = (unsigned)(fr >> 3) * 512 + (unsigned)g * 128 + (unsigned)(8 * ((fr >> 2) & 1) + (fr & 3)) * 8;
  asm volatile("" : "+v"(o[0]), "+v"(o[1]), "+v"(o[2]), "+v"(o[3]), "+v"(lt));
  wait_seen(rs, 0, F_CH + h * 128, (unsigned)seg + 1u, chs, cap);
  u32x2 vmf[4];
  for (;;) {
    asm volatile("" ::: "memory");
    unsigned mn = 0xFFFFFFFFu;
    bf16x8 kmf[4];
#pragma unroll
    for (int ks = 0; ks < 4; ++ks) kmf[ks] = __builtin_bit_cast(bf16x8, ldx16(rs, l16, WS_KM + ub * 4096 + ks * 1024));
#pragma unroll
    for (int i = 0; i < 4; ++i) vmf[i] = ldx8(rs, em, X_VM + ub * 4096 + (2 * eh + (i >> 1)) * 1024 + (i & 1) * 32);
    s0 = f32x4{0.f, 0.f, 0.f, 0.f};
#pragma unroll
    for (int ks = 0; ks < 4; ++ks) s0 = mfma32(kmf[ks], q[ks], s0);
#pragma unroll
    for (int i = 0; i < 4; ++i) mn = min(mn, min(vmf[i][0], vmf[i][1]));
    if (__all(mn != 0u) || cap == 0u) break;
    cap = __builtin_amdgcn_readfirstlane(cap - 1u);
  }
  cap = __builtin_amdgcn_readfirstlane(cap);
#pragma unroll
  for (int r = 0; r < 4; ++r) a[r] = rbf(rbf(s0[r]) * scale);
  const float mx = fmaxf(mt, gmax(fmaxf(fmaxf(a[0], a[1]), fmaxf(a[2], a[3]))));
#pragma unroll
  for (int r = 0; r < 4; ++r) a[r] = __builtin_amdgcn_exp2f((a[r] - mx) * L2E);
  const u32x2 pm = {pack2(a[0], a[1]), pack2(a[2], a[3])};
  f32x4 om[4];
#pragma unroll
  for (int i = 0; i < 4; ++i) om[i] = mfma16n(vmf[i], pm, f32x4{0.f, 0.f, 0.f, 0.f});
  const float al = __builtin_amdgcn_exp2f((mt - mx) * L2E);
  const float inv = __builtin_amdgcn_rcpf(fmaf(al, lt, 0.5f * gsum((a[0] + a[1]) + (a[2] + a[3])))), ai = al * inv;
#pragma unroll
  for (int i = 0; i < 4; ++i) o[i] = o[i] * ai + om[i] * inv;
#pragma unroll
  for (int u = 0; u < 2; ++u)
    stx16(rs, l16, WS_O + (unsigned)seg * 262144 + (unsigned)((h * 4 + 2 * eh + u) * 2 + t) * 1024,
          u32x4{pack2(o[2 * u][0], o[2 * u][1]), pack2(o[2 * u][2], o[2 * u][3]), pack2(o[2 * u + 1][0], o[2 * u + 1][1]), pack2(o[2 * u + 1][2], o[2 * u + 1][3])});
}

template <int SG>
__device__ __forceinline__ void stage_gemm(LAS unsigned char* lds, __amdgpu_buffer_rsrc_t rs, const u16* Wg, int wk, int kq, int cb, int pid, unsigned& cap) {
  constexpr bool KV = (SG == SG_K || SG == SG_V), PROJ = KV || SG == SG_Q, ROWM = (SG == SG_W1 || PROJ);
  constexpr int KS = (SG == SG_W4) ? 28 : 32;
  constexpr int LDW = PROJ ? HD : (SG == SG_W3) ? FF : D_;
  constexpr int WROWS = (SG == SG_WO) ? HD : (SG == SG_W4 ? FF : D_);
  constexpr int NBLK = KV ? 32 : (SG == SG_WO || SG == SG_W1 || SG == SG_Q) ? 64 : 65;
  constexpr unsigned ABASE = PROJ ? WS_H : SG == SG_WO ? WS_O : SG == SG_W1 ? WS_G : SG == SG_W2 ? WS_H1S : SG == SG_W3 ? WS_X2 : WS_U;
  constexpr unsigned ASTR = SG == SG_WO ? 262144u : SG == SG_W4 ? 229376u : 65536u;
  constexpr int NPC = KS / 4;
  const unsigned myword = F_ST + (unsigned)pid * 128u;
  int tid = threadIdx.x; asm volatile("" : "+v"(tid));
  const int lane = tid & 63, w = __builtin_amdgcn_readfirstlane(tid >> 6), row = lane & 31, hf = lane >> 5;
  const int kh = w >> 2, wl = w & 3;
  const int koff = PROJ ? 0 : kq;
  const unsigned lbase = (unsigned)(size_t)lds, l16 = lane * 16;
  const int col0 = cb * 128 + wl * 32;
  bf16x8 Wf[KS];
  {
    const __amdgpu_buffer_rsrc_t rsW = mk_rs(Wg, (unsigned)WROWS * LDW * 2u);
    const int ub = lane >> 3, tu = 4 * hf + ((lane & 15) >> 2);
    const unsigned vB = (unsigned)(((ub & 3) + 8 * (ub >> 2)) * LDW + 16 * (((lane >> 1) & 3) ^ (ub >> 1)) + 8 * (lane & 1)) * 2u;
    const unsigned sB = (unsigned)((koff * KS * 32 + 32 * (w >> 2) + 16 * ((w >> 1) & 1) + 4 * (w & 1)) * LDW + cb * 128) * 2u;
    const int boff = kh * 65536 + (wl >> 1) * 8192 + tu * 128 + (((tu >> 1) ^ (2 * (wl & 1) + ((lane >> 4) & 1))) << 5) + (lane & 3) * 8;
#pragma unroll
    for (int ps = 0; ps < 2; ++ps) {
      BAR();
#pragma unroll
      for (int hh = 0; hh < 2; ++hh)
#pragma unroll
        for (int i = 0; i < 4; ++i)
          if (4 * ps + i < NPC) {
            const unsigned so = sB + (unsigned)((hh * NPC + 4 * ps + i) * 64 * LDW) * 2u, d = lbase + hh * 65536 + i * 16384 + w * 1024;
            dma16(rsW, vB, so, d); dma16(rsW, vB, so + 128, d + 8192);
          }
      WAIT_V(0); BAR();
#pragma unroll
      for (int i = 0; i < 4; ++i)
        if (4 * ps + i < NPC) {
#pragma unroll
          for (int k = 0; k < 4; ++k) { LAS unsigned char* q = lds + (boff + i * 16384 + k * 2048); Wf[(4 * ps + i) * 4 + k] = cat8(trread(q), trread(q + 1024)); }
        }
      WAIT_L0();
    }
    if (tid < 8) *(LAS unsigned*)(lds + G_CTL + tid * 4) = 0u;
    LBAR();
  }
  const f32x16 zero16 = {0.f, 0.f, 0.f, 0.f, 0.f, 0.f, 0.f, 0.f, 0.f, 0.f, 0.f, 0.f, 0.f, 0.f, 0.f, 0.f};
  auto put16 = [&](LAS unsigned char* p, const f32x16& a) {
#pragma unroll
    for (int q = 0; q < 4; ++q) *(LAS f32x4*)(p + q * 1024) = f32x4{a[4 * q], a[4 * q + 1], a[4 * q + 2], a[4 * q + 3]};
  };
  if (SG == SG_W3) {
    const u32x4 one4 = {0x3F803F80u, 0x3F803F80u, 0x3F803F80u, 0x3F803F80u};
    f32x16 cs = zero16;
#pragma unroll
    for (int k = 0; k < KS; ++k) cs = mfma3232(Wf[k], __builtin_bit_cast(bf16x8, one4), cs);
    if (kh == 0) put16(lds + wl * 4096 + l16, cs);
    LBAR();
    if (kh == 1 && row == 0) {
#pragma unroll
      for (int q = 0; q < 4; ++q)
        *(LAS f32x4*)(lds + G_K + wl * 128 + hf * 64 + q * 16) = *(const LAS f32x4*)(lds + wl * 4096 + q * 1024 + l16) + f32x4{cs[4 * q], cs[4 * q + 1], cs[4 * q + 2], cs[4 * q + 3]};
    }
    LBAR();
  }
  auto blk = [&](int i) -> int { return KV ? 2 * i + kq : i; };
  const unsigned aoff = ROWM ? (unsigned)(row * 128 + ((hf ^ ((row >> 1) & 7)) << 4)) : (unsigned)((row >> 4) * 1024 + hf * 256 + (row & 15) * 16);
  const unsigned vA = ROWM ? (unsigned)((lane >> 3) * 2048 + (((lane & 7) ^ ((4 * wl + (lane >> 4)) & 7)) << 4)) : l16;
  auto fetch = [&](int hb) {
    const int i = hb >> 1, hh = hb & 1;
    const unsigned so = ABASE + (unsigned)blk(i) * ASTR + (unsigned)koff * (KS * 2048) + (ROWM ? wl * 16384 + hh * 1024 : (hh * KS + wl) * 1024);
#pragma unroll
    for (int q = 0; q < NPC; ++q) dma16x(rs, vA, so + q * (ROWM ? 128 : 4096), lbase + (i & 1) * 65536 + hh * 32768 + wl * 1024 + q * 4096);
  };
  auto products = [&](int i, f32x16& acc, auto&& first, auto&& early, auto&& handover) {
    const unsigned ub = lbase + (i & 1) * 65536 + kh * 32768;
    LAS unsigned char* ab[4];
    ab[0] = lad(aoff, ub);
    if (ROWM) { ab[1] = lad<32>(aoff, ub); ab[2] = lad<64>(aoff, ub); ab[3] = lad<96>(aoff, ub); }
    constexpr int PD = 10;
    bf16x8 af[PD + 1];
    auto rd = [&](int k) { af[k % (PD + 1)] = ROWM ? *(const LAS bf16x8*)(ab[k & 3] + (k >> 2) * 4096) : *(const LAS bf16x8*)(ab[0] + (k >> 1) * 2048 + (k & 1) * 512); };
#pragma unroll
    for (int k = 0; k < PD; ++k) rd(k);
    SCHED();
#pragma unroll
    for (int k = 0; k < KS; ++k) {
      if (k + PD < KS) rd(k + PD);
      acc = SG == SG_V ? mfma3232(af[k % (PD + 1)], Wf[k], k ? acc : zero16) : mfma3232(Wf[k], af[k % (PD + 1)], k ? acc : zero16);
      SCHED();
      if (k == 8) { first(); SCHED(); }
      if (k == KS - 10) { early(); SCHED(); }
      if (k == KS - 5) { handover(); SCHED(); }
    }
    asm volatile("" : "+v"(acc));
  };
  WST(rs, wk, KV ? 2 : 10);
#define SEG(x)
  if (kh == 0) {
    auto pvo = [&](int i) -> unsigned {
      const int b = blk(i);
      if (PROJ) return F_LN + (unsigned)((b % (NLN / 4)) * 4 + (lane & 3)) * 128u;
      if (SG == SG_WO) return F_ST + (unsigned)(P_S1 + kq * 8 + (lane & 7)) * 128u;
      if (SG == SG_W1) return F_ST + (unsigned)(P_S3 + (b % 3) * 32 + (lane & 31)) * 128u;
      if (SG == SG_W2) return F_ST + (unsigned)(P_S4 + (lane & 7)) * 128u;
      if (SG == SG_W3) return F_ST + (unsigned)(P_S5 + (lane & 7)) * 128u;
      return F_ST + (unsigned)(P_S7 + kq * 7 + min(lane & 7, 6)) * 128u;
    };
    auto need = [&](int i) -> unsigned { return PROJ ? 1u : (SG == SG_W2 && i == 64) ? 64u : (unsigned)i + 1u; };
    int R = 0, Rp = 0;
    for (int i = 0; i < NBLK; ++i) {
      if (R <= i) { wait_ge(rs, pvo(i), 0, need(i), cap); fetch(2 * i); fetch(2 * i + 1); }
      const unsigned pk = w == 0 ? (lane < 32 ? pvo(i + 1) : pvo(i + 2)) : 0xF0000000u;
      if (R <= i + 1) wait_v<NPC>(); else wait_v<2 * NPC>();
      BAR();
      SEG(1);
      f32x16 acc;
      unsigned peek;
      asm volatile("buffer_load_dword %0, %1, %2, 0 offen sc1" : "=v"(peek) : "v"(pk), "s"(rs) : "memory");
      u32x4 st[8];
      if (SG == SG_W3) {
#pragma unroll
        for (int q = 0; q < 8; ++q) st[q] = ldx16(rs, l16, WS_ST5 + (unsigned)i * 8192 + q * 1024);
      }
      products(i, acc, []() {}, []() {}, [&]() {
        SEG(4);
        if (SG == SG_Q && R >= i + 2) fetch(2 * i + 3);
        WAIT_V(0);
        SEG(0);
        asm volatile("" : "+v"(peek));
        if (w == 0) {
          const unsigned long long ok = __ballot(peek >= (lane < 32 ? need(i + 1) : need(i + 2)));
          const bool r1 = (R >= i + 2 || (unsigned)ok == 0xFFFFFFFFu) && i + 1 < NBLK, r2 = r1 && (unsigned)(ok >> 32) == 0xFFFFFFFFu && i + 2 < NBLK;
          if (lane == 0) *(volatile LAS unsigned*)(lds + G_CTL + 4) = (unsigned)(i + 1 + (r1 ? 1 : 0) + (r2 ? 1 : 0));
        }
        LBAR();
      });
      SEG(2);
      const unsigned rv = *(const volatile LAS unsigned*)(lds + G_CTL + 4);
      if (SG == SG_W3) {
        float ms = 0.f;
#pragma unroll
        for (int q = 0; q < 8; ++q) ms += u2f(st[q][0]) + u2f(st[q][2]);
        const float mean = hsum(ms) * (1.0f / 32.0f);
        float q2 = 0.f;
#pragma unroll
        for (int q = 0; q < 8; ++q) { const float d0 = u2f(st[q][0]) - mean, d1 = u2f(st[q][2]) - mean; q2 += u2f(st[q][1]) + u2f(st[q][3]) + 32.0f * (d0 * d0 + d1 * d1); }
        const float var = hsum(q2) * (1.0f / 1024.0f);
        const float m = rbf(mean);
        *(LAS float*)(lds + G_K + 1024 + wl * 256 + lane * 4) = __builtin_amdgcn_rcpf(rbf(__builtin_amdgcn_sqrtf(rbf(rbf(var) + 1e-5f))));
#pragma unroll
        for (int q = 0; q < 4; ++q) {
          const f32x4 cs = *(const LAS f32x4*)(lds + G_K + wl * 128 + hf * 64 + q * 16);
#pragma unroll
          for (int e = 0; e < 4; ++e) acc[4 * q + e] = fmaf(-m, cs[e], acc[4 * q + e]);
        }
      }
      put16(lad(l16, lbase + G_XCH + wl * 4096), acc);
      asm volatile("" ::: "memory");
      if (lane == 0) *(volatile LAS unsigned*)(lds + G_CTL + 16 + wl * 4) = (unsigned)i + 1u;
      Rp = R; R = __builtin_amdgcn_readfirstlane(rv);
      SEG(3);
      if (R >= i + 2 && Rp < i + 2) { fetch(2 * i + 2); fetch(2 * i + 3); }
      if (R >= i + 3) fetch(2 * i + 4);
      SEG(5);
    }
    BAR();
  } else {
    bool pending = false;
    auto announce = [&](int i) {
      if (lane == 0) {
        const unsigned c = __hip_atomic_fetch_add((LAS unsigned*)(lds + G_CTL), 1u, __ATOMIC_RELAXED, __HIP_MEMORY_SCOPE_WORKGROUP);
        if ((c & 3u) == 3u) stx4(rs, 0, myword, (unsigned)blk(i) + 1u);
      }
    };
    unsigned kvs[2] = {0u, 0u}, chs = 0u;
    BAR();
    for (int i = 0; i < NBLK; ++i) {
      const int b = blk(i);
      if (SG == SG_Q) {
        if (i & 1) wait_seen(rs, (unsigned)(lane & 1) * 4096, F_ST + (unsigned)(P_PJ + 64 + cb) * 128, (unsigned)i + 1u, kvs[1], cap);
        else wait_seen(rs, (unsigned)(lane & 1) * 4096, F_ST + (unsigned)(P_PJ + cb) * 128, (unsigned)i + 1u, kvs[0], cap);
      }
      BAR();
      SEG(0);
      const unsigned rv = *(const volatile LAS unsigned*)(lds + G_CTL + 4);
      u32x4 res[2], vv[4];
      float isd;
      if (SG == SG_W2) {
#pragma unroll
        for (int u = 0; u < 2; ++u) {
          const int t = 32 * b - 2 + 16 * u + (lane >> 2);
          dma16x(rs, (t >= 0 && t < S_) ? (unsigned)(t * D_ + col0 + ((lane ^ (lane >> 3)) & 3) * 8) * 2u : 0xF0000000u, WS_H2, lbase + G_K + wl * 2048 + u * 1024);
        }
      }
      if (SG == SG_Q) {
        const unsigned ub = (unsigned)(b * NH + cb) * 8192;
        dma16x(rs, l16, WS_K + ub + wl * 2048, lbase + G_K + wl * 2048); dma16x(rs, l16, WS_K + ub + wl * 2048 + 1024, lbase + G_K + wl * 2048 + 1024);
        const unsigned ev = (unsigned)((lane >> 3) & 1) * 1024 + (unsigned)((lane >> 4) * 16 + 8 * ((lane >> 2) & 1) + (lane & 3)) * 16;
#pragma unroll
        for (int q = 0; q < 4; ++q) vv[q] = ldx16(rs, ev, WS_VT + ub + (2 * (wl >> 1) + (q >> 1)) * 2048 + (q & 1) * 64);
      }
      f32x16 acc;
      int R = 0;
      LAS unsigned char* xr = lad(l16, lbase + G_XCH + wl * 4096);
      f32x4 xa[4];
      unsigned xf;
      auto take = [&]() {
        asm volatile("" ::: "memory");
        xf = *(const volatile LAS unsigned*)(lds + G_CTL + 16 + wl * 4);
        asm volatile("" ::: "memory");
#pragma unroll
        for (int q = 0; q < 4; ++q) xa[q] = *(const LAS f32x4*)(xr + q * 1024);
        if (SG == SG_W3) isd = *(const LAS float*)(lds + G_K + 1024 + wl * 256 + lane * 4);
        asm volatile("" ::: "memory");
      };
      products(i, acc, [&]() {
        if (SG == SG_Q && i > 0) { wait_v<6>(); announce(i - 1); }
      }, [&]() {
        take();
      }, [&]() {
        SEG(5);
        R = __builtin_amdgcn_readfirstlane(rv);
        if (R >= i + 2) {
          WAIT_V(0);
          LBAR();
          if (pending && tid == 256) stx4(rs, 0, myword, (unsigned)blk(i - 1) + 1u);
        }
      });
      const bool more = R >= i + 2;
      SEG(1);
      while (__builtin_amdgcn_readfirstlane(xf) <= (unsigned)i) take();
#pragma unroll
      for (int q = 0; q < 16; ++q) { acc[q] += xa[q >> 2][q & 3]; if (SG == SG_W3) acc[q] *= isd; }
      SEG(2);
      const unsigned fo = (unsigned)((col0 >> 5) * 2048 + hf * 256), ro = (unsigned)((row >> 4) * 1024 + (row & 15) * 16);
      u32x4 v[2];
      if (SG == SG_W1 || SG == SG_W3) {
#pragma unroll
        for (int q = 0; q < 16; ++q) acc[q] = gelu_tanh(acc[q]);
      }
      if (SG != SG_V) widen32(acc, v);
      if (SG == SG_WO || SG == SG_W4) {
        LAS unsigned char* tw = lds + G_XCH + wl * 4096 + row * 64;
        *(LAS u32x4*)(tw + ((hf ^ (row >> 1)) & 3) * 16) = v[0]; *(LAS u32x4*)(tw + (((2 + hf) ^ (row >> 1)) & 3) * 16) = v[1];
        asm volatile("" ::: "memory");
        LAS unsigned char* tr = lds + G_XCH + wl * 4096 + (lane >> 2) * 64 + ((lane ^ (lane >> 3)) & 3) * 16;
        const unsigned so = (SG == SG_WO ? WS_P2 : WS_P8) + (unsigned)(b * 4 + kq) * 65536u, vo = (unsigned)((lane >> 2) * D_ + col0 + (lane & 3) * 8) * 2u;
        stx16(rs, vo, so, *(const LAS u32x4*)(tr)); stx16(rs, vo, so + 32768, *(const LAS u32x4*)(tr + 1024));
      } else if (SG == SG_K) {
        const unsigned so = WS_K + (unsigned)(b * NH + cb) * 8192 + wl * 1024, vo = (unsigned)((row >> 4) * 4096 + hf * 256 + (row & 15) * 16);
        stx16(rs, vo, so, v[0]); stx16(rs, vo, so + 512, v[1]);
      } else if (SG == SG_V) {
        const unsigned so = WS_VT + (unsigned)(b * NH + cb) * 8192 + wl * 2048, vo = ro + hf * 256;
#pragma unroll
        for (int u = 0; u < 2; ++u)
          stx16(rs, vo, so + u * 512, u32x4{pack2(acc[4 * u], acc[4 * u + 1]), pack2(acc[4 * u + 2], acc[4 * u + 3]), pack2(acc[8 + 4 * u], acc[9 + 4 * u]), pack2(acc[10 + 4 * u], acc[11 + 4 * u])});
      } else if (SG == SG_Q) {
        LAS unsigned char* qw = lds + G_XCH + wl * 4096 + (row >> 4) * 1024 + hf * 256 + (row & 15) * 16;
        *(LAS u32x4*)(qw) = v[0]; *(LAS u32x4*)(qw + 512) = v[1];
        WAIT_V(0);
        if (lane == 0) __hip_atomic_fetch_add((LAS unsigned*)(lds + G_CTL + 8), 1u, __ATOMIC_RELAXED, __HIP_MEMORY_SCOPE_WORKGROUP);
        while (__builtin_amdgcn_readfirstlane(*(const volatile LAS unsigned*)(lds + G_CTL + 8)) < 4u * (unsigned)i + 4u) {}
        asm volatile("" ::: "memory");
        attend(rs, lds + G_XCH + (wl & 1) * 1024 + l16, lds + G_K + l16, b, cb, wl & 1, wl >> 1, lane, vv, chs, cap);
      } else if (SG == SG_W1) {
        const int grp = cb >> 1, sh = (grp & 1) ? -(1 + (grp >> 1)) : (1 + (grp >> 1));
        const int rho = 32 * b + row + sh + 2;
        const unsigned vo = (rho >= 2 && rho < S_ + 2) ? (unsigned)(rho >> 5) * 65536u + fo + (unsigned)((rho >> 4) & 1) * 1024u + (unsigned)(rho & 15) * 16u : 0xF0000000u;
        stx16(rs, vo, WS_H1S, v[0]); stx16(rs, vo, WS_H1S + 512, v[1]);
      } else if (SG == SG_W2) {
        if (!more) WAIT_V(0);
        res[0] = *(const LAS u32x4*)(lds + G_K + wl * 2048 + row * 64 + ((hf ^ (row >> 1)) & 3) * 16);
        res[1] = *(const LAS u32x4*)(lds + G_K + wl * 2048 + row * 64 + (((2 + hf) ^ (row >> 1)) & 3) * 16);
        float z[16], zs = 0.f;
#pragma unroll
        for (int q = 0; q < 8; ++q) {
          const unsigned x = pack2(lo2f(res[q >> 2][q & 3]) + lo2f(v[q >> 2][q & 3]), hi2f(res[q >> 2][q & 3]) + hi2f(v[q >> 2][q & 3]));
          v[q >> 2][q & 3] = x; z[2 * q] = lo2f(x); z[2 * q + 1] = hi2f(x); zs += z[2 * q] + z[2 * q + 1];
        }
        stx16(rs, fo + ro, WS_X2 + (unsigned)b * 65536u, v[0]); stx16(rs, fo + ro, WS_X2 + (unsigned)b * 65536u + 512, v[1]);
        const float mw = hsum(zs) * (1.0f / 32.0f);
        float q2 = 0.f;
#pragma unroll
        for (int q = 0; q < 16; ++q) { const float d = z[q] - mw; q2 += d * d; }
        q2 = hsum(q2);
        if (hf == 0) stx8(rs, (unsigned)((cb * 2 + (wl >> 1)) * 512 + row * 16 + (wl & 1) * 8), WS_ST5 + (unsigned)b * 8192, u32x2{f2u(mw), f2u(q2)});
      } else {
        stx16(rs, fo + ro, WS_U + (unsigned)b * 229376u, v[0]); stx16(rs, fo + ro, WS_U + (unsigned)b * 229376u + 512, v[1]);
      }
      SEG(3);
      pending = R >= i + 3 && i >= 2 && SG != SG_Q;
      if (SG == SG_Q ? i == NBLK - 1 : !pending) { WAIT_V(0); announce(i); }
      if (SG != SG_Q && R >= i + 3) fetch(2 * i + 5);
      if (!KV) WST(rs, wk, 16 + b);
      SEG(4);
      if (!more) BAR();
    }
  }
}

__device__ __forceinline__ void wait32(__amdgpu_buffer_rsrc_t rs, int first, int mine, int lane, unsigned need, unsigned& cap) {
  wait_ge(rs, 0, F_ST + (unsigned)(first + mine) * 128u, need, cap);
  wait_ge(rs, (unsigned)(lane & 31) * 128u, F_ST + (unsigned)first * 128u, need, cap);
}
__device__ __forceinline__ void sum_partials(__amdgpu_buffer_rsrc_t rs, unsigned l16, unsigned so, float (&a)[16]) {
  u32x4 pp[4][2];
#pragma unroll
  for (int k = 0; k < 4; ++k) { pp[k][0] = ldx16(rs, l16, so + k * 65536u); pp[k][1] = ldx16(rs, l16, so + k * 65536u + 1024); }
#pragma unroll
  for (int i = 0; i < 8; ++i) {
    f32x2 t = {0.f, 0.f};
#pragma unroll
    for (int k = 0; k < 4; ++k) t += f32x2{lo2f(pp[k][i >> 2][i & 3]), hi2f(pp[k][i >> 2][i & 3])};
    a[2 * i] = rbf(t[0]); a[2 * i + 1] = rbf(t[1]);
  }
}
__device__ __forceinline__ void put_row(__amdgpu_buffer_rsrc_t rs, unsigned l16, unsigned so, const float (&v)[16]) {
  stx16(rs, l16, so, u32x4{pack2(v[0], v[1]), pack2(v[2], v[3]), pack2(v[4], v[5]), pack2(v[6], v[7])});
  stx16(rs, l16, so + 1024, u32x4{pack2(v[8], v[9]), pack2(v[10], v[11]), pack2(v[12], v[13]), pack2(v[14], v[15])});
}
__device__ __forceinline__ void load_vec16(const float* p, int lane, float (&v)[16]) {
#pragma unroll
  for (int i = 0; i < 4; ++i) { const f32x4 t = *(const f32x4*)(p + (i >> 1) * 512 + lane * 8 + (i & 1) * 4); v[4 * i] = t[0]; v[4 * i + 1] = t[1]; v[4 * i + 2] = t[2]; v[4 * i + 3] = t[3]; }
}

__device__ __forceinline__ void stage_mid(__amdgpu_buffer_rsrc_t rs, const u16* x, const float* mod, int wk, int cu, unsigned& cap) {
  const int tid = threadIdx.x, lane = tid & 63, w = __builtin_amdgcn_readfirstlane(tid >> 6);
  const int id = w * 12 + cu, grp = id >> 5, rl = id & 31;
  const unsigned l16 = lane * 16;
  float ga[16], sc[16], sh[16];
  load_vec16(mod + 2048, lane, ga); load_vec16(mod + 4096, lane, sc); load_vec16(mod + 3072, lane, sh);
#pragma unroll
  for (int i = 0; i < 16; ++i) sc[i] = rbf(1.0f + sc[i]);
  for (int seg = grp; seg < NSEG; seg += 3) {
    const int row = seg * 32 + rl;
    float v[16], a[16];
    load_row16(x + (size_t)row * D_, lane, v);
    wait32(rs, P_S2, rl, lane, (unsigned)seg + 1u, cap);
    sum_partials(rs, l16, WS_P2 + (unsigned)seg * 262144u + (unsigned)rl * 2048, a);
#pragma unroll
    for (int i = 0; i < 16; ++i) v[i] = rbf(v[i] + rbf(ga[i] * a[i]));
    put_row(rs, l16, WS_X1 + (unsigned)row * 2048, v);
    ln_row(v);
#pragma unroll
    for (int i = 0; i < 16; ++i) v[i] = rbf(rbf(v[i] * sc[i]) + sh[i]);
    put_row(rs, l16, WS_H2 + (unsigned)row * 2048, v);
    ln_row(v);
    put_row(rs, l16, WS_G + (unsigned)row * 2048, v);
    WAIT_V(0);
    if (lane == 0) stx4(rs, 0, F_ST + (unsigned)(P_S3 + grp * 32 + rl) * 128, (unsigned)seg + 1u);
    WST(rs, wk, 16 + seg);
  }
}

__device__ __forceinline__ void stage_out(__amdgpu_buffer_rsrc_t rs, u16* out, const float* mod, int wk, int cu, unsigned& cap) {
  const int tid = threadIdx.x, lane = tid & 63, w = __builtin_amdgcn_readfirstlane(tid >> 6);
  const int grp = w >> 2, rl = cu * 4 + (w & 3);
  const unsigned l16 = lane * 16;
  float gb[16];
  load_vec16(mod + 5120, lane, gb);
  for (int b = grp; b < 65; b += 2) {
    const int t = 32 * b - 2 + rl;
    if (t < 0 || t >= S_) continue;
    wait_ge(rs, (unsigned)(lane & 7) * 128u, F_ST + P_S5 * 128u, (unsigned)b + 1u, cap);
    const unsigned xo = (unsigned)((lane >> 2) * 2048 + (lane & 3) * 256), xs = WS_X2 + (unsigned)b * 65536u + (unsigned)((rl >> 4) * 1024 + (rl & 15) * 16);
    const u32x4 x2a = ldx16(rs, xo, xs), x2b = ldx16(rs, xo, xs + 32768);
    const u32x4 x1a = ldx16(rs, l16, WS_X1 + (unsigned)t * 2048), x1b = ldx16(rs, l16, WS_X1 + (unsigned)t * 2048 + 1024);
    wait32(rs, P_S8, rl, lane, (unsigned)b + 1u, cap);
    float a[16];
    sum_partials(rs, l16, WS_P8 + (unsigned)b * 262144u + (unsigned)rl * 2048, a);
#pragma unroll
    for (int i = 0; i < 4; ++i) {
      a[2 * i] = lo2f(x1a[i]) + rbf(gb[2 * i] * rbf(lo2f(x2a[i]) + a[2 * i])); a[2 * i + 1] = hi2f(x1a[i]) + rbf(gb[2 * i + 1] * rbf(hi2f(x2a[i]) + a[2 * i + 1]));
      a[8 + 2 * i] = lo2f(x1b[i]) + rbf(gb[8 + 2 * i] * rbf(lo2f(x2b[i]) + a[8 + 2 * i])); a[9 + 2 * i] = hi2f(x1b[i]) + rbf(gb[9 + 2 * i] * rbf(hi2f(x2b[i]) + a[9 + 2 * i]));
    }
    store_row16(out + (size_t)t * D_, lane, a);
    WST(rs, wk, 16 + b);
  }
}

__device__ __forceinline__ void worker(const MainP& p, LAS unsigned char* lds, int xw, int j) {
  const int tid = threadIdx.x, lane = tid & 63, w = __builtin_amdgcn_readfirstlane(tid >> 6);
  const __amdgpu_buffer_rsrc_t rs = mk_rs(p.ws, WS_END);
  const float* mod = (const float*)(p.ws + WS_MOD);
  unsigned cap = SPIN_CAP;
  const int wk = xw * 32 + j;
  WST(rs, wk, 0);
  {
    const int c = j * 5 + xw;
    for (int row = c * 8 + w; row < S_; row += 8 * NLN) {
      float v[16];
      load_row16(p.x + (size_t)row * D_, lane, v);
      ln_row(v);
      modulate(v, mod + 1024, mod, lane);
      put_row(rs, (unsigned)lane * 16, WS_H + (unsigned)row * 2048, v);
    }
    publish(rs, F_LN + c * 128, 1u);
    WST(rs, wk, 1);
  }
  if (xw == 4) stage_gemm<SG_Q>(lds, rs, p.Wq, wk, 0, j, P_S1 + j, cap);
  else {
    if (xw & 1) stage_gemm<SG_V>(lds, rs, p.Wv, wk, xw >> 1, j, P_PJ + xw * 32 + j, cap);
    else stage_gemm<SG_K>(lds, rs, p.Wk, wk, xw >> 1, j, P_PJ + xw * 32 + j, cap);
    WST(rs, wk, 3);
    if (xw == 0) stage_gemm<SG_WO>(lds, rs, p.Wo, wk, j >> 3, j & 7, P_S2 + j, cap);
    else if (xw == 1) stage_gemm<SG_W4>(lds, rs, p.w4, wk, j >> 3, j & 7, P_S8 + j, cap);
    else if (j >= 28) stage_out(rs, p.out, mod, wk, (xw - 2) * 4 + j - 28, cap);
    else if (xw == 2) stage_gemm<SG_W3>(lds, rs, p.w3, wk, 0, j, P_S7 + j, cap);
    else if (j < 8) stage_gemm<SG_W1>(lds, rs, p.w1, wk, 0, j, P_S4 + j, cap);
    else if (j < 16) stage_gemm<SG_W2>(lds, rs, p.w2, wk, 0, j - 8, P_S5 + j - 8, cap);
    else stage_mid(rs, p.x, mod, wk, j - 16, cap);
  }
  if (cap == 0u && lane == 0) stx4(rs, 0, X_CTL, 1u);
}

#define ST_DECL
#define ST0()
#define ST(i)
#define ST_FLUSH()
constexpr int R_W = 0, R_MEM = 131072, R_QK = R_MEM + 16384, R_KT = R_QK + 8192, R_RED = R_MEM + 16384, R_LDS = 163840;
__device__ __forceinline__ void bar_lo(int w) { asm volatile("s_cmp_ge_u32 %0, 4\n\ts_cbranch_scc1 1f\n\ts_barrier\n\t1:" :: "s"(w) : "scc", "memory"); }
__device__ __forceinline__ void bar_hi(int w) { asm volatile("s_cmp_lt_u32 %0, 4\n\ts_cbranch_scc1 1f\n\ts_setprio 3\n\ts_barrier\n\t1:" :: "s"(w) : "scc", "memory"); }
__device__ __forceinline__ void issue4(__amdgpu_buffer_rsrc_t rs, unsigned vo, unsigned s0, unsigned s1, unsigned s2, unsigned s3, u32x4& a0, u32x4& a1, u32x4& a2, u32x4& a3) {
  asm volatile("buffer_load_dwordx4 v[240:243], %[vo], %[rs], %[s0] offen sc1\n\tbuffer_load_dwordx4 v[244:247], %[vo], %[rs], %[s1] offen sc1\n\t"
               "buffer_load_dwordx4 v[248:251], %[vo], %[rs], %[s2] offen sc1\n\tbuffer_load_dwordx4 v[252:255], %[vo], %[rs], %[s3] offen sc1"
               : "=&{v[240:243]}"(a0), "=&{v[244:247]}"(a1), "=&{v[248:251]}"(a2), "=&{v[252:255]}"(a3)
               : [vo] "v"(vo), [rs] "s"(rs), [s0] "s"(s0), [s1] "s"(s1), [s2] "s"(s2), [s3] "s"(s3) : "memory");
}
#define ARRIVE(R0, R1, R2, R3, CNT) \
  asm volatile("s_waitcnt vmcnt(" #CNT ")\n\t" \
               "1:\n\t" \
               "v_min3_u32 %[t], v" #R0 ", v" #R1 ", v" #R2 "\n\t" \
               "v_min_u32 %[t], %[t], v" #R3 "\n\t" \
               "v_cmp_eq_u32 vcc, 0, %[t]\n\t" \
               "s_cbranch_vccz 2f\n\t" \
               "s_mov_b64 exec, vcc\n\t" \
               "buffer_load_dwordx4 v[" #R0 ":" #R3 "], %[vo], %[rs], %[so] offen sc1\n\t" \
               "s_mov_b64 exec, -1\n\t" \
               "s_waitcnt vmcnt(0)\n\t" \
               "s_sub_u32 %[cap], %[cap], 1\n\t" \
               "s_cbranch_scc0 1b\n\t" \
               "s_mov_b32 %[cap], 0\n\t" \
               "2:" \
               : "+{v[" #R0 ":" #R3 "]}"(a), [t] "=&v"(t), [cap] "+s"(cap) : [vo] "v"(vo), [rs] "s"(rs), [so] "s"(so) : "vcc", "scc", "memory")
template <int I> __device__ __forceinline__ void arrive(__amdgpu_buffer_rsrc_t rs, unsigned vo, unsigned so, u32x4& a, unsigned& cap) {
  unsigned t;
  if (I == 0) ARRIVE(240, 241, 242, 243, 3);
  else if (I == 1) ARRIVE(244, 245, 246, 247, 2);
  else if (I == 2) ARRIVE(248, 249, 250, 251, 1);
  else ARRIVE(252, 253, 254, 255, 0);
}
__device__ __forceinline__ unsigned half_of(unsigned v) { unsigned r; asm volatile("v_lshrrev_b32 %0, 1, %1" : "=v"(r) : "v"(v)); return r; }
__device__ __forceinline__ void issue2(__amdgpu_buffer_rsrc_t rs, unsigned l8, unsigned so, int w, u32x4& a) {
  asm volatile("s_cmp_ge_u32 %[w], 4\n\ts_cbranch_scc1 1f\n\tv_lshrrev_b32 v255, 1, %[l8]\n\t"
               "buffer_load_dwordx2 v[252:253], v255, %[rs], %[so] offen sc1\n\tbuffer_load_dwordx2 v[254:255], v255, %[rs], %[so] offen offset:512 sc1\n\t1:"
               : "=&{v[252:255]}"(a) : [l8] "v"(l8), [rs] "s"(rs), [so] "s"(so), [w] "s"(w) : "scc", "memory");
}
__device__ __forceinline__ void arrive2(__amdgpu_buffer_rsrc_t rs, unsigned l8, unsigned so, u32x4& a, unsigned& cap) {
  unsigned t;
  asm volatile("s_waitcnt vmcnt(0)\n\t"
               "1:\n\t"
               "v_min3_u32 %[t], v252, v253, v254\n\t"
               "v_min_u32 %[t], %[t], v255\n\t"
               "v_cmp_eq_u32 vcc, 0, %[t]\n\t"
               "s_cbranch_vccz 2f\n\t"
               "v_lshrrev_b32 %[t], 1, %[l8]\n\t"
               "buffer_load_dwordx2 v[252:253], %[t], %[rs], %[so] offen sc1\n\t"
               "buffer_load_dwordx2 v[254:255], %[t], %[rs], %[so] offen offset:512 sc1\n\t"
               "s_waitcnt vmcnt(0)\n\t"
               "s_sub_u32 %[cap], %[cap], 1\n\t"
               "s_cbranch_scc0 1b\n\t"
               "s_mov_b32 %[cap], 0\n\t"
               "2:"
               : "+{v[252:255]}"(a), [t] "=&v"(t), [cap] "+s"(cap) : [l8] "v"(l8), [rs] "s"(rs), [so] "s"(so) : "vcc", "scc", "memory");
}
__device__ __forceinline__ unsigned ror8(unsigned v) { return __builtin_amdgcn_update_dpp(0u, v, 0x128, 0xF, 0xF, true); }
__device__ __forceinline__ void gather_mem(__amdgpu_buffer_rsrc_t rs, unsigned lbase, int s, int w, unsigned l16, unsigned& cap) {
  const unsigned so = X_MEM + s * 16384 + w * 2048;
  u32x4 a0, a1;
  asm volatile("buffer_load_dwordx4 v[248:251], %[vo], %[rs], %[so] offen sc1\n\tbuffer_load_dwordx4 v[252:255], %[vo], %[rs], %[so] offen offset:1024 sc1"
               : "=&{v[248:251]}"(a0), "=&{v[252:255]}"(a1) : [vo] "v"(l16), [rs] "s"(rs), [so] "s"(so) : "memory");
  LAS unsigned char* d = lad(l16, lbase + R_MEM + w * 2048);
  arrive<2>(rs, l16, so, a0, cap);
  *(LAS u32x4*)(d) = a0;
  arrive<3>(rs, l16, so + 1024, a1, cap);
  *(LAS u32x4*)(d + 1024) = a1;
}
__device__ __forceinline__ unsigned mem_off(int lane) { return (unsigned)((lane & 1) * 512 + (((lane >> 1) ^ ((lane & 1) * 16)) * 8)); }
__device__ __forceinline__ bf16x8 mem_frag(LAS unsigned char* mp, int chunk) { return cat8(trread(mp + chunk * 1024), trread(mp + chunk * 1024 + 256)); }
__device__ __forceinline__ bf16x16 w_frag(LAS unsigned char* p) {
  return cat16(cat8(trread(p), trread(p + 2048)), cat8(trread(p + 4096), trread(p + 6144)));
}
__device__ __forceinline__ bf16x16 l_frag(LAS unsigned char* p, int i) { return cat16(*(const LAS bf16x8*)(p + i * 2048), *(const LAS bf16x8*)(p + i * 2048 + 1024)); }
__device__ __forceinline__ void make_resident(LAS unsigned char* wb, unsigned lo, unsigned l16) {
#pragma unroll
  for (int c = 0; c < 2; ++c) {
    bf16x16 f[4];
#pragma unroll
    for (int t = 0; t < 4; ++t) f[t] = w_frag(wb + c * 8192 + (lo ^ (t << 5)));
    WAIT_L0(); SCHED();
#pragma unroll
    for (int t = 0; t < 4; ++t) {
      *(LAS bf16x8*)(wb + (c * 4 + t) * 2048 + l16) = __builtin_shufflevector(f[t], f[t], 0, 1, 2, 3, 4, 5, 6, 7);
      *(LAS bf16x8*)(wb + (c * 4 + t) * 2048 + 1024 + l16) = __builtin_shufflevector(f[t], f[t], 8, 9, 10, 11, 12, 13, 14, 15);
    }
    WAIT_L0(); SCHED();
  }
}
__device__ __forceinline__ f32x4 fold(f32x4 a, f32x4 b) {
  f32x4 r;
#pragma unroll
  for (int i = 0; i < 4; ++i) {
    const auto t = __builtin_amdgcn_permlane32_swap(f2u(a[i]), f2u(b[i]), false, false);
    r[i] = u2f(t[0]) + u2f(t[1]);
  }
  return r;
}

struct Sched { unsigned tau, prev; int ea, eb, d1; bool moved; };
__device__ __forceinline__ void retime(__amdgpu_buffer_rsrc_t rs, unsigned l16, int s, Sched& t, const MainP& p, unsigned& cap) {
  if (s > 0) {
    unsigned vo;
    asm volatile("v_lshlrev_b32 %0, 3, %1" : "=v"(vo) : "v"(l16));
    unsigned va;
    asm volatile("v_and_b32 %0, 0xF80, %1" : "=v"(va) : "v"(vo));
    wait_ge(rs, vo, X_TS + (unsigned)s * 12288, 1u, cap);
    wait_ge(rs, va, X_TS + (unsigned)s * 12288 + 8192, 1u, cap);
    int vb = (int)(ldx4(rs, vo, X_TS + (unsigned)s * 12288) - t.prev), vc = (int)(ldx4(rs, va, X_TS + (unsigned)s * 12288 + 8192) - t.prev);
#pragma unroll
    for (int o = 32; o >= 1; o >>= 1) { vb = max(vb, __shfl_xor(vb, o)); vc = max(vc, __shfl_xor(vc, o)); }
    const int tb = __builtin_amdgcn_readfirstlane(vb), ta = __builtin_amdgcn_readfirstlane(vc);
    const int late = tb + p.d3 - (int)(t.tau - t.prev);
    if (s > 2) {
      const int up = s < 5 ? 80 : 8, dn = s < 10 ? 40 : 8;
      if (!t.moved) t.ea = max(t.ea - dn, min(ta * 8, t.ea + up));
      if (ta + p.d3 <= t.d1) t.eb = max(t.eb - dn, min((tb - t.d1) * 8, t.eb + up));
    }
    t.moved = late > 0;
    if (late > 0) t.tau += late + min(p.d4, 8 * late);
  }
  t.d1 = (t.ea >> 3) + p.d1;
}
__device__ __forceinline__ void advance(Sched& t, const MainP& p) { t.prev = t.tau; t.tau += t.d1 + (t.eb >> 3) + p.d2; }

__device__ __forceinline__ void role_a(const MainP& p, LAS unsigned char* lds, int h) {
  const int tid = threadIdx.x, lane = tid & 63, w = __builtin_amdgcn_readfirstlane(tid >> 6);
  const int cg = w & 3, kh = w >> 2;
  const __amdgpu_buffer_rsrc_t rs = mk_rs(p.ws, WS_END);
  const unsigned lbase = (unsigned)(size_t)lds;
  unsigned l16 = lane * 16;
  asm volatile("" : "+v"(l16));
  const int sidx = (lane & 8) ? 0xEEEE : 0x4444;
  bf16x16 W[24];
  unsigned qoff;
  {
    const int fr = lane & 15, g = lane >> 4, tu = 4 * (g & 1) + (fr >> 2), ub = lane >> 3;
    const unsigned lo = (g >> 1) * 1024 + tu * 128 + ((tu >> 1) << 5) + (fr & 3) * 8;
    qoff = R_QK + ((fr >> 1) & 1) * 512 + (g >> 1) * 256 + (fr & 1) * 128 + (8 * (g & 1) + (fr >> 2)) * 8;
    const __amdgpu_buffer_rsrc_t rw = mk_rs(cg < 2 ? p.Wq : p.Wk, D_ * HD * 2);
    const unsigned vo = (unsigned)(ub * HD + 16 * (((lane >> 1) & 3) ^ (ub >> 1)) + 8 * (lane & 1)) * 2u;
    const unsigned so = (unsigned)(kh * 512 * HD + h * DH + (cg & 1) * 64) * 2u;
    auto stage = [&](int c) {
#pragma unroll
      for (int rho = 0; rho < 8; ++rho) dma16(rw, vo, so + (unsigned)((c * 64 + rho * 8) * HD) * 2u, lbase + R_W + w * 16384 + (c & 1) * 8192 + rho * 1024);
    };
    stage(0); stage(1);
#pragma unroll
    for (int c = 0; c < 6; ++c) {
      WAIT_V(8);
#pragma unroll
      for (int t = 0; t < 4; ++t) W[4 * c + t] = w_frag(lds + R_W + w * 16384 + (c & 1) * 8192 + (lo ^ (t << 5)));
      WAIT_L0(); SCHED();
      stage(c + 2);
    }
    WAIT_V(0);
    make_resident(lds + R_W + w * 16384, lo, l16);
  }
  WST(rs, 160 + (h >> 1), 4);
  unsigned cap = SPIN_CAP;
  wait_ge(rs, (l16 & 16) * 256, F_ST + (unsigned)(P_PJ + h) * 128, 1u, cap);
  dma16x(rs, l16, WS_K + h * 8192 + w * 1024, lbase + R_KT + w * 1024);
  Sched tt; tt.tau = rt32() - 100000; tt.prev = tt.tau; tt.ea = 195 * 8; tt.eb = 118 * 8; tt.d1 = 0; tt.moved = true;
  const int cu_ = h; (void)cu_;
  ST_DECL
  for (int s = 0; s < NSEG; ++s) {
    LAS unsigned char* lp = lad(l16, lbase + R_W + w * 16384);
    bf16x16 L[2];
    L[0] = l_frag(lp, 0); L[1] = l_frag(lp, 1);
    unsigned lz = l16;
    asm volatile("" : "+v"(lz));
    LAS unsigned char* mp = lad(mem_off((int)(lz >> 4)), lbase + R_MEM + kh * 8192);
    SCHED();
    if (w == 7) wait_until(tt.tau);
    BAR();
    ST0();
    gather_mem(rs, lbase, s, (w + h) & 7, l16, cap);
    ST(1);
    LBAR();
    ST(2);
    f32x4 c[4];
#pragma unroll
    for (int t = 0; t < 4; ++t) c[t] = f32x4{0.f, 0.f, 0.f, 0.f};
    bf16x8 al, af[2];
    u32x4 vt0, vt1;
    u32x4 vmv;
    const unsigned vso = X_VM + (unsigned)(s * NH + h) * 4096 + cg * 1024;
    al = mem_frag(mp, 6); af[0] = mem_frag(mp, 0); af[1] = mem_frag(mp, 1);
#pragma unroll
    for (int i = 0; i < 8; ++i) {
      c[i & 3] = smf(al, L[i & 1], c[i & 3], sidx);
      SCHED();
      if (i == 3) al = mem_frag(mp, 7);
      if (i < 6) L[i & 1] = l_frag(lp, i + 2);
      if (i == 6) {
        const unsigned vo = WS_VT + (unsigned)(s * NH + h) * 8192 + cg * 2048;
        asm volatile("s_cmp_ge_u32 %5, 4\n\ts_cbranch_scc1 1f\n\t"
                     "buffer_load_dwordx4 %0, %2, %3, %4 offen sc1\n\tbuffer_load_dwordx4 %1, %2, %3, %4 offen offset:1024 sc1\n\t1:"
                     : "=&v"(vt0), "=&v"(vt1) : "v"(l16), "s"(rs), "s"(vo), "s"(w) : "scc", "memory");
      }
      SCHED();
#pragma unroll
      for (int r = 3 * i; r < 3 * i + 3; ++r) {
        c[(r + 1) & 3] = smf(af[(r >> 2) & 1], W[(r & ~3) + ((r + 1) & 3)], c[(r + 1) & 3], sidx);
        SCHED();
        if ((r & 3) == 3 && (r >> 2) + 2 < 6) af[(r >> 2) & 1] = mem_frag(mp, (r >> 2) + 2);
        SCHED();
      }
    }
    {
      LAS unsigned char* qw = lad(half_of(l16), lbase + R_QK + cg * 2048 + kh * 512);
      const f32x4 f0 = fold(c[0], c[1]);
      *(LAS u32x2*)(qw) = u32x2{pack2(f0[0], f0[1]), pack2(f0[2], f0[3])};
      const f32x4 f1 = fold(c[2], c[3]);
      *(LAS u32x2*)(qw + 1024) = u32x2{pack2(f1[0], f1[1]), pack2(f1[2], f1[3])};

    }
    ST(3);
    if (w < 4) {
      bf16x8 k1[4], k2[4];
      LAS unsigned char* kb = lad(l16, lbase + R_KT);
#pragma unroll
      for (int ks = 0; ks < 2; ++ks) { k1[ks] = *(const LAS bf16x8*)(kb + ks * 1024); k2[ks] = *(const LAS bf16x8*)(kb + 4096 + ks * 1024); }
      LBAR();
      ST(4);
      issue2(rs, l16, vso, w, vmv);
      f32x4 t0 = {0.f, 0.f, 0.f, 0.f}, t1 = t0, t2 = t0;
      LAS unsigned char* qb = lad(qoff, lbase);
      bf16x8 qf[4], kf[4];
#pragma unroll
      for (int ks = 0; ks < 2; ++ks) {
        qf[ks] = cat8(trread(qb + ks * 1024), trread(qb + ks * 1024 + 32));
        kf[ks] = cat8(trread(qb + 4096 + ks * 1024), trread(qb + 4096 + ks * 1024 + 32));
      }
      SCHED();
#pragma unroll
      for (int ks = 0; ks < 4; ++ks) {
        t1 = mfma32(k1[ks], qf[ks], t1); t2 = mfma32(k2[ks], qf[ks], t2); t0 = mfma32(kf[ks], qf[ks], t0);
        SCHED();
        if (ks < 2) {
          qf[ks + 2] = cat8(trread(qb + (ks + 2) * 1024), trread(qb + (ks + 2) * 1024 + 32));
          k1[ks + 2] = *(const LAS bf16x8*)(kb + (ks + 2) * 1024); k2[ks + 2] = *(const LAS bf16x8*)(kb + 4096 + (ks + 2) * 1024);
          kf[ks + 2] = cat8(trread(qb + 4096 + (ks + 2) * 1024), trread(qb + 4096 + (ks + 2) * 1024 + 32));
        }
        SCHED();
      }
      ST(5);
      {
        const float sl = 0.08838834764831845f * 1.4426950408889634f;
        float z[4] = {t1[0], t1[1], t1[2], t1[3]}, y = t0[0], mx;
        {
          float u1 = t0[1], u2 = t0[2], u3 = t0[3];
#define M8(Z, B) "v_add_f32_dpp " Z ", " Z ", " Z " row_ror:8 row_mask:0xf bank_mask:0x3\n\tv_add_f32_dpp " Z ", " B ", " B " row_ror:8 row_mask:0xf bank_mask:0xc\n\t"
          asm("s_nop 7\n\t"
              "v_permlane32_swap_b32 %4, %6\n\tv_permlane32_swap_b32 %5, %7\n\t"
              M8("%0", "%9") "v_add_f32 %4, %4, %6\n\tv_add_f32 %5, %5, %7\n\t" M8("%1", "%10") M8("%2", "%11") M8("%4", "%5") M8("%3", "%12")
              "v_max3_f32 %8, %0, %1, %2\n\tv_max3_f32 %8, %8, %4, %3\n\ts_nop 1\n\t"
              "v_max_f32_dpp %8, %8, %8 row_ror:8 row_mask:0xf bank_mask:0xf\n\tv_mov_b32 %5, %8\n\ts_nop 1\n\t"
              "v_permlane16_swap_b32 %8, %5\n\tv_max_f32 %8, %8, %5\n\tv_mov_b32 %5, %8\n\ts_nop 1\n\t"
              "v_permlane32_swap_b32 %8, %5\n\tv_max_f32 %8, %8, %5"
              : "+v"(z[0]), "+v"(z[1]), "+v"(z[2]), "+v"(z[3]), "+v"(y), "+v"(u1), "+v"(u2), "+v"(u3), "=&v"(mx) : "v"(t2[0]), "v"(t2[1]), "v"(t2[2]), "v"(t2[3]));
#undef M8
        }
        const float nm = -mx * sl;
#pragma unroll
        for (int r = 0; r < 4; ++r) z[r] = __builtin_amdgcn_exp2f(fmaf(z[r], sl, nm));
        const unsigned p0 = pack2(z[0], z[1]), p1 = pack2(z[2], z[3]);
        const u32x4 pt = {p0, p1, ror8(p0), ror8(p1)};
        SCHED();
        asm volatile("s_waitcnt vmcnt(2)" : "+v"(vt0), "+v"(vt1) :: "memory");
        f32x4 o0 = {0.f, 0.f, 0.f, 0.f}, o1 = o0;
        o0 = mfma32(__builtin_bit_cast(bf16x8, vt0), __builtin_bit_cast(bf16x8, pt), o0);
        o1 = mfma32(__builtin_bit_cast(bf16x8, vt1), __builtin_bit_cast(bf16x8, pt), o1);
        y = __builtin_amdgcn_exp2f(fmaf(y, sl, nm));
        const unsigned pm1 = pack2(y, u2f(ror8(f2u(y))));
        const auto pms = __builtin_amdgcn_permlane32_swap(pm1, pm1, false, false);
        const u32x2 pm = {pms[0], pms[1]};
        float inv = __builtin_amdgcn_rcpf(gsum(dpp_add<0x128>(((z[0] + z[1]) + (z[2] + z[3])) + y)));
        o0 *= inv; o1 *= inv;
        unsigned ov;
        unsigned ot;
        asm volatile("v_and_b32 %0, 0x30, %5\n\tv_lshrrev_b32 %1, 2, %5\n\tv_and_b32 %1, 0xC0, %1\n\tv_or_b32 %0, %0, %1\n\t"
                     "v_and_b32 %1, 0x40, %5\n\tv_lshl_or_b32 %0, %1, 9, %0\n\tv_and_b32 %1, 0x80, %5\n\tv_lshl_or_b32 %0, %1, 24, %0"
                     : "=&v"(ov), "=&v"(ot), "+v"(o0), "+v"(o1), "+v"(inv) : "v"(l16));
        ST(6);
        arrive2(rs, l16, vso, vmv, cap);
        ST(7);
        const f32x4 n0 = mfma16n(u32x2{vmv[0], vmv[1]}, pm, f32x4{0.f, 0.f, 0.f, 0.f}), n1 = mfma16n(u32x2{vmv[2], vmv[3]}, pm, f32x4{0.f, 0.f, 0.f, 0.f});
        {
          const u32x4 v = {nz(pack2(fmaf(n0[0], inv, o0[0]), fmaf(n0[1], inv, o0[1]))), nz(pack2(fmaf(n0[2], inv, o0[2]), fmaf(n0[3], inv, o0[3]))),
                           nz(pack2(fmaf(n1[0], inv, o1[0]), fmaf(n1[1], inv, o1[1]))), nz(pack2(fmaf(n1[2], inv, o1[2]), fmaf(n1[3], inv, o1[3])))};
          stx16(rs, ov, X_OX + s * 65536 + h * 1024 + w * 256, v);
        }
        if (w == 3) { const unsigned t = max(rt32(), 1u); if (l16 == 0) stx4(rs, 0, X_TS + (unsigned)(s + 1) * 12288 + (unsigned)(64 + h) * 128, t); }
        ST(8);
      }
    } else {
      LBAR();
      LAS unsigned char* qb = lad(qoff, lbase + 4096 + (w - 4) * 1024);
      u32x4 kf = __builtin_bit_cast(u32x4, cat8(trread(qb), trread(qb + 32)));
#pragma unroll
      for (int i = 0; i < 4; ++i) kf[i] = pack2(dpp_add<0x128>(lo2f(kf[i])), dpp_add<0x128>(hi2f(kf[i])));
      stx16(rs, l16, WS_KM + (unsigned)(s * NH + h) * 4096 + (w - 4) * 1024, kf);
      WAIT_V(0);
    }
    LBAR();
    if (w == 4 && l16 == 0) stx4(rs, 0, F_CH + h * 128, (unsigned)s + 1u);
    ST(9);
    if (w == 7) { retime(rs, l16, s, tt, p, cap); advance(tt, p); }
    if (s + 1 < NSEG) {
      unsigned fo;
      asm volatile("v_and_b32 %0, 16, %1\n\tv_lshlrev_b32 %0, 8, %0" : "=v"(fo) : "v"(l16));
      wait_ge(rs, fo, F_ST + (unsigned)(P_PJ + ((s + 1) & 1) * 64 + h) * 128, (unsigned)s + 2u, cap);
      dma16x(rs, l16, WS_K + (unsigned)((s + 1) * NH + h) * 8192 + w * 1024, lbase + R_KT + w * 1024);
    }
  }
  ST_FLUSH();
  if (cap == 0u && l16 == 0) stx4(rs, 0, X_CTL, 1u);
}

__device__ __forceinline__ void role_vb(const MainP& p, LAS unsigned char* lds, int rg, int cb) {
  const int tid = threadIdx.x, lane = tid & 63, w = __builtin_amdgcn_readfirstlane(tid >> 6);
  const int kh = w >> 2, wr = (w + cb) & 7;
  const __amdgpu_buffer_rsrc_t rs = mk_rs(p.ws, WS_END);
  const unsigned lbase = (unsigned)(size_t)lds;
  unsigned l16 = lane * 16, l8 = lane * 8, moff = mem_off(lane), roff, soff = (unsigned)((lane >> 5) * 256 + (lane & 31) * 4);
  const int sidx = (lane & 8) ? 0xEEEE : 0x4444;
  bf16x16 WV[8], WB[16];
  {
    const int fr = lane & 15, g = lane >> 4, tu = 4 * (g & 1) + (fr >> 2), ub = lane >> 3;
    { const int sl = mem_slot(16 * (g >> 1) + fr); roff = R_RED + w * 512 + ((sl >> 5) * 32 + (sl & 15) * 2 + (g & 1)) * 8; }
    const __amdgpu_buffer_rsrc_t rv = mk_rs(p.Wv, D_ * HD * 2), ro = mk_rs(p.Wo, D_ * HD * 2);
    const unsigned vv = (unsigned)(ub * HD + 16 * (((lane >> 1) & 3) ^ (ub >> 1)) + 8 * (lane & 1)) * 2u, sv = (unsigned)(8 * w * HD + cb * DH + rg * 64) * 2u;
#pragma unroll
    for (int c = 0; c < 16; ++c) dma16(rv, vv, sv + (unsigned)c * 64 * HD * 2, lbase + R_W + c * 8192 + w * 1024);
    WAIT_V(0); BAR();
    const unsigned lo = (g >> 1) * 1024 + tu * 128 + (((tu >> 1) ^ (w & 3)) << 5) + (fr & 3) * 8;
#pragma unroll
    for (int c = 0; c < 8; ++c) WV[c] = w_frag(lds + R_W + (kh * 8 + c) * 8192 + lo);
    LBAR();
    const int u = (lane >> 2) & 7;
    const unsigned vw = (unsigned)((16 * (u >> 2) + 8 * ((u >> 1) & 1) + 4 * (lane >> 5) + (u & 1)) * D_ + (lane & 3) * 8) * 2u;
    const unsigned sw = (unsigned)(wr * 512 * D_ + cb * 32) * 2u;
    const unsigned bo = (g >> 1) * 1024 + tu * 64 + (fr & 3) * 8;
#pragma unroll
    for (int ps = 0; ps < 2; ++ps) {
#pragma unroll
      for (int q = 0; q < 16; ++q) {
        const int r = (q >> 1) & 3, hb = q & 1;
        dma16(ro, vw, sw + (unsigned)(((ps * 2 + (q >> 3)) * 128 + 64 * (r >> 1) + 32 * hb + 2 * (r & 1)) * D_) * 2u, lbase + R_W + w * 16384 + q * 1024);
      }
      WAIT_V(0);
#pragma unroll
      for (int f = 0; f < 8; ++f) WB[ps * 8 + f] = w_frag(lds + R_W + w * 16384 + (f >> 2) * 8192 + ((f >> 1) & 1) * 512 + (f & 1) * 32 + bo);
      WAIT_L0(); SCHED();
    }
  }
  WST(rs, 176 + rg * 8 + (cb >> 2), 4);
  asm volatile("" : "+v"(l16), "+v"(l8), "+v"(moff), "+v"(roff), "+v"(soff));
  unsigned cap = SPIN_CAP;
  Sched tt; tt.tau = rt32() - 100000; tt.prev = tt.tau; tt.ea = 195 * 8; tt.eb = 118 * 8; tt.d1 = 0; tt.moved = true;
  const int cu_ = 32 + (rg * 32 + cb) / 2; (void)cu_;
  ST_DECL
  for (int s = 0; s < NSEG; ++s) {
    if (w == 7) wait_until(tt.tau);
    BAR();
    ST0();
    gather_mem(rs, lbase, s, wr, l16, cap);
    ST(1);
    LBAR();
    ST(2);
    {
      f32x4 c0 = {0.f, 0.f, 0.f, 0.f};
      LAS unsigned char* mp = lad(moff, lbase + R_MEM + kh * 8192);
      bf16x8 af[8];
#pragma unroll
      for (int c = 0; c < 8; ++c) af[c] = mem_frag(mp, c);
      SCHED();
#pragma unroll
      for (int c = 0; c < 8; ++c) c0 = smf(af[c], WV[c], c0, sidx);
      const f32x4 f = fold(c0, c0);
      if (l16 < 512) stx8(rs, l8, X_VM + (unsigned)(s * NH + cb) * 4096 + (rg * 4 + (w & 3)) * 512 + kh * 256, u32x2{nz(pack2(f[0], f[1])), nz(pack2(f[2], f[3]))});
    }
    ST(3);
    if (w == 7) retime(rs, l16, s, tt, p, cap);
    if (s + 1 < NSEG) {
      u32x4 a[4];
      const unsigned so = X_OX + s * 65536 + rg * 32768 + wr * 4096;
      if (w == 7) wait_until(tt.tau + tt.d1);
      BAR();
      ST(4);
      issue4(rs, l16, so, so + 1024, so + 2048, so + 3072, a[0], a[1], a[2], a[3]);
      f32x4 c[4];
#pragma unroll
      for (int t = 0; t < 4; ++t) c[t] = f32x4{0.f, 0.f, 0.f, 0.f};
#pragma unroll
      for (int i = 0; i < 4; ++i) {
        if (i == 0) arrive<0>(rs, l16, so, a[0], cap); else if (i == 1) arrive<1>(rs, l16, so + 1024, a[1], cap);
        else if (i == 2) arrive<2>(rs, l16, so + 2048, a[2], cap); else arrive<3>(rs, l16, so + 3072, a[3], cap);
#pragma unroll
        for (int t = 0; t < 4; ++t) c[t] = smf(__builtin_bit_cast(bf16x8, a[i]), WB[i * 4 + t], c[t], sidx);
        SCHED();
      }
      ST(5);
      {
        const bool odd = (l16 & 256) != 0;
        const f32x4 f = fold(odd ? c[2] : c[0], odd ? c[3] : c[1]);
        const auto e0 = __builtin_amdgcn_permlane16_swap(f2u(f[0]), f2u(f[2]), false, false), e1 = __builtin_amdgcn_permlane16_swap(f2u(f[1]), f2u(f[3]), false, false);
        *(LAS f32x2*)(lad(roff, lbase)) = f32x2{u2f(e0[0]) + u2f(e0[1]), u2f(e1[0]) + u2f(e1[1])};
      }
      ST(6);
      LBAR();
      ST(7);
      if (w == 0) {
        LAS unsigned char* rp = lad(l8, lbase + R_RED);
        f32x2 v[8];
#pragma unroll
        for (int i = 0; i < 8; ++i) v[i] = *(const LAS f32x2*)(rp + i * 512);
        const f32x2 sum = ((v[0] + v[1]) + (v[2] + v[3])) + ((v[4] + v[5]) + (v[6] + v[7]));
        stx4(rs, soff, X_MEM + (s + 1) * 16384 + (cb >> 1) * 1024 + rg * 512 + ((cb ^ rg) & 1) * 128, nz(pack2(sum[0], sum[1])));
        const unsigned t = max(rt32(), 1u);
        if (l16 == 0) stx4(rs, 0, X_TS + (unsigned)(s + 1) * 12288 + (unsigned)(rg * 32 + cb) * 128, t);
      }
      ST(8);
    }
    advance(tt, p);
  }
  ST_FLUSH();
  if (cap == 0u && l16 == 0) stx4(rs, 0, X_CTL, 1u);
}

#if defined(__HIP_DEVICE_COMPILE__)
__attribute__((target("no-packed-fp32-ops")))
#endif
__global__ void __launch_bounds__(512, 2) k_main(MainP p) {
  extern __shared__ __attribute__((aligned(16))) unsigned char smem[];
  const int b = blockIdx.x, x = b & 7, j = b >> 3;
  if (x >= 3) worker(p, (LAS unsigned char*)smem, x - 3, j);
  else if (x == 0) role_a(p, (LAS unsigned char*)smem, j);
  else role_vb(p, (LAS unsigned char*)smem, x - 1, j);
}

extern "C" void kernel_launch(void* const* d_in, const int* in_sizes, int n_in, void* d_out, int out_size, void* d_ws, size_t ws_size,
                              hipStream_t stream) {
  unsigned char* ws = (unsigned char*)d_ws;
  k_prep<<<256, 512, 0, stream>>>((const u16*)d_in[12], (const u16*)d_in[13], (const u16*)d_in[1], ws);
  MainP p;
  p.x = (const u16*)d_in[0]; p.Wq = (const u16*)d_in[2]; p.Wk = (const u16*)d_in[3]; p.Wv = (const u16*)d_in[4]; p.Wo = (const u16*)d_in[5];
  p.w1 = (const u16*)d_in[8]; p.w2 = (const u16*)d_in[9]; p.w3 = (const u16*)d_in[10]; p.w4 = (const u16*)d_in[11];
  p.out = (u16*)d_out; p.ws = ws; p.d1 = 10; p.d2 = 10; p.d3 = 2; p.d4 = 100;
  (void)hipFuncSetAttribute((const void*)k_main, hipFuncAttributeMaxDynamicSharedMemorySize, R_LDS);
  k_main<<<256, 512, R_LDS, stream>>>(p);
}
```

```cpp
#include <hip/hip_runtime.h>
#include <hip/hip_bf16.h>
#include <cstdlib>
#pragma clang diagnostic ignored "-Winline-asm"

#define LAS __attribute__((address_space(3)))
#define GAS __attribute__((address_space(1)))
typedef unsigned short u16;
typedef __bf16 bf16x2 __attribute__((ext_vector_type(2)));
typedef __bf16 bf16x4 __attribute__((ext_vector_type(4)));
typedef __bf16 bf16x8 __attribute__((ext_vector_type(8)));
typedef __bf16 bf16x16 __attribute__((ext_vector_type(16)));
typedef short s16x4 __attribute__((ext_vector_type(4)));
typedef float f32x2 __attribute__((ext_vector_type(2)));
typedef float f32x4 __attribute__((ext_vector_type(4)));
typedef float f32x16 __attribute__((ext_vector_type(16)));
typedef unsigned u32x2 __attribute__((ext_vector_type(2)));
typedef unsigned u32x4 __attribute__((ext_vector_type(4)));

constexpr int S_ = 2048, D_ = 1024, NH = 32, DH = 128, HD = 4096, NSEG = 64, FF = 3584;

constexpr unsigned MB = 1u << 20;
constexpr unsigned F_LN = 0;
constexpr unsigned F_ST = 24576;
constexpr unsigned F_CH = F_ST + 49152;
constexpr unsigned X_CTL = F_CH + 4096;
constexpr unsigned X_MOD = X_CTL + 4096;
constexpr unsigned X_REC = X_MOD + 24576;
constexpr unsigned RSTR = 225280;
constexpr unsigned X_MEM = X_REC;
constexpr unsigned X_OX = X_REC + 16384;
constexpr unsigned X_VM = X_REC + 81920;
constexpr unsigned X_TS = X_REC + 212992;
constexpr unsigned X_ZERO = X_REC + 2 * RSTR;
constexpr unsigned WS_H = 16 * MB;
constexpr unsigned WS_Q = 20 * MB;
constexpr unsigned WS_K = 36 * MB;
constexpr unsigned WS_VT = 52 * MB;
constexpr unsigned WS_O = 68 * MB;
constexpr unsigned WS_X1 = 84 * MB;
constexpr unsigned WS_H2 = 88 * MB;
constexpr unsigned WS_G = 92 * MB;
constexpr unsigned WS_H1S = 96 * MB;
constexpr unsigned WS_X2 = 101 * MB;
constexpr unsigned WS_ST5 = 106 * MB;
constexpr unsigned WS_U = 108 * MB;
constexpr unsigned WS_KM = 124 * MB;
constexpr unsigned WS_P2 = 132 * MB;
constexpr unsigned WS_P8 = 164 * MB;
constexpr unsigned WS_END = 198 * MB;

__device__ __forceinline__ float bf2f(unsigned short b) { return __builtin_bit_cast(float, (unsigned)b << 16); }
__device__ __forceinline__ unsigned f2u(float f) { return __builtin_bit_cast(unsigned, f); }
__device__ __forceinline__ float u2f(unsigned u) { return __builtin_bit_cast(float, u); }
__device__ __forceinline__ float lo2f(unsigned w) { return __builtin_bit_cast(float, w << 16); }
__device__ __forceinline__ float hi2f(unsigned w) { return __builtin_bit_cast(float, w & 0xFFFF0000u); }
__device__ __forceinline__ unsigned pack2(float a, float b) { bf16x2 v; v[0] = (__bf16)a; v[1] = (__bf16)b; return __builtin_bit_cast(unsigned, v); }
__device__ __forceinline__ float rbf(float a) { return (float)(__bf16)a; }
__device__ __forceinline__ unsigned nz(unsigned w) { return max(w, 1u); }
__device__ __forceinline__ float gelu_tanh(float x) {
  const float e = __builtin_amdgcn_exp2f(x * fmaf(x * x, -0.1029432397f, -2.302208198f));
  return x * __builtin_amdgcn_rcpf(1.0f + e);
}
template <int CTRL> __device__ __forceinline__ float dpp_add(float v) { return v + u2f(__builtin_amdgcn_update_dpp(0u, f2u(v), CTRL, 0xF, 0xF, true)); }
__device__ __forceinline__ float wave_sum(float v) {
  v = dpp_add<0xB1>(v); v = dpp_add<0x4E>(v); v = dpp_add<0x141>(v); v = dpp_add<0x140>(v);
  auto t = __builtin_amdgcn_permlane16_swap(f2u(v), f2u(v), false, false);
  v = u2f(t[0]) + u2f(t[1]);
  t = __builtin_amdgcn_permlane32_swap(f2u(v), f2u(v), false, false);
  return u2f(t[0]) + u2f(t[1]);
}
#define WAIT_V(n) asm volatile("s_waitcnt vmcnt(" #n ")" ::: "memory")
#define WAIT_L0() asm volatile("s_waitcnt lgkmcnt(0)" ::: "memory")
#define BAR() do { asm volatile("" ::: "memory"); __builtin_amdgcn_s_barrier(); asm volatile("" ::: "memory"); } while (0)
#define LBAR() do { WAIT_L0(); BAR(); } while (0)
template <int N> __device__ __forceinline__ void wait_v() { asm volatile("s_waitcnt vmcnt(%0)" :: "n"(N) : "memory"); }
__device__ __forceinline__ void dma16(__amdgpu_buffer_rsrc_t rs, unsigned voff, unsigned soff, unsigned ldsa) {
  asm volatile("s_mov_b32 m0, %0\n\ts_nop 3\n\tbuffer_load_dwordx4 %1, %2, %3 offen lds" :: "s"(ldsa), "v"(voff), "s"(rs), "s"(soff) : "m0", "memory");
}
__device__ __forceinline__ void dma16x(__amdgpu_buffer_rsrc_t rs, unsigned voff, unsigned soff, unsigned ldsa) {
  asm volatile("s_mov_b32 m0, %0\n\ts_nop 3\n\tbuffer_load_dwordx4 %1, %2, %3 offen sc1 lds" :: "s"(ldsa), "v"(voff), "s"(rs), "s"(soff) : "m0", "memory");
}
__device__ __forceinline__ u32x4 ldx16(__amdgpu_buffer_rsrc_t rs, unsigned voff, unsigned soff) { return __builtin_amdgcn_raw_buffer_load_b128(rs, voff, soff, 16); }
__device__ __forceinline__ u32x2 ldx8(__amdgpu_buffer_rsrc_t rs, unsigned voff, unsigned soff) { return __builtin_amdgcn_raw_buffer_load_b64(rs, voff, soff, 16); }
__device__ __forceinline__ unsigned ldx4(__amdgpu_buffer_rsrc_t rs, unsigned voff, unsigned soff) { return __builtin_amdgcn_raw_buffer_load_b32(rs, voff, soff, 16); }
__device__ __forceinline__ void stx16(__amdgpu_buffer_rsrc_t rs, unsigned voff, unsigned soff, u32x4 v) {
  __builtin_amdgcn_raw_buffer_store_b128(v, rs, voff, soff, 16);
  asm volatile("s_nop 1" :: "v"(v) : "memory");
}
__device__ __forceinline__ void stx8(__amdgpu_buffer_rsrc_t rs, unsigned voff, unsigned soff, u32x2 v) { __builtin_amdgcn_raw_buffer_store_b64(v, rs, voff, soff, 16); }
__device__ __forceinline__ void stx4(__amdgpu_buffer_rsrc_t rs, unsigned voff, unsigned soff, unsigned v) { __builtin_amdgcn_raw_buffer_store_b32(v, rs, voff, soff, 16); }
__device__ __forceinline__ void wait_ge(__amdgpu_buffer_rsrc_t rs, unsigned voff, unsigned soff, unsigned need, unsigned& cap) {
  unsigned t;
  asm volatile("s_nop 4\n\t"
               "1:\n\t"
               "buffer_load_dword %[t], %[vo], %[rs], %[so] offen sc1\n\t"
               "s_waitcnt vmcnt(0)\n\t"
               "v_cmp_gt_u32 vcc, %[need], %[t]\n\t"
               "s_cmp_eq_u64 vcc, 0\n\t"
               "s_cbranch_scc1 2f\n\t"
               "s_sleep 2\n\t"
               "s_sub_u32 %[cap], %[cap], 1\n\t"
               "s_cbranch_scc0 1b\n\t"
               "s_mov_b32 %[cap], 0\n\t"
               "2:"
               : [t] "=&v"(t), [cap] "+s"(cap) : [vo] "v"(voff), [rs] "s"(rs), [so] "s"(soff), [need] "s"(need) : "vcc", "scc", "memory");
}
__device__ __forceinline__ void wait_seen(__amdgpu_buffer_rsrc_t rs, unsigned voff, unsigned soff, unsigned need, unsigned& seen, unsigned& cap) {
  if (seen >= need) return;
  unsigned t;
  asm volatile("s_nop 4\n\t"
               "1:\n\t"
               "buffer_load_dword %[t], %[vo], %[rs], %[so] offen sc1\n\t"
               "s_waitcnt vmcnt(0)\n\t"
               "v_cmp_gt_u32 vcc, %[need], %[t]\n\t"
               "s_cbranch_vccz 2f\n\t"
               "s_sleep 1\n\t"
               "s_sub_u32 %[cap], %[cap], 1\n\t"
               "s_cbranch_scc0 1b\n\t"
               "s_mov_b32 %[cap], 0\n\t"
               "2:"
               : [t] "=&v"(t), [cap] "+s"(cap) : [vo] "v"(voff), [rs] "s"(rs), [so] "s"(soff), [need] "s"(need) : "vcc", "scc", "memory");
  seen = min((unsigned)__builtin_amdgcn_readlane(t, 0), (unsigned)__builtin_amdgcn_readlane(t, 1));
}
__device__ __forceinline__ __amdgpu_buffer_rsrc_t mk_rs(const void* p, unsigned bytes) { return __builtin_amdgcn_make_buffer_rsrc((void*)p, 0, (int)bytes, 0x00020000); }
#define SCHED() __builtin_amdgcn_sched_barrier(0)
__device__ __forceinline__ unsigned now32() { unsigned long long t; asm volatile("s_memtime %0\n\ts_waitcnt lgkmcnt(0)" : "=s"(t) :: "memory"); return (unsigned)t; }
__device__ __forceinline__ unsigned rt32() { unsigned long long t; asm volatile("s_memrealtime %0\n\ts_waitcnt lgkmcnt(0)" : "=s"(t) :: "memory"); return (unsigned)t; }
__device__ __forceinline__ void wait_until(unsigned t) { while ((int)(rt32() - t) < 0) {} }
template <int X = 0> __device__ __forceinline__ LAS unsigned char* lad(unsigned v, unsigned u) {
  unsigned a;
  if (X == 0) asm volatile("v_add_u32 %0, %1, %2" : "=v"(a) : "s"(u), "v"(v));
  else asm volatile("v_xor_b32 %0, %3, %2\n\tv_add_u32 %0, %1, %0" : "=&v"(a) : "s"(u), "v"(v), "n"(X));
  return (LAS unsigned char*)(size_t)a;
}
__device__ __forceinline__ bf16x4 trread(LAS unsigned char* p) { return __builtin_amdgcn_ds_read_tr16_b64_v4bf16((LAS bf16x4*)p); }
__device__ __forceinline__ bf16x8 cat8(bf16x4 a, bf16x4 b) { return __builtin_shufflevector(a, b, 0, 1, 2, 3, 4, 5, 6, 7); }
__device__ __forceinline__ bf16x16 cat16(bf16x8 a, bf16x8 b) { return __builtin_shufflevector(a, b, 0, 1, 2, 3, 4, 5, 6, 7, 8, 9, 10, 11, 12, 13, 14, 15); }
__device__ __forceinline__ f32x4 mfma32(bf16x8 a, bf16x8 b, f32x4 c) { return __builtin_amdgcn_mfma_f32_16x16x32_bf16(a, b, c, 0, 0, 0); }
__device__ __forceinline__ f32x4 mfma16(u32x2 a, u32x2 b, f32x4 c) {
  return mfma32(__builtin_bit_cast(bf16x8, u32x4{a[0], a[1], 0u, 0u}), __builtin_bit_cast(bf16x8, u32x4{b[0], b[1], 0u, 0u}), c);
}
__device__ __forceinline__ f32x4 mfma16n(u32x2 a, u32x2 b, f32x4 c) { return __builtin_amdgcn_mfma_f32_16x16x16bf16_1k(__builtin_bit_cast(s16x4, a), __builtin_bit_cast(s16x4, b), c, 0, 0, 0); }
__device__ __forceinline__ f32x4 smf(bf16x8 a, bf16x16 b, f32x4 c, int idx) { return __builtin_amdgcn_smfmac_f32_16x16x64_bf16(a, b, c, idx, 0, 0); }

__device__ __forceinline__ void ln_row(float (&x)[16]) {
  float s = 0.f;
#pragma unroll
  for (int i = 0; i < 16; ++i) s += x[i];
  const float mean = wave_sum(s) * (1.0f / 1024.0f);
  float q = 0.f;
#pragma unroll
  for (int i = 0; i < 16; ++i) { const float d = x[i] - mean; q += d * d; }
  const float var = wave_sum(q) * (1.0f / 1024.0f);
  const float m = rbf(mean), isd = __builtin_amdgcn_rcpf(rbf(__builtin_amdgcn_sqrtf(rbf(rbf(var) + 1e-5f))));
#pragma unroll
  for (int i = 0; i < 16; ++i) x[i] = rbf(rbf(x[i] - m) * isd);
}
__device__ __forceinline__ void load_row16(const u16* rowp, int lane, float (&x)[16]) {
  const u32x4 a = *(const u32x4*)(rowp + lane * 8), b = *(const u32x4*)(rowp + 512 + lane * 8);
#pragma unroll
  for (int i = 0; i < 4; ++i) { x[2 * i] = lo2f(a[i]); x[2 * i + 1] = hi2f(a[i]); x[8 + 2 * i] = lo2f(b[i]); x[9 + 2 * i] = hi2f(b[i]); }
}
__device__ __forceinline__ void store_row16(u16* rowp, int lane, const float (&x)[16]) {
  u32x4 a, b;
#pragma unroll
  for (int i = 0; i < 4; ++i) { a[i] = pack2(x[2 * i], x[2 * i + 1]); b[i] = pack2(x[8 + 2 * i], x[9 + 2 * i]); }
  *(u32x4*)(rowp + lane * 8) = a; *(u32x4*)(rowp + 512 + lane * 8) = b;
}
__host__ __device__ __forceinline__ int mem_slot(int dl) {
  return ((dl >> 2) & 1) * 32 + (((dl >> 5) & 1) * 2 + ((dl >> 3) & 1)) * 8 + (((dl >> 4) & 1) * 2 + (dl & 1)) * 2 + ((dl >> 1) & 1);
}

#define WST(rs, wk, i)
struct MainP { const u16 *x, *mem0, *Wq, *Wk, *Wv, *Wo, *w1, *w2, *w3, *w4, *c, *wmod; u16* out; unsigned char* ws; int d1, d2, d3, d4; };
constexpr unsigned SPIN_CAP = 1u << 18;

__device__ __forceinline__ float gmax(float v) {
  auto t = __builtin_amdgcn_permlane16_swap(f2u(v), f2u(v), false, false);
  v = fmaxf(u2f(t[0]), u2f(t[1]));
  t = __builtin_amdgcn_permlane32_swap(f2u(v), f2u(v), false, false);
  return fmaxf(u2f(t[0]), u2f(t[1]));
}
__device__ __forceinline__ float gsum(float v) {
  auto t = __builtin_amdgcn_permlane16_swap(f2u(v), f2u(v), false, false);
  v = u2f(t[0]) + u2f(t[1]);
  t = __builtin_amdgcn_permlane32_swap(f2u(v), f2u(v), false, false);
  return u2f(t[0]) + u2f(t[1]);
}
__device__ __forceinline__ void publish(__amdgpu_buffer_rsrc_t rs, unsigned word, unsigned val) {
  WAIT_V(0); BAR();
  if (threadIdx.x == 0) stx4(rs, 0, word, val);
}

constexpr int P_S2 = 0, P_S8 = 32, P_S7 = 64, P_S4 = 96, P_S5 = 104, P_S1 = 112, P_S3 = 144, P_PJ = 240;
constexpr int NLN = 160;

enum { SG_WO = 0, SG_W1 = 1, SG_W2 = 2, SG_W3 = 3, SG_W4 = 4, SG_K = 5, SG_V = 6, SG_Q = 7 };
constexpr int G_XCH = 131072, G_K = G_XCH + 16384, G_CTL = G_K + 8192;
__device__ __forceinline__ f32x16 mfma3232(bf16x8 a, bf16x8 b, f32x16 c) { return __builtin_amdgcn_mfma_f32_32x32x16_bf16(a, b, c, 0, 0, 0); }
__device__ __forceinline__ void widen32(const f32x16& a, u32x4 (&v)[2]) {
#pragma unroll
  for (int u = 0; u < 2; ++u) {
    const auto r0 = __builtin_amdgcn_permlane32_swap(pack2(a[8 * u], a[8 * u + 1]), pack2(a[8 * u + 4], a[8 * u + 5]), false, false);
    const auto r1 = __builtin_amdgcn_permlane32_swap(pack2(a[8 * u + 2], a[8 * u + 3]), pack2(a[8 * u + 6], a[8 * u + 7]), false, false);
    v[u] = u32x4{r0[0], r1[0], r0[1], r1[1]};
  }
}
__device__ __forceinline__ float hsum(float v) { const auto t = __builtin_amdgcn_permlane32_swap(f2u(v), f2u(v), false, false); return u2f(t[0]) + u2f(t[1]); }

__device__ __forceinline__ void attend(__amdgpu_buffer_rsrc_t rs, LAS unsigned char* qb, LAS unsigned char* kb, int seg, int h, int t, int eh, int lane, const u32x4 (&vv)[4],
                                       unsigned& chs, unsigned& cap) {
  const int fr = lane & 15, g = lane >> 4;
  const unsigned l16 = lane * 16, ub = (unsigned)(seg * NH + h);
  f32x4 s0 = {0.f, 0.f, 0.f, 0.f}, s1 = s0, s2 = s0;
  bf16x8 q[4];
#pragma unroll
  for (int ks = 0; ks < 4; ++ks) {
    q[ks] = *(const LAS bf16x8*)(qb + ks * 4096);
    s1 = mfma32(*(const LAS bf16x8*)(kb + ks * 1024), q[ks], s1); s2 = mfma32(*(const LAS bf16x8*)(kb + 4096 + ks * 1024), q[ks], s2);
  }
  const float scale = 0.08838834764831845f, L2E = 1.4426950408889634f;
  float a[4], b[4], c[4], mt = -3.0e38f, lt = 0.f;
#pragma unroll
  for (int r = 0; r < 4; ++r) {
    const int j = 4 * g + r;
    b[r] = (t == 1 || j <= fr) ? rbf(rbf(s1[r]) * scale) : -3.0e38f;
    c[r] = (t == 1 && j <= fr) ? rbf(rbf(s2[r]) * scale) : -3.0e38f;
    mt = fmaxf(mt, fmaxf(b[r], c[r]));
  }
  mt = gmax(mt);
#pragma unroll
  for (int r = 0; r < 4; ++r) { b[r] = __builtin_amdgcn_exp2f((b[r] - mt) * L2E); c[r] = __builtin_amdgcn_exp2f((c[r] - mt) * L2E); lt += b[r] + c[r]; }
  const u32x4 pt = {pack2(b[0], b[1]), pack2(b[2], b[3]), pack2(c[0], c[1]), pack2(c[2], c[3])};
  f32x4 o[4];
#pragma unroll
  for (int i = 0; i < 4; ++i) o[i] = mfma32(__builtin_bit_cast(bf16x8, vv[i]), __builtin_bit_cast(bf16x8, pt), f32x4{0.f, 0.f, 0.f, 0.f});
  lt = gsum(lt);
  const unsigned em = (unsigned)(fr >> 3) * 512 + (unsigned)g * 128 + (unsigned)(8 * ((fr >> 2) & 1) + (fr & 3)) * 8;
  asm volatile("" : "+v"(o[0]), "+v"(o[1]), "+v"(o[2]), "+v"(o[3]), "+v"(lt));
  wait_seen(rs, 0, F_CH + h * 128, (unsigned)seg + 1u, chs, cap);
  u32x2 vmf[4];
  for (;;) {
    asm volatile("" ::: "memory");
    unsigned mn = 0xFFFFFFFFu;
    bf16x8 kmf[4];
#pragma unroll
    for (int ks = 0; ks < 4; ++ks) kmf[ks] = __builtin_bit_cast(bf16x8, ldx16(rs, l16, WS_KM + ub * 4096 + ks * 1024));
#pragma unroll
    for (int i = 0; i < 4; ++i) vmf[i] = ldx8(rs, em, X_VM + (unsigned)seg * RSTR + h * 4096 + (2 * eh + (i >> 1)) * 1024 + (i & 1) * 32);
    s0 = f32x4{0.f, 0.f, 0.f, 0.f};
#pragma unroll
    for (int ks = 0; ks < 4; ++ks) s0 = mfma32(kmf[ks], q[ks], s0);
#pragma unroll
    for (int i = 0; i < 4; ++i) mn = min(mn, min(vmf[i][0], vmf[i][1]));
    if (__all(mn != 0u) || cap == 0u) break;
    cap = __builtin_amdgcn_readfirstlane(cap - 1u);
  }
  cap = __builtin_amdgcn_readfirstlane(cap);
#pragma unroll
  for (int r = 0; r < 4; ++r) a[r] = rbf(rbf(s0[r]) * scale);
  const float mx = fmaxf(mt, gmax(fmaxf(fmaxf(a[0], a[1]), fmaxf(a[2], a[3]))));
#pragma unroll
  for (int r = 0; r < 4; ++r) a[r] = __builtin_amdgcn_exp2f((a[r] - mx) * L2E);
  const u32x2 pm = {pack2(a[0], a[1]), pack2(a[2], a[3])};
  f32x4 om[4];
#pragma unroll
  for (int i = 0; i < 4; ++i) om[i] = mfma16n(vmf[i], pm, f32x4{0.f, 0.f, 0.f, 0.f});
  const float al = __builtin_amdgcn_exp2f((mt - mx) * L2E);
  const float inv = __builtin_amdgcn_rcpf(fmaf(al, lt, 0.5f * gsum((a[0] + a[1]) + (a[2] + a[3])))), ai = al * inv;
#pragma unroll
  for (int i = 0; i < 4; ++i) o[i] = o[i] * ai + om[i] * inv;
#pragma unroll
  for (int u = 0; u < 2; ++u)
    stx16(rs, l16, WS_O + (unsigned)seg * 262144 + (unsigned)((h * 4 + 2 * eh + u) * 2 + t) * 1024,
          u32x4{pack2(o[2 * u][0], o[2 * u][1]), pack2(o[2 * u][2], o[2 * u][3]), pack2(o[2 * u + 1][0], o[2 * u + 1][1]), pack2(o[2 * u + 1][2], o[2 * u + 1][3])});
}

template <int SG>
__device__ __forceinline__ void stage_gemm(LAS unsigned char* lds, __amdgpu_buffer_rsrc_t rs, const u16* Wg, int wk, int kq, int cb, int pid, unsigned& cap) {
  constexpr bool KV = (SG == SG_K || SG == SG_V), PROJ = KV || SG == SG_Q, ROWM = (SG == SG_W1 || PROJ);
  constexpr int KS = (SG == SG_W4) ? 28 : 32;
  constexpr int LDW = PROJ ? HD : (SG == SG_W3) ? FF : D_;
  constexpr int WROWS = (SG == SG_WO) ? HD : (SG == SG_W4 ? FF : D_);
  constexpr int NBLK = KV ? 32 : (SG == SG_WO || SG == SG_W1 || SG == SG_Q) ? 64 : 65;
  constexpr unsigned ABASE = PROJ ? WS_H : SG == SG_WO ? WS_O : SG == SG_W1 ? WS_G : SG == SG_W2 ? WS_H1S : SG == SG_W3 ? WS_X2 : WS_U;
  constexpr unsigned ASTR = SG == SG_WO ? 262144u : SG == SG_W4 ? 229376u : 65536u;
  constexpr int NPC = KS / 4;
  const unsigned myword = F_ST + (unsigned)pid * 128u;
  int tid = threadIdx.x; asm volatile("" : "+v"(tid));
  const int lane = tid & 63, w = __builtin_amdgcn_readfirstlane(tid >> 6), row = lane & 31, hf = lane >> 5;
  const int kh = w >> 2, wl = w & 3;
  const int koff = PROJ ? 0 : kq;
  const unsigned lbase = (unsigned)(size_t)lds, l16 = lane * 16;
  const int col0 = cb * 128 + wl * 32;
  bf16x8 Wf[KS];
  {
    const __amdgpu_buffer_rsrc_t rsW = mk_rs(Wg, (unsigned)WROWS * LDW * 2u);
    const int ub = lane >> 3, tu = 4 * hf + ((lane & 15) >> 2);
    const unsigned vB = (unsigned)(((ub & 3) + 8 * (ub >> 2)) * LDW + 16 * (((lane >> 1) & 3) ^ (ub >> 1)) + 8 * (lane & 1)) * 2u;
    const unsigned sB = (unsigned)((koff * KS * 32 + 32 * (w >> 2) + 16 * ((w >> 1) & 1) + 4 * (w & 1)) * LDW + cb * 128) * 2u;
    const int boff = kh * 65536 + (wl >> 1) * 8192 + tu * 128 + (((tu >> 1) ^ (2 * (wl & 1) + ((lane >> 4) & 1))) << 5) + (lane & 3) * 8;
#pragma unroll
    for (int ps = 0; ps < 2; ++ps) {
      BAR();
#pragma unroll
      for (int hh = 0; hh < 2; ++hh)
#pragma unroll
        for (int i = 0; i < 4; ++i)
          if (4 * ps + i < NPC && !(PROJ && ps == 0)) {
            const unsigned so = sB + (unsigned)((hh * NPC + 4 * ps + i) * 64 * LDW) * 2u, d = lbase + hh * 65536 + i * 16384 + w * 1024;
            dma16(rsW, vB, so, d); dma16(rsW, vB, so + 128, d + 8192);
          }
      WAIT_V(0); BAR();
#pragma unroll
      for (int i = 0; i < 4; ++i)
        if (4 * ps + i < NPC) {
#pragma unroll
          for (int k = 0; k < 4; ++k) { LAS unsigned char* q = lds + (boff + i * 16384 + k * 2048); Wf[(4 * ps + i) * 4 + k] = cat8(trread(q), trread(q + 1024)); }
        }
      WAIT_L0();
    }
    if (SG == SG_W1) {
      const u32x4 z = {0u, 0u, 0u, 0u};
      stx16(rs, (unsigned)tid * 16, WS_H1S + cb * 8192, z); stx16(rs, (unsigned)tid * 16, WS_H1S + 64 * 65536 + cb * 8192, z);
      WAIT_V(0);
    }
    if (tid < 8) *(LAS unsigned*)(lds + G_CTL + tid * 4) = 0u;
    LBAR();
  }
  const f32x16 zero16 = {0.f, 0.f, 0.f, 0.f, 0.f, 0.f, 0.f, 0.f, 0.f, 0.f, 0.f, 0.f, 0.f, 0.f, 0.f, 0.f};
  auto put16 = [&](LAS unsigned char* p, const f32x16& a) {
#pragma unroll
    for (int q = 0; q < 4; ++q) *(LAS f32x4*)(p + q * 1024) = f32x4{a[4 * q], a[4 * q + 1], a[4 * q + 2], a[4 * q + 3]};
  };
  if (SG == SG_W3) {
    const u32x4 one4 = {0x3F803F80u, 0x3F803F80u, 0x3F803F80u, 0x3F803F80u};
    f32x16 cs = zero16;
#pragma unroll
    for (int k = 0; k < KS; ++k) cs = mfma3232(Wf[k], __builtin_bit_cast(bf16x8, one4), cs);
    if (kh == 0) put16(lds + wl * 4096 + l16, cs);
    LBAR();
    if (kh == 1 && row == 0) {
#pragma unroll
      for (int q = 0; q < 4; ++q)
        *(LAS f32x4*)(lds + G_K + wl * 128 + hf * 64 + q * 16) = *(const LAS f32x4*)(lds + wl * 4096 + q * 1024 + l16) + f32x4{cs[4 * q], cs[4 * q + 1], cs[4 * q + 2], cs[4 * q + 3]};
    }
    LBAR();
  }
  auto blk = [&](int i) -> int { return KV ? 2 * i + kq : i; };
  const unsigned aoff = ROWM ? (unsigned)(row * 128 + ((hf ^ ((row >> 1) & 7)) << 4)) : (unsigned)((row >> 4) * 1024 + hf * 256 + (row & 15) * 16);
  const unsigned vA = ROWM ? (unsigned)((lane >> 3) * 2048 + (((lane & 7) ^ ((4 * wl + (lane >> 4)) & 7)) << 4)) : l16;
  auto fetch = [&](int hb) {
    const int i = hb >> 1, hh = hb & 1;
    const unsigned so = ABASE + (unsigned)blk(i) * ASTR + (unsigned)koff * (KS * 2048) + (ROWM ? wl * 16384 + hh * 1024 : (hh * KS + wl) * 1024);
#pragma unroll
    for (int q = 0; q < NPC; ++q) dma16x(rs, vA, so + q * (ROWM ? 128 : 4096), lbase + (i & 1) * 65536 + hh * 32768 + wl * 1024 + q * 4096);
  };
  auto products = [&](int i, f32x16& acc, auto&& first, auto&& early, auto&& handover) {
    const unsigned ub = lbase + (i & 1) * 65536 + kh * 32768;
    LAS unsigned char* ab[4];
    ab[0] = lad(aoff, ub);
    if (ROWM) { ab[1] = lad<32>(aoff, ub); ab[2] = lad<64>(aoff, ub); ab[3] = lad<96>(aoff, ub); }
    constexpr int PD = 10;
    bf16x8 af[PD + 1];
    auto rd = [&](int k) { af[k % (PD + 1)] = ROWM ? *(const LAS bf16x8*)(ab[k & 3] + (k >> 2) * 4096) : *(const LAS bf16x8*)(ab[0] + (k >> 1) * 2048 + (k & 1) * 512); };
#pragma unroll
    for (int k = 0; k < PD; ++k) rd(k);
    SCHED();
#pragma unroll
    for (int k = 0; k < KS; ++k) {
      if (k + PD < KS) rd(k + PD);
      acc = SG == SG_V ? mfma3232(af[k % (PD + 1)], Wf[k], k ? acc : zero16) : mfma3232(Wf[k], af[k % (PD + 1)], k ? acc : zero16);
      SCHED();
      first(k);
      SCHED();
      if (k == KS - 10) { early(); SCHED(); }
      if (k == KS - 5) { handover(); SCHED(); }
    }
    asm volatile("" : "+v"(acc));
  };
  WST(rs, wk, KV ? 2 : 10);
#define SEG(x)
  if (kh == 0) {
    auto pvo = [&](int i) -> unsigned {
      const int b = blk(i);
      if (PROJ) return F_LN + (unsigned)((b % (NLN / 4)) * 4 + (lane & 3)) * 128u;
      if (SG == SG_WO) return F_ST + (unsigned)(P_S1 + kq * 8 + (lane & 7)) * 128u;
      if (SG == SG_W1) return F_ST + (unsigned)(P_S3 + (b % 3) * 32 + (lane & 31)) * 128u;
      if (SG == SG_W2) return F_ST + (unsigned)(P_S4 + (lane & 7)) * 128u;
      if (SG == SG_W3) return F_ST + (unsigned)(P_S5 + (lane & 7)) * 128u;
      return F_ST + (unsigned)(P_S7 + kq * 7 + min(lane & 7, 6)) * 128u;
    };
    auto need = [&](int i) -> unsigned { return PROJ ? 1u : (SG == SG_W2 && i == 64) ? 64u : (unsigned)i + 1u; };
    int R = 0, Rp = 0;
    for (int i = 0; i < NBLK; ++i) {
      if (R <= i) { wait_ge(rs, pvo(i), 0, need(i), cap); fetch(2 * i); fetch(2 * i + 1); }
      const unsigned pk = w == 0 ? (lane < 32 ? pvo(i + 1) : pvo(i + 2)) : 0xF0000000u;
      if (R <= i + 1) wait_v<NPC>(); else wait_v<2 * NPC>();
      BAR();
      SEG(1);
      f32x16 acc;
      unsigned peek;
      asm volatile("buffer_load_dword %0, %1, %2, 0 offen sc1" : "=v"(peek) : "v"(pk), "s"(rs) : "memory");
      u32x4 st[8];
      float mean = 0.f, q2 = 0.f;
      if (SG == SG_W3) {
#pragma unroll
        for (int q = 0; q < 8; ++q) st[q] = ldx16(rs, l16, WS_ST5 + (unsigned)i * 8192 + q * 1024);
      }
      products(i, acc, [&](int k) {
        if (SG == SG_W3) {
          if (k >= 14 && k < 18) { mean += (u2f(st[2 * k - 28][0]) + u2f(st[2 * k - 28][2])) + (u2f(st[2 * k - 27][0]) + u2f(st[2 * k - 27][2])); asm volatile("" : "+v"(mean)); }
          if (k == 18) { mean = hsum(mean) * (1.0f / 32.0f); asm volatile("" : "+v"(mean)); }
          if (k >= 19 && k < 27) {
            const float d0 = u2f(st[k - 19][0]) - mean, d1 = u2f(st[k - 19][2]) - mean;
            q2 += fmaf(32.0f, fmaf(d0, d0, d1 * d1), u2f(st[k - 19][1]) + u2f(st[k - 19][3])); asm volatile("" : "+v"(q2));
          }
          if (k == 28) { q2 = rbf(rbf(hsum(q2) * (1.0f / 1024.0f)) + 1e-5f); asm volatile("" : "+v"(q2)); }
          if (k == 29) { q2 = __builtin_amdgcn_rcpf(rbf(__builtin_amdgcn_sqrtf(q2))); mean = rbf(mean); asm volatile("" : "+v"(q2), "+v"(mean)); }
        }
      }, []() {}, [&]() {
        SEG(4);
        if (SG == SG_Q && R >= i + 2) fetch(2 * i + 3);
        WAIT_V(0);
        SEG(0);
        asm volatile("" : "+v"(peek));
        if (w == 0) {
          const unsigned long long ok = __ballot(peek >= (lane < 32 ? need(i + 1) : need(i + 2)));
          const bool r1 = (R >= i + 2 || (unsigned)ok == 0xFFFFFFFFu) && i + 1 < NBLK, r2 = r1 && (unsigned)(ok >> 32) == 0xFFFFFFFFu && i + 2 < NBLK;
          if (lane == 0) *(volatile LAS unsigned*)(lds + G_CTL + 4) = (unsigned)(i + 1 + (r1 ? 1 : 0) + (r2 ? 1 : 0));
        }
        LBAR();
      });
      SEG(2);
      const unsigned rv = *(const volatile LAS unsigned*)(lds + G_CTL + 4);
      if (SG == SG_W3) {
        const float m = mean;
        *(LAS float*)(lds + G_K + 1024 + wl * 256 + lane * 4) = q2;
#pragma unroll
        for (int q = 0; q < 4; ++q) {
          const f32x4 cs = *(const LAS f32x4*)(lds + G_K + wl * 128 + hf * 64 + q * 16);
#pragma unroll
          for (int e = 0; e < 4; ++e) acc[4 * q + e] = fmaf(-m, cs[e], acc[4 * q + e]);
        }
      }
      put16(lad(l16, lbase + G_XCH + wl * 4096), acc);
      asm volatile("" ::: "memory");
      if (lane == 0) *(volatile LAS unsigned*)(lds + G_CTL + 16 + wl * 4) = (unsigned)i + 1u;
      Rp = R; R = __builtin_amdgcn_readfirstlane(rv);
      SEG(3);
      if (R >= i + 2 && Rp < i + 2) { fetch(2 * i + 2); fetch(2 * i + 3); }
      if (R >= i + 3) fetch(2 * i + 4);
      SEG(5);
    }
    BAR();
  } else {
    bool pending = false;
    auto announce = [&](int i) {
      if (lane == 0) {
        const unsigned c = __hip_atomic_fetch_add((LAS unsigned*)(lds + G_CTL), 1u, __ATOMIC_RELAXED, __HIP_MEMORY_SCOPE_WORKGROUP);
        if ((c & 3u) == 3u) stx4(rs, 0, myword, (unsigned)blk(i) + 1u);
      }
    };
    unsigned kvs[2] = {0u, 0u}, chs = 0u;
    BAR();
    for (int i = 0; i < NBLK; ++i) {
      const int b = blk(i);
      if (SG == SG_Q) {
        if (i & 1) wait_seen(rs, (unsigned)(lane & 1) * 4096, F_ST + (unsigned)(P_PJ + 64 + cb) * 128, (unsigned)i + 1u, kvs[1], cap);
        else wait_seen(rs, (unsigned)(lane & 1) * 4096, F_ST + (unsigned)(P_PJ + cb) * 128, (unsigned)i + 1u, kvs[0], cap);
      }
      BAR();
      SEG(0);
      const unsigned rv = *(const volatile LAS unsigned*)(lds + G_CTL + 4);
      u32x4 res[2], vv[4];
      float isd;
      if (SG == SG_W2) {
#pragma unroll
        for (int u = 0; u < 2; ++u) {
          const int t = 32 * b - 2 + 16 * u + (lane >> 2);
          dma16x(rs, (t >= 0 && t < S_) ? (unsigned)(t * D_ + col0 + ((lane ^ (lane >> 3)) & 3) * 8) * 2u : 0xF0000000u, WS_H2, lbase + G_K + wl * 2048 + u * 1024);
        }
      }
      if (SG == SG_Q) {
        const unsigned ub = (unsigned)(b * NH + cb) * 8192;
        dma16x(rs, l16, WS_K + ub + wl * 2048, lbase + G_K + wl * 2048); dma16x(rs, l16, WS_K + ub + wl * 2048 + 1024, lbase + G_K + wl * 2048 + 1024);
        const unsigned ev = (unsigned)((lane >> 3) & 1) * 1024 + (unsigned)((lane >> 4) * 16 + 8 * ((lane >> 2) & 1) + (lane & 3)) * 16;
#pragma unroll
        for (int q = 0; q < 4; ++q) vv[q] = ldx16(rs, ev, WS_VT + ub + (2 * (wl >> 1) + (q >> 1)) * 2048 + (q & 1) * 64);
      }
      f32x16 acc;
      int R = 0;
      LAS unsigned char* xr = lad(l16, lbase + G_XCH + wl * 4096);
      f32x4 xa[4];
      unsigned xf;
      auto take = [&]() {
        asm volatile("" ::: "memory");
        xf = *(const volatile LAS unsigned*)(lds + G_CTL + 16 + wl * 4);
        asm volatile("" ::: "memory");
#pragma unroll
        for (int q = 0; q < 4; ++q) xa[q] = *(const LAS f32x4*)(xr + q * 1024);
        if (SG == SG_W3) isd = *(const LAS float*)(lds + G_K + 1024 + wl * 256 + lane * 4);
        asm volatile("" ::: "memory");
      };
      products(i, acc, [&](int k) {
        if (SG == SG_Q && k == 8 && i > 0) { wait_v<6>(); announce(i - 1); }
      }, [&]() {
        take();
      }, [&]() {
        SEG(5);
        R = __builtin_amdgcn_readfirstlane(rv);
        if (R >= i + 2) {
          WAIT_V(0);
          LBAR();
          if (pending && tid == 256) stx4(rs, 0, myword, (unsigned)blk(i - 1) + 1u);
        }
      });
      const bool more = R >= i + 2;
      SEG(1);
      while (__builtin_amdgcn_readfirstlane(xf) <= (unsigned)i) take();
#pragma unroll
      for (int q = 0; q < 16; ++q) { acc[q] += xa[q >> 2][q & 3]; if (SG == SG_W3) acc[q] *= isd; }
      SEG(2);
      const unsigned fo = (unsigned)((col0 >> 5) * 2048 + hf * 256), ro = (unsigned)((row >> 4) * 1024 + (row & 15) * 16);
      u32x4 v[2];
      if (SG == SG_W1 || SG == SG_W3) {
#pragma unroll
        for (int q = 0; q < 16; ++q) acc[q] = gelu_tanh(acc[q]);
      }
      if (SG != SG_V) widen32(acc, v);
      if (SG == SG_WO || SG == SG_W4) {
        LAS unsigned char* tw = lds + G_XCH + wl * 4096 + row * 64;
        *(LAS u32x4*)(tw + ((hf ^ (row >> 1)) & 3) * 16) = v[0]; *(LAS u32x4*)(tw + (((2 + hf) ^ (row >> 1)) & 3) * 16) = v[1];
        asm volatile("" ::: "memory");
        LAS unsigned char* tr = lds + G_XCH + wl * 4096 + (lane >> 2) * 64 + ((lane ^ (lane >> 3)) & 3) * 16;
        const unsigned so = (SG == SG_WO ? WS_P2 : WS_P8) + (unsigned)(b * 4 + kq) * 65536u, vo = (unsigned)((lane >> 2) * D_ + col0 + (lane & 3) * 8) * 2u;
        stx16(rs, vo, so, *(const LAS u32x4*)(tr)); stx16(rs, vo, so + 32768, *(const LAS u32x4*)(tr + 1024));
      } else if (SG == SG_K) {
        const unsigned so = WS_K + (unsigned)(b * NH + cb) * 8192 + wl * 1024, vo = (unsigned)((row >> 4) * 4096 + hf * 256 + (row & 15) * 16);
        stx16(rs, vo, so, v[0]); stx16(rs, vo, so + 512, v[1]);
      } else if (SG == SG_V) {
        const unsigned so = WS_VT + (unsigned)(b * NH + cb) * 8192 + wl * 2048, vo = ro + hf * 256;
#pragma unroll
        for (int u = 0; u < 2; ++u)
          stx16(rs, vo, so + u * 512, u32x4{pack2(acc[4 * u], acc[4 * u + 1]), pack2(acc[4 * u + 2], acc[4 * u + 3]), pack2(acc[8 + 4 * u], acc[9 + 4 * u]), pack2(acc[10 + 4 * u], acc[11 + 4 * u])});
      } else if (SG == SG_Q) {
        LAS unsigned char* qw = lds + G_XCH + wl * 4096 + (row >> 4) * 1024 + hf * 256 + (row & 15) * 16;
        *(LAS u32x4*)(qw) = v[0]; *(LAS u32x4*)(qw + 512) = v[1];
        WAIT_V(0);
        if (lane == 0) __hip_atomic_fetch_add((LAS unsigned*)(lds + G_CTL + 8), 1u, __ATOMIC_RELAXED, __HIP_MEMORY_SCOPE_WORKGROUP);
        while (__builtin_amdgcn_readfirstlane(*(const volatile LAS unsigned*)(lds + G_CTL + 8)) < 4u * (unsigned)i + 4u) {}
        asm volatile("" ::: "memory");
        attend(rs, lds + G_XCH + (wl & 1) * 1024 + l16, lds + G_K + l16, b, cb, wl & 1, wl >> 1, lane, vv, chs, cap);
      } else if (SG == SG_W1) {
        const int grp = cb >> 1, sh = (grp & 1) ? -(1 + (grp >> 1)) : (1 + (grp >> 1));
        const int rho = 32 * b + row + sh + 2;
        const unsigned vo = (rho >= 2 && rho < S_ + 2) ? (unsigned)(rho >> 5) * 65536u + fo + (unsigned)((rho >> 4) & 1) * 1024u + (unsigned)(rho & 15) * 16u : 0xF0000000u;
        stx16(rs, vo, WS_H1S, v[0]); stx16(rs, vo, WS_H1S + 512, v[1]);
      } else if (SG == SG_W2) {
        if (!more) WAIT_V(0);
        res[0] = *(const LAS u32x4*)(lds + G_K + wl * 2048 + row * 64 + ((hf ^ (row >> 1)) & 3) * 16);
        res[1] = *(const LAS u32x4*)(lds + G_K + wl * 2048 + row * 64 + (((2 + hf) ^ (row >> 1)) & 3) * 16);
        float z[16], zs = 0.f;
#pragma unroll
        for (int q = 0; q < 8; ++q) {
          const unsigned x = pack2(lo2f(res[q >> 2][q & 3]) + lo2f(v[q >> 2][q & 3]), hi2f(res[q >> 2][q & 3]) + hi2f(v[q >> 2][q & 3]));
          v[q >> 2][q & 3] = x; z[2 * q] = lo2f(x); z[2 * q + 1] = hi2f(x); zs += z[2 * q] + z[2 * q + 1];
        }
        stx16(rs, fo + ro, WS_X2 + (unsigned)b * 65536u, v[0]); stx16(rs, fo + ro, WS_X2 + (unsigned)b * 65536u + 512, v[1]);
        const float mw = hsum(zs) * (1.0f / 32.0f);
        float q2 = 0.f;
#pragma unroll
        for (int q = 0; q < 16; ++q) { const float d = z[q] - mw; q2 += d * d; }
        q2 = hsum(q2);
        if (hf == 0) stx8(rs, (unsigned)((cb * 2 + (wl >> 1)) * 512 + row * 16 + (wl & 1) * 8), WS_ST5 + (unsigned)b * 8192, u32x2{f2u(mw), f2u(q2)});
      } else {
        stx16(rs, fo + ro, WS_U + (unsigned)b * 229376u, v[0]); stx16(rs, fo + ro, WS_U + (unsigned)b * 229376u + 512, v[1]);
      }
      SEG(3);
      pending = R >= i + 3 && i >= 2 && SG != SG_Q;
      if (SG == SG_Q ? i == NBLK - 1 : !pending) { WAIT_V(0); announce(i); }
      if (SG != SG_Q && R >= i + 3) fetch(2 * i + 5);
      if (!KV) WST(rs, wk, 16 + b);
      SEG(4);
      if (!more) BAR();
    }
  }
}

__device__ __forceinline__ void wait32(__amdgpu_buffer_rsrc_t rs, int first, int mine, int lane, unsigned need, unsigned& cap) {
  wait_ge(rs, 0, F_ST + (unsigned)(first + mine) * 128u, need, cap);
  wait_ge(rs, (unsigned)(lane & 31) * 128u, F_ST + (unsigned)first * 128u, need, cap);
}
__device__ __forceinline__ void sum_partials(__amdgpu_buffer_rsrc_t rs, unsigned l16, unsigned so, float (&a)[16]) {
  u32x4 pp[4][2];
#pragma unroll
  for (int k = 0; k < 4; ++k) { pp[k][0] = ldx16(rs, l16, so + k * 65536u); pp[k][1] = ldx16(rs, l16, so + k * 65536u + 1024); }
#pragma unroll
  for (int i = 0; i < 8; ++i) {
    f32x2 t = {0.f, 0.f};
#pragma unroll
    for (int k = 0; k < 4; ++k) t += f32x2{lo2f(pp[k][i >> 2][i & 3]), hi2f(pp[k][i >> 2][i & 3])};
    a[2 * i] = rbf(t[0]); a[2 * i + 1] = rbf(t[1]);
  }
}
__device__ __forceinline__ void put_row(__amdgpu_buffer_rsrc_t rs, unsigned l16, unsigned so, const float (&v)[16]) {
  stx16(rs, l16, so, u32x4{pack2(v[0], v[1]), pack2(v[2], v[3]), pack2(v[4], v[5]), pack2(v[6], v[7])});
  stx16(rs, l16, so + 1024, u32x4{pack2(v[8], v[9]), pack2(v[10], v[11]), pack2(v[12], v[13]), pack2(v[14], v[15])});
}
__device__ __forceinline__ void load_mod(__amdgpu_buffer_rsrc_t rs, int part, int lane, float (&v)[16], unsigned& cap) {
  for (;;) {
    asm volatile("" ::: "memory");
    u32x4 t[4];
#pragma unroll
    for (int i = 0; i < 4; ++i) t[i] = ldx16(rs, (unsigned)lane * 32, X_MOD + part * 4096 + (i >> 1) * 2048 + (i & 1) * 16);
    unsigned mn = 0xFFFFFFFFu;
#pragma unroll
    for (int i = 0; i < 16; ++i) { mn = min(mn, t[i >> 2][i & 3]); v[i] = hi2f(t[i >> 2][i & 3]); }
    if (__all(mn != 0u) || cap == 0u) break;
    cap = __builtin_amdgcn_readfirstlane(cap - 1u);
    __builtin_amdgcn_s_sleep(4);
  }
  cap = __builtin_amdgcn_readfirstlane(cap);
}
struct ModLoad { u32x4 w[8]; u16 c[8]; };
__device__ __forceinline__ void mod_load(const MainP& p, int grp, ModLoad& m) {
  const int kg = threadIdx.x >> 2, c8 = threadIdx.x & 3;
#pragma unroll
  for (int i = 0; i < 8; ++i) { m.w[i] = *(const u32x4*)(p.wmod + (size_t)(kg + 128 * i) * 6144 + grp * 32 + c8 * 8); m.c[i] = p.c[kg + 128 * i]; }
}
__device__ __forceinline__ void mod_finish(__amdgpu_buffer_rsrc_t rs, LAS float* red, int grp, const ModLoad& m) {
  const int tid = threadIdx.x, kg = tid >> 2, c8 = tid & 3;
  float acc[8] = {0.f, 0.f, 0.f, 0.f, 0.f, 0.f, 0.f, 0.f};
#pragma unroll
  for (int i = 0; i < 8; ++i) {
    const float v = bf2f(m.c[i]), sc = rbf(v / (1.0f + __expf(-v)));
#pragma unroll
    for (int j = 0; j < 4; ++j) { acc[2 * j] += sc * lo2f(m.w[i][j]); acc[2 * j + 1] += sc * hi2f(m.w[i][j]); }
  }
#pragma unroll
  for (int j = 0; j < 8; ++j) red[kg * 33 + c8 * 8 + j] = acc[j];
  LBAR();
  float t = 0.f;
#pragma unroll
  for (int i = 0; i < 8; ++i) t += red[((tid >> 5) * 8 + i) * 33 + (tid & 31)];
  t = hsum(t);
  LBAR();
  if ((tid & 63) < 32) red[(tid >> 6) * 33 + (tid & 31)] = t;
  LBAR();
  if (tid < 32) {
    float u = 0.f;
#pragma unroll
    for (int i = 0; i < 8; ++i) u += red[i * 33 + tid];
    stx4(rs, (unsigned)tid * 4, X_MOD + (unsigned)grp * 128, f2u(rbf(u)) | 1u);
  }
}

__device__ __forceinline__ void stage_mid(__amdgpu_buffer_rsrc_t rs, const u16* x, int wk, int cu, unsigned& cap) {
  const int tid = threadIdx.x, lane = tid & 63, w = __builtin_amdgcn_readfirstlane(tid >> 6);
  const int id = w * 12 + cu, grp = id >> 5, rl = id & 31;
  const unsigned l16 = lane * 16;
  float ga[16], sc[16], sh[16];
  load_mod(rs, 2, lane, ga, cap); load_mod(rs, 4, lane, sc, cap); load_mod(rs, 3, lane, sh, cap);
#pragma unroll
  for (int i = 0; i < 16; ++i) sc[i] = rbf(1.0f + sc[i]);
  for (int seg = grp; seg < NSEG; seg += 3) {
    const int row = seg * 32 + rl;
    float v[16], a[16];
    load_row16(x + (size_t)row * D_, lane, v);
    wait32(rs, P_S2, rl, lane, (unsigned)seg + 1u, cap);
    sum_partials(rs, l16, WS_P2 + (unsigned)seg * 262144u + (unsigned)rl * 2048, a);
#pragma unroll
    for (int i = 0; i < 16; ++i) v[i] = rbf(v[i] + rbf(ga[i] * a[i]));
    put_row(rs, l16, WS_X1 + (unsigned)row * 2048, v);
    ln_row(v);
#pragma unroll
    for (int i = 0; i < 16; ++i) v[i] = rbf(rbf(v[i] * sc[i]) + sh[i]);
    put_row(rs, l16, WS_H2 + (unsigned)row * 2048, v);
    ln_row(v);
    put_row(rs, l16, WS_G + (unsigned)row * 2048, v);
    WAIT_V(0);
    if (lane == 0) stx4(rs, 0, F_ST + (unsigned)(P_S3 + grp * 32 + rl) * 128, (unsigned)seg + 1u);
    WST(rs, wk, 16 + seg);
  }
}

__device__ __forceinline__ void stage_out(__amdgpu_buffer_rsrc_t rs, u16* out, int wk, int cu, unsigned& cap) {
  const int tid = threadIdx.x, lane = tid & 63, w = __builtin_amdgcn_readfirstlane(tid >> 6);
  const int grp = w >> 2, rl = cu * 4 + (w & 3);
  const unsigned l16 = lane * 16;
  float gb[16];
  load_mod(rs, 5, lane, gb, cap);
  for (int b = grp; b < 65; b += 2) {
    const int t = 32 * b - 2 + rl;
    if (t < 0 || t >= S_) continue;
    wait_ge(rs, (unsigned)(lane & 7) * 128u, F_ST + P_S5 * 128u, (unsigned)b + 1u, cap);
    const unsigned xo = (unsigned)((lane >> 2) * 2048 + (lane & 3) * 256), xs = WS_X2 + (unsigned)b * 65536u + (unsigned)((rl >> 4) * 1024 + (rl & 15) * 16);
    const u32x4 x2a = ldx16(rs, xo, xs), x2b = ldx16(rs, xo, xs + 32768);
    const u32x4 x1a = ldx16(rs, l16, WS_X1 + (unsigned)t * 2048), x1b = ldx16(rs, l16, WS_X1 + (unsigned)t * 2048 + 1024);
    wait32(rs, P_S8, rl, lane, (unsigned)b + 1u, cap);
    float a[16];
    sum_partials(rs, l16, WS_P8 + (unsigned)b * 262144u + (unsigned)rl * 2048, a);
#pragma unroll
    for (int i = 0; i < 4; ++i) {
      a[2 * i] = lo2f(x1a[i]) + rbf(gb[2 * i] * rbf(lo2f(x2a[i]) + a[2 * i])); a[2 * i + 1] = hi2f(x1a[i]) + rbf(gb[2 * i + 1] * rbf(hi2f(x2a[i]) + a[2 * i + 1]));
      a[8 + 2 * i] = lo2f(x1b[i]) + rbf(gb[8 + 2 * i] * rbf(lo2f(x2b[i]) + a[8 + 2 * i])); a[9 + 2 * i] = hi2f(x1b[i]) + rbf(gb[9 + 2 * i] * rbf(hi2f(x2b[i]) + a[9 + 2 * i]));
    }
    store_row16(out + (size_t)t * D_, lane, a);
    WST(rs, wk, 16 + b);
  }
}

#define WPRE 0
__device__ __forceinline__ void worker(const MainP& p, LAS unsigned char* lds, int xw, int j) {
  const int tid = threadIdx.x, lane = tid & 63, w = __builtin_amdgcn_readfirstlane(tid >> 6);
  const __amdgpu_buffer_rsrc_t rs = mk_rs(p.ws, WS_END);
  unsigned cap = SPIN_CAP;
  const int wk = xw * 32 + j;
  WST(rs, wk, 0);
  {
    const int c = j * 5 + xw, r0 = c * 8 + w, r1 = min(r0 + 8 * NLN, S_ - 1);
    float v0[16], v1[16];
    ModLoad m;
    if (c >= NLN - 64) mod_load(p, c - (NLN - 64), m);
    load_row16(p.x + (size_t)r0 * D_, lane, v0); load_row16(p.x + (size_t)r1 * D_, lane, v1);
    if (c >= NLN - 64) mod_finish(rs, (LAS float*)(lds + G_XCH), c - (NLN - 64), m);
    ln_row(v0);
    SCHED();
    {
      const __amdgpu_buffer_rsrc_t rsW = mk_rs(xw == 4 ? p.Wq : (xw & 1) ? p.Wv : p.Wk, D_ * HD * 2);
      const int ub = lane >> 3;
      const unsigned vB = (unsigned)(((ub & 3) + 8 * (ub >> 2)) * HD + 16 * (((lane >> 1) & 3) ^ (ub >> 1)) + 8 * (lane & 1)) * 2u;
      const unsigned sB = (unsigned)((32 * (w >> 2) + 16 * ((w >> 1) & 1) + 4 * (w & 1)) * HD + j * 128) * 2u, lbase = (unsigned)(size_t)lds;
      float sc[16], sh[16];
#pragma unroll
      for (int q = 0; q < 8; ++q) {
        const unsigned so = sB + (unsigned)(((q >> 2) * 8 + (q & 3)) * 64 * HD) * 2u, d = lbase + (q >> 2) * 65536 + (q & 3) * 16384 + w * 1024;
        dma16(rsW, vB, so, d); dma16(rsW, vB, so + 128, d + 8192);
      }
      ln_row(v1);
      load_mod(rs, 1, lane, sc, cap); load_mod(rs, 0, lane, sh, cap);
#pragma unroll
      for (int i = 0; i < 16; ++i) { const float f = rbf(1.0f + sc[i]); v0[i] = rbf(rbf(v0[i] * f) + sh[i]); v1[i] = rbf(rbf(v1[i] * f) + sh[i]); }
    }
    put_row(rs, (unsigned)lane * 16, WS_H + (unsigned)r0 * 2048, v0);
    if (r0 + 8 * NLN < S_) put_row(rs, (unsigned)lane * 16, WS_H + (unsigned)r1 * 2048, v1);
    publish(rs, F_LN + c * 128, 1u);
    WST(rs, wk, 1);
  }
  if (xw == 4) stage_gemm<SG_Q>(lds, rs, p.Wq, wk, 0, j, P_S1 + j, cap);
  else {
    if (xw & 1) stage_gemm<SG_V>(lds, rs, p.Wv, wk, xw >> 1, j, P_PJ + xw * 32 + j, cap);
    else stage_gemm<SG_K>(lds, rs, p.Wk, wk, xw >> 1, j, P_PJ + xw * 32 + j, cap);
    WST(rs, wk, 3);
    { ModLoad m; mod_load(p, 64 + wk, m); mod_finish(rs, (LAS float*)(lds + 65536), 64 + wk, m); }
    if (xw == 0) stage_gemm<SG_WO>(lds, rs, p.Wo, wk, j >> 3, j & 7, P_S2 + j, cap);
    else if (xw == 1) stage_gemm<SG_W4>(lds, rs, p.w4, wk, j >> 3, j & 7, P_S8 + j, cap);
    else if (j >= 28) stage_out(rs, p.out, wk, (xw - 2) * 4 + j - 28, cap);
    else if (xw == 2) stage_gemm<SG_W3>(lds, rs, p.w3, wk, 0, j, P_S7 + j, cap);
    else if (j < 8) stage_gemm<SG_W1>(lds, rs, p.w1, wk, 0, j, P_S4 + j, cap);
    else if (j < 16) stage_gemm<SG_W2>(lds, rs, p.w2, wk, 0, j - 8, P_S5 + j - 8, cap);
    else stage_mid(rs, p.x, wk, j - 16, cap);
  }
  if (cap == 0u && lane == 0) stx4(rs, 0, X_CTL, 1u);
}

#define ST_DECL
#define ST0()
#define ST(i)
#define ST_FLUSH()
constexpr int R_W = 0, R_MEM = 131072, R_QK = R_MEM + 16384, R_KT = R_QK + 8192, R_RED = R_MEM + 16384, R_LDS = 163840;
__device__ __forceinline__ void bar_lo(int w) { asm volatile("s_cmp_ge_u32 %0, 4\n\ts_cbranch_scc1 1f\n\ts_barrier\n\t1:" :: "s"(w) : "scc", "memory"); }
__device__ __forceinline__ void bar_hi(int w) { asm volatile("s_cmp_lt_u32 %0, 4\n\ts_cbranch_scc1 1f\n\ts_setprio 3\n\ts_barrier\n\t1:" :: "s"(w) : "scc", "memory"); }
__device__ __forceinline__ void issue4(__amdgpu_buffer_rsrc_t rs, unsigned vo, unsigned s0, unsigned s1, unsigned s2, unsigned s3, u32x4& a0, u32x4& a1, u32x4& a2, u32x4& a3) {
  asm volatile("buffer_load_dwordx4 v[240:243], %[vo], %[rs], %[s0] offen sc1\n\tbuffer_load_dwordx4 v[244:247], %[vo], %[rs], %[s1] offen sc1\n\t"
               "buffer_load_dwordx4 v[248:251], %[vo], %[rs], %[s2] offen sc1\n\tbuffer_load_dwordx4 v[252:255], %[vo], %[rs], %[s3] offen sc1"
               : "=&{v[240:243]}"(a0), "=&{v[244:247]}"(a1), "=&{v[248:251]}"(a2), "=&{v[252:255]}"(a3)
               : [vo] "v"(vo), [rs] "s"(rs), [s0] "s"(s0), [s1] "s"(s1), [s2] "s"(s2), [s3] "s"(s3) : "memory");
}
#define ARRIVE(R0, R1, R2, R3, CNT) \
  asm volatile("s_waitcnt vmcnt(" #CNT ")\n\t" \
               "1:\n\t" \
               "v_min3_u32 %[t], v" #R0 ", v" #R1 ", v" #R2 "\n\t" \
               "v_min_u32 %[t], %[t], v" #R3 "\n\t" \
               "v_cmp_eq_u32 vcc, 0, %[t]\n\t" \
               "s_cbranch_vccz 2f\n\t" \
               "s_mov_b64 exec, vcc\n\t" \
               "buffer_load_dwordx4 v[" #R0 ":" #R3 "], %[vo], %[rs], %[so] offen sc1\n\t" \
               "s_mov_b64 exec, -1\n\t" \
               "s_waitcnt vmcnt(0)\n\t" \
               "s_sub_u32 %[cap], %[cap], 1\n\t" \
               "s_cbranch_scc0 1b\n\t" \
               "s_mov_b32 %[cap], 0\n\t" \
               "2:" \
               : "+{v[" #R0 ":" #R3 "]}"(a), [t] "=&v"(t), [cap] "+s"(cap) : [vo] "v"(vo), [rs] "s"(rs), [so] "s"(so) : "vcc", "scc", "memory")
template <int I> __device__ __forceinline__ void arrive(__amdgpu_buffer_rsrc_t rs, unsigned vo, unsigned so, u32x4& a, unsigned& cap) {
  unsigned t;
  if (I == 0) ARRIVE(240, 241, 242, 243, 3);
  else if (I == 1) ARRIVE(244, 245, 246, 247, 2);
  else if (I == 2) ARRIVE(248, 249, 250, 251, 1);
  else ARRIVE(252, 253, 254, 255, 0);
}
__device__ __forceinline__ unsigned half_of(unsigned v) { unsigned r; asm volatile("v_lshrrev_b32 %0, 1, %1" : "=v"(r) : "v"(v)); return r; }
__device__ __forceinline__ void issue2(__amdgpu_buffer_rsrc_t rs, unsigned l8, unsigned so, int w, u32x4& a) {
  asm volatile("s_cmp_ge_u32 %[w], 4\n\ts_cbranch_scc1 1f\n\tv_lshrrev_b32 v255, 1, %[l8]\n\t"
               "buffer_load_dwordx2 v[252:253], v255, %[rs], %[so] offen sc1\n\tbuffer_load_dwordx2 v[254:255], v255, %[rs], %[so] offen offset:512 sc1\n\t1:"
               : "=&{v[252:255]}"(a) : [l8] "v"(l8), [rs] "s"(rs), [so] "s"(so), [w] "s"(w) : "scc", "memory");
}
__device__ __forceinline__ void arrive2(__amdgpu_buffer_rsrc_t rs, unsigned l8, unsigned so, u32x4& a, unsigned& cap) {
  unsigned t;
  asm volatile("s_waitcnt vmcnt(0)\n\t"
               "1:\n\t"
               "v_min3_u32 %[t], v252, v253, v254\n\t"
               "v_min_u32 %[t], %[t], v255\n\t"
               "v_cmp_eq_u32 vcc, 0, %[t]\n\t"
               "s_cbranch_vccz 2f\n\t"
               "v_lshrrev_b32 %[t], 1, %[l8]\n\t"
               "buffer_load_dwordx2 v[252:253], %[t], %[rs], %[so] offen sc1\n\t"
               "buffer_load_dwordx2 v[254:255], %[t], %[rs], %[so] offen offset:512 sc1\n\t"
               "s_waitcnt vmcnt(0)\n\t"
               "s_sub_u32 %[cap], %[cap], 1\n\t"
               "s_cbranch_scc0 1b\n\t"
               "s_mov_b32 %[cap], 0\n\t"
               "2:"
               : "+{v[252:255]}"(a), [t] "=&v"(t), [cap] "+s"(cap) : [l8] "v"(l8), [rs] "s"(rs), [so] "s"(so) : "vcc", "scc", "memory");
}
__device__ __forceinline__ unsigned ror8(unsigned v) { return __builtin_amdgcn_update_dpp(0u, v, 0x128, 0xF, 0xF, true); }
__device__ __forceinline__ void gather_mem(__amdgpu_buffer_rsrc_t rs, unsigned lbase, int s, int w, unsigned l16, unsigned& cap) {
  const unsigned so = X_MEM + s * RSTR + w * 2048;
  u32x4 a0, a1;
  asm volatile("buffer_load_dwordx4 v[248:251], %[vo], %[rs], %[so] offen sc1\n\tbuffer_load_dwordx4 v[252:255], %[vo], %[rs], %[so] offen offset:1024 sc1"
               : "=&{v[248:251]}"(a0), "=&{v[252:255]}"(a1) : [vo] "v"(l16), [rs] "s"(rs), [so] "s"(so) : "memory");
  LAS unsigned char* d = lad(l16, lbase + R_MEM + w * 2048);
  arrive<2>(rs, l16, so, a0, cap);
  *(LAS u32x4*)(d) = a0;
  arrive<3>(rs, l16, so + 1024, a1, cap);
  *(LAS u32x4*)(d + 1024) = a1;
}
__device__ __forceinline__ unsigned mem_off(int lane) { return (unsigned)((lane & 1) * 512 + (((lane >> 1) ^ ((lane & 1) * 16)) * 8)); }
__device__ __forceinline__ bf16x8 mem_frag(LAS unsigned char* mp, int chunk) { return cat8(trread(mp + chunk * 1024), trread(mp + chunk * 1024 + 256)); }
__device__ __forceinline__ bf16x16 w_frag(LAS unsigned char* p) {
  return cat16(cat8(trread(p), trread(p + 2048)), cat8(trread(p + 4096), trread(p + 6144)));
}
__device__ __forceinline__ bf16x16 l_frag(LAS unsigned char* p, int i) { return cat16(*(const LAS bf16x8*)(p + i * 2048), *(const LAS bf16x8*)(p + i * 2048 + 1024)); }
__device__ __forceinline__ void make_resident(LAS unsigned char* wb, unsigned lo, unsigned l16) {
#pragma unroll
  for (int c = 0; c < 2; ++c) {
    bf16x16 f[4];
#pragma unroll
    for (int t = 0; t < 4; ++t) f[t] = w_frag(wb + c * 8192 + (lo ^ (t << 5)));
    WAIT_L0(); SCHED();
#pragma unroll
    for (int t = 0; t < 4; ++t) {
      *(LAS bf16x8*)(wb + (c * 4 + t) * 2048 + l16) = __builtin_shufflevector(f[t], f[t], 0, 1, 2, 3, 4, 5, 6, 7);
      *(LAS bf16x8*)(wb + (c * 4 + t) * 2048 + 1024 + l16) = __builtin_shufflevector(f[t], f[t], 8, 9, 10, 11, 12, 13, 14, 15);
    }
    WAIT_L0(); SCHED();
  }
}
__device__ __forceinline__ f32x4 fold(f32x4 a, f32x4 b) {
  f32x4 r;
#pragma unroll
  for (int i = 0; i < 4; ++i) {
    const auto t = __builtin_amdgcn_permlane32_swap(f2u(a[i]), f2u(b[i]), false, false);
    r[i] = u2f(t[0]) + u2f(t[1]);
  }
  return r;
}

struct Sched { unsigned tau, prev; int ea, eb, d1; bool moved; };
__device__ __forceinline__ void retime(__amdgpu_buffer_rsrc_t rs, unsigned l16, int s, Sched& t, const MainP& p, unsigned& cap) {
  if (s > 0) {
    unsigned vo;
    asm volatile("v_lshlrev_b32 %0, 3, %1" : "=v"(vo) : "v"(l16));
    unsigned va;
    asm volatile("v_and_b32 %0, 0xF80, %1" : "=v"(va) : "v"(vo));
    wait_ge(rs, vo, X_TS + (unsigned)s * RSTR, 1u, cap);
    wait_ge(rs, va, X_TS + (unsigned)s * RSTR + 8192, 1u, cap);
    int vb = (int)(ldx4(rs, vo, X_TS + (unsigned)s * RSTR) - t.prev), vc = (int)(ldx4(rs, va, X_TS + (unsigned)s * RSTR + 8192) - t.prev);
#pragma unroll
    for (int o = 32; o >= 1; o >>= 1) { vb = max(vb, __shfl_xor(vb, o)); vc = max(vc, __shfl_xor(vc, o)); }
    const int tb = __builtin_amdgcn_readfirstlane(vb), ta = __builtin_amdgcn_readfirstlane(vc);
    const int late = tb + p.d3 - (int)(t.tau - t.prev);
    if (s > 2) {
      const int up = s < 5 ? 80 : 8, dn = s < 10 ? 40 : 8;
      if (!t.moved) t.ea = max(t.ea - dn, min(ta * 8, t.ea + up));
      if (ta + p.d3 <= t.d1) t.eb = max(t.eb - dn, min((tb - t.d1) * 8, t.eb + up));
    }
    t.moved = late > 0;
    if (late > 0) t.tau += late + min(p.d4, 8 * late);
  }
  t.d1 = (t.ea >> 3) + p.d1;
}
__device__ __forceinline__ void advance(Sched& t, const MainP& p) { t.prev = t.tau; t.tau += t.d1 + (t.eb >> 3) + p.d2; }

__device__ __forceinline__ void role_a(const MainP& p, LAS unsigned char* lds, int h) {
  const int tid = threadIdx.x, lane = tid & 63, w = __builtin_amdgcn_readfirstlane(tid >> 6);
  const int cg = w & 3, kh = w >> 2;
  const __amdgpu_buffer_rsrc_t rs = mk_rs(p.ws, WS_END);
  const unsigned lbase = (unsigned)(size_t)lds;
  unsigned l16 = lane * 16;
  asm volatile("" : "+v"(l16));
  const int sidx = (lane & 8) ? 0xEEEE : 0x4444;
  bf16x16 W[24];
  unsigned qoff;
  {
    const int fr = lane & 15, g = lane >> 4, tu = 4 * (g & 1) + (fr >> 2), ub = lane >> 3;
    const unsigned lo = (g >> 1) * 1024 + tu * 128 + ((tu >> 1) << 5) + (fr & 3) * 8;
    qoff = R_QK + ((fr >> 1) & 1) * 512 + (g >> 1) * 256 + (fr & 1) * 128 + (8 * (g & 1) + (fr >> 2)) * 8;
    const __amdgpu_buffer_rsrc_t rw = mk_rs(cg < 2 ? p.Wq : p.Wk, D_ * HD * 2);
    const unsigned vo = (unsigned)(ub * HD + 16 * (((lane >> 1) & 3) ^ (ub >> 1)) + 8 * (lane & 1)) * 2u;
    const unsigned so = (unsigned)(kh * 512 * HD + h * DH + (cg & 1) * 64) * 2u;
    auto stage = [&](int c) {
#pragma unroll
      for (int rho = 0; rho < 8; ++rho) dma16(rw, vo, so + (unsigned)((c * 64 + rho * 8) * HD) * 2u, lbase + R_W + w * 16384 + (c & 1) * 8192 + rho * 1024);
    };
    stage(0); stage(1);
#pragma unroll
    for (int c = 0; c < 6; ++c) {
      WAIT_V(8);
#pragma unroll
      for (int t = 0; t < 4; ++t) W[4 * c + t] = w_frag(lds + R_W + w * 16384 + (c & 1) * 8192 + (lo ^ (t << 5)));
      WAIT_L0(); SCHED();
      stage(c + 2);
    }
    WAIT_V(0);
    make_resident(lds + R_W + w * 16384, lo, l16);
  }
  WST(rs, 160 + (h >> 1), 4);
  unsigned cap = SPIN_CAP;
  wait_ge(rs, (l16 & 16) * 256, F_ST + (unsigned)(P_PJ + h) * 128, 1u, cap);
  dma16x(rs, l16, WS_K + h * 8192 + w * 1024, lbase + R_KT + w * 1024);
  Sched tt; tt.tau = rt32() - 100000; tt.prev = tt.tau; tt.ea = 195 * 8; tt.eb = 118 * 8; tt.d1 = 0; tt.moved = true;
  const int cu_ = h; (void)cu_;
  ST_DECL
  for (int s = 0; s < NSEG; ++s) {
    LAS unsigned char* lp = lad(l16, lbase + R_W + w * 16384);
    bf16x16 L[2];
    L[0] = l_frag(lp, 0); L[1] = l_frag(lp, 1);
    unsigned lz = l16;
    asm volatile("" : "+v"(lz));
    LAS unsigned char* mp = lad(mem_off((int)(lz >> 4)), lbase + R_MEM + kh * 8192);
    SCHED();
    if (w == 7) wait_until(tt.tau);
    BAR();
    ST0();
    gather_mem(rs, lbase, s, (w + h) & 7, l16, cap);
    ST(1);
    LBAR();
    ST(2);
    f32x4 c[4];
#pragma unroll
    for (int t = 0; t < 4; ++t) c[t] = f32x4{0.f, 0.f, 0.f, 0.f};
    bf16x8 al, af[2];
    u32x4 vt0, vt1;
    u32x4 vmv;
    const unsigned vso = X_VM + (unsigned)s * RSTR + h * 4096 + cg * 1024;
    al = mem_frag(mp, 6); af[0] = mem_frag(mp, 0); af[1] = mem_frag(mp, 1);
#pragma unroll
    for (int i = 0; i < 8; ++i) {
      c[i & 3] = smf(al, L[i & 1], c[i & 3], sidx);
      SCHED();
      if (i == 3) al = mem_frag(mp, 7);
      if (i < 6) L[i & 1] = l_frag(lp, i + 2);
      if (i == 6) {
        const unsigned vo = WS_VT + (unsigned)(s * NH + h) * 8192 + cg * 2048;
        asm volatile("s_cmp_ge_u32 %5, 4\n\ts_cbranch_scc1 1f\n\t"
                     "buffer_load_dwordx4 %0, %2, %3, %4 offen sc1\n\tbuffer_load_dwordx4 %1, %2, %3, %4 offen offset:1024 sc1\n\t1:"
                     : "=&v"(vt0), "=&v"(vt1) : "v"(l16), "s"(rs), "s"(vo), "s"(w) : "scc", "memory");
      }
      SCHED();
#pragma unroll
      for (int r = 3 * i; r < 3 * i + 3; ++r) {
        c[(r + 1) & 3] = smf(af[(r >> 2) & 1], W[(r & ~3) + ((r + 1) & 3)], c[(r + 1) & 3], sidx);
        SCHED();
        if ((r & 3) == 3 && (r >> 2) + 2 < 6) af[(r >> 2) & 1] = mem_frag(mp, (r >> 2) + 2);
        SCHED();
      }
    }
    {
      LAS unsigned char* qw = lad(half_of(l16), lbase + R_QK + cg * 2048 + kh * 512);
      const f32x4 f0 = fold(c[0], c[1]);
      *(LAS u32x2*)(qw) = u32x2{pack2(f0[0], f0[1]), pack2(f0[2], f0[3])};
      const f32x4 f1 = fold(c[2], c[3]);
      *(LAS u32x2*)(qw + 1024) = u32x2{pack2(f1[0], f1[1]), pack2(f1[2], f1[3])};

    }
    ST(3);
    if (w < 4) {
      bf16x8 k1[4], k2[4];
      LAS unsigned char* kb = lad(l16, lbase + R_KT);
#pragma unroll
      for (int ks = 0; ks < 2; ++ks) { k1[ks] = *(const LAS bf16x8*)(kb + ks * 1024); k2[ks] = *(const LAS bf16x8*)(kb + 4096 + ks * 1024); }
      LBAR();
      ST(4);
      issue2(rs, l16, vso, w, vmv);
      f32x4 t0 = {0.f, 0.f, 0.f, 0.f}, t1 = t0, t2 = t0;
      LAS unsigned char* qb = lad(qoff, lbase);
      bf16x8 qf[4], kf[4];
#pragma unroll
      for (int ks = 0; ks < 2; ++ks) {
        qf[ks] = cat8(trread(qb + ks * 1024), trread(qb + ks * 1024 + 32));
        kf[ks] = cat8(trread(qb + 4096 + ks * 1024), trread(qb + 4096 + ks * 1024 + 32));
      }
      SCHED();
#pragma unroll
      for (int ks = 0; ks < 4; ++ks) {
        t1 = mfma32(k1[ks], qf[ks], t1); t2 = mfma32(k2[ks], qf[ks], t2); t0 = mfma32(kf[ks], qf[ks], t0);
        SCHED();
        if (ks < 2) {
          qf[ks + 2] = cat8(trread(qb + (ks + 2) * 1024), trread(qb + (ks + 2) * 1024 + 32));
          k1[ks + 2] = *(const LAS bf16x8*)(kb + (ks + 2) * 1024); k2[ks + 2] = *(const LAS bf16x8*)(kb + 4096 + (ks + 2) * 1024);
          kf[ks + 2] = cat8(trread(qb + 4096 + (ks + 2) * 1024), trread(qb + 4096 + (ks + 2) * 1024 + 32));
        }
        SCHED();
      }
      ST(5);
      {
        const float sl = 0.08838834764831845f * 1.4426950408889634f;
        float z[4] = {t1[0], t1[1], t1[2], t1[3]}, y = t0[0], mx;
        {
          float u1 = t0[1], u2 = t0[2], u3 = t0[3];
#define M8(Z, B) "v_add_f32_dpp " Z ", " Z ", " Z " row_ror:8 row_mask:0xf bank_mask:0x3\n\tv_add_f32_dpp " Z ", " B ", " B " row_ror:8 row_mask:0xf bank_mask:0xc\n\t"
          asm("s_nop 7\n\t"
              "v_permlane32_swap_b32 %4, %6\n\tv_permlane32_swap_b32 %5, %7\n\t"
              M8("%0", "%9") "v_add_f32 %4, %4, %6\n\tv_add_f32 %5, %5, %7\n\t" M8("%1", "%10") M8("%2", "%11") M8("%4", "%5") M8("%3", "%12")
              "v_max3_f32 %8, %0, %1, %2\n\tv_max3_f32 %8, %8, %4, %3\n\ts_nop 1\n\t"
              "v_max_f32_dpp %8, %8, %8 row_ror:8 row_mask:0xf bank_mask:0xf\n\tv_mov_b32 %5, %8\n\ts_nop 1\n\t"
              "v_permlane16_swap_b32 %8, %5\n\tv_max_f32 %8, %8, %5\n\tv_mov_b32 %5, %8\n\ts_nop 1\n\t"
              "v_permlane32_swap_b32 %8, %5\n\tv_max_f32 %8, %8, %5"
              : "+v"(z[0]), "+v"(z[1]), "+v"(z[2]), "+v"(z[3]), "+v"(y), "+v"(u1), "+v"(u2), "+v"(u3), "=&v"(mx) : "v"(t2[0]), "v"(t2[1]), "v"(t2[2]), "v"(t2[3]));
#undef M8
        }
        const float nm = -mx * sl;
#pragma unroll
        for (int r = 0; r < 4; ++r) z[r] = __builtin_amdgcn_exp2f(fmaf(z[r], sl, nm));
        const unsigned p0 = pack2(z[0], z[1]), p1 = pack2(z[2], z[3]);
        const u32x4 pt = {p0, p1, ror8(p0), ror8(p1)};
        SCHED();
        asm volatile("s_waitcnt vmcnt(2)" : "+v"(vt0), "+v"(vt1) :: "memory");
        f32x4 o0 = {0.f, 0.f, 0.f, 0.f}, o1 = o0;
        o0 = mfma32(__builtin_bit_cast(bf16x8, vt0), __builtin_bit_cast(bf16x8, pt), o0);
        o1 = mfma32(__builtin_bit_cast(bf16x8, vt1), __builtin_bit_cast(bf16x8, pt), o1);
        y = __builtin_amdgcn_exp2f(fmaf(y, sl, nm));
        const unsigned pm1 = pack2(y, u2f(ror8(f2u(y))));
        const auto pms = __builtin_amdgcn_permlane32_swap(pm1, pm1, false, false);
        const u32x2 pm = {pms[0], pms[1]};
        float inv = __builtin_amdgcn_rcpf(gsum(dpp_add<0x128>(((z[0] + z[1]) + (z[2] + z[3])) + y)));
        o0 *= inv; o1 *= inv;
        unsigned ov;
        unsigned ot;
        asm volatile("v_and_b32 %0, 0x30, %5\n\tv_lshrrev_b32 %1, 2, %5\n\tv_and_b32 %1, 0xC0, %1\n\tv_or_b32 %0, %0, %1\n\t"
                     "v_and_b32 %1, 0x40, %5\n\tv_lshl_or_b32 %0, %1, 9, %0\n\tv_and_b32 %1, 0x80, %5\n\tv_lshl_or_b32 %0, %1, 24, %0"
                     : "=&v"(ov), "=&v"(ot), "+v"(o0), "+v"(o1), "+v"(inv) : "v"(l16));
        ST(6);
        arrive2(rs, l16, vso, vmv, cap);
        ST(7);
        const f32x4 n0 = mfma16n(u32x2{vmv[0], vmv[1]}, pm, f32x4{0.f, 0.f, 0.f, 0.f}), n1 = mfma16n(u32x2{vmv[2], vmv[3]}, pm, f32x4{0.f, 0.f, 0.f, 0.f});
        {
          const u32x4 v = {nz(pack2(fmaf(n0[0], inv, o0[0]), fmaf(n0[1], inv, o0[1]))), nz(pack2(fmaf(n0[2], inv, o0[2]), fmaf(n0[3], inv, o0[3]))),
                           nz(pack2(fmaf(n1[0], inv, o1[0]), fmaf(n1[1], inv, o1[1]))), nz(pack2(fmaf(n1[2], inv, o1[2]), fmaf(n1[3], inv, o1[3])))};
          stx16(rs, ov, X_OX + s * RSTR + h * 1024 + w * 256, v);
        }
        if (w == 3) { const unsigned t = max(rt32(), 1u); if (l16 == 0) stx4(rs, 0, X_TS + (unsigned)(s + 1) * RSTR + (unsigned)(64 + h) * 128, t); }
        ST(8);
      }
    } else {
      LBAR();
      LAS unsigned char* qb = lad(qoff, lbase + 4096 + (w - 4) * 1024);
      u32x4 kf = __builtin_bit_cast(u32x4, cat8(trread(qb), trread(qb + 32)));
#pragma unroll
      for (int i = 0; i < 4; ++i) kf[i] = pack2(dpp_add<0x128>(lo2f(kf[i])), dpp_add<0x128>(hi2f(kf[i])));
      stx16(rs, l16, WS_KM + (unsigned)(s * NH + h) * 4096 + (w - 4) * 1024, kf);
      WAIT_V(0);
    }
    LBAR();
    if (w == 4 && l16 == 0) stx4(rs, 0, F_CH + h * 128, (unsigned)s + 1u);
    ST(9);
    if (w == 7) { retime(rs, l16, s, tt, p, cap); advance(tt, p); }
    if (s + 1 < NSEG) {
      unsigned fo;
      asm volatile("v_and_b32 %0, 16, %1\n\tv_lshlrev_b32 %0, 8, %0" : "=v"(fo) : "v"(l16));
      wait_ge(rs, fo, F_ST + (unsigned)(P_PJ + ((s + 1) & 1) * 64 + h) * 128, (unsigned)s + 2u, cap);
      dma16x(rs, l16, WS_K + (unsigned)((s + 1) * NH + h) * 8192 + w * 1024, lbase + R_KT + w * 1024);
    }
  }
  ST_FLUSH();
  if (cap == 0u && l16 == 0) stx4(rs, 0, X_CTL, 1u);
}

__device__ __forceinline__ void role_vb(const MainP& p, LAS unsigned char* lds, int rg, int cb) {
  const int tid = threadIdx.x, lane = tid & 63, w = __builtin_amdgcn_readfirstlane(tid >> 6);
  const int kh = w >> 2, wr = (w + cb) & 7;
  const __amdgpu_buffer_rsrc_t rs = mk_rs(p.ws, WS_END);
  const unsigned lbase = (unsigned)(size_t)lds;
  unsigned l16 = lane * 16, l8 = lane * 8, moff = mem_off(lane), roff, soff = (unsigned)((lane >> 5) * 256 + (lane & 31) * 4);
  const int sidx = (lane & 8) ? 0xEEEE : 0x4444;
  bf16x16 WV[8], WB[16];
  {
    const int fr = lane & 15, g = lane >> 4, tu = 4 * (g & 1) + (fr >> 2), ub = lane >> 3;
    { const int sl = mem_slot(16 * (g >> 1) + fr); roff = R_RED + w * 512 + ((sl >> 5) * 32 + (sl & 15) * 2 + (g & 1)) * 8; }
    if (w == 0) {
      const int sl = (lane >> 1) & 15, col = cb * 32 + ((sl >> 2) & 1) * 16 + (sl >> 3) * 8 + (lane >> 5) * 4 + (sl & 1) * 2 + ((sl >> 1) & 1), r = rg * 4 + (lane & 1) * 2;
      stx4(rs, soff, X_MEM + (cb >> 1) * 1024 + rg * 512 + ((cb ^ rg) & 1) * 128, nz((unsigned)p.mem0[r * D_ + col] | ((unsigned)p.mem0[(r + 1) * D_ + col] << 16)));
    }
    const __amdgpu_buffer_rsrc_t rv = mk_rs(p.Wv, D_ * HD * 2), ro = mk_rs(p.Wo, D_ * HD * 2);
    const unsigned vv = (unsigned)(ub * HD + 16 * (((lane >> 1) & 3) ^ (ub >> 1)) + 8 * (lane & 1)) * 2u, sv = (unsigned)(8 * w * HD + cb * DH + rg * 64) * 2u;
#pragma unroll
    for (int c = 0; c < 16; ++c) dma16(rv, vv, sv + (unsigned)c * 64 * HD * 2, lbase + R_W + c * 8192 + w * 1024);
    WAIT_V(0); BAR();
    const unsigned lo = (g >> 1) * 1024 + tu * 128 + (((tu >> 1) ^ (w & 3)) << 5) + (fr & 3) * 8;
#pragma unroll
    for (int c = 0; c < 8; ++c) WV[c] = w_frag(lds + R_W + (kh * 8 + c) * 8192 + lo);
    LBAR();
    const int u = (lane >> 2) & 7;
    const unsigned vw = (unsigned)((16 * (u >> 2) + 8 * ((u >> 1) & 1) + 4 * (lane >> 5) + (u & 1)) * D_ + (lane & 3) * 8) * 2u;
    const unsigned sw = (unsigned)(wr * 512 * D_ + cb * 32) * 2u;
    const unsigned bo = (g >> 1) * 1024 + tu * 64 + (fr & 3) * 8;
    auto stageo = [&](int hd) {
#pragma unroll
      for (int q = 0; q < 8; ++q)
        dma16(ro, vw, sw + (unsigned)((hd * 128 + 64 * (q >> 2) + 32 * (q & 1) + 2 * ((q >> 1) & 1)) * D_) * 2u, lbase + R_W + w * 16384 + (hd & 1) * 8192 + q * 1024);
    };
    stageo(0); stageo(1);
#pragma unroll
    for (int hd = 0; hd < 4; ++hd) {
      if (hd < 3) wait_v<8>(); else wait_v<0>();
#pragma unroll
      for (int f = 0; f < 4; ++f) WB[hd * 4 + f] = w_frag(lds + R_W + w * 16384 + (hd & 1) * 8192 + (f >> 1) * 512 + (f & 1) * 32 + bo);
      WAIT_L0(); SCHED();
      if (hd < 2) stageo(hd + 2);
    }
  }
  WST(rs, 176 + rg * 8 + (cb >> 2), 4);
  asm volatile("" : "+v"(l16), "+v"(l8), "+v"(moff), "+v"(roff), "+v"(soff));
  unsigned cap = SPIN_CAP;
  Sched tt; tt.tau = rt32() - 100000; tt.prev = tt.tau; tt.ea = 195 * 8; tt.eb = 118 * 8; tt.d1 = 0; tt.moved = true;
  const int cu_ = 32 + (rg * 32 + cb) / 2; (void)cu_;
  ST_DECL
  for (int s = 0; s < NSEG; ++s) {
    if (w == 7) wait_until(tt.tau);
    BAR();
    ST0();
    gather_mem(rs, lbase, s, wr, l16, cap);
    ST(1);
    LBAR();
    ST(2);
    {
      f32x4 c0 = {0.f, 0.f, 0.f, 0.f};
      LAS unsigned char* mp = lad(moff, lbase + R_MEM + kh * 8192);
      bf16x8 af[8];
#pragma unroll
      for (int c = 0; c < 8; ++c) af[c] = mem_frag(mp, c);
      SCHED();
#pragma unroll
      for (int c = 0; c < 8; ++c) c0 = smf(af[c], WV[c], c0, sidx);
      const f32x4 f = fold(c0, c0);
      if (l16 < 512) stx8(rs, l8, X_VM + (unsigned)s * RSTR + cb * 4096 + (rg * 4 + (w & 3)) * 512 + kh * 256, u32x2{nz(pack2(f[0], f[1])), nz(pack2(f[2], f[3]))});
    }
    ST(3);
    if (w < 4 && s + 2 <= NSEG && (rg * 32 + cb) * 4 + w < 220) {
      u32x4 z; asm volatile("v_mov_b32 %0, 0" : "=v"(z[0])); z[1] = z[2] = z[3] = z[0];
      stx16(rs, l16, X_REC + (unsigned)(s + 2) * RSTR + (unsigned)((rg * 32 + cb) * 4 + w) * 1024, z);
    }
    if (w == 7) retime(rs, l16, s, tt, p, cap);
    if (s + 1 < NSEG) {
      u32x4 a[4];
      const unsigned so = X_OX + s * RSTR + rg * 32768 + wr * 4096;
      if (w == 7) wait_until(tt.tau + tt.d1);
      BAR();
      ST(4);
      issue4(rs, l16, so, so + 1024, so + 2048, so + 3072, a[0], a[1], a[2], a[3]);
      f32x4 c[4];
#pragma unroll
      for (int t = 0; t < 4; ++t) c[t] = f32x4{0.f, 0.f, 0.f, 0.f};
#pragma unroll
      for (int i = 0; i < 4; ++i) {
        if (i == 0) arrive<0>(rs, l16, so, a[0], cap); else if (i == 1) arrive<1>(rs, l16, so + 1024, a[1], cap);
        else if (i == 2) arrive<2>(rs, l16, so + 2048, a[2], cap); else arrive<3>(rs, l16, so + 3072, a[3], cap);
#pragma unroll
        for (int t = 0; t < 4; ++t) c[t] = smf(__builtin_bit_cast(bf16x8, a[i]), WB[i * 4 + t], c[t], sidx);
        SCHED();
      }
      ST(5);
      {
        const bool odd = (l16 & 256) != 0;
        const f32x4 f = fold(odd ? c[2] : c[0], odd ? c[3] : c[1]);
        const auto e0 = __builtin_amdgcn_permlane16_swap(f2u(f[0]), f2u(f[2]), false, false), e1 = __builtin_amdgcn_permlane16_swap(f2u(f[1]), f2u(f[3]), false, false);
        *(LAS f32x2*)(lad(roff, lbase)) = f32x2{u2f(e0[0]) + u2f(e0[1]), u2f(e1[0]) + u2f(e1[1])};
      }
      ST(6);
      LBAR();
      ST(7);
      if (w == 0) {
        LAS unsigned char* rp = lad(l8, lbase + R_RED);
        f32x2 v[8];
#pragma unroll
        for (int i = 0; i < 8; ++i) v[i] = *(const LAS f32x2*)(rp + i * 512);
        const f32x2 sum = ((v[0] + v[1]) + (v[2] + v[3])) + ((v[4] + v[5]) + (v[6] + v[7]));
        stx4(rs, soff, X_MEM + (s + 1) * RSTR + (cb >> 1) * 1024 + rg * 512 + ((cb ^ rg) & 1) * 128, nz(pack2(sum[0], sum[1])));
        const unsigned t = max(rt32(), 1u);
        if (l16 == 0) stx4(rs, 0, X_TS + (unsigned)(s + 1) * RSTR + (unsigned)(rg * 32 + cb) * 128, t);
      }
      ST(8);
    }
    advance(tt, p);
  }
  ST_FLUSH();
  if (cap == 0u && l16 == 0) stx4(rs, 0, X_CTL, 1u);
}

#if defined(__HIP_DEVICE_COMPILE__)
__attribute__((target("no-packed-fp32-ops")))
#endif
__global__ void __launch_bounds__(512, 2) k_main(MainP p) {
  extern __shared__ __attribute__((aligned(16))) unsigned char smem[];
  const int b = blockIdx.x, x = b & 7, j = b >> 3;
  if (x >= 3) worker(p, (LAS unsigned char*)smem, x - 3, j);
  else if (x == 0) role_a(p, (LAS unsigned char*)smem, j);
  else role_vb(p, (LAS unsigned char*)smem, x - 1, j);
}

extern "C" void kernel_launch(void* const* d_in, const int* in_sizes, int n_in, void* d_out, int out_size, void* d_ws, size_t ws_size,
                              hipStream_t stream) {
  unsigned char* ws = (unsigned char*)d_ws;
  (void)hipMemsetAsync(ws, 0, X_ZERO, stream);
  MainP p;
  p.x = (const u16*)d_in[0]; p.mem0 = (const u16*)d_in[1]; p.c = (const u16*)d_in[12]; p.wmod = (const u16*)d_in[13]; p.Wq = (const u16*)d_in[2]; p.Wk = (const u16*)d_in[3]; p.Wv = (const u16*)d_in[4]; p.Wo = (const u16*)d_in[5];
  p.w1 = (const u16*)d_in[8]; p.w2 = (const u16*)d_in[9]; p.w3 = (const u16*)d_in[10]; p.w4 = (const u16*)d_in[11];
  p.out = (u16*)d_out; p.ws = ws; p.d1 = 10; p.d2 = 10; p.d3 = 2; p.d4 = 100;
  (void)hipFuncSetAttribute((const void*)k_main, hipFuncAttributeMaxDynamicSharedMemorySize, R_LDS);
  k_main<<<256, 512, R_LDS, stream>>>(p);
}
```

```cpp
#include <hip/hip_runtime.h>
#include <hip/hip_bf16.h>
#include <cstdlib>
#pragma clang diagnostic ignored "-Winline-asm"

#define LAS __attribute__((address_space(3)))
#define GAS __attribute__((address_space(1)))
typedef unsigned short u16;
typedef __bf16 bf16x2 __attribute__((ext_vector_type(2)));
typedef __bf16 bf16x4 __attribute__((ext_vector_type(4)));
typedef __bf16 bf16x8 __attribute__((ext_vector_type(8)));
typedef __bf16 bf16x16 __attribute__((ext_vector_type(16)));
typedef short s16x4 __attribute__((ext_vector_type(4)));
typedef float f32x2 __attribute__((ext_vector_type(2)));
typedef float f32x4 __attribute__((ext_vector_type(4)));
typedef float f32x16 __attribute__((ext_vector_type(16)));
typedef unsigned u32x2 __attribute__((ext_vector_type(2)));
typedef unsigned u32x4 __attribute__((ext_vector_type(4)));

constexpr int S_ = 2048, D_ = 1024, NH = 32, DH = 128, HD = 4096, NSEG = 64, FF = 3584;

constexpr unsigned MB = 1u << 20;
constexpr unsigned F_LN = 0;
constexpr unsigned F_ST = 24576;
constexpr unsigned F_CH = F_ST + 49152;
constexpr unsigned X_CTL = F_CH + 4096;
constexpr unsigned X_MOD = X_CTL + 4096;
constexpr unsigned X_REC = X_MOD + 24576;
constexpr unsigned RSTR = 225280;
constexpr unsigned X_MEM = X_REC;
constexpr unsigned X_OX = X_REC + 16384;
constexpr unsigned X_VM = X_REC + 81920;
constexpr unsigned X_TS = X_REC + 212992;
constexpr unsigned X_ZERO = X_REC + 2 * RSTR;
constexpr unsigned WS_H = 16 * MB;
constexpr unsigned WS_Q = 20 * MB;
constexpr unsigned WS_K = 36 * MB;
constexpr unsigned WS_VT = 52 * MB;
constexpr unsigned WS_O = 68 * MB;
constexpr unsigned WS_X1 = 84 * MB;
constexpr unsigned WS_H2 = 88 * MB;
constexpr unsigned WS_G = 92 * MB;
constexpr unsigned WS_H1S = 96 * MB;
constexpr unsigned WS_X2 = 101 * MB;
constexpr unsigned WS_ST5 = 106 * MB;
constexpr unsigned WS_U = 108 * MB;
constexpr unsigned WS_KM = 124 * MB;
constexpr unsigned WS_P2 = 132 * MB;
constexpr unsigned WS_P8 = 164 * MB;
constexpr unsigned WS_END = 198 * MB;

__device__ __forceinline__ float bf2f(unsigned short b) { return __builtin_bit_cast(float, (unsigned)b << 16); }
__device__ __forceinline__ unsigned f2u(float f) { return __builtin_bit_cast(unsigned, f); }
__device__ __forceinline__ float u2f(unsigned u) { return __builtin_bit_cast(float, u); }
__device__ __forceinline__ float lo2f(unsigned w) { return __builtin_bit_cast(float, w << 16); }
__device__ __forceinline__ float hi2f(unsigned w) { return __builtin_bit_cast(float, w & 0xFFFF0000u); }
__device__ __forceinline__ unsigned pack2(float a, float b) { bf16x2 v; v[0] = (__bf16)a; v[1] = (__bf16)b; return __builtin_bit_cast(unsigned, v); }
__device__ __forceinline__ float rbf(float a) { return (float)(__bf16)a; }
__device__ __forceinline__ unsigned nz(unsigned w) { return max(w, 1u); }
__device__ __forceinline__ float gelu_tanh(float x) {
  const float e = __builtin_amdgcn_exp2f(x * fmaf(x * x, -0.1029432397f, -2.302208198f));
  return x * __builtin_amdgcn_rcpf(1.0f + e);
}
template <int CTRL> __device__ __forceinline__ float dpp_add(float v) { return v + u2f(__builtin_amdgcn_update_dpp(0u, f2u(v), CTRL, 0xF, 0xF, true)); }
__device__ __forceinline__ float wave_sum(float v) {
  v = dpp_add<0xB1>(v); v = dpp_add<0x4E>(v); v = dpp_add<0x141>(v); v = dpp_add<0x140>(v);
  auto t = __builtin_amdgcn_permlane16_swap(f2u(v), f2u(v), false, false);
  v = u2f(t[0]) + u2f(t[1]);
  t = __builtin_amdgcn_permlane32_swap(f2u(v), f2u(v), false, false);
  return u2f(t[0]) + u2f(t[1]);
}
#define WAIT_V(n) asm volatile("s_waitcnt vmcnt(" #n ")" ::: "memory")
#define WAIT_L0() asm volatile("s_waitcnt lgkmcnt(0)" ::: "memory")
#define BAR() do { asm volatile("" ::: "memory"); __builtin_amdgcn_s_barrier(); asm volatile("" ::: "memory"); } while (0)
#define LBAR() do { WAIT_L0(); BAR(); } while (0)
template <int N> __device__ __forceinline__ void wait_v() { asm volatile("s_waitcnt vmcnt(%0)" :: "n"(N) : "memory"); }
__device__ __forceinline__ void dma16(__amdgpu_buffer_rsrc_t rs, unsigned voff, unsigned soff, unsigned ldsa) {
  asm volatile("s_mov_b32 m0, %0\n\ts_nop 3\n\tbuffer_load_dwordx4 %1, %2, %3 offen lds" :: "s"(ldsa), "v"(voff), "s"(rs), "s"(soff) : "m0", "memory");
}
__device__ __forceinline__ void dma16x(__amdgpu_buffer_rsrc_t rs, unsigned voff, unsigned soff, unsigned ldsa) {
  asm volatile("s_mov_b32 m0, %0\n\ts_nop 3\n\tbuffer_load_dwordx4 %1, %2, %3 offen sc1 lds" :: "s"(ldsa), "v"(voff), "s"(rs), "s"(soff) : "m0", "memory");
}
__device__ __forceinline__ u32x4 ldx16(__amdgpu_buffer_rsrc_t rs, unsigned voff, unsigned soff) { return __builtin_amdgcn_raw_buffer_load_b128(rs, voff, soff, 16); }
__device__ __forceinline__ u32x2 ldx8(__amdgpu_buffer_rsrc_t rs, unsigned voff, unsigned soff) { return __builtin_amdgcn_raw_buffer_load_b64(rs, voff, soff, 16); }
__device__ __forceinline__ unsigned ldx4(__amdgpu_buffer_rsrc_t rs, unsigned voff, unsigned soff) { return __builtin_amdgcn_raw_buffer_load_b32(rs, voff, soff, 16); }
__device__ __forceinline__ void stx16(__amdgpu_buffer_rsrc_t rs, unsigned voff, unsigned soff, u32x4 v) {
  __builtin_amdgcn_raw_buffer_store_b128(v, rs, voff, soff, 16);
  asm volatile("s_nop 1" :: "v"(v) : "memory");
}
__device__ __forceinline__ void stx8(__amdgpu_buffer_rsrc_t rs, unsigned voff, unsigned soff, u32x2 v) { __builtin_amdgcn_raw_buffer_store_b64(v, rs, voff, soff, 16); }
__device__ __forceinline__ void stx4(__amdgpu_buffer_rsrc_t rs, unsigned voff, unsigned soff, unsigned v) { __builtin_amdgcn_raw_buffer_store_b32(v, rs, voff, soff, 16); }
__device__ __forceinline__ void wait_ge(__amdgpu_buffer_rsrc_t rs, unsigned voff, unsigned soff, unsigned need, unsigned& cap) {
  unsigned t;
  asm volatile("s_nop 4\n\t"
               "1:\n\t"
               "buffer_load_dword %[t], %[vo], %[rs], %[so] offen sc1\n\t"
               "s_waitcnt vmcnt(0)\n\t"
               "v_cmp_gt_u32 vcc, %[need], %[t]\n\t"
               "s_cmp_eq_u64 vcc, 0\n\t"
               "s_cbranch_scc1 2f\n\t"
               "s_sleep 2\n\t"
               "s_sub_u32 %[cap], %[cap], 1\n\t"
               "s_cbranch_scc0 1b\n\t"
               "s_mov_b32 %[cap], 0\n\t"
               "2:"
               : [t] "=&v"(t), [cap] "+s"(cap) : [vo] "v"(voff), [rs] "s"(rs), [so] "s"(soff), [need] "s"(need) : "vcc", "scc", "memory");
}
__device__ __forceinline__ void wait_seen(__amdgpu_buffer_rsrc_t rs, unsigned voff, unsigned soff, unsigned need, unsigned& seen, unsigned& cap) {
  if (seen >= need) return;
  unsigned t;
  asm volatile("s_nop 4\n\t"
               "1:\n\t"
               "buffer_load_dword %[t], %[vo], %[rs], %[so] offen sc1\n\t"
               "s_waitcnt vmcnt(0)\n\t"
               "v_cmp_gt_u32 vcc, %[need], %[t]\n\t"
               "s_cbranch_vccz 2f\n\t"
               "s_sleep 1\n\t"
               "s_sub_u32 %[cap], %[cap], 1\n\t"
               "s_cbranch_scc0 1b\n\t"
               "s_mov_b32 %[cap], 0\n\t"
               "2:"
               : [t] "=&v"(t), [cap] "+s"(cap) : [vo] "v"(voff), [rs] "s"(rs), [so] "s"(soff), [need] "s"(need) : "vcc", "scc", "memory");
  seen = min((unsigned)__builtin_amdgcn_readlane(t, 0), (unsigned)__builtin_amdgcn_readlane(t, 1));
}
__device__ __forceinline__ __amdgpu_buffer_rsrc_t mk_rs(const void* p, unsigned bytes) { return __builtin_amdgcn_make_buffer_rsrc((void*)p, 0, (int)bytes, 0x00020000); }
#define SCHED() __builtin_amdgcn_sched_barrier(0)
__device__ __forceinline__ unsigned now32() { unsigned long long t; asm volatile("s_memtime %0\n\ts_waitcnt lgkmcnt(0)" : "=s"(t) :: "memory"); return (unsigned)t; }
__device__ __forceinline__ unsigned rt32() { unsigned long long t; asm volatile("s_memrealtime %0\n\ts_waitcnt lgkmcnt(0)" : "=s"(t) :: "memory"); return (unsigned)t; }
__device__ __forceinline__ void wait_until(unsigned t) { while ((int)(rt32() - t) < 0) {} }
template <int X = 0> __device__ __forceinline__ LAS unsigned char* lad(unsigned v, unsigned u) {
  unsigned a;
  if (X == 0) asm volatile("v_add_u32 %0, %1, %2" : "=v"(a) : "s"(u), "v"(v));
  else asm volatile("v_xor_b32 %0, %3, %2\n\tv_add_u32 %0, %1, %0" : "=&v"(a) : "s"(u), "v"(v), "n"(X));
  return (LAS unsigned char*)(size_t)a;
}
__device__ __forceinline__ bf16x4 trread(LAS unsigned char* p) { return __builtin_amdgcn_ds_read_tr16_b64_v4bf16((LAS bf16x4*)p); }
__device__ __forceinline__ bf16x8 cat8(bf16x4 a, bf16x4 b) { return __builtin_shufflevector(a, b, 0, 1, 2, 3, 4, 5, 6, 7); }
__device__ __forceinline__ bf16x16 cat16(bf16x8 a, bf16x8 b) { return __builtin_shufflevector(a, b, 0, 1, 2, 3, 4, 5, 6, 7, 8, 9, 10, 11, 12, 13, 14, 15); }
__device__ __forceinline__ f32x4 mfma32(bf16x8 a, bf16x8 b, f32x4 c) { return __builtin_amdgcn_mfma_f32_16x16x32_bf16(a, b, c, 0, 0, 0); }
__device__ __forceinline__ f32x4 mfma16(u32x2 a, u32x2 b, f32x4 c) {
  return mfma32(__builtin_bit_cast(bf16x8, u32x4{a[0], a[1], 0u, 0u}), __builtin_bit_cast(bf16x8, u32x4{b[0], b[1], 0u, 0u}), c);
}
__device__ __forceinline__ f32x4 mfma16n(u32x2 a, u32x2 b, f32x4 c) { return __builtin_amdgcn_mfma_f32_16x16x16bf16_1k(__builtin_bit_cast(s16x4, a), __builtin_bit_cast(s16x4, b), c, 0, 0, 0); }
__device__ __forceinline__ f32x4 smf(bf16x8 a, bf16x16 b, f32x4 c, int idx) { return __builtin_amdgcn_smfmac_f32_16x16x64_bf16(a, b, c, idx, 0, 0); }

__device__ __forceinline__ void ln_row(float (&x)[16]) {
  float s = 0.f;
#pragma unroll
  for (int i = 0; i < 16; ++i) s += x[i];
  const float mean = wave_sum(s) * (1.0f / 1024.0f);
  float q = 0.f;
#pragma unroll
  for (int i = 0; i < 16; ++i) { const float d = x[i] - mean; q += d * d; }
  const float var = wave_sum(q) * (1.0f / 1024.0f);
  const float m = rbf(mean), isd = __builtin_amdgcn_rcpf(rbf(__builtin_amdgcn_sqrtf(rbf(rbf(var) + 1e-5f))));
#pragma unroll
  for (int i = 0; i < 16; ++i) x[i] = rbf(rbf(x[i] - m) * isd);
}
__device__ __forceinline__ void load_row16(const u16* rowp, int lane, float (&x)[16]) {
  const u32x4 a = *(const u32x4*)(rowp + lane * 8), b = *(const u32x4*)(rowp + 512 + lane * 8);
#pragma unroll
  for (int i = 0; i < 4; ++i) { x[2 * i] = lo2f(a[i]); x[2 * i + 1] = hi2f(a[i]); x[8 + 2 * i] = lo2f(b[i]); x[9 + 2 * i] = hi2f(b[i]); }
}
__device__ __forceinline__ void store_row16(u16* rowp, int lane, const float (&x)[16]) {
  u32x4 a, b;
#pragma unroll
  for (int i = 0; i < 4; ++i) { a[i] = pack2(x[2 * i], x[2 * i + 1]); b[i] = pack2(x[8 + 2 * i], x[9 + 2 * i]); }
  *(u32x4*)(rowp + lane * 8) = a; *(u32x4*)(rowp + 512 + lane * 8) = b;
}
__host__ __device__ __forceinline__ int mem_slot(int dl) {
  return ((dl >> 2) & 1) * 32 + (((dl >> 5) & 1) * 2 + ((dl >> 3) & 1)) * 8 + (((dl >> 4) & 1) * 2 + (dl & 1)) * 2 + ((dl >> 1) & 1);
}

#define WST(rs, wk, i)
struct MainP { const u16 *x, *mem0, *Wq, *Wk, *Wv, *Wo, *w1, *w2, *w3, *w4, *c, *wmod; u16* out; unsigned char* ws; int d1, d2, d3, d4; };
constexpr unsigned SPIN_CAP = 1u << 18;

__device__ __forceinline__ float gmax(float v) {
  auto t = __builtin_amdgcn_permlane16_swap(f2u(v), f2u(v), false, false);
  v = fmaxf(u2f(t[0]), u2f(t[1]));
  t = __builtin_amdgcn_permlane32_swap(f2u(v), f2u(v), false, false);
  return fmaxf(u2f(t[0]), u2f(t[1]));
}
__device__ __forceinline__ float gsum(float v) {
  auto t = __builtin_amdgcn_permlane16_swap(f2u(v), f2u(v), false, false);
  v = u2f(t[0]) + u2f(t[1]);
  t = __builtin_amdgcn_permlane32_swap(f2u(v), f2u(v), false, false);
  return u2f(t[0]) + u2f(t[1]);
}
__device__ __forceinline__ void publish(__amdgpu_buffer_rsrc_t rs, unsigned word, unsigned val) {
  WAIT_V(0); BAR();
  if (threadIdx.x == 0) stx4(rs, 0, word, val);
}

constexpr int P_S2 = 0, P_S8 = 32, P_S7 = 64, P_S4 = 96, P_S5 = 104, P_S1 = 112, P_S3 = 144, P_PJ = 240;
constexpr int NLN = 160;

enum { SG_WO = 0, SG_W1 = 1, SG_W2 = 2, SG_W3 = 3, SG_W4 = 4, SG_K = 5, SG_V = 6, SG_Q = 7 };
constexpr int G_XCH = 131072, G_K = G_XCH + 16384, G_CTL = G_K + 8192;
__device__ __forceinline__ f32x16 mfma3232(bf16x8 a, bf16x8 b, f32x16 c) { return __builtin_amdgcn_mfma_f32_32x32x16_bf16(a, b, c, 0, 0, 0); }
__device__ __forceinline__ void widen32(const f32x16& a, u32x4 (&v)[2]) {
#pragma unroll
  for (int u = 0; u < 2; ++u) {
    const auto r0 = __builtin_amdgcn_permlane32_swap(pack2(a[8 * u], a[8 * u + 1]), pack2(a[8 * u + 4], a[8 * u + 5]), false, false);
    const auto r1 = __builtin_amdgcn_permlane32_swap(pack2(a[8 * u + 2], a[8 * u + 3]), pack2(a[8 * u + 6], a[8 * u + 7]), false, false);
    v[u] = u32x4{r0[0], r1[0], r0[1], r1[1]};
  }
}
__device__ __forceinline__ float hsum(float v) { const auto t = __builtin_amdgcn_permlane32_swap(f2u(v), f2u(v), false, false); return u2f(t[0]) + u2f(t[1]); }

__device__ __forceinline__ void attend(__amdgpu_buffer_rsrc_t rs, LAS unsigned char* qb, LAS unsigned char* kb, int seg, int h, int t, int eh, int lane, const u32x4 (&vv)[4],
                                       unsigned& chs, unsigned& cap) {
  const int fr = lane & 15, g = lane >> 4;
  const unsigned l16 = lane * 16, ub = (unsigned)(seg * NH + h);
  f32x4 s0 = {0.f, 0.f, 0.f, 0.f}, s1 = s0, s2 = s0;
  bf16x8 q[4];
#pragma unroll
  for (int ks = 0; ks < 4; ++ks) {
    q[ks] = *(const LAS bf16x8*)(qb + ks * 4096);
    s1 = mfma32(*(const LAS bf16x8*)(kb + ks * 1024), q[ks], s1); s2 = mfma32(*(const LAS bf16x8*)(kb + 4096 + ks * 1024), q[ks], s2);
  }
  const float scale = 0.08838834764831845f, L2E = 1.4426950408889634f;
  float a[4], b[4], c[4], mt = -3.0e38f, lt = 0.f;
#pragma unroll
  for (int r = 0; r < 4; ++r) {
    const int j = 4 * g + r;
    b[r] = (t == 1 || j <= fr) ? rbf(rbf(s1[r]) * scale) : -3.0e38f;
    c[r] = (t == 1 && j <= fr) ? rbf(rbf(s2[r]) * scale) : -3.0e38f;
    mt = fmaxf(mt, fmaxf(b[r], c[r]));
  }
  mt = gmax(mt);
#pragma unroll
  for (int r = 0; r < 4; ++r) { b[r] = __builtin_amdgcn_exp2f((b[r] - mt) * L2E); c[r] = __builtin_amdgcn_exp2f((c[r] - mt) * L2E); lt += b[r] + c[r]; }
  const u32x4 pt = {pack2(b[0], b[1]), pack2(b[2], b[3]), pack2(c[0], c[1]), pack2(c[2], c[3])};
  f32x4 o[4];
#pragma unroll
  for (int i = 0; i < 4; ++i) o[i] = mfma32(__builtin_bit_cast(bf16x8, vv[i]), __builtin_bit_cast(bf16x8, pt), f32x4{0.f, 0.f, 0.f, 0.f});
  lt = gsum(lt);
  const unsigned em = (unsigned)(fr >> 3) * 512 + (unsigned)g * 128 + (unsigned)(8 * ((fr >> 2) & 1) + (fr & 3)) * 8;
  asm volatile("" : "+v"(o[0]), "+v"(o[1]), "+v"(o[2]), "+v"(o[3]), "+v"(lt));
  wait_seen(rs, 0, F_CH + h * 128, (unsigned)seg + 1u, chs, cap);
  u32x2 vmf[4];
  for (;;) {
    asm volatile("" ::: "memory");
    unsigned mn = 0xFFFFFFFFu;
    bf16x8 kmf[4];
#pragma unroll
    for (int ks = 0; ks < 4; ++ks) kmf[ks] = __builtin_bit_cast(bf16x8, ldx16(rs, l16, WS_KM + ub * 4096 + ks * 1024));
#pragma unroll
    for (int i = 0; i < 4; ++i) vmf[i] = ldx8(rs, em, X_VM + (unsigned)seg * RSTR + h * 4096 + (2 * eh + (i >> 1)) * 1024 + (i & 1) * 32);
    s0 = f32x4{0.f, 0.f, 0.f, 0.f};
#pragma unroll
    for (int ks = 0; ks < 4; ++ks) s0 = mfma32(kmf[ks], q[ks], s0);
#pragma unroll
    for (int i = 0; i < 4; ++i) mn = min(mn, min(vmf[i][0], vmf[i][1]));
    if (__all(mn != 0u) || cap == 0u) break;
    cap = __builtin_amdgcn_readfirstlane(cap - 1u);
  }
  cap = __builtin_amdgcn_readfirstlane(cap);
#pragma unroll
  for (int r = 0; r < 4; ++r) a[r] = rbf(rbf(s0[r]) * scale);
  const float mx = fmaxf(mt, gmax(fmaxf(fmaxf(a[0], a[1]), fmaxf(a[2], a[3]))));
#pragma unroll
  for (int r = 0; r < 4; ++r) a[r] = __builtin_amdgcn_exp2f((a[r] - mx) * L2E);
  const u32x2 pm = {pack2(a[0], a[1]), pack2(a[2], a[3])};
  f32x4 om[4];
#pragma unroll
  for (int i = 0; i < 4; ++i) om[i] = mfma16n(vmf[i], pm, f32x4{0.f, 0.f, 0.f, 0.f});
  const float al = __builtin_amdgcn_exp2f((mt - mx) * L2E);
  const float inv = __builtin_amdgcn_rcpf(fmaf(al, lt, 0.5f * gsum((a[0] + a[1]) + (a[2] + a[3])))), ai = al * inv;
#pragma unroll
  for (int i = 0; i < 4; ++i) o[i] = o[i] * ai + om[i] * inv;
#pragma unroll
  for (int u = 0; u < 2; ++u)
    stx16(rs, l16, WS_O + (unsigned)seg * 262144 + (unsigned)((h * 4 + 2 * eh + u) * 2 + t) * 1024,
          u32x4{pack2(o[2 * u][0], o[2 * u][1]), pack2(o[2 * u][2], o[2 * u][3]), pack2(o[2 * u + 1][0], o[2 * u + 1][1]), pack2(o[2 * u + 1][2], o[2 * u + 1][3])});
}

template <int SG>
__device__ __forceinline__ void stage_gemm(LAS unsigned char* lds, __amdgpu_buffer_rsrc_t rs, const u16* Wg, int wk, int kq, int cb, int pid, unsigned& cap) {
  constexpr bool KV = (SG == SG_K || SG == SG_V), PROJ = KV || SG == SG_Q, ROWM = (SG == SG_W1 || PROJ);
  constexpr int KS = (SG == SG_W4) ? 28 : 32;
  constexpr int LDW = PROJ ? HD : (SG == SG_W3) ? FF : D_;
  constexpr int WROWS = (SG == SG_WO) ? HD : (SG == SG_W4 ? FF : D_);
  constexpr int NBLK = KV ? 32 : (SG == SG_WO || SG == SG_W1 || SG == SG_Q) ? 64 : 65;
  constexpr unsigned ABASE = PROJ ? WS_H : SG == SG_WO ? WS_O : SG == SG_W1 ? WS_G : SG == SG_W2 ? WS_H1S : SG == SG_W3 ? WS_X2 : WS_U;
  constexpr unsigned ASTR = SG == SG_WO ? 262144u : SG == SG_W4 ? 229376u : 65536u;
  constexpr int NPC = KS / 4;
  const unsigned myword = F_ST + (unsigned)pid * 128u;
  int tid = threadIdx.x; asm volatile("" : "+v"(tid));
  const int lane = tid & 63, w = __builtin_amdgcn_readfirstlane(tid >> 6), row = lane & 31, hf = lane >> 5;
  const int kh = w >> 2, wl = w & 3;
  const int koff = PROJ ? 0 : kq;
  const unsigned lbase = (unsigned)(size_t)lds, l16 = lane * 16;
  const int col0 = cb * 128 + wl * 32;
  bf16x8 Wf[KS];
  {
    const __amdgpu_buffer_rsrc_t rsW = mk_rs(Wg, (unsigned)WROWS * LDW * 2u);
    const int ub = lane >> 3, tu = 4 * hf + ((lane & 15) >> 2);
    const unsigned vB = (unsigned)(((ub & 3) + 8 * (ub >> 2)) * LDW + 16 * (((lane >> 1) & 3) ^ (ub >> 1)) + 8 * (lane & 1)) * 2u;
    const unsigned sB = (unsigned)((koff * KS * 32 + 32 * (w >> 2) + 16 * ((w >> 1) & 1) + 4 * (w & 1)) * LDW + cb * 128) * 2u;
    const int boff = kh * 65536 + (wl >> 1) * 8192 + tu * 128 + (((tu >> 1) ^ (2 * (wl & 1) + ((lane >> 4) & 1))) << 5) + (lane & 3) * 8;
#pragma unroll
    for (int ps = 0; ps < 2; ++ps) {
      BAR();
#pragma unroll
      for (int hh = 0; hh < 2; ++hh)
#pragma unroll
        for (int i = 0; i < 4; ++i)
          if (4 * ps + i < NPC && !(PROJ && ps == 0)) {
            const unsigned so = sB + (unsigned)((hh * NPC + 4 * ps + i) * 64 * LDW) * 2u, d = lbase + hh * 65536 + i * 16384 + w * 1024;
            dma16(rsW, vB, so, d); dma16(rsW, vB, so + 128, d + 8192);
          }
      WAIT_V(0); BAR();
#pragma unroll
      for (int i = 0; i < 4; ++i)
        if (4 * ps + i < NPC) {
#pragma unroll
          for (int k = 0; k < 4; ++k) { LAS unsigned char* q = lds + (boff + i * 16384 + k * 2048); Wf[(4 * ps + i) * 4 + k] = cat8(trread(q), trread(q + 1024)); }
        }
      WAIT_L0();
    }
    if (SG == SG_W1) {
      const u32x4 z = {0u, 0u, 0u, 0u};
      stx16(rs, (unsigned)tid * 16, WS_H1S + cb * 8192, z); stx16(rs, (unsigned)tid * 16, WS_H1S + 64 * 65536 + cb * 8192, z);
      WAIT_V(0);
    }
    if (tid < 8) *(LAS unsigned*)(lds + G_CTL + tid * 4) = 0u;
    LBAR();
  }
  const f32x16 zero16 = {0.f, 0.f, 0.f, 0.f, 0.f, 0.f, 0.f, 0.f, 0.f, 0.f, 0.f, 0.f, 0.f, 0.f, 0.f, 0.f};
  auto put16 = [&](LAS unsigned char* p, const f32x16& a) {
#pragma unroll
    for (int q = 0; q < 4; ++q) *(LAS f32x4*)(p + q * 1024) = f32x4{a[4 * q], a[4 * q + 1], a[4 * q + 2], a[4 * q + 3]};
  };
  if (SG == SG_W3) {
    const u32x4 one4 = {0x3F803F80u, 0x3F803F80u, 0x3F803F80u, 0x3F803F80u};
    f32x16 cs = zero16;
#pragma unroll
    for (int k = 0; k < KS; ++k) cs = mfma3232(Wf[k], __builtin_bit_cast(bf16x8, one4), cs);
    if (kh == 0) put16(lds + wl * 4096 + l16, cs);
    LBAR();
    if (kh == 1 && row == 0) {
#pragma unroll
      for (int q = 0; q < 4; ++q)
        *(LAS f32x4*)(lds + G_K + wl * 128 + hf * 64 + q * 16) = *(const LAS f32x4*)(lds + wl * 4096 + q * 1024 + l16) + f32x4{cs[4 * q], cs[4 * q + 1], cs[4 * q + 2], cs[4 * q + 3]};
    }
    LBAR();
  }
  auto blk = [&](int i) -> int { return KV ? 2 * i + kq : i; };
  const unsigned aoff = ROWM ? (unsigned)(row * 128 + ((hf ^ ((row >> 1) & 7)) << 4)) : (unsigned)((row >> 4) * 1024 + hf * 256 + (row & 15) * 16);
  const unsigned vA = ROWM ? (unsigned)((lane >> 3) * 2048 + (((lane & 7) ^ ((4 * wl + (lane >> 4)) & 7)) << 4)) : l16;
  auto piece = [&](int hb, int q, bool on) {
    const int i = hb >> 1, hh = hb & 1;
    const unsigned so = ABASE + (unsigned)blk(i) * ASTR + (unsigned)koff * (KS * 2048) + (ROWM ? wl * 16384 + hh * 1024 : (hh * KS + wl) * 1024);
    if (on) dma16x(rs, vA, so + q * (ROWM ? 128 : 4096), lbase + (i & 1) * 65536 + hh * 32768 + wl * 1024 + q * 4096);
  };
  auto fetch = [&](int hb) {
#pragma unroll
    for (int q = 0; q < NPC; ++q) piece(hb, q, true);
  };
  constexpr bool WEAVE = (SG == SG_W1 || SG == SG_W2 || SG == SG_W3);
  auto products = [&](int i, f32x16& acc, auto&& first, auto&& early, auto&& handover) {
    const unsigned ub = lbase + (i & 1) * 65536 + kh * 32768;
    LAS unsigned char* ab[4];
    ab[0] = lad(aoff, ub);
    if (ROWM) { ab[1] = lad<32>(aoff, ub); ab[2] = lad<64>(aoff, ub); ab[3] = lad<96>(aoff, ub); }
    constexpr int PD = 10;
    bf16x8 af[PD + 1];
    auto rd = [&](int k) { af[k % (PD + 1)] = ROWM ? *(const LAS bf16x8*)(ab[k & 3] + (k >> 2) * 4096) : *(const LAS bf16x8*)(ab[0] + (k >> 1) * 2048 + (k & 1) * 512); };
#pragma unroll
    for (int k = 0; k < PD; ++k) rd(k);
    SCHED();
#pragma unroll
    for (int k = 0; k < KS; ++k) {
      if (k + PD < KS) rd(k + PD);
      acc = SG == SG_V ? mfma3232(af[k % (PD + 1)], Wf[k], k ? acc : zero16) : mfma3232(Wf[k], af[k % (PD + 1)], k ? acc : zero16);
      SCHED();
      first(k);
      SCHED();
      if (k == KS - 10) { early(); SCHED(); }
      if (k == KS - 5) { handover(); SCHED(); }
    }
    asm volatile("" : "+v"(acc));
  };
  WST(rs, wk, KV ? 2 : 10);
#define SEG(x)
  if (kh == 0) {
    auto pvo = [&](int i) -> unsigned {
      const int b = blk(i);
      if (PROJ) return F_LN + (unsigned)((b % (NLN / 4)) * 4 + (lane & 3)) * 128u;
      if (SG == SG_WO) return F_ST + (unsigned)(P_S1 + kq * 8 + (lane & 7)) * 128u;
      if (SG == SG_W1) return F_ST + (unsigned)(P_S3 + (b % 3) * 32 + (lane & 31)) * 128u;
      if (SG == SG_W2) return F_ST + (unsigned)(P_S4 + (lane & 7)) * 128u;
      if (SG == SG_W3) return F_ST + (unsigned)(P_S5 + (lane & 7)) * 128u;
      return F_ST + (unsigned)(P_S7 + kq * 7 + min(lane & 7, 6)) * 128u;
    };
    auto need = [&](int i) -> unsigned { return PROJ ? 1u : (SG == SG_W2 && i == 64) ? 64u : (unsigned)i + 1u; };
    int R = 0, Rp = 0;
    for (int i = 0; i < NBLK; ++i) {
      if (R <= i) { wait_ge(rs, pvo(i), 0, need(i), cap); fetch(2 * i); fetch(2 * i + 1); }
      const unsigned pk = w == 0 ? (lane < 32 ? pvo(i + 1) : pvo(i + 2)) : 0xF0000000u;
      if (R <= i + 1) wait_v<NPC>(); else wait_v<2 * NPC>();
      BAR();
      SEG(1);
      f32x16 acc;
      unsigned peek;
      asm volatile("buffer_load_dword %0, %1, %2, 0 offen sc1" : "=v"(peek) : "v"(pk), "s"(rs) : "memory");
      u32x4 st[8];
      float mean = 0.f, q2 = 0.f;
      if (SG == SG_W3) {
#pragma unroll
        for (int q = 0; q < 8; ++q) st[q] = ldx16(rs, l16, WS_ST5 + (unsigned)i * 8192 + q * 1024);
      }
      products(i, acc, [&](int k) {
        if (SG == SG_W3) {
          if (k >= 18 && k < 26) { mean += u2f(st[k - 18][0]) + u2f(st[k - 18][2]); q2 += u2f(st[k - 18][1]) + u2f(st[k - 18][3]); asm volatile("" : "+v"(mean), "+v"(q2)); }
          if (k == 26) { mean = hsum(mean) * (1.0f / 1024.0f); q2 = hsum(q2); asm volatile("" : "+v"(mean), "+v"(q2)); }
          if (k == 28) { q2 = rbf(rbf(fmaxf(fmaf(-mean, mean, q2 * (1.0f / 1024.0f)), 0.f)) + 1e-5f); asm volatile("" : "+v"(q2)); }
          if (k == 29) { q2 = __builtin_amdgcn_rcpf(rbf(__builtin_amdgcn_sqrtf(q2))); mean = rbf(mean); asm volatile("" : "+v"(q2), "+v"(mean)); }
        }
      }, []() {}, [&]() {
        SEG(4);
        if (SG == SG_Q && R >= i + 2) fetch(2 * i + 3);
        WAIT_V(0);
        SEG(0);
        asm volatile("" : "+v"(peek));
        if (w == 0) {
          const unsigned long long ok = __ballot(peek >= (lane < 32 ? need(i + 1) : need(i + 2)));
          const bool r1 = (R >= i + 2 || (unsigned)ok == 0xFFFFFFFFu) && i + 1 < NBLK, r2 = r1 && (unsigned)(ok >> 32) == 0xFFFFFFFFu && i + 2 < NBLK;
          if (lane == 0) *(volatile LAS unsigned*)(lds + G_CTL + 4) = (unsigned)(i + 1 + (r1 ? 1 : 0) + (r2 ? 1 : 0));
        }
        LBAR();
      });
      SEG(2);
      const unsigned rv = *(const volatile LAS unsigned*)(lds + G_CTL + 4);
      if (SG == SG_W3) {
        const float m = mean;
        *(LAS float*)(lds + G_K + 1024 + wl * 256 + lane * 4) = q2;
#pragma unroll
        for (int q = 0; q < 4; ++q) {
          const f32x4 cs = *(const LAS f32x4*)(lds + G_K + wl * 128 + hf * 64 + q * 16);
#pragma unroll
          for (int e = 0; e < 4; ++e) acc[4 * q + e] = fmaf(-m, cs[e], acc[4 * q + e]);
        }
      }
      put16(lad(l16, lbase + G_XCH + wl * 4096), acc);
      asm volatile("" ::: "memory");
      if (lane == 0) *(volatile LAS unsigned*)(lds + G_CTL + 16 + wl * 4) = (unsigned)i + 1u;
      Rp = R; R = __builtin_amdgcn_readfirstlane(rv);
      SEG(3);
      if (R >= i + 2 && Rp < i + 2) { fetch(2 * i + 2); fetch(2 * i + 3); }
      if (R >= i + 3) fetch(2 * i + 4);
      SEG(5);
    }
    BAR();
  } else {
    bool pending = false;
    auto announce = [&](int i) {
      if (lane == 0) {
        const unsigned c = __hip_atomic_fetch_add((LAS unsigned*)(lds + G_CTL), 1u, __ATOMIC_RELAXED, __HIP_MEMORY_SCOPE_WORKGROUP);
        if ((c & 3u) == 3u) stx4(rs, 0, myword, (unsigned)blk(i) + 1u);
      }
    };
    unsigned kvs[2] = {0u, 0u}, chs = 0u;
    BAR();
    for (int i = 0; i < NBLK; ++i) {
      const int b = blk(i);
      if (SG == SG_Q) {
        if (i & 1) wait_seen(rs, (unsigned)(lane & 1) * 4096, F_ST + (unsigned)(P_PJ + 64 + cb) * 128, (unsigned)i + 1u, kvs[1], cap);
        else wait_seen(rs, (unsigned)(lane & 1) * 4096, F_ST + (unsigned)(P_PJ + cb) * 128, (unsigned)i + 1u, kvs[0], cap);
      }
      BAR();
      SEG(0);
      const unsigned rv = *(const volatile LAS unsigned*)(lds + G_CTL + 4);
      u32x4 res[2], vv[4];
      float isd;
      if (SG == SG_W2) {
#pragma unroll
        for (int u = 0; u < 2; ++u) {
          const int t = 32 * b - 2 + 16 * u + (lane >> 2);
          dma16x(rs, (t >= 0 && t < S_) ? (unsigned)(t * D_ + col0 + ((lane ^ (lane >> 3)) & 3) * 8) * 2u : 0xF0000000u, WS_H2, lbase + G_K + wl * 2048 + u * 1024);
        }
      }
      if (SG == SG_Q) {
        const unsigned ub = (unsigned)(b * NH + cb) * 8192;
        dma16x(rs, l16, WS_K + ub + wl * 2048, lbase + G_K + wl * 2048); dma16x(rs, l16, WS_K + ub + wl * 2048 + 1024, lbase + G_K + wl * 2048 + 1024);
        const unsigned ev = (unsigned)((lane >> 3) & 1) * 1024 + (unsigned)((lane >> 4) * 16 + 8 * ((lane >> 2) & 1) + (lane & 3)) * 16;
#pragma unroll
        for (int q = 0; q < 4; ++q) vv[q] = ldx16(rs, ev, WS_VT + ub + (2 * (wl >> 1) + (q >> 1)) * 2048 + (q & 1) * 64);
      }
      f32x16 acc;
      int R = 0;
      LAS unsigned char* xr = lad(l16, lbase + G_XCH + wl * 4096);
      f32x4 xa[4];
      unsigned xf;
      auto take = [&]() {
        asm volatile("" ::: "memory");
        xf = *(const volatile LAS unsigned*)(lds + G_CTL + 16 + wl * 4);
        asm volatile("" ::: "memory");
#pragma unroll
        for (int q = 0; q < 4; ++q) xa[q] = *(const LAS f32x4*)(xr + q * 1024);
        if (SG == SG_W3) isd = *(const LAS float*)(lds + G_K + 1024 + wl * 256 + lane * 4);
        asm volatile("" ::: "memory");
      };
      products(i, acc, [&](int k) {
        if (SG == SG_Q && k == 8 && i > 0) { wait_v<6>(); announce(i - 1); }
      }, [&]() {
        take();
      }, [&]() {
        SEG(5);
        R = __builtin_amdgcn_readfirstlane(rv);
        if (R >= i + 2) {
          WAIT_V(0);
          LBAR();
          if (pending && tid == 256) stx4(rs, 0, myword, (unsigned)blk(i - 1) + 1u);
        }
      });
      const bool more = R >= i + 2;
      SEG(1);
      while (__builtin_amdgcn_readfirstlane(xf) <= (unsigned)i) take();
#pragma unroll
      for (int q = 0; q < 16; ++q) { acc[q] += xa[q >> 2][q & 3]; if (SG == SG_W3) acc[q] *= isd; }
      SEG(2);
      const unsigned fo = (unsigned)((col0 >> 5) * 2048 + hf * 256), ro = (unsigned)((row >> 4) * 1024 + (row & 15) * 16);
      u32x4 v[2];
      if (SG == SG_W1 || SG == SG_W3) {
#pragma unroll
        for (int q = 0; q < 16; q += 2) {
          float g0 = gelu_tanh(acc[q]), g1 = gelu_tanh(acc[q + 1]);
          asm volatile("" : "+v"(g0), "+v"(g1));
          acc[q] = g0; acc[q + 1] = g1;
          piece(2 * i + 5, q >> 1, R >= i + 3 && i >= 2);
        }
      }
      if (SG != SG_V) widen32(acc, v);
      if (SG == SG_WO || SG == SG_W4) {
        LAS unsigned char* tw = lds + G_XCH + wl * 4096 + row * 64;
        *(LAS u32x4*)(tw + ((hf ^ (row >> 1)) & 3) * 16) = v[0]; *(LAS u32x4*)(tw + (((2 + hf) ^ (row >> 1)) & 3) * 16) = v[1];
        asm volatile("" ::: "memory");
        LAS unsigned char* tr = lds + G_XCH + wl * 4096 + (lane >> 2) * 64 + ((lane ^ (lane >> 3)) & 3) * 16;
        const unsigned so = (SG == SG_WO ? WS_P2 : WS_P8) + (unsigned)(b * 4 + kq) * 65536u, vo = (unsigned)((lane >> 2) * D_ + col0 + (lane & 3) * 8) * 2u;
        stx16(rs, vo, so, *(const LAS u32x4*)(tr)); stx16(rs, vo, so + 32768, *(const LAS u32x4*)(tr + 1024));
      } else if (SG == SG_K) {
        const unsigned so = WS_K + (unsigned)(b * NH + cb) * 8192 + wl * 1024, vo = (unsigned)((row >> 4) * 4096 + hf * 256 + (row & 15) * 16);
        stx16(rs, vo, so, v[0]); stx16(rs, vo, so + 512, v[1]);
      } else if (SG == SG_V) {
        const unsigned so = WS_VT + (unsigned)(b * NH + cb) * 8192 + wl * 2048, vo = ro + hf * 256;
#pragma unroll
        for (int u = 0; u < 2; ++u)
          stx16(rs, vo, so + u * 512, u32x4{pack2(acc[4 * u], acc[4 * u + 1]), pack2(acc[4 * u + 2], acc[4 * u + 3]), pack2(acc[8 + 4 * u], acc[9 + 4 * u]), pack2(acc[10 + 4 * u], acc[11 + 4 * u])});
      } else if (SG == SG_Q) {
        LAS unsigned char* qw = lds + G_XCH + wl * 4096 + (row >> 4) * 1024 + hf * 256 + (row & 15) * 16;
        *(LAS u32x4*)(qw) = v[0]; *(LAS u32x4*)(qw + 512) = v[1];
        WAIT_V(0);
        if (lane == 0) __hip_atomic_fetch_add((LAS unsigned*)(lds + G_CTL + 8), 1u, __ATOMIC_RELAXED, __HIP_MEMORY_SCOPE_WORKGROUP);
        while (__builtin_amdgcn_readfirstlane(*(const volatile LAS unsigned*)(lds + G_CTL + 8)) < 4u * (unsigned)i + 4u) {}
        asm volatile("" ::: "memory");
        attend(rs, lds + G_XCH + (wl & 1) * 1024 + l16, lds + G_K + l16, b, cb, wl & 1, wl >> 1, lane, vv, chs, cap);
      } else if (SG == SG_W1) {
        const int grp = cb >> 1, sh = (grp & 1) ? -(1 + (grp >> 1)) : (1 + (grp >> 1));
        const int rho = 32 * b + row + sh + 2;
        const unsigned vo = (rho >= 2 && rho < S_ + 2) ? (unsigned)(rho >> 5) * 65536u + fo + (unsigned)((rho >> 4) & 1) * 1024u + (unsigned)(rho & 15) * 16u : 0xF0000000u;
        stx16(rs, vo, WS_H1S, v[0]); stx16(rs, vo, WS_H1S + 512, v[1]);
      } else if (SG == SG_W2) {
        if (!more) WAIT_V(0);
        res[0] = *(const LAS u32x4*)(lds + G_K + wl * 2048 + row * 64 + ((hf ^ (row >> 1)) & 3) * 16);
        res[1] = *(const LAS u32x4*)(lds + G_K + wl * 2048 + row * 64 + (((2 + hf) ^ (row >> 1)) & 3) * 16);
        float zs = 0.f, q2 = 0.f;
#pragma unroll
        for (int q = 0; q < 8; ++q) {
          const unsigned x = pack2(lo2f(res[q >> 2][q & 3]) + lo2f(v[q >> 2][q & 3]), hi2f(res[q >> 2][q & 3]) + hi2f(v[q >> 2][q & 3]));
          v[q >> 2][q & 3] = x; zs += lo2f(x) + hi2f(x); q2 = fmaf(lo2f(x), lo2f(x), fmaf(hi2f(x), hi2f(x), q2));
          asm volatile("" : "+v"(zs), "+v"(q2));
          piece(2 * i + 5, q, R >= i + 3 && i >= 2);
        }
        stx16(rs, fo + ro, WS_X2 + (unsigned)b * 65536u, v[0]); stx16(rs, fo + ro, WS_X2 + (unsigned)b * 65536u + 512, v[1]);
        const float mw = hsum(zs);
        q2 = hsum(q2);
        if (hf == 0) stx8(rs, (unsigned)((cb * 2 + (wl >> 1)) * 512 + row * 16 + (wl & 1) * 8), WS_ST5 + (unsigned)b * 8192, u32x2{f2u(mw), f2u(q2)});
      } else {
        stx16(rs, fo + ro, WS_U + (unsigned)b * 229376u, v[0]); stx16(rs, fo + ro, WS_U + (unsigned)b * 229376u + 512, v[1]);
      }
      SEG(3);
      pending = R >= i + 3 && i >= 2 && SG != SG_Q;
      if (SG == SG_Q ? i == NBLK - 1 : !pending) { WAIT_V(0); announce(i); }
      if (SG != SG_Q && R >= i + 3 && !(WEAVE && i >= 2)) fetch(2 * i + 5);
      if (!KV) WST(rs, wk, 16 + b);
      SEG(4);
      if (!more) BAR();
    }
  }
}

__device__ __forceinline__ void wait32(__amdgpu_buffer_rsrc_t rs, int first, int mine, int lane, unsigned need, unsigned& cap) {
  wait_ge(rs, 0, F_ST + (unsigned)(first + mine) * 128u, need, cap);
  wait_ge(rs, (unsigned)(lane & 31) * 128u, F_ST + (unsigned)first * 128u, need, cap);
}
__device__ __forceinline__ void sum_partials(__amdgpu_buffer_rsrc_t rs, unsigned l16, unsigned so, float (&a)[16]) {
  u32x4 pp[4][2];
#pragma unroll
  for (int k = 0; k < 4; ++k) { pp[k][0] = ldx16(rs, l16, so + k * 65536u); pp[k][1] = ldx16(rs, l16, so + k * 65536u + 1024); }
#pragma unroll
  for (int i = 0; i < 8; ++i) {
    f32x2 t = {0.f, 0.f};
#pragma unroll
    for (int k = 0; k < 4; ++k) t += f32x2{lo2f(pp[k][i >> 2][i & 3]), hi2f(pp[k][i >> 2][i & 3])};
    a[2 * i] = rbf(t[0]); a[2 * i + 1] = rbf(t[1]);
  }
}
__device__ __forceinline__ void put_row(__amdgpu_buffer_rsrc_t rs, unsigned l16, unsigned so, const float (&v)[16]) {
  stx16(rs, l16, so, u32x4{pack2(v[0], v[1]), pack2(v[2], v[3]), pack2(v[4], v[5]), pack2(v[6], v[7])});
  stx16(rs, l16, so + 1024, u32x4{pack2(v[8], v[9]), pack2(v[10], v[11]), pack2(v[12], v[13]), pack2(v[14], v[15])});
}
__device__ __forceinline__ void load_mod(__amdgpu_buffer_rsrc_t rs, int part, int lane, float (&v)[16], unsigned& cap) {
  for (;;) {
    asm volatile("" ::: "memory");
    u32x4 t[4];
#pragma unroll
    for (int i = 0; i < 4; ++i) t[i] = ldx16(rs, (unsigned)lane * 32, X_MOD + part * 4096 + (i >> 1) * 2048 + (i & 1) * 16);
    unsigned mn = 0xFFFFFFFFu;
#pragma unroll
    for (int i = 0; i < 16; ++i) { mn = min(mn, t[i >> 2][i & 3]); v[i] = hi2f(t[i >> 2][i & 3]); }
    if (__all(mn != 0u) || cap == 0u) break;
    cap = __builtin_amdgcn_readfirstlane(cap - 1u);
    __builtin_amdgcn_s_sleep(4);
  }
  cap = __builtin_amdgcn_readfirstlane(cap);
}
struct ModLoad { u32x4 w[8]; u16 c[8]; };
__device__ __forceinline__ void mod_load(const MainP& p, int grp, ModLoad& m) {
  const int kg = threadIdx.x >> 2, c8 = threadIdx.x & 3;
#pragma unroll
  for (int i = 0; i < 8; ++i) { m.w[i] = *(const u32x4*)(p.wmod + (size_t)(kg + 128 * i) * 6144 + grp * 32 + c8 * 8); m.c[i] = p.c[kg + 128 * i]; }
}
__device__ __forceinline__ void mod_finish(__amdgpu_buffer_rsrc_t rs, LAS float* red, int grp, const ModLoad& m) {
  const int tid = threadIdx.x, kg = tid >> 2, c8 = tid & 3;
  float acc[8] = {0.f, 0.f, 0.f, 0.f, 0.f, 0.f, 0.f, 0.f};
#pragma unroll
  for (int i = 0; i < 8; ++i) {
    const float v = bf2f(m.c[i]), sc = rbf(v / (1.0f + __expf(-v)));
#pragma unroll
    for (int j = 0; j < 4; ++j) { acc[2 * j] += sc * lo2f(m.w[i][j]); acc[2 * j + 1] += sc * hi2f(m.w[i][j]); }
  }
#pragma unroll
  for (int j = 0; j < 8; ++j) red[kg * 33 + c8 * 8 + j] = acc[j];
  LBAR();
  float t = 0.f;
#pragma unroll
  for (int i = 0; i < 8; ++i) t += red[((tid >> 5) * 8 + i) * 33 + (tid & 31)];
  t = hsum(t);
  LBAR();
  if ((tid & 63) < 32) red[(tid >> 6) * 33 + (tid & 31)] = t;
  LBAR();
  if (tid < 32) {
    float u = 0.f;
#pragma unroll
    for (int i = 0; i < 8; ++i) u += red[i * 33 + tid];
    stx4(rs, (unsigned)tid * 4, X_MOD + (unsigned)grp * 128, f2u(rbf(u)) | 1u);
  }
}

__device__ __forceinline__ void stage_mid(__amdgpu_buffer_rsrc_t rs, const u16* x, int wk, int cu, unsigned& cap) {
  const int tid = threadIdx.x, lane = tid & 63, w = __builtin_amdgcn_readfirstlane(tid >> 6);
  const int id = w * 12 + cu, grp = id >> 5, rl = id & 31;
  const unsigned l16 = lane * 16;
  float ga[16], sc[16], sh[16];
  load_mod(rs, 2, lane, ga, cap); load_mod(rs, 4, lane, sc, cap); load_mod(rs, 3, lane, sh, cap);
#pragma unroll
  for (int i = 0; i < 16; ++i) sc[i] = rbf(1.0f + sc[i]);
  for (int seg = grp; seg < NSEG; seg += 3) {
    const int row = seg * 32 + rl;
    float v[16], a[16];
    load_row16(x + (size_t)row * D_, lane, v);
    wait32(rs, P_S2, rl, lane, (unsigned)seg + 1u, cap);
    sum_partials(rs, l16, WS_P2 + (unsigned)seg * 262144u + (unsigned)rl * 2048, a);
#pragma unroll
    for (int i = 0; i < 16; ++i) v[i] = rbf(v[i] + rbf(ga[i] * a[i]));
    put_row(rs, l16, WS_X1 + (unsigned)row * 2048, v);
    ln_row(v);
#pragma unroll
    for (int i = 0; i < 16; ++i) v[i] = rbf(rbf(v[i] * sc[i]) + sh[i]);
    put_row(rs, l16, WS_H2 + (unsigned)row * 2048, v);
    ln_row(v);
    put_row(rs, l16, WS_G + (unsigned)row * 2048, v);
    WAIT_V(0);
    if (lane == 0) stx4(rs, 0, F_ST + (unsigned)(P_S3 + grp * 32 + rl) * 128, (unsigned)seg + 1u);
    WST(rs, wk, 16 + seg);
  }
}

__device__ __forceinline__ void stage_out(__amdgpu_buffer_rsrc_t rs, u16* out, int wk, int cu, unsigned& cap) {
  const int tid = threadIdx.x, lane = tid & 63, w = __builtin_amdgcn_readfirstlane(tid >> 6);
  const int grp = w >> 2, rl = cu * 4 + (w & 3);
  const unsigned l16 = lane * 16;
  float gb[16];
  load_mod(rs, 5, lane, gb, cap);
  for (int b = grp; b < 65; b += 2) {
    const int t = 32 * b - 2 + rl;
    if (t < 0 || t >= S_) continue;
    wait_ge(rs, (unsigned)(lane & 7) * 128u, F_ST + P_S5 * 128u, (unsigned)b + 1u, cap);
    const unsigned xo = (unsigned)((lane >> 2) * 2048 + (lane & 3) * 256), xs = WS_X2 + (unsigned)b * 65536u + (unsigned)((rl >> 4) * 1024 + (rl & 15) * 16);
    const u32x4 x2a = ldx16(rs, xo, xs), x2b = ldx16(rs, xo, xs + 32768);
    const u32x4 x1a = ldx16(rs, l16, WS_X1 + (unsigned)t * 2048), x1b = ldx16(rs, l16, WS_X1 + (unsigned)t * 2048 + 1024);
    wait32(rs, P_S8, rl, lane, (unsigned)b + 1u, cap);
    float a[16];
    sum_partials(rs, l16, WS_P8 + (unsigned)b * 262144u + (unsigned)rl * 2048, a);
#pragma unroll
    for (int i = 0; i < 4; ++i) {
      a[2 * i] = lo2f(x1a[i]) + rbf(gb[2 * i] * rbf(lo2f(x2a[i]) + a[2 * i])); a[2 * i + 1] = hi2f(x1a[i]) + rbf(gb[2 * i + 1] * rbf(hi2f(x2a[i]) + a[2 * i + 1]));
      a[8 + 2 * i] = lo2f(x1b[i]) + rbf(gb[8 + 2 * i] * rbf(lo2f(x2b[i]) + a[8 + 2 * i])); a[9 + 2 * i] = hi2f(x1b[i]) + rbf(gb[9 + 2 * i] * rbf(hi2f(x2b[i]) + a[9 + 2 * i]));
    }
    store_row16(out + (size_t)t * D_, lane, a);
    WST(rs, wk, 16 + b);
  }
}

#define WPRE 0
__device__ __forceinline__ void worker(const MainP& p, LAS unsigned char* lds, int xw, int j) {
  const int tid = threadIdx.x, lane = tid & 63, w = __builtin_amdgcn_readfirstlane(tid >> 6);
  const __amdgpu_buffer_rsrc_t rs = mk_rs(p.ws, WS_END);
  unsigned cap = SPIN_CAP;
  const int wk = xw * 32 + j;
  WST(rs, wk, 0);
  {
    const int c = j * 5 + xw, r0 = c * 8 + w, r1 = min(r0 + 8 * NLN, S_ - 1);
    float v0[16], v1[16];
    ModLoad m;
    if (c >= NLN - 64) mod_load(p, c - (NLN - 64), m);
    load_row16(p.x + (size_t)r0 * D_, lane, v0); load_row16(p.x + (size_t)r1 * D_, lane, v1);
    if (c >= NLN - 64) mod_finish(rs, (LAS float*)(lds + G_XCH), c - (NLN - 64), m);
    ln_row(v0);
    SCHED();
    {
      const __amdgpu_buffer_rsrc_t rsW = mk_rs(xw == 4 ? p.Wq : (xw & 1) ? p.Wv : p.Wk, D_ * HD * 2);
      const int ub = lane >> 3;
      const unsigned vB = (unsigned)(((ub & 3) + 8 * (ub >> 2)) * HD + 16 * (((lane >> 1) & 3) ^ (ub >> 1)) + 8 * (lane & 1)) * 2u;
      const unsigned sB = (unsigned)((32 * (w >> 2) + 16 * ((w >> 1) & 1) + 4 * (w & 1)) * HD + j * 128) * 2u, lbase = (unsigned)(size_t)lds;
      float sc[16], sh[16];
#pragma unroll
      for (int q = 0; q < 8; ++q) {
        const unsigned so = sB + (unsigned)(((q >> 2) * 8 + (q & 3)) * 64 * HD) * 2u, d = lbase + (q >> 2) * 65536 + (q & 3) * 16384 + w * 1024;
        dma16(rsW, vB, so, d); dma16(rsW, vB, so + 128, d + 8192);
      }
      ln_row(v1);
      load_mod(rs, 1, lane, sc, cap); load_mod(rs, 0, lane, sh, cap);
#pragma unroll
      for (int i = 0; i < 16; ++i) { const float f = rbf(1.0f + sc[i]); v0[i] = rbf(rbf(v0[i] * f) + sh[i]); v1[i] = rbf(rbf(v1[i] * f) + sh[i]); }
    }
    put_row(rs, (unsigned)lane * 16, WS_H + (unsigned)r0 * 2048, v0);
    if (r0 + 8 * NLN < S_) put_row(rs, (unsigned)lane * 16, WS_H + (unsigned)r1 * 2048, v1);
    publish(rs, F_LN + c * 128, 1u);
    WST(rs, wk, 1);
  }
  if (xw == 4) stage_gemm<SG_Q>(lds, rs, p.Wq, wk, 0, j, P_S1 + j, cap);
  else {
    if (xw & 1) stage_gemm<SG_V>(lds, rs, p.Wv, wk, xw >> 1, j, P_PJ + xw * 32 + j, cap);
    else stage_gemm<SG_K>(lds, rs, p.Wk, wk, xw >> 1, j, P_PJ + xw * 32 + j, cap);
    WST(rs, wk, 3);
    { ModLoad m; mod_load(p, 64 + wk, m); mod_finish(rs, (LAS float*)(lds + 65536), 64 + wk, m); }
    if (xw == 0) stage_gemm<SG_WO>(lds, rs, p.Wo, wk, j >> 3, j & 7, P_S2 + j, cap);
    else if (xw == 1) stage_gemm<SG_W4>(lds, rs, p.w4, wk, j >> 3, j & 7, P_S8 + j, cap);
    else if (j >= 28) stage_out(rs, p.out, wk, (xw - 2) * 4 + j - 28, cap);
    else if (xw == 3) stage_gemm<SG_W3>(lds, rs, p.w3, wk, 0, j, P_S7 + j, cap);
    else if (j < 8) stage_gemm<SG_W1>(lds, rs, p.w1, wk, 0, j, P_S4 + j, cap);
    else if (j < 16) stage_gemm<SG_W2>(lds, rs, p.w2, wk, 0, j - 8, P_S5 + j - 8, cap);
    else stage_mid(rs, p.x, wk, j - 16, cap);
  }
  if (cap == 0u && lane == 0) stx4(rs, 0, X_CTL, 1u);
}

#define ST_DECL
#define ST0()
#define ST(i)
#define ST_FLUSH()
constexpr int R_W = 0, R_MEM = 131072, R_QK = R_MEM + 16384, R_KT = R_QK + 8192, R_RED = R_MEM + 16384, R_LDS = 163840;
__device__ __forceinline__ void bar_lo(int w) { asm volatile("s_cmp_ge_u32 %0, 4\n\ts_cbranch_scc1 1f\n\ts_barrier\n\t1:" :: "s"(w) : "scc", "memory"); }
__device__ __forceinline__ void bar_hi(int w) { asm volatile("s_cmp_lt_u32 %0, 4\n\ts_cbranch_scc1 1f\n\ts_setprio 3\n\ts_barrier\n\t1:" :: "s"(w) : "scc", "memory"); }
__device__ __forceinline__ void issue4(__amdgpu_buffer_rsrc_t rs, unsigned vo, unsigned s0, unsigned s1, unsigned s2, unsigned s3, u32x4& a0, u32x4& a1, u32x4& a2, u32x4& a3) {
  asm volatile("buffer_load_dwordx4 v[240:243], %[vo], %[rs], %[s0] offen sc1\n\tbuffer_load_dwordx4 v[244:247], %[vo], %[rs], %[s1] offen sc1\n\t"
               "buffer_load_dwordx4 v[248:251], %[vo], %[rs], %[s2] offen sc1\n\tbuffer_load_dwordx4 v[252:255], %[vo], %[rs], %[s3] offen sc1"
               : "=&{v[240:243]}"(a0), "=&{v[244:247]}"(a1), "=&{v[248:251]}"(a2), "=&{v[252:255]}"(a3)
               : [vo] "v"(vo), [rs] "s"(rs), [s0] "s"(s0), [s1] "s"(s1), [s2] "s"(s2), [s3] "s"(s3) : "memory");
}
#define ARRIVE(R0, R1, R2, R3, CNT) \
  asm volatile("s_waitcnt vmcnt(" #CNT ")\n\t" \
               "1:\n\t" \
               "v_min3_u32 %[t], v" #R0 ", v" #R1 ", v" #R2 "\n\t" \
               "v_min_u32 %[t], %[t], v" #R3 "\n\t" \
               "v_cmp_eq_u32 vcc, 0, %[t]\n\t" \
               "s_cbranch_vccz 2f\n\t" \
               "s_mov_b64 exec, vcc\n\t" \
               "buffer_load_dwordx4 v[" #R0 ":" #R3 "], %[vo], %[rs], %[so] offen sc1\n\t" \
               "s_mov_b64 exec, -1\n\t" \
               "s_waitcnt vmcnt(0)\n\t" \
               "s_sub_u32 %[cap], %[cap], 1\n\t" \
               "s_cbranch_scc0 1b\n\t" \
               "s_mov_b32 %[cap], 0\n\t" \
               "2:" \
               : "+{v[" #R0 ":" #R3 "]}"(a), [t] "=&v"(t), [cap] "+s"(cap) : [vo] "v"(vo), [rs] "s"(rs), [so] "s"(so) : "vcc", "scc", "memory")
template <int I> __device__ __forceinline__ void arrive(__amdgpu_buffer_rsrc_t rs, unsigned vo, unsigned so, u32x4& a, unsigned& cap) {
  unsigned t;
  if (I == 0) ARRIVE(240, 241, 242, 243, 3);
  else if (I == 1) ARRIVE(244, 245, 246, 247, 2);
  else if (I == 2) ARRIVE(248, 249, 250, 251, 1);
  else ARRIVE(252, 253, 254, 255, 0);
}
__device__ __forceinline__ unsigned half_of(unsigned v) { unsigned r; asm volatile("v_lshrrev_b32 %0, 1, %1" : "=v"(r) : "v"(v)); return r; }
__device__ __forceinline__ void issue2(__amdgpu_buffer_rsrc_t rs, unsigned l8, unsigned so, int w, u32x4& a) {
  asm volatile("s_cmp_ge_u32 %[w], 4\n\ts_cbranch_scc1 1f\n\tv_lshrrev_b32 v255, 1, %[l8]\n\t"
               "buffer_load_dwordx2 v[252:253], v255, %[rs], %[so] offen sc1\n\tbuffer_load_dwordx2 v[254:255], v255, %[rs], %[so] offen offset:512 sc1\n\t1:"
               : "=&{v[252:255]}"(a) : [l8] "v"(l8), [rs] "s"(rs), [so] "s"(so), [w] "s"(w) : "scc", "memory");
}
__device__ __forceinline__ void arrive2(__amdgpu_buffer_rsrc_t rs, unsigned l8, unsigned so, u32x4& a, unsigned& cap) {
  unsigned t;
  asm volatile("s_waitcnt vmcnt(0)\n\t"
               "1:\n\t"
               "v_min3_u32 %[t], v252, v253, v254\n\t"
               "v_min_u32 %[t], %[t], v255\n\t"
               "v_cmp_eq_u32 vcc, 0, %[t]\n\t"
               "s_cbranch_vccz 2f\n\t"
               "v_lshrrev_b32 %[t], 1, %[l8]\n\t"
               "buffer_load_dwordx2 v[252:253], %[t], %[rs], %[so] offen sc1\n\t"
               "buffer_load_dwordx2 v[254:255], %[t], %[rs], %[so] offen offset:512 sc1\n\t"
               "s_waitcnt vmcnt(0)\n\t"
               "s_sub_u32 %[cap], %[cap], 1\n\t"
               "s_cbranch_scc0 1b\n\t"
               "s_mov_b32 %[cap], 0\n\t"
               "2:"
               : "+{v[252:255]}"(a), [t] "=&v"(t), [cap] "+s"(cap) : [l8] "v"(l8), [rs] "s"(rs), [so] "s"(so) : "vcc", "scc", "memory");
}
__device__ __forceinline__ unsigned ror8(unsigned v) { return __builtin_amdgcn_update_dpp(0u, v, 0x128, 0xF, 0xF, true); }
__device__ __forceinline__ void gather_mem(__amdgpu_buffer_rsrc_t rs, unsigned lbase, int s, int w, unsigned l16, unsigned& cap) {
  const unsigned so = X_MEM + s * RSTR + w * 2048;
  u32x4 a0, a1;
  asm volatile("buffer_load_dwordx4 v[248:251], %[vo], %[rs], %[so] offen sc1\n\tbuffer_load_dwordx4 v[252:255], %[vo], %[rs], %[so] offen offset:1024 sc1"
               : "=&{v[248:251]}"(a0), "=&{v[252:255]}"(a1) : [vo] "v"(l16), [rs] "s"(rs), [so] "s"(so) : "memory");
  LAS unsigned char* d = lad(l16, lbase + R_MEM + w * 2048);
  arrive<2>(rs, l16, so, a0, cap);
  *(LAS u32x4*)(d) = a0;
  arrive<3>(rs, l16, so + 1024, a1, cap);
  *(LAS u32x4*)(d + 1024) = a1;
}
__device__ __forceinline__ unsigned mem_off(int lane) { return (unsigned)((lane & 1) * 512 + (((lane >> 1) ^ ((lane & 1) * 16)) * 8)); }
__device__ __forceinline__ bf16x8 mem_frag(LAS unsigned char* mp, int chunk) { return cat8(trread(mp + chunk * 1024), trread(mp + chunk * 1024 + 256)); }
__device__ __forceinline__ bf16x16 w_frag(LAS unsigned char* p) {
  return cat16(cat8(trread(p), trread(p + 2048)), cat8(trread(p + 4096), trread(p + 6144)));
}
__device__ __forceinline__ bf16x16 l_frag(LAS unsigned char* p, int i) { return cat16(*(const LAS bf16x8*)(p + i * 2048), *(const LAS bf16x8*)(p + i * 2048 + 1024)); }
__device__ __forceinline__ void make_resident(LAS unsigned char* wb, unsigned lo, unsigned l16) {
#pragma unroll
  for (int c = 0; c < 2; ++c) {
    bf16x16 f[4];
#pragma unroll
    for (int t = 0; t < 4; ++t) f[t] = w_frag(wb + c * 8192 + (lo ^ (t << 5)));
    WAIT_L0(); SCHED();
#pragma unroll
    for (int t = 0; t < 4; ++t) {
      *(LAS bf16x8*)(wb + (c * 4 + t) * 2048 + l16) = __builtin_shufflevector(f[t], f[t], 0, 1, 2, 3, 4, 5, 6, 7);
      *(LAS bf16x8*)(wb + (c * 4 + t) * 2048 + 1024 + l16) = __builtin_shufflevector(f[t], f[t], 8, 9, 10, 11, 12, 13, 14, 15);
    }
    WAIT_L0(); SCHED();
  }
}
__device__ __forceinline__ f32x4 fold(f32x4 a, f32x4 b) {
  f32x4 r;
#pragma unroll
  for (int i = 0; i < 4; ++i) {
    const auto t = __builtin_amdgcn_permlane32_swap(f2u(a[i]), f2u(b[i]), false, false);
    r[i] = u2f(t[0]) + u2f(t[1]);
  }
  return r;
}

struct Sched { unsigned tau, prev; int ea, eb, d1; bool moved; };
__device__ __forceinline__ void retime(__amdgpu_buffer_rsrc_t rs, unsigned l16, int s, Sched& t, const MainP& p, unsigned& cap) {
  if (s > 0) {
    unsigned vo;
    asm volatile("v_lshlrev_b32 %0, 3, %1" : "=v"(vo) : "v"(l16));
    unsigned va;
    asm volatile("v_and_b32 %0, 0xF80, %1" : "=v"(va) : "v"(vo));
    wait_ge(rs, vo, X_TS + (unsigned)s * RSTR, 1u, cap);
    wait_ge(rs, va, X_TS + (unsigned)s * RSTR + 8192, 1u, cap);
    int vb = (int)(ldx4(rs, vo, X_TS + (unsigned)s * RSTR) - t.prev), vc = (int)(ldx4(rs, va, X_TS + (unsigned)s * RSTR + 8192) - t.prev);
#pragma unroll
    for (int o = 32; o >= 1; o >>= 1) { vb = max(vb, __shfl_xor(vb, o)); vc = max(vc, __shfl_xor(vc, o)); }
    const int tb = __builtin_amdgcn_readfirstlane(vb), ta = __builtin_amdgcn_readfirstlane(vc);
    const int late = tb + p.d3 - (int)(t.tau - t.prev);
    if (s > 2) {
      const int up = s < 5 ? 80 : 8, dn = s < 10 ? 40 : 8;
      if (!t.moved) t.ea = max(t.ea - dn, min(ta * 8, t.ea + up));
      if (ta + p.d3 <= t.d1) t.eb = max(t.eb - dn, min((tb - t.d1) * 8, t.eb + up));
    }
    t.moved = late > 0;
    if (late > 0) t.tau += late + min(p.d4, 8 * late);
  }
  t.d1 = (t.ea >> 3) + p.d1;
}
__device__ __forceinline__ void advance(Sched& t, const MainP& p) { t.prev = t.tau; t.tau += t.d1 + (t.eb >> 3) + p.d2; }

__device__ __forceinline__ void role_a(const MainP& p, LAS unsigned char* lds, int h) {
  const int tid = threadIdx.x, lane = tid & 63, w = __builtin_amdgcn_readfirstlane(tid >> 6);
  const int cg = w & 3, kh = w >> 2;
  const __amdgpu_buffer_rsrc_t rs = mk_rs(p.ws, WS_END);
  const unsigned lbase = (unsigned)(size_t)lds;
  unsigned l16 = lane * 16;
  asm volatile("" : "+v"(l16));
  const int sidx = (lane & 8) ? 0xEEEE : 0x4444;
  bf16x16 W[24];
  unsigned qoff;
  {
    const int fr = lane & 15, g = lane >> 4, tu = 4 * (g & 1) + (fr >> 2), ub = lane >> 3;
    const unsigned lo = (g >> 1) * 1024 + tu * 128 + ((tu >> 1) << 5) + (fr & 3) * 8;
    qoff = R_QK + ((fr >> 1) & 1) * 512 + (g >> 1) * 256 + (fr & 1) * 128 + (8 * (g & 1) + (fr >> 2)) * 8;
    const __amdgpu_buffer_rsrc_t rw = mk_rs(cg < 2 ? p.Wq : p.Wk, D_ * HD * 2);
    const unsigned vo = (unsigned)(ub * HD + 16 * (((lane >> 1) & 3) ^ (ub >> 1)) + 8 * (lane & 1)) * 2u;
    const unsigned so = (unsigned)(kh * 512 * HD + h * DH + (cg & 1) * 64) * 2u;
    auto stage = [&](int c) {
#pragma unroll
      for (int rho = 0; rho < 8; ++rho) dma16(rw, vo, so + (unsigned)((c * 64 + rho * 8) * HD) * 2u, lbase + R_W + w * 16384 + (c & 1) * 8192 + rho * 1024);
    };
    stage(0); stage(1);
#pragma unroll
    for (int c = 0; c < 6; ++c) {
      WAIT_V(8);
#pragma unroll
      for (int t = 0; t < 4; ++t) W[4 * c + t] = w_frag(lds + R_W + w * 16384 + (c & 1) * 8192 + (lo ^ (t << 5)));
      WAIT_L0(); SCHED();
      stage(c + 2);
    }
    WAIT_V(0);
    make_resident(lds + R_W + w * 16384, lo, l16);
  }
  WST(rs, 160 + (h >> 1), 4);
  unsigned cap = SPIN_CAP;
  wait_ge(rs, (l16 & 16) * 256, F_ST + (unsigned)(P_PJ + h) * 128, 1u, cap);
  dma16x(rs, l16, WS_K + h * 8192 + w * 1024, lbase + R_KT + w * 1024);
  Sched tt; tt.tau = rt32() - 100000; tt.prev = tt.tau; tt.ea = 195 * 8; tt.eb = 118 * 8; tt.d1 = 0; tt.moved = true;
  const int cu_ = h; (void)cu_;
  ST_DECL
  for (int s = 0; s < NSEG; ++s) {
    LAS unsigned char* lp = lad(l16, lbase + R_W + w * 16384);
    bf16x16 L[2];
    L[0] = l_frag(lp, 0); L[1] = l_frag(lp, 1);
    unsigned lz = l16;
    asm volatile("" : "+v"(lz));
    LAS unsigned char* mp = lad(mem_off((int)(lz >> 4)), lbase + R_MEM + kh * 8192);
    SCHED();
    if (w == 7) wait_until(tt.tau);
    BAR();
    ST0();
    gather_mem(rs, lbase, s, (w + h) & 7, l16, cap);
    ST(1);
    LBAR();
    ST(2);
    f32x4 c[4];
#pragma unroll
    for (int t = 0; t < 4; ++t) c[t] = f32x4{0.f, 0.f, 0.f, 0.f};
    bf16x8 al, af[2];
    u32x4 vt0, vt1;
    u32x4 vmv;
    const unsigned vso = X_VM + (unsigned)s * RSTR + h * 4096 + cg * 1024;
    al = mem_frag(mp, 6); af[0] = mem_frag(mp, 0); af[1] = mem_frag(mp, 1);
#pragma unroll
    for (int i = 0; i < 8; ++i) {
      c[i & 3] = smf(al, L[i & 1], c[i & 3], sidx);
      SCHED();
      if (i == 3) al = mem_frag(mp, 7);
      if (i < 6) L[i & 1] = l_frag(lp, i + 2);
      if (i == 6) {
        const unsigned vo = WS_VT + (unsigned)(s * NH + h) * 8192 + cg * 2048;
        asm volatile("s_cmp_ge_u32 %5, 4\n\ts_cbranch_scc1 1f\n\t"
                     "buffer_load_dwordx4 %0, %2, %3, %4 offen sc1\n\tbuffer_load_dwordx4 %1, %2, %3, %4 offen offset:1024 sc1\n\t1:"
                     : "=&v"(vt0), "=&v"(vt1) : "v"(l16), "s"(rs), "s"(vo), "s"(w) : "scc", "memory");
      }
      SCHED();
#pragma unroll
      for (int r = 3 * i; r < 3 * i + 3; ++r) {
        c[(r + 1) & 3] = smf(af[(r >> 2) & 1], W[(r & ~3) + ((r + 1) & 3)], c[(r + 1) & 3], sidx);
        SCHED();
        if ((r & 3) == 3 && (r >> 2) + 2 < 6) af[(r >> 2) & 1] = mem_frag(mp, (r >> 2) + 2);
        SCHED();
      }
    }
    {
      LAS unsigned char* qw = lad(half_of(l16), lbase + R_QK + cg * 2048 + kh * 512);
      const f32x4 f0 = fold(c[0], c[1]);
      *(LAS u32x2*)(qw) = u32x2{pack2(f0[0], f0[1]), pack2(f0[2], f0[3])};
      const f32x4 f1 = fold(c[2], c[3]);
      *(LAS u32x2*)(qw + 1024) = u32x2{pack2(f1[0], f1[1]), pack2(f1[2], f1[3])};

    }
    ST(3);
    if (w < 4) {
      bf16x8 k1[4], k2[4];
      LAS unsigned char* kb = lad(l16, lbase + R_KT);
#pragma unroll
      for (int ks = 0; ks < 2; ++ks) { k1[ks] = *(const LAS bf16x8*)(kb + ks * 1024); k2[ks] = *(const LAS bf16x8*)(kb + 4096 + ks * 1024); }
      LBAR();
      ST(4);
      issue2(rs, l16, vso, w, vmv);
      f32x4 t0 = {0.f, 0.f, 0.f, 0.f}, t1 = t0, t2 = t0;
      LAS unsigned char* qb = lad(qoff, lbase);
      bf16x8 qf[4], kf[4];
#pragma unroll
      for (int ks = 0; ks < 2; ++ks) {
        qf[ks] = cat8(trread(qb + ks * 1024), trread(qb + ks * 1024 + 32));
        kf[ks] = cat8(trread(qb + 4096 + ks * 1024), trread(qb + 4096 + ks * 1024 + 32));
      }
      SCHED();
#pragma unroll
      for (int ks = 0; ks < 4; ++ks) {
        t1 = mfma32(k1[ks], qf[ks], t1); t2 = mfma32(k2[ks], qf[ks], t2); t0 = mfma32(kf[ks], qf[ks], t0);
        SCHED();
        if (ks < 2) {
          qf[ks + 2] = cat8(trread(qb + (ks + 2) * 1024), trread(qb + (ks + 2) * 1024 + 32));
          k1[ks + 2] = *(const LAS bf16x8*)(kb + (ks + 2) * 1024); k2[ks + 2] = *(const LAS bf16x8*)(kb + 4096 + (ks + 2) * 1024);
          kf[ks + 2] = cat8(trread(qb + 4096 + (ks + 2) * 1024), trread(qb + 4096 + (ks + 2) * 1024 + 32));
        }
        SCHED();
      }
      ST(5);
      {
        const float sl = 0.08838834764831845f * 1.4426950408889634f;
        float z[4] = {t1[0], t1[1], t1[2], t1[3]}, y = t0[0], mx;
        {
          float u1 = t0[1], u2 = t0[2], u3 = t0[3];
#define M8(Z, B) "v_add_f32_dpp " Z ", " Z ", " Z " row_ror:8 row_mask:0xf bank_mask:0x3\n\tv_add_f32_dpp " Z ", " B ", " B " row_ror:8 row_mask:0xf bank_mask:0xc\n\t"
          asm("s_nop 7\n\t"
              "v_permlane32_swap_b32 %4, %6\n\tv_permlane32_swap_b32 %5, %7\n\t"
              M8("%0", "%9") "v_add_f32 %4, %4, %6\n\tv_add_f32 %5, %5, %7\n\t" M8("%1", "%10") M8("%2", "%11") M8("%4", "%5") M8("%3", "%12")
              "v_max3_f32 %8, %0, %1, %2\n\tv_max3_f32 %8, %8, %4, %3\n\ts_nop 1\n\t"
              "v_max_f32_dpp %8, %8, %8 row_ror:8 row_mask:0xf bank_mask:0xf\n\tv_mov_b32 %5, %8\n\ts_nop 1\n\t"
              "v_permlane16_swap_b32 %8, %5\n\tv_max_f32 %8, %8, %5\n\tv_mov_b32 %5, %8\n\ts_nop 1\n\t"
              "v_permlane32_swap_b32 %8, %5\n\tv_max_f32 %8, %8, %5"
              : "+v"(z[0]), "+v"(z[1]), "+v"(z[2]), "+v"(z[3]), "+v"(y), "+v"(u1), "+v"(u2), "+v"(u3), "=&v"(mx) : "v"(t2[0]), "v"(t2[1]), "v"(t2[2]), "v"(t2[3]));
#undef M8
        }
        const float nm = -mx * sl;
#pragma unroll
        for (int r = 0; r < 4; ++r) z[r] = __builtin_amdgcn_exp2f(fmaf(z[r], sl, nm));
        const unsigned p0 = pack2(z[0], z[1]), p1 = pack2(z[2], z[3]);
        const u32x4 pt = {p0, p1, ror8(p0), ror8(p1)};
        SCHED();
        asm volatile("s_waitcnt vmcnt(2)" : "+v"(vt0), "+v"(vt1) :: "memory");
        f32x4 o0 = {0.f, 0.f, 0.f, 0.f}, o1 = o0;
        o0 = mfma32(__builtin_bit_cast(bf16x8, vt0), __builtin_bit_cast(bf16x8, pt), o0);
        o1 = mfma32(__builtin_bit_cast(bf16x8, vt1), __builtin_bit_cast(bf16x8, pt), o1);
        y = __builtin_amdgcn_exp2f(fmaf(y, sl, nm));
        const unsigned pm1 = pack2(y, u2f(ror8(f2u(y))));
        const auto pms = __builtin_amdgcn_permlane32_swap(pm1, pm1, false, false);
        const u32x2 pm = {pms[0], pms[1]};
        float inv = __builtin_amdgcn_rcpf(gsum(dpp_add<0x128>(((z[0] + z[1]) + (z[2] + z[3])) + y)));
        o0 *= inv; o1 *= inv;
        unsigned ov;
        unsigned ot;
        asm volatile("v_and_b32 %0, 0x30, %5\n\tv_lshrrev_b32 %1, 2, %5\n\tv_and_b32 %1, 0xC0, %1\n\tv_or_b32 %0, %0, %1\n\t"
                     "v_and_b32 %1, 0x40, %5\n\tv_lshl_or_b32 %0, %1, 9, %0\n\tv_and_b32 %1, 0x80, %5\n\tv_lshl_or_b32 %0, %1, 24, %0"
                     : "=&v"(ov), "=&v"(ot), "+v"(o0), "+v"(o1), "+v"(inv) : "v"(l16));
        ST(6);
        arrive2(rs, l16, vso, vmv, cap);
        ST(7);
        const f32x4 n0 = mfma16n(u32x2{vmv[0], vmv[1]}, pm, f32x4{0.f, 0.f, 0.f, 0.f}), n1 = mfma16n(u32x2{vmv[2], vmv[3]}, pm, f32x4{0.f, 0.f, 0.f, 0.f});
        {
          const u32x4 v = {nz(pack2(fmaf(n0[0], inv, o0[0]), fmaf(n0[1], inv, o0[1]))), nz(pack2(fmaf(n0[2], inv, o0[2]), fmaf(n0[3], inv, o0[3]))),
                           nz(pack2(fmaf(n1[0], inv, o1[0]), fmaf(n1[1], inv, o1[1]))), nz(pack2(fmaf(n1[2], inv, o1[2]), fmaf(n1[3], inv, o1[3])))};
          stx16(rs, ov, X_OX + s * RSTR + h * 1024 + w * 256, v);
        }
        if (w == 3) { const unsigned t = max(rt32(), 1u); if (l16 == 0) stx4(rs, 0, X_TS + (unsigned)(s + 1) * RSTR + (unsigned)(64 + h) * 128, t); }
        ST(8);
      }
    } else {
      LBAR();
      LAS unsigned char* qb = lad(qoff, lbase + 4096 + (w - 4) * 1024);
      u32x4 kf = __builtin_bit_cast(u32x4, cat8(trread(qb), trread(qb + 32)));
#pragma unroll
      for (int i = 0; i < 4; ++i) kf[i] = pack2(dpp_add<0x128>(lo2f(kf[i])), dpp_add<0x128>(hi2f(kf[i])));
      stx16(rs, l16, WS_KM + (unsigned)(s * NH + h) * 4096 + (w - 4) * 1024, kf);
      WAIT_V(0);
    }
    LBAR();
    if (w == 4 && l16 == 0) stx4(rs, 0, F_CH + h * 128, (unsigned)s + 1u);
    ST(9);
    if (w == 7) { retime(rs, l16, s, tt, p, cap); advance(tt, p); }
    if (s + 1 < NSEG) {
      unsigned fo;
      asm volatile("v_and_b32 %0, 16, %1\n\tv_lshlrev_b32 %0, 8, %0" : "=v"(fo) : "v"(l16));
      wait_ge(rs, fo, F_ST + (unsigned)(P_PJ + ((s + 1) & 1) * 64 + h) * 128, (unsigned)s + 2u, cap);
      dma16x(rs, l16, WS_K + (unsigned)((s + 1) * NH + h) * 8192 + w * 1024, lbase + R_KT + w * 1024);
    }
  }
  ST_FLUSH();
  if (cap == 0u && l16 == 0) stx4(rs, 0, X_CTL, 1u);
}

__device__ __forceinline__ void role_vb(const MainP& p, LAS unsigned char* lds, int rg, int cb) {
  const int tid = threadIdx.x, lane = tid & 63, w = __builtin_amdgcn_readfirstlane(tid >> 6);
  const int kh = w >> 2, wr = (w + cb) & 7;
  const __amdgpu_buffer_rsrc_t rs = mk_rs(p.ws, WS_END);
  const unsigned lbase = (unsigned)(size_t)lds;
  unsigned l16 = lane * 16, l8 = lane * 8, moff = mem_off(lane), roff, soff = (unsigned)((lane >> 5) * 256 + (lane & 31) * 4);
  const int sidx = (lane & 8) ? 0xEEEE : 0x4444;
  bf16x16 WV[8], WB[16];
  {
    const int fr = lane & 15, g = lane >> 4, tu = 4 * (g & 1) + (fr >> 2), ub = lane >> 3;
    { const int sl = mem_slot(16 * (g >> 1) + fr); roff = R_RED + w * 512 + ((sl >> 5) * 32 + (sl & 15) * 2 + (g & 1)) * 8; }
    if (w == 0) {
      const int sl = (lane >> 1) & 15, col = cb * 32 + ((sl >> 2) & 1) * 16 + (sl >> 3) * 8 + (lane >> 5) * 4 + (sl & 1) * 2 + ((sl >> 1) & 1), r = rg * 4 + (lane & 1) * 2;
      stx4(rs, soff, X_MEM + (cb >> 1) * 1024 + rg * 512 + ((cb ^ rg) & 1) * 128, nz((unsigned)p.mem0[r * D_ + col] | ((unsigned)p.mem0[(r + 1) * D_ + col] << 16)));
    }
    const __amdgpu_buffer_rsrc_t rv = mk_rs(p.Wv, D_ * HD * 2), ro = mk_rs(p.Wo, D_ * HD * 2);
    const unsigned vv = (unsigned)(ub * HD + 16 * (((lane >> 1) & 3) ^ (ub >> 1)) + 8 * (lane & 1)) * 2u, sv = (unsigned)(8 * w * HD + cb * DH + rg * 64) * 2u;
#pragma unroll
    for (int c = 0; c < 16; ++c) dma16(rv, vv, sv + (unsigned)c * 64 * HD * 2, lbase + R_W + c * 8192 + w * 1024);
    WAIT_V(0); BAR();
    const unsigned lo = (g >> 1) * 1024 + tu * 128 + (((tu >> 1) ^ (w & 3)) << 5) + (fr & 3) * 8;
#pragma unroll
    for (int c = 0; c < 8; ++c) WV[c] = w_frag(lds + R_W + (kh * 8 + c) * 8192 + lo);
    LBAR();
    const int u = (lane >> 2) & 7;
    const unsigned vw = (unsigned)((16 * (u >> 2) + 8 * ((u >> 1) & 1) + 4 * (lane >> 5) + (u & 1)) * D_ + (lane & 3) * 8) * 2u;
    const unsigned sw = (unsigned)(wr * 512 * D_ + cb * 32) * 2u;
    const unsigned bo = (g >> 1) * 1024 + tu * 64 + (fr & 3) * 8;
    auto stageo = [&](int hd) {
#pragma unroll
      for (int q = 0; q < 8; ++q)
        dma16(ro, vw, sw + (unsigned)((hd * 128 + 64 * (q >> 2) + 32 * (q & 1) + 2 * ((q >> 1) & 1)) * D_) * 2u, lbase + R_W + w * 16384 + (hd & 1) * 8192 + q * 1024);
    };
    stageo(0); stageo(1);
#pragma unroll
    for (int hd = 0; hd < 4; ++hd) {
      if (hd < 3) wait_v<8>(); else wait_v<0>();
#pragma unroll
      for (int f = 0; f < 4; ++f) WB[hd * 4 + f] = w_frag(lds + R_W + w * 16384 + (hd & 1) * 8192 + (f >> 1) * 512 + (f & 1) * 32 + bo);
      WAIT_L0(); SCHED();
      if (hd < 2) stageo(hd + 2);
    }
  }
  WST(rs, 176 + rg * 8 + (cb >> 2), 4);
  asm volatile("" : "+v"(l16), "+v"(l8), "+v"(moff), "+v"(roff), "+v"(soff));
  unsigned cap = SPIN_CAP;
  Sched tt; tt.tau = rt32() - 100000; tt.prev = tt.tau; tt.ea = 195 * 8; tt.eb = 118 * 8; tt.d1 = 0; tt.moved = true;
  const int cu_ = 32 + (rg * 32 + cb) / 2; (void)cu_;
  ST_DECL
  for (int s = 0; s < NSEG; ++s) {
    if (w == 7) wait_until(tt.tau);
    BAR();
    ST0();
    gather_mem(rs, lbase, s, wr, l16, cap);
    ST(1);
    LBAR();
    ST(2);
    {
      f32x4 c0 = {0.f, 0.f, 0.f, 0.f};
      LAS unsigned char* mp = lad(moff, lbase + R_MEM + kh * 8192);
      bf16x8 af[8];
#pragma unroll
      for (int c = 0; c < 8; ++c) af[c] = mem_frag(mp, c);
      SCHED();
#pragma unroll
      for (int c = 0; c < 8; ++c) c0 = smf(af[c], WV[c], c0, sidx);
      const f32x4 f = fold(c0, c0);
      if (l16 < 512) stx8(rs, l8, X_VM + (unsigned)s * RSTR + cb * 4096 + (rg * 4 + (w & 3)) * 512 + kh * 256, u32x2{nz(pack2(f[0], f[1])), nz(pack2(f[2], f[3]))});
    }
    ST(3);
    if (w < 4 && s + 2 <= NSEG && (rg * 32 + cb) * 4 + w < 220) {
      u32x4 z; asm volatile("v_mov_b32 %0, 0" : "=v"(z[0])); z[1] = z[2] = z[3] = z[0];
      stx16(rs, l16, X_REC + (unsigned)(s + 2) * RSTR + (unsigned)((rg * 32 + cb) * 4 + w) * 1024, z);
    }
    if (w == 7) retime(rs, l16, s, tt, p, cap);
    if (s + 1 < NSEG) {
      u32x4 a[4];
      const unsigned so = X_OX + s * RSTR + rg * 32768 + wr * 4096;
      if (w == 7) wait_until(tt.tau + tt.d1);
      BAR();
      ST(4);
      issue4(rs, l16, so, so + 1024, so + 2048, so + 3072, a[0], a[1], a[2], a[3]);
      f32x4 c[4];
#pragma unroll
      for (int t = 0; t < 4; ++t) c[t] = f32x4{0.f, 0.f, 0.f, 0.f};
#pragma unroll
      for (int i = 0; i < 4; ++i) {
        if (i == 0) arrive<0>(rs, l16, so, a[0], cap); else if (i == 1) arrive<1>(rs, l16, so + 1024, a[1], cap);
        else if (i == 2) arrive<2>(rs, l16, so + 2048, a[2], cap); else arrive<3>(rs, l16, so + 3072, a[3], cap);
#pragma unroll
        for (int t = 0; t < 4; ++t) c[t] = smf(__builtin_bit_cast(bf16x8, a[i]), WB[i * 4 + t], c[t], sidx);
        SCHED();
      }
      ST(5);
      {
        const bool odd = (l16 & 256) != 0;
        const f32x4 f = fold(odd ? c[2] : c[0], odd ? c[3] : c[1]);
        const auto e0 = __builtin_amdgcn_permlane16_swap(f2u(f[0]), f2u(f[2]), false, false), e1 = __builtin_amdgcn_permlane16_swap(f2u(f[1]), f2u(f[3]), false, false);
        *(LAS f32x2*)(lad(roff, lbase)) = f32x2{u2f(e0[0]) + u2f(e0[1]), u2f(e1[0]) + u2f(e1[1])};
      }
      ST(6);
      LBAR();
      ST(7);
      if (w == 0) {
        LAS unsigned char* rp = lad(l8, lbase + R_RED);
        f32x2 v[8];
#pragma unroll
        for (int i = 0; i < 8; ++i) v[i] = *(const LAS f32x2*)(rp + i * 512);
        const f32x2 sum = ((v[0] + v[1]) + (v[2] + v[3])) + ((v[4] + v[5]) + (v[6] + v[7]));
        stx4(rs, soff, X_MEM + (s + 1) * RSTR + (cb >> 1) * 1024 + rg * 512 + ((cb ^ rg) & 1) * 128, nz(pack2(sum[0], sum[1])));
        const unsigned t = max(rt32(), 1u);
        if (l16 == 0) stx4(rs, 0, X_TS + (unsigned)(s + 1) * RSTR + (unsigned)(rg * 32 + cb) * 128, t);
      }
      ST(8);
    }
    advance(tt, p);
  }
  ST_FLUSH();
  if (cap == 0u && l16 == 0) stx4(rs, 0, X_CTL, 1u);
}

#if defined(__HIP_DEVICE_COMPILE__)
__attribute__((target("no-packed-fp32-ops")))
#endif
__global__ void __launch_bounds__(512, 2) k_main(MainP p) {
  extern __shared__ __attribute__((aligned(16))) unsigned char smem[];
  const int b = blockIdx.x, x = b & 7, j = b >> 3;
  if (x >= 3) worker(p, (LAS unsigned char*)smem, x - 3, j);
  else if (x == 0) role_a(p, (LAS unsigned char*)smem, j);
  else role_vb(p, (LAS unsigned char*)smem, x - 1, j);
}

extern "C" void kernel_launch(void* const* d_in, const int* in_sizes, int n_in, void* d_out, int out_size, void* d_ws, size_t ws_size,
                              hipStream_t stream) {
  unsigned char* ws = (unsigned char*)d_ws;
  (void)hipMemsetAsync(ws, 0, X_ZERO, stream);
  MainP p;
  p.x = (const u16*)d_in[0]; p.mem0 = (const u16*)d_in[1]; p.c = (const u16*)d_in[12]; p.wmod = (const u16*)d_in[13]; p.Wq = (const u16*)d_in[2]; p.Wk = (const u16*)d_in[3]; p.Wv = (const u16*)d_in[4]; p.Wo = (const u16*)d_in[5];
  p.w1 = (const u16*)d_in[8]; p.w2 = (const u16*)d_in[9]; p.w3 = (const u16*)d_in[10]; p.w4 = (const u16*)d_in[11];
  p.out = (u16*)d_out; p.ws = ws; p.d1 = 10; p.d2 = 10; p.d3 = 2; p.d4 = 100;
  (void)hipFuncSetAttribute((const void*)k_main, hipFuncAttributeMaxDynamicSharedMemorySize, R_LDS);
  k_main<<<256, 512, R_LDS, stream>>>(p);
}
```

```cpp
#include <hip/hip_runtime.h>
#include <hip/hip_bf16.h>
#include <cstdlib>
#pragma clang diagnostic ignored "-Winline-asm"

#define LAS __attribute__((address_space(3)))
#define GAS __attribute__((address_space(1)))
typedef unsigned short u16;
typedef __bf16 bf16x2 __attribute__((ext_vector_type(2)));
typedef __bf16 bf16x4 __attribute__((ext_vector_type(4)));
typedef __bf16 bf16x8 __attribute__((ext_vector_type(8)));
typedef __bf16 bf16x16 __attribute__((ext_vector_type(16)));
typedef short s16x4 __attribute__((ext_vector_type(4)));
typedef float f32x2 __attribute__((ext_vector_type(2)));
typedef float f32x4 __attribute__((ext_vector_type(4)));
typedef float f32x16 __attribute__((ext_vector_type(16)));
typedef unsigned u32x2 __attribute__((ext_vector_type(2)));
typedef unsigned u32x4 __attribute__((ext_vector_type(4)));

constexpr int S_ = 2048, D_ = 1024, NH = 32, DH = 128, HD = 4096, NSEG = 64, FF = 3584;

constexpr unsigned MB = 1u << 20;
constexpr unsigned F_LN = 0;
constexpr unsigned F_ST = 24576;
constexpr unsigned F_CH = F_ST + 81920;
constexpr unsigned X_CTL = F_CH + 4096;
constexpr unsigned X_MOD = X_CTL + 4096;
constexpr unsigned X_REC = X_MOD + 24576;
constexpr unsigned RSTR = 225280;
constexpr int ZA = 6;
constexpr unsigned X_MEM = X_REC;
constexpr unsigned X_OX = X_REC + 16384;
constexpr unsigned X_VM = X_REC + 81920;
constexpr unsigned X_TS = X_REC + 212992;
constexpr unsigned X_ZERO = X_REC + ZA * RSTR;
constexpr unsigned WS_H = 16 * MB;
constexpr unsigned WS_Q = 20 * MB;
constexpr unsigned WS_K = 36 * MB;
constexpr unsigned WS_VT = 52 * MB;
constexpr unsigned WS_O = 68 * MB;
constexpr unsigned WS_X1 = 84 * MB;
constexpr unsigned WS_H2 = 88 * MB;
constexpr unsigned WS_G = 92 * MB;
constexpr unsigned WS_H1S = 96 * MB;
constexpr unsigned WS_X2 = 101 * MB;
constexpr unsigned WS_ST5 = 106 * MB;
constexpr unsigned WS_U = 108 * MB;
constexpr unsigned WS_KM = 124 * MB;
constexpr unsigned WS_P2 = 132 * MB;
constexpr unsigned WS_P8 = 164 * MB;
constexpr unsigned WS_END = 198 * MB;

__device__ __forceinline__ float bf2f(unsigned short b) { return __builtin_bit_cast(float, (unsigned)b << 16); }
__device__ __forceinline__ unsigned f2u(float f) { return __builtin_bit_cast(unsigned, f); }
__device__ __forceinline__ float u2f(unsigned u) { return __builtin_bit_cast(float, u); }
__device__ __forceinline__ float lo2f(unsigned w) { return __builtin_bit_cast(float, w << 16); }
__device__ __forceinline__ float hi2f(unsigned w) { return __builtin_bit_cast(float, w & 0xFFFF0000u); }
__device__ __forceinline__ unsigned pack2(float a, float b) { bf16x2 v; v[0] = (__bf16)a; v[1] = (__bf16)b; return __builtin_bit_cast(unsigned, v); }
__device__ __forceinline__ float rbf(float a) { return (float)(__bf16)a; }
__device__ __forceinline__ unsigned nz(unsigned w) { return max(w, 1u); }
__device__ __forceinline__ float gelu_tanh(float x) {
  const float e = __builtin_amdgcn_exp2f(x * fmaf(x * x, -0.1029432397f, -2.302208198f));
  return x * __builtin_amdgcn_rcpf(1.0f + e);
}
template <int CTRL> __device__ __forceinline__ float dpp_add(float v) { return v + u2f(__builtin_amdgcn_update_dpp(0u, f2u(v), CTRL, 0xF, 0xF, true)); }
__device__ __forceinline__ float wave_sum(float v) {
  v = dpp_add<0xB1>(v); v = dpp_add<0x4E>(v); v = dpp_add<0x141>(v); v = dpp_add<0x140>(v);
  auto t = __builtin_amdgcn_permlane16_swap(f2u(v), f2u(v), false, false);
  v = u2f(t[0]) + u2f(t[1]);
  t = __builtin_amdgcn_permlane32_swap(f2u(v), f2u(v), false, false);
  return u2f(t[0]) + u2f(t[1]);
}
#define WAIT_V(n) asm volatile("s_waitcnt vmcnt(" #n ")" ::: "memory")
#define WAIT_L0() asm volatile("s_waitcnt lgkmcnt(0)" ::: "memory")
#define BAR() do { asm volatile("" ::: "memory"); __builtin_amdgcn_s_barrier(); asm volatile("" ::: "memory"); } while (0)
#define LBAR() do { WAIT_L0(); BAR(); } while (0)
template <int N> __device__ __forceinline__ void wait_v() { asm volatile("s_waitcnt vmcnt(%0)" :: "n"(N) : "memory"); }
__device__ __forceinline__ void dma16(__amdgpu_buffer_rsrc_t rs, unsigned voff, unsigned soff, unsigned ldsa) {
  asm volatile("s_mov_b32 m0, %0\n\ts_nop 3\n\tbuffer_load_dwordx4 %1, %2, %3 offen lds" :: "s"(ldsa), "v"(voff), "s"(rs), "s"(soff) : "m0", "memory");
}
__device__ __forceinline__ void dma16x(__amdgpu_buffer_rsrc_t rs, unsigned voff, unsigned soff, unsigned ldsa) {
  asm volatile("s_mov_b32 m0, %0\n\ts_nop 3\n\tbuffer_load_dwordx4 %1, %2, %3 offen sc1 lds" :: "s"(ldsa), "v"(voff), "s"(rs), "s"(soff) : "m0", "memory");
}
__device__ __forceinline__ u32x4 ldx16(__amdgpu_buffer_rsrc_t rs, unsigned voff, unsigned soff) { return __builtin_amdgcn_raw_buffer_load_b128(rs, voff, soff, 16); }
__device__ __forceinline__ u32x2 ldx8(__amdgpu_buffer_rsrc_t rs, unsigned voff, unsigned soff) { return __builtin_amdgcn_raw_buffer_load_b64(rs, voff, soff, 16); }
__device__ __forceinline__ unsigned ldx4(__amdgpu_buffer_rsrc_t rs, unsigned voff, unsigned soff) { return __builtin_amdgcn_raw_buffer_load_b32(rs, voff, soff, 16); }
__device__ __forceinline__ void stx16(__amdgpu_buffer_rsrc_t rs, unsigned voff, unsigned soff, u32x4 v) {
  __builtin_amdgcn_raw_buffer_store_b128(v, rs, voff, soff, 16);
  asm volatile("s_nop 1" :: "v"(v) : "memory");
}
__device__ __forceinline__ void stx8(__amdgpu_buffer_rsrc_t rs, unsigned voff, unsigned soff, u32x2 v) { __builtin_amdgcn_raw_buffer_store_b64(v, rs, voff, soff, 16); }
__device__ __forceinline__ void stx4(__amdgpu_buffer_rsrc_t rs, unsigned voff, unsigned soff, unsigned v) { __builtin_amdgcn_raw_buffer_store_b32(v, rs, voff, soff, 16); }
__device__ __forceinline__ void wait_ge(__amdgpu_buffer_rsrc_t rs, unsigned voff, unsigned soff, unsigned need, unsigned& cap) {
  unsigned t;
  asm volatile("s_nop 4\n\t"
               "1:\n\t"
               "buffer_load_dword %[t], %[vo], %[rs], %[so] offen sc1\n\t"
               "s_waitcnt vmcnt(0)\n\t"
               "v_cmp_gt_u32 vcc, %[need], %[t]\n\t"
               "s_cmp_eq_u64 vcc, 0\n\t"
               "s_cbranch_scc1 2f\n\t"
               "s_sleep 2\n\t"
               "s_sub_u32 %[cap], %[cap], 1\n\t"
               "s_cbranch_scc0 1b\n\t"
               "s_mov_b32 %[cap], 0\n\t"
               "2:"
               : [t] "=&v"(t), [cap] "+s"(cap) : [vo] "v"(voff), [rs] "s"(rs), [so] "s"(soff), [need] "s"(need) : "vcc", "scc", "memory");
}
__device__ __forceinline__ void wait_seen(__amdgpu_buffer_rsrc_t rs, unsigned voff, unsigned soff, unsigned need, unsigned& seen, unsigned& cap) {
  if (seen >= need) return;
  unsigned t;
  asm volatile("s_nop 4\n\t"
               "1:\n\t"
               "buffer_load_dword %[t], %[vo], %[rs], %[so] offen sc1\n\t"
               "s_waitcnt vmcnt(0)\n\t"
               "v_cmp_gt_u32 vcc, %[need], %[t]\n\t"
               "s_cbranch_vccz 2f\n\t"
               "s_sleep 1\n\t"
               "s_sub_u32 %[cap], %[cap], 1\n\t"
               "s_cbranch_scc0 1b\n\t"
               "s_mov_b32 %[cap], 0\n\t"
               "2:"
               : [t] "=&v"(t), [cap] "+s"(cap) : [vo] "v"(voff), [rs] "s"(rs), [so] "s"(soff), [need] "s"(need) : "vcc", "scc", "memory");
  seen = min((unsigned)__builtin_amdgcn_readlane(t, 0), (unsigned)__builtin_amdgcn_readlane(t, 1));
}
__device__ __forceinline__ __amdgpu_buffer_rsrc_t mk_rs(const void* p, unsigned bytes) { return __builtin_amdgcn_make_buffer_rsrc((void*)p, 0, (int)bytes, 0x00020000); }
#define SCHED() __builtin_amdgcn_sched_barrier(0)
__device__ __forceinline__ unsigned now32() { unsigned long long t; asm volatile("s_memtime %0\n\ts_waitcnt lgkmcnt(0)" : "=s"(t) :: "memory"); return (unsigned)t; }
__device__ __forceinline__ unsigned rt32() { unsigned long long t; asm volatile("s_memrealtime %0\n\ts_waitcnt lgkmcnt(0)" : "=s"(t) :: "memory"); return (unsigned)t; }
__device__ __forceinline__ void wait_until(unsigned t) { while ((int)(rt32() - t) < 0) {} }
template <int X = 0> __device__ __forceinline__ LAS unsigned char* lad(unsigned v, unsigned u) {
  unsigned a;
  if (X == 0) asm volatile("v_add_u32 %0, %1, %2" : "=v"(a) : "s"(u), "v"(v));
  else asm volatile("v_xor_b32 %0, %3, %2\n\tv_add_u32 %0, %1, %0" : "=&v"(a) : "s"(u), "v"(v), "n"(X));
  return (LAS unsigned char*)(size_t)a;
}
__device__ __forceinline__ bf16x4 trread(LAS unsigned char* p) { return __builtin_amdgcn_ds_read_tr16_b64_v4bf16((LAS bf16x4*)p); }
__device__ __forceinline__ bf16x8 cat8(bf16x4 a, bf16x4 b) { return __builtin_shufflevector(a, b, 0, 1, 2, 3, 4, 5, 6, 7); }
__device__ __forceinline__ bf16x16 cat16(bf16x8 a, bf16x8 b) { return __builtin_shufflevector(a, b, 0, 1, 2, 3, 4, 5, 6, 7, 8, 9, 10, 11, 12, 13, 14, 15); }
__device__ __forceinline__ f32x4 mfma32(bf16x8 a, bf16x8 b, f32x4 c) { return __builtin_amdgcn_mfma_f32_16x16x32_bf16(a, b, c, 0, 0, 0); }
__device__ __forceinline__ f32x4 mfma16(u32x2 a, u32x2 b, f32x4 c) {
  return mfma32(__builtin_bit_cast(bf16x8, u32x4{a[0], a[1], 0u, 0u}), __builtin_bit_cast(bf16x8, u32x4{b[0], b[1], 0u, 0u}), c);
}
__device__ __forceinline__ f32x4 mfma16n(u32x2 a, u32x2 b, f32x4 c) { return __builtin_amdgcn_mfma_f32_16x16x16bf16_1k(__builtin_bit_cast(s16x4, a), __builtin_bit_cast(s16x4, b), c, 0, 0, 0); }
__device__ __forceinline__ f32x4 smf(bf16x8 a, bf16x16 b, f32x4 c, int idx) { return __builtin_amdgcn_smfmac_f32_16x16x64_bf16(a, b, c, idx, 0, 0); }

__device__ __forceinline__ void ln_row(float (&x)[16]) {
  float s = 0.f;
#pragma unroll
  for (int i = 0; i < 16; ++i) s += x[i];
  const float mean = wave_sum(s) * (1.0f / 1024.0f);
  float q = 0.f;
#pragma unroll
  for (int i = 0; i < 16; ++i) { const float d = x[i] - mean; q += d * d; }
  const float var = wave_sum(q) * (1.0f / 1024.0f);
  const float m = rbf(mean), isd = __builtin_amdgcn_rcpf(rbf(__builtin_amdgcn_sqrtf(rbf(rbf(var) + 1e-5f))));
#pragma unroll
  for (int i = 0; i < 16; ++i) x[i] = rbf(rbf(x[i] - m) * isd);
}
__device__ __forceinline__ void load_row16(const u16* rowp, int lane, float (&x)[16]) {
  const u32x4 a = *(const u32x4*)(rowp + lane * 8), b = *(const u32x4*)(rowp + 512 + lane * 8);
#pragma unroll
  for (int i = 0; i < 4; ++i) { x[2 * i] = lo2f(a[i]); x[2 * i + 1] = hi2f(a[i]); x[8 + 2 * i] = lo2f(b[i]); x[9 + 2 * i] = hi2f(b[i]); }
}
__device__ __forceinline__ void store_row16(u16* rowp, int lane, const float (&x)[16]) {
  u32x4 a, b;
#pragma unroll
  for (int i = 0; i < 4; ++i) { a[i] = pack2(x[2 * i], x[2 * i + 1]); b[i] = pack2(x[8 + 2 * i], x[9 + 2 * i]); }
  *(u32x4*)(rowp + lane * 8) = a; *(u32x4*)(rowp + 512 + lane * 8) = b;
}
__host__ __device__ __forceinline__ int mem_slot(int dl) {
  return ((dl >> 2) & 1) * 32 + (((dl >> 5) & 1) * 2 + ((dl >> 3) & 1)) * 8 + (((dl >> 4) & 1) * 2 + (dl & 1)) * 2 + ((dl >> 1) & 1);
}

#define WST(rs, wk, i)
struct MainP { const u16 *x, *mem0, *Wq, *Wk, *Wv, *Wo, *w1, *w2, *w3, *w4, *c, *wmod; u16* out; unsigned char* ws; int d1, d2, d3, d4; };
constexpr unsigned SPIN_CAP = 1u << 18;

__device__ __forceinline__ float gmax(float v) {
  auto t = __builtin_amdgcn_permlane16_swap(f2u(v), f2u(v), false, false);
  v = fmaxf(u2f(t[0]), u2f(t[1]));
  t = __builtin_amdgcn_permlane32_swap(f2u(v), f2u(v), false, false);
  return fmaxf(u2f(t[0]), u2f(t[1]));
}
__device__ __forceinline__ float gsum(float v) {
  auto t = __builtin_amdgcn_permlane16_swap(f2u(v), f2u(v), false, false);
  v = u2f(t[0]) + u2f(t[1]);
  t = __builtin_amdgcn_permlane32_swap(f2u(v), f2u(v), false, false);
  return u2f(t[0]) + u2f(t[1]);
}
__device__ __forceinline__ void publish(__amdgpu_buffer_rsrc_t rs, unsigned word, unsigned val) {
  WAIT_V(0); BAR();
  if (threadIdx.x == 0) stx4(rs, 0, word, val);
}

constexpr int P_S2 = 0, P_S8 = 32, P_S7 = 64, P_S4 = 96, P_S5 = 104, P_S1 = 112, P_S3 = 144, P_PJ = 240;
constexpr int NLN = 160;

enum { SG_WO = 0, SG_W1 = 1, SG_W2 = 2, SG_W3 = 3, SG_W4 = 4, SG_K = 5, SG_V = 6, SG_Q = 7 };
constexpr int G_XCH = 131072, G_K = G_XCH + 16384, G_CTL = G_K + 8192;
__device__ __forceinline__ f32x16 mfma3232(bf16x8 a, bf16x8 b, f32x16 c) { return __builtin_amdgcn_mfma_f32_32x32x16_bf16(a, b, c, 0, 0, 0); }
__device__ __forceinline__ void widen32(const f32x16& a, u32x4 (&v)[2]) {
#pragma unroll
  for (int u = 0; u < 2; ++u) {
    const auto r0 = __builtin_amdgcn_permlane32_swap(pack2(a[8 * u], a[8 * u + 1]), pack2(a[8 * u + 4], a[8 * u + 5]), false, false);
    const auto r1 = __builtin_amdgcn_permlane32_swap(pack2(a[8 * u + 2], a[8 * u + 3]), pack2(a[8 * u + 6], a[8 * u + 7]), false, false);
    v[u] = u32x4{r0[0], r1[0], r0[1], r1[1]};
  }
}
__device__ __forceinline__ float hsum(float v) { const auto t = __builtin_amdgcn_permlane32_swap(f2u(v), f2u(v), false, false); return u2f(t[0]) + u2f(t[1]); }

__device__ __forceinline__ void attend(__amdgpu_buffer_rsrc_t rs, LAS unsigned char* qb, LAS unsigned char* kb, int seg, int h, int t, int eh, int lane, const u32x4 (&vv)[4],
                                       unsigned& chs, unsigned& cap) {
  const int fr = lane & 15, g = lane >> 4;
  const unsigned l16 = lane * 16, ub = (unsigned)(seg * NH + h);
  f32x4 s0 = {0.f, 0.f, 0.f, 0.f}, s1 = s0, s2 = s0;
  bf16x8 q[4];
#pragma unroll
  for (int ks = 0; ks < 4; ++ks) {
    q[ks] = *(const LAS bf16x8*)(qb + ks * 4096);
    s1 = mfma32(*(const LAS bf16x8*)(kb + ks * 1024), q[ks], s1); s2 = mfma32(*(const LAS bf16x8*)(kb + 4096 + ks * 1024), q[ks], s2);
  }
  const float scale = 0.08838834764831845f, L2E = 1.4426950408889634f;
  float a[4], b[4], c[4], mt = -3.0e38f, lt = 0.f;
#pragma unroll
  for (int r = 0; r < 4; ++r) {
    const int j = 4 * g + r;
    b[r] = (t == 1 || j <= fr) ? rbf(rbf(s1[r]) * scale) : -3.0e38f;
    c[r] = (t == 1 && j <= fr) ? rbf(rbf(s2[r]) * scale) : -3.0e38f;
    mt = fmaxf(mt, fmaxf(b[r], c[r]));
  }
  mt = gmax(mt);
#pragma unroll
  for (int r = 0; r < 4; ++r) { b[r] = __builtin_amdgcn_exp2f((b[r] - mt) * L2E); c[r] = __builtin_amdgcn_exp2f((c[r] - mt) * L2E); lt += b[r] + c[r]; }
  const u32x4 pt = {pack2(b[0], b[1]), pack2(b[2], b[3]), pack2(c[0], c[1]), pack2(c[2], c[3])};
  f32x4 o[4];
#pragma unroll
  for (int i = 0; i < 4; ++i) o[i] = mfma32(__builtin_bit_cast(bf16x8, vv[i]), __builtin_bit_cast(bf16x8, pt), f32x4{0.f, 0.f, 0.f, 0.f});
  lt = gsum(lt);
  const unsigned em = (unsigned)(fr >> 3) * 512 + (unsigned)g * 128 + (unsigned)(8 * ((fr >> 2) & 1) + (fr & 3)) * 8;
  asm volatile("" : "+v"(o[0]), "+v"(o[1]), "+v"(o[2]), "+v"(o[3]), "+v"(lt));
  wait_seen(rs, 0, F_CH + h * 128, (unsigned)seg + 1u, chs, cap);
  u32x2 vmf[4];
  for (;;) {
    asm volatile("" ::: "memory");
    unsigned mn = 0xFFFFFFFFu;
    bf16x8 kmf[4];
#pragma unroll
    for (int ks = 0; ks < 4; ++ks) kmf[ks] = __builtin_bit_cast(bf16x8, ldx16(rs, l16, WS_KM + ub * 4096 + ks * 1024));
#pragma unroll
    for (int i = 0; i < 4; ++i) vmf[i] = ldx8(rs, em, X_VM + (unsigned)seg * RSTR + h * 4096 + (2 * eh + (i >> 1)) * 1024 + (i & 1) * 32);
    s0 = f32x4{0.f, 0.f, 0.f, 0.f};
#pragma unroll
    for (int ks = 0; ks < 4; ++ks) s0 = mfma32(kmf[ks], q[ks], s0);
#pragma unroll
    for (int i = 0; i < 4; ++i) mn = min(mn, min(vmf[i][0], vmf[i][1]));
    if (__all(mn != 0u) || cap == 0u) break;
    cap = __builtin_amdgcn_readfirstlane(cap - 1u);
  }
  cap = __builtin_amdgcn_readfirstlane(cap);
#pragma unroll
  for (int r = 0; r < 4; ++r) a[r] = rbf(rbf(s0[r]) * scale);
  const float mx = fmaxf(mt, gmax(fmaxf(fmaxf(a[0], a[1]), fmaxf(a[2], a[3]))));
#pragma unroll
  for (int r = 0; r < 4; ++r) a[r] = __builtin_amdgcn_exp2f((a[r] - mx) * L2E);
  const u32x2 pm = {pack2(a[0], a[1]), pack2(a[2], a[3])};
  f32x4 om[4];
#pragma unroll
  for (int i = 0; i < 4; ++i) om[i] = mfma16n(vmf[i], pm, f32x4{0.f, 0.f, 0.f, 0.f});
  const float al = __builtin_amdgcn_exp2f((mt - mx) * L2E);
  const float inv = __builtin_amdgcn_rcpf(fmaf(al, lt, 0.5f * gsum((a[0] + a[1]) + (a[2] + a[3])))), ai = al * inv;
#pragma unroll
  for (int i = 0; i < 4; ++i) o[i] = o[i] * ai + om[i] * inv;
#pragma unroll
  for (int u = 0; u < 2; ++u)
    stx16(rs, l16, WS_O + (unsigned)seg * 262144 + (unsigned)((h * 4 + 2 * eh + u) * 2 + t) * 1024,
          u32x4{pack2(o[2 * u][0], o[2 * u][1]), pack2(o[2 * u][2], o[2 * u][3]), pack2(o[2 * u + 1][0], o[2 * u + 1][1]), pack2(o[2 * u + 1][2], o[2 * u + 1][3])});
}

template <int SG>
__device__ __forceinline__ void stage_gemm(LAS unsigned char* lds, __amdgpu_buffer_rsrc_t rs, const u16* Wg, int wk, int kq, int cb, int pid, unsigned& cap,
                                           const u16* side = nullptr, const u16* side2 = nullptr, int sld = 0, const u16* side3 = nullptr) {
  constexpr bool KV = (SG == SG_K || SG == SG_V), PROJ = KV || SG == SG_Q, ROWM = (SG == SG_W1 || PROJ);
  constexpr int KS = (SG == SG_W4) ? 28 : 32;
  constexpr int LDW = PROJ ? HD : (SG == SG_W3) ? FF : D_;
  constexpr int WROWS = (SG == SG_WO) ? HD : (SG == SG_W4 ? FF : D_);
  constexpr int NBLK = KV ? 32 : (SG == SG_WO || SG == SG_W1 || SG == SG_Q) ? 64 : 65;
  constexpr unsigned ABASE = PROJ ? WS_H : SG == SG_WO ? WS_O : SG == SG_W1 ? WS_G : SG == SG_W2 ? WS_H1S : SG == SG_W3 ? WS_X2 : WS_U;
  constexpr unsigned ASTR = SG == SG_WO ? 262144u : SG == SG_W4 ? 229376u : 65536u;
  constexpr int NPC = KS / 4;
  const unsigned myword = F_ST + (unsigned)pid * 128u;
  int tid = threadIdx.x; asm volatile("" : "+v"(tid));
  const int lane = tid & 63, w = __builtin_amdgcn_readfirstlane(tid >> 6), row = lane & 31, hf = lane >> 5;
  const int kh = w >> 2, wl = w & 3;
  const int koff = PROJ ? 0 : kq;
  const unsigned lbase = (unsigned)(size_t)lds, l16 = lane * 16;
  const int col0 = cb * 128 + wl * 32;
  bf16x8 Wf[KS];
  {
    const __amdgpu_buffer_rsrc_t rsW = mk_rs(Wg, (unsigned)WROWS * LDW * 2u);
    const int ub = lane >> 3, tu = 4 * hf + ((lane & 15) >> 2);
    const unsigned vB = (unsigned)(((ub & 3) + 8 * (ub >> 2)) * LDW + 16 * (((lane >> 1) & 3) ^ (ub >> 1)) + 8 * (lane & 1)) * 2u;
    const unsigned sB = (unsigned)((koff * KS * 32 + 32 * (w >> 2) + 16 * ((w >> 1) & 1) + 4 * (w & 1)) * LDW + cb * 128) * 2u;
    const int boff = kh * 65536 + (wl >> 1) * 8192 + tu * 128 + (((tu >> 1) ^ (2 * (wl & 1) + ((lane >> 4) & 1))) << 5) + (lane & 3) * 8;
#pragma unroll
    for (int ps = 0; ps < 2; ++ps) {
      BAR();
#pragma unroll
      for (int hh = 0; hh < 2; ++hh)
#pragma unroll
        for (int i = 0; i < 4; ++i)
          if (4 * ps + i < NPC && !(PROJ && ps == 0)) {
            const unsigned so = sB + (unsigned)((hh * NPC + 4 * ps + i) * 64 * LDW) * 2u, d = lbase + hh * 65536 + i * 16384 + w * 1024;
            dma16(rsW, vB, so, d); dma16(rsW, vB, so + 128, d + 8192);
          }
      WAIT_V(0); BAR();
#pragma unroll
      for (int i = 0; i < 4; ++i)
        if (4 * ps + i < NPC) {
#pragma unroll
          for (int k = 0; k < 4; ++k) { LAS unsigned char* q = lds + (boff + i * 16384 + k * 2048); Wf[(4 * ps + i) * 4 + k] = cat8(trread(q), trread(q + 1024)); }
        }
      WAIT_L0();
    }
    if (SG == SG_W1) {
      const u32x4 z = {0u, 0u, 0u, 0u};
      stx16(rs, (unsigned)tid * 16, WS_H1S + cb * 8192, z); stx16(rs, (unsigned)tid * 16, WS_H1S + 64 * 65536 + cb * 8192, z);
      WAIT_V(0);
    }
    if (tid < 8) *(LAS unsigned*)(lds + G_CTL + tid * 4) = 0u;
    LBAR();
  }
  const f32x16 zero16 = {0.f, 0.f, 0.f, 0.f, 0.f, 0.f, 0.f, 0.f, 0.f, 0.f, 0.f, 0.f, 0.f, 0.f, 0.f, 0.f};
  auto put16 = [&](LAS unsigned char* p, const f32x16& a) {
#pragma unroll
    for (int q = 0; q < 4; ++q) *(LAS f32x4*)(p + q * 1024) = f32x4{a[4 * q], a[4 * q + 1], a[4 * q + 2], a[4 * q + 3]};
  };
  if (SG == SG_W3) {
    const u32x4 one4 = {0x3F803F80u, 0x3F803F80u, 0x3F803F80u, 0x3F803F80u};
    f32x16 cs = zero16;
#pragma unroll
    for (int k = 0; k < KS; ++k) cs = mfma3232(Wf[k], __builtin_bit_cast(bf16x8, one4), cs);
    if (kh == 0) put16(lds + wl * 4096 + l16, cs);
    LBAR();
    if (kh == 1 && row == 0) {
#pragma unroll
      for (int q = 0; q < 4; ++q)
        *(LAS f32x4*)(lds + G_K + wl * 128 + hf * 64 + q * 16) = *(const LAS f32x4*)(lds + wl * 4096 + q * 1024 + l16) + f32x4{cs[4 * q], cs[4 * q + 1], cs[4 * q + 2], cs[4 * q + 3]};
    }
    LBAR();
  }
  auto blk = [&](int i) -> int { return KV ? 2 * i + kq : i; };
  const unsigned aoff = ROWM ? (unsigned)(row * 128 + ((hf ^ ((row >> 1) & 7)) << 4)) : (unsigned)((row >> 4) * 1024 + hf * 256 + (row & 15) * 16);
  const unsigned vA = ROWM ? (unsigned)((lane >> 3) * 2048 + (((lane & 7) ^ ((4 * wl + (lane >> 4)) & 7)) << 4)) : l16;
  auto piece = [&](int hb, int q, bool on) {
    const int i = hb >> 1, hh = hb & 1;
    const unsigned so = ABASE + (unsigned)blk(i) * ASTR + (unsigned)koff * (KS * 2048) + (ROWM ? wl * 16384 + hh * 1024 : (hh * KS + wl) * 1024);
    if (on) dma16x(rs, vA, so + q * (ROWM ? 128 : 4096), lbase + (i & 1) * 65536 + hh * 32768 + wl * 1024 + q * 4096);
  };
  auto fetch = [&](int hb) {
#pragma unroll
    for (int q = 0; q < NPC; ++q) piece(hb, q, true);
  };
  constexpr bool WEAVE = (SG == SG_W1 || SG == SG_W2 || SG == SG_W3);
  auto products = [&](int i, f32x16& acc, auto&& first, auto&& early, auto&& handover) {
    const unsigned ub = lbase + (i & 1) * 65536 + kh * 32768;
    LAS unsigned char* ab[4];
    ab[0] = lad(aoff, ub);
    if (ROWM) { ab[1] = lad<32>(aoff, ub); ab[2] = lad<64>(aoff, ub); ab[3] = lad<96>(aoff, ub); }
    constexpr int PD = 10;
    bf16x8 af[PD + 1];
    auto rd = [&](int k) { af[k % (PD + 1)] = ROWM ? *(const LAS bf16x8*)(ab[k & 3] + (k >> 2) * 4096) : *(const LAS bf16x8*)(ab[0] + (k >> 1) * 2048 + (k & 1) * 512); };
#pragma unroll
    for (int k = 0; k < PD; ++k) rd(k);
    SCHED();
#pragma unroll
    for (int k = 0; k < KS; ++k) {
      if (k + PD < KS) rd(k + PD);
      acc = SG == SG_V ? mfma3232(af[k % (PD + 1)], Wf[k], k ? acc : zero16) : mfma3232(Wf[k], af[k % (PD + 1)], k ? acc : zero16);
      SCHED();
      first(k);
      SCHED();
      if (k == KS - 10) { early(); SCHED(); }
      if (k == KS - 5) { handover(); SCHED(); }
    }
    asm volatile("" : "+v"(acc));
  };
  WST(rs, wk, KV ? 2 : 10);
#define SEG(x)
  if (kh == 0) {
    auto pvo = [&](int i) -> unsigned {
      const int b = blk(i);
      if (PROJ) return F_LN + (unsigned)((b % (NLN / 4)) * 4 + (lane & 3)) * 128u;
      if (SG == SG_WO) return F_ST + (unsigned)(P_S1 + kq * 8 + (lane & 7)) * 128u;
      if (SG == SG_W1) return F_ST + (unsigned)(P_S3 + (b % 3) * 32 + (lane & 31)) * 128u;
      if (SG == SG_W2) return F_ST + (unsigned)(P_S4 + (lane & 7)) * 128u;
      if (SG == SG_W3) return F_ST + (unsigned)(P_S5 + (lane & 7)) * 128u;
      return F_ST + (unsigned)(P_S7 + kq * 7 + min(lane & 7, 6)) * 128u;
    };
    auto need = [&](int i) -> unsigned { return PROJ ? 1u : (SG == SG_W2 && i == 64) ? 64u : (unsigned)i + 1u; };
    int R = 0, Rp = 0;
    for (int i = 0; i < NBLK; ++i) {
      if (R <= i) { wait_ge(rs, pvo(i), 0, need(i), cap); fetch(2 * i); fetch(2 * i + 1); }
      const unsigned pk = w == 0 ? (lane < 32 ? pvo(i + 1) : pvo(i + 2)) : 0xF0000000u;
      if (Rp <= i) { if (R <= i + 1) wait_v<NPC>(); else wait_v<2 * NPC>(); }
      BAR();
      SEG(1);
      f32x16 acc;
      unsigned peek;
      asm volatile("buffer_load_dword %0, %1, %2, 0 offen sc1" : "=v"(peek) : "v"(pk), "s"(rs) : "memory");
      u32x4 st[8];
      float mean = 0.f, q2 = 0.f;
      if (SG == SG_W3) {
#pragma unroll
        for (int q = 0; q < 8; ++q) st[q] = ldx16(rs, l16, WS_ST5 + (unsigned)i * 8192 + q * 1024);
      }
      products(i, acc, [&](int k) {
        if (SG == SG_W3) {
          if (k >= 18 && k < 26) { mean += u2f(st[k - 18][0]) + u2f(st[k - 18][2]); q2 += u2f(st[k - 18][1]) + u2f(st[k - 18][3]); asm volatile("" : "+v"(mean), "+v"(q2)); }
          if (k == 26) { mean = hsum(mean) * (1.0f / 1024.0f); q2 = hsum(q2); asm volatile("" : "+v"(mean), "+v"(q2)); }
          if (k == 28) { q2 = rbf(rbf(fmaxf(fmaf(-mean, mean, q2 * (1.0f / 1024.0f)), 0.f)) + 1e-5f); asm volatile("" : "+v"(q2)); }
          if (k == 29) { q2 = __builtin_amdgcn_rcpf(rbf(__builtin_amdgcn_sqrtf(q2))); mean = rbf(mean); asm volatile("" : "+v"(q2), "+v"(mean)); }
        }
      }, []() {}, [&]() {
        SEG(4);
        if (SG == SG_Q && R >= i + 2) fetch(2 * i + 3);
        WAIT_V(0);
        SEG(0);
        asm volatile("" : "+v"(peek));
        if (w == 0) {
          const unsigned long long ok = __ballot(peek >= (lane < 32 ? need(i + 1) : need(i + 2)));
          const bool r1 = (R >= i + 2 || (unsigned)ok == 0xFFFFFFFFu) && i + 1 < NBLK, r2 = r1 && (unsigned)(ok >> 32) == 0xFFFFFFFFu && i + 2 < NBLK;
          if (lane == 0) *(volatile LAS unsigned*)(lds + G_CTL + 4) = (unsigned)(i + 1 + (r1 ? 1 : 0) + (r2 ? 1 : 0));
        }
        LBAR();
      });
      SEG(2);
      const unsigned rv = *(const volatile LAS unsigned*)(lds + G_CTL + 4);
      if (SG == SG_W3) {
        const float m = mean;
        *(LAS float*)(lds + G_K + 1024 + wl * 256 + lane * 4) = q2;
#pragma unroll
        for (int q = 0; q < 4; ++q) {
          const f32x4 cs = *(const LAS f32x4*)(lds + G_K + wl * 128 + hf * 64 + q * 16);
#pragma unroll
          for (int e = 0; e < 4; ++e) acc[4 * q + e] = fmaf(-m, cs[e], acc[4 * q + e]);
        }
      }
      put16(lad(l16, lbase + G_XCH + wl * 4096), acc);
      asm volatile("" ::: "memory");
      if (lane == 0) *(volatile LAS unsigned*)(lds + G_CTL + 16 + wl * 4) = (unsigned)i + 1u;
      Rp = R; R = __builtin_amdgcn_readfirstlane(rv);
      SEG(3);
      if (R >= i + 2 && Rp < i + 2) { fetch(2 * i + 2); fetch(2 * i + 3); }
      if (R >= i + 3) fetch(2 * i + 4);
      if (KV) {
        if (wl < 2) dma16(mk_rs(side3, 0x7FFFFFFFu), (unsigned)((lane >> 2) * 6144 + (lane & 3) * 8) * 2u, (unsigned)((32 * i + 16 * wl) * 6144) * 2u, lbase + G_K);
        if (sld && 32 * i < (int)(side2 - side)) {
          const __amdgpu_buffer_rsrc_t rsN = mk_rs(side, 0x7FFFFFFFu);
#pragma unroll
          for (int q = 0; q < 2; ++q) dma16(rsN, (unsigned)((lane >> 4) * sld + (lane & 15) * 8) * 2u, (unsigned)((32 * i + 8 * wl + 4 * q) * sld) * 2u, lbase + G_K + wl * 2048 + q * 1024);
        }
      }
      SEG(5);
    }
    BAR();
  } else {
    bool pending = false;
    auto announce = [&](int i) {
      if (lane == 0) {
        const unsigned c = __hip_atomic_fetch_add((LAS unsigned*)(lds + G_CTL), 1u, __ATOMIC_RELAXED, __HIP_MEMORY_SCOPE_WORKGROUP);
        if ((c & 3u) == 3u) stx4(rs, 0, myword, (unsigned)blk(i) + 1u);
      }
    };
    unsigned kvs[2] = {0u, 0u}, chs = 0u;
    BAR();
    for (int i = 0; i < NBLK; ++i) {
      const int b = blk(i);
      if (SG == SG_Q) {
        if (i & 1) wait_seen(rs, (unsigned)(lane & 1) * 4096, F_ST + (unsigned)(P_PJ + 64 + cb) * 128, (unsigned)i + 1u, kvs[1], cap);
        else wait_seen(rs, (unsigned)(lane & 1) * 4096, F_ST + (unsigned)(P_PJ + cb) * 128, (unsigned)i + 1u, kvs[0], cap);
      }
      BAR();
      SEG(0);
      const unsigned rv = *(const volatile LAS unsigned*)(lds + G_CTL + 4);
      u32x4 res[2], vv[4];
      float isd;
      if (SG == SG_W2) {
#pragma unroll
        for (int u = 0; u < 2; ++u) {
          const int t = 32 * b - 2 + 16 * u + (lane >> 2);
          dma16x(rs, (t >= 0 && t < S_) ? (unsigned)(t * D_ + col0 + ((lane ^ (lane >> 3)) & 3) * 8) * 2u : 0xF0000000u, WS_H2, lbase + G_K + wl * 2048 + u * 1024);
        }
      }
      if (SG == SG_Q) {
        const unsigned ub = (unsigned)(b * NH + cb) * 8192;
        dma16x(rs, l16, WS_K + ub + wl * 2048, lbase + G_K + wl * 2048); dma16x(rs, l16, WS_K + ub + wl * 2048 + 1024, lbase + G_K + wl * 2048 + 1024);
        const unsigned ev = (unsigned)((lane >> 3) & 1) * 1024 + (unsigned)((lane >> 4) * 16 + 8 * ((lane >> 2) & 1) + (lane & 3)) * 16;
#pragma unroll
        for (int q = 0; q < 4; ++q) vv[q] = ldx16(rs, ev, WS_VT + ub + (2 * (wl >> 1) + (q >> 1)) * 2048 + (q & 1) * 64);
      }
      f32x16 acc;
      int R = 0;
      LAS unsigned char* xr = lad(l16, lbase + G_XCH + wl * 4096);
      f32x4 xa[4];
      unsigned xf;
      auto take = [&]() {
        asm volatile("" ::: "memory");
        xf = *(const volatile LAS unsigned*)(lds + G_CTL + 16 + wl * 4);
        asm volatile("" ::: "memory");
#pragma unroll
        for (int q = 0; q < 4; ++q) xa[q] = *(const LAS f32x4*)(xr + q * 1024);
        if (SG == SG_W3) isd = *(const LAS float*)(lds + G_K + 1024 + wl * 256 + lane * 4);
        asm volatile("" ::: "memory");
      };
      products(i, acc, [&](int k) {
        if (SG == SG_Q && k == 8 && i > 0) { wait_v<6>(); announce(i - 1); }
      }, [&]() {
        take();
      }, [&]() {
        SEG(5);
        R = __builtin_amdgcn_readfirstlane(rv);
        if (R >= i + 2) {
          WAIT_V(0);
          LBAR();
          if (pending && tid == 256) stx4(rs, 0, myword, (unsigned)blk(i - 1) + 1u);
        }
      });
      const bool more = R >= i + 2;
      SEG(1);
      while (__builtin_amdgcn_readfirstlane(xf) <= (unsigned)i) take();
#pragma unroll
      for (int q = 0; q < 16; ++q) { acc[q] += xa[q >> 2][q & 3]; if (SG == SG_W3) acc[q] *= isd; }
      SEG(2);
      const unsigned fo = (unsigned)((col0 >> 5) * 2048 + hf * 256), ro = (unsigned)((row >> 4) * 1024 + (row & 15) * 16);
      u32x4 v[2];
      if (SG == SG_W1 || SG == SG_W3) {
#pragma unroll
        for (int q = 0; q < 16; q += 2) {
          float g0 = gelu_tanh(acc[q]), g1 = gelu_tanh(acc[q + 1]);
          asm volatile("" : "+v"(g0), "+v"(g1));
          acc[q] = g0; acc[q + 1] = g1;
          piece(2 * i + 5, q >> 1, R >= i + 3 && i >= 2);
        }
      }
      if (SG != SG_V) widen32(acc, v);
      if (SG == SG_WO || SG == SG_W4) {
        LAS unsigned char* tw = lds + G_XCH + wl * 4096 + row * 64;
        *(LAS u32x4*)(tw + ((hf ^ (row >> 1)) & 3) * 16) = v[0]; *(LAS u32x4*)(tw + (((2 + hf) ^ (row >> 1)) & 3) * 16) = v[1];
        asm volatile("" ::: "memory");
        LAS unsigned char* tr = lds + G_XCH + wl * 4096 + (lane >> 2) * 64 + ((lane ^ (lane >> 3)) & 3) * 16;
        const unsigned so = (SG == SG_WO ? WS_P2 : WS_P8) + (unsigned)(b * 4 + kq) * 65536u, vo = (unsigned)((lane >> 2) * D_ + col0 + (lane & 3) * 8) * 2u;
        stx16(rs, vo, so, *(const LAS u32x4*)(tr)); stx16(rs, vo, so + 32768, *(const LAS u32x4*)(tr + 1024));
      } else if (SG == SG_K) {
        const unsigned so = WS_K + (unsigned)(b * NH + cb) * 8192 + wl * 1024, vo = (unsigned)((row >> 4) * 4096 + hf * 256 + (row & 15) * 16);
        stx16(rs, vo, so, v[0]); stx16(rs, vo, so + 512, v[1]);
      } else if (SG == SG_V) {
        const unsigned so = WS_VT + (unsigned)(b * NH + cb) * 8192 + wl * 2048, vo = ro + hf * 256;
#pragma unroll
        for (int u = 0; u < 2; ++u)
          stx16(rs, vo, so + u * 512, u32x4{pack2(acc[4 * u], acc[4 * u + 1]), pack2(acc[4 * u + 2], acc[4 * u + 3]), pack2(acc[8 + 4 * u], acc[9 + 4 * u]), pack2(acc[10 + 4 * u], acc[11 + 4 * u])});
      } else if (SG == SG_Q) {
        LAS unsigned char* qw = lds + G_XCH + wl * 4096 + (row >> 4) * 1024 + hf * 256 + (row & 15) * 16;
        *(LAS u32x4*)(qw) = v[0]; *(LAS u32x4*)(qw + 512) = v[1];
        WAIT_V(0);
        if (lane == 0) __hip_atomic_fetch_add((LAS unsigned*)(lds + G_CTL + 8), 1u, __ATOMIC_RELAXED, __HIP_MEMORY_SCOPE_WORKGROUP);
        while (__builtin_amdgcn_readfirstlane(*(const volatile LAS unsigned*)(lds + G_CTL + 8)) < 4u * (unsigned)i + 4u) {}
        asm volatile("" ::: "memory");
        attend(rs, lds + G_XCH + (wl & 1) * 1024 + l16, lds + G_K + l16, b, cb, wl & 1, wl >> 1, lane, vv, chs, cap);
      } else if (SG == SG_W1) {
        const int grp = cb >> 1, sh = (grp & 1) ? -(1 + (grp >> 1)) : (1 + (grp >> 1));
        const int rho = 32 * b + row + sh + 2;
        const unsigned vo = (rho >= 2 && rho < S_ + 2) ? (unsigned)(rho >> 5) * 65536u + fo + (unsigned)((rho >> 4) & 1) * 1024u + (unsigned)(rho & 15) * 16u : 0xF0000000u;
        stx16(rs, vo, WS_H1S, v[0]); stx16(rs, vo, WS_H1S + 512, v[1]);
      } else if (SG == SG_W2) {
        if (!more) WAIT_V(0);
        res[0] = *(const LAS u32x4*)(lds + G_K + wl * 2048 + row * 64 + ((hf ^ (row >> 1)) & 3) * 16);
        res[1] = *(const LAS u32x4*)(lds + G_K + wl * 2048 + row * 64 + (((2 + hf) ^ (row >> 1)) & 3) * 16);
        float zs = 0.f, q2 = 0.f;
#pragma unroll
        for (int q = 0; q < 8; ++q) {
          const unsigned x = pack2(lo2f(res[q >> 2][q & 3]) + lo2f(v[q >> 2][q & 3]), hi2f(res[q >> 2][q & 3]) + hi2f(v[q >> 2][q & 3]));
          v[q >> 2][q & 3] = x; zs += lo2f(x) + hi2f(x); q2 = fmaf(lo2f(x), lo2f(x), fmaf(hi2f(x), hi2f(x), q2));
          asm volatile("" : "+v"(zs), "+v"(q2));
          piece(2 * i + 5, q, R >= i + 3 && i >= 2);
        }
        stx16(rs, fo + ro, WS_X2 + (unsigned)b * 65536u, v[0]); stx16(rs, fo + ro, WS_X2 + (unsigned)b * 65536u + 512, v[1]);
        const float mw = hsum(zs);
        q2 = hsum(q2);
        if (hf == 0) stx8(rs, (unsigned)((cb * 2 + (wl >> 1)) * 512 + row * 16 + (wl & 1) * 8), WS_ST5 + (unsigned)b * 8192, u32x2{f2u(mw), f2u(q2)});
      } else {
        stx16(rs, fo + ro, WS_U + (unsigned)b * 229376u, v[0]); stx16(rs, fo + ro, WS_U + (unsigned)b * 229376u + 512, v[1]);
      }
      if (KV && 2 * i + ZA + (wl >> 1) <= NSEG && wk * 2 + (wl & 1) < 220) stx16(rs, l16, X_REC + (unsigned)(2 * i + ZA + (wl >> 1)) * RSTR + (unsigned)(wk * 2 + (wl & 1)) * 1024, u32x4{0u, 0u, 0u, 0u});
      SEG(3);
      pending = R >= i + 3 && i >= 2 && SG != SG_Q;
      if (SG == SG_Q ? i == NBLK - 1 : !pending) { WAIT_V(0); announce(i); }
      if (SG != SG_Q && R >= i + 3 && !(WEAVE && i >= 2)) fetch(2 * i + 5);
      if (!KV) WST(rs, wk, 16 + b);
      SEG(4);
      if (!more) BAR();
    }
  }
}

__device__ __forceinline__ void wait32(__amdgpu_buffer_rsrc_t rs, int first, int mine, int lane, unsigned need, unsigned& cap) {
  wait_ge(rs, 0, F_ST + (unsigned)(first + mine) * 128u, need, cap);
  wait_ge(rs, (unsigned)(lane & 31) * 128u, F_ST + (unsigned)first * 128u, need, cap);
}
__device__ __forceinline__ void sum_partials(__amdgpu_buffer_rsrc_t rs, unsigned l16, unsigned so, float (&a)[16]) {
  u32x4 pp[4][2];
#pragma unroll
  for (int k = 0; k < 4; ++k) { pp[k][0] = ldx16(rs, l16, so + k * 65536u); pp[k][1] = ldx16(rs, l16, so + k * 65536u + 1024); }
#pragma unroll
  for (int i = 0; i < 8; ++i) {
    f32x2 t = {0.f, 0.f};
#pragma unroll
    for (int k = 0; k < 4; ++k) t += f32x2{lo2f(pp[k][i >> 2][i & 3]), hi2f(pp[k][i >> 2][i & 3])};
    a[2 * i] = rbf(t[0]); a[2 * i + 1] = rbf(t[1]);
  }
}
__device__ __forceinline__ void put_row(__amdgpu_buffer_rsrc_t rs, unsigned l16, unsigned so, const float (&v)[16]) {
  stx16(rs, l16, so, u32x4{pack2(v[0], v[1]), pack2(v[2], v[3]), pack2(v[4], v[5]), pack2(v[6], v[7])});
  stx16(rs, l16, so + 1024, u32x4{pack2(v[8], v[9]), pack2(v[10], v[11]), pack2(v[12], v[13]), pack2(v[14], v[15])});
}
__device__ __forceinline__ void load_mod(__amdgpu_buffer_rsrc_t rs, int part, int lane, float (&v)[16], unsigned& cap) {
  for (;;) {
    asm volatile("" ::: "memory");
    u32x4 t[4];
#pragma unroll
    for (int i = 0; i < 4; ++i) t[i] = ldx16(rs, (unsigned)lane * 32, X_MOD + part * 4096 + (i >> 1) * 2048 + (i & 1) * 16);
    unsigned mn = 0xFFFFFFFFu;
#pragma unroll
    for (int i = 0; i < 16; ++i) { mn = min(mn, t[i >> 2][i & 3]); v[i] = hi2f(t[i >> 2][i & 3]); }
    if (__all(mn != 0u) || cap == 0u) break;
    cap = __builtin_amdgcn_readfirstlane(cap - 1u);
    __builtin_amdgcn_s_sleep(4);
  }
  cap = __builtin_amdgcn_readfirstlane(cap);
}
struct ModLoad { u32x4 w[8]; u16 c[8]; };
__device__ __forceinline__ void mod_load(const MainP& p, int grp, ModLoad& m) {
  const int kg = threadIdx.x >> 2, c8 = threadIdx.x & 3;
#pragma unroll
  for (int i = 0; i < 8; ++i) { m.w[i] = *(const u32x4*)(p.wmod + (size_t)(kg + 128 * i) * 6144 + grp * 32 + c8 * 8); m.c[i] = p.c[kg + 128 * i]; }
}
__device__ __forceinline__ void mod_finish(__amdgpu_buffer_rsrc_t rs, LAS float* red, int grp, const ModLoad& m) {
  const int tid = threadIdx.x, kg = tid >> 2, c8 = tid & 3;
  float acc[8] = {0.f, 0.f, 0.f, 0.f, 0.f, 0.f, 0.f, 0.f};
#pragma unroll
  for (int i = 0; i < 8; ++i) {
    const float v = bf2f(m.c[i]), sc = rbf(v / (1.0f + __expf(-v)));
#pragma unroll
    for (int j = 0; j < 4; ++j) { acc[2 * j] += sc * lo2f(m.w[i][j]); acc[2 * j + 1] += sc * hi2f(m.w[i][j]); }
  }
#pragma unroll
  for (int j = 0; j < 8; ++j) red[kg * 33 + c8 * 8 + j] = acc[j];
  LBAR();
  float t = 0.f;
#pragma unroll
  for (int i = 0; i < 8; ++i) t += red[((tid >> 5) * 8 + i) * 33 + (tid & 31)];
  t = hsum(t);
  LBAR();
  if ((tid & 63) < 32) red[(tid >> 6) * 33 + (tid & 31)] = t;
  LBAR();
  if (tid < 32) {
    float u = 0.f;
#pragma unroll
    for (int i = 0; i < 8; ++i) u += red[i * 33 + tid];
    stx4(rs, (unsigned)tid * 4, X_MOD + (unsigned)grp * 128, f2u(rbf(u)) | 1u);
  }
}

__device__ __forceinline__ void stage_mid(__amdgpu_buffer_rsrc_t rs, const u16* x, int wk, int cu, unsigned& cap) {
  const int tid = threadIdx.x, lane = tid & 63, w = __builtin_amdgcn_readfirstlane(tid >> 6);
  const int id = w * 12 + cu, grp = id >> 5, rl = id & 31;
  const unsigned l16 = lane * 16;
  float ga[16], sc[16], sh[16];
  load_mod(rs, 2, lane, ga, cap); load_mod(rs, 4, lane, sc, cap); load_mod(rs, 3, lane, sh, cap);
#pragma unroll
  for (int i = 0; i < 16; ++i) sc[i] = rbf(1.0f + sc[i]);
  for (int seg = grp; seg < NSEG; seg += 3) {
    const int row = seg * 32 + rl;
    float v[16], a[16];
    load_row16(x + (size_t)row * D_, lane, v);
    wait32(rs, P_S2, rl, lane, (unsigned)seg + 1u, cap);
    sum_partials(rs, l16, WS_P2 + (unsigned)seg * 262144u + (unsigned)rl * 2048, a);
#pragma unroll
    for (int i = 0; i < 16; ++i) v[i] = rbf(v[i] + rbf(ga[i] * a[i]));
    put_row(rs, l16, WS_X1 + (unsigned)row * 2048, v);
    ln_row(v);
#pragma unroll
    for (int i = 0; i < 16; ++i) v[i] = rbf(rbf(v[i] * sc[i]) + sh[i]);
    put_row(rs, l16, WS_H2 + (unsigned)row * 2048, v);
    ln_row(v);
    put_row(rs, l16, WS_G + (unsigned)row * 2048, v);
    WAIT_V(0);
    if (lane == 0) stx4(rs, 0, F_ST + (unsigned)(P_S3 + grp * 32 + rl) * 128, (unsigned)seg + 1u);
    WST(rs, wk, 16 + seg);
  }
}

__device__ __forceinline__ void stage_out(__amdgpu_buffer_rsrc_t rs, u16* out, int wk, int cu, unsigned& cap) {
  const int tid = threadIdx.x, lane = tid & 63, w = __builtin_amdgcn_readfirstlane(tid >> 6);
  const int grp = w >> 2, rl = cu * 4 + (w & 3);
  const unsigned l16 = lane * 16;
  float gb[16];
  load_mod(rs, 5, lane, gb, cap);
  for (int b = grp; b < 65; b += 2) {
    const int t = 32 * b - 2 + rl;
    if (t < 0 || t >= S_) continue;
    wait_ge(rs, (unsigned)(lane & 7) * 128u, F_ST + P_S5 * 128u, (unsigned)b + 1u, cap);
    const unsigned xo = (unsigned)((lane >> 2) * 2048 + (lane & 3) * 256), xs = WS_X2 + (unsigned)b * 65536u + (unsigned)((rl >> 4) * 1024 + (rl & 15) * 16);
    const u32x4 x2a = ldx16(rs, xo, xs), x2b = ldx16(rs, xo, xs + 32768);
    const u32x4 x1a = ldx16(rs, l16, WS_X1 + (unsigned)t * 2048), x1b = ldx16(rs, l16, WS_X1 + (unsigned)t * 2048 + 1024);
    wait32(rs, P_S8, rl, lane, (unsigned)b + 1u, cap);
    float a[16];
    sum_partials(rs, l16, WS_P8 + (unsigned)b * 262144u + (unsigned)rl * 2048, a);
#pragma unroll
    for (int i = 0; i < 4; ++i) {
      a[2 * i] = lo2f(x1a[i]) + rbf(gb[2 * i] * rbf(lo2f(x2a[i]) + a[2 * i])); a[2 * i + 1] = hi2f(x1a[i]) + rbf(gb[2 * i + 1] * rbf(hi2f(x2a[i]) + a[2 * i + 1]));
      a[8 + 2 * i] = lo2f(x1b[i]) + rbf(gb[8 + 2 * i] * rbf(lo2f(x2b[i]) + a[8 + 2 * i])); a[9 + 2 * i] = hi2f(x1b[i]) + rbf(gb[9 + 2 * i] * rbf(hi2f(x2b[i]) + a[9 + 2 * i]));
    }
    store_row16(out + (size_t)t * D_, lane, a);
    WST(rs, wk, 16 + b);
  }
}

__device__ __forceinline__ void worker(const MainP& p, LAS unsigned char* lds, int xw, int j) {
  const int tid = threadIdx.x, lane = tid & 63, w = __builtin_amdgcn_readfirstlane(tid >> 6);
  const __amdgpu_buffer_rsrc_t rs = mk_rs(p.ws, WS_END);
  unsigned cap = SPIN_CAP;
  const int wk = xw * 32 + j;
  WST(rs, wk, 0);
  {
    const int c = j * 5 + xw, r0 = c * 8 + 2 * (w & 3);
    const bool two = r0 + 8 * NLN < S_;
    float v[4][16];
    ModLoad m;
    if (c >= NLN - 64) mod_load(p, c - (NLN - 64), m);
    if (w < 4) {
#pragma unroll
      for (int q = 0; q < 4; ++q) load_row16(p.x + (size_t)(r0 + (q & 1) + (two ? (q >> 1) * 8 * NLN : 0)) * D_, lane, v[q]);
    }
    if (c >= NLN - 64) mod_finish(rs, (LAS float*)(lds + G_XCH), c - (NLN - 64), m);
    WST(rs, wk, 5);
    if (w >= 4) {
      const __amdgpu_buffer_rsrc_t rsW = mk_rs(xw == 4 ? p.Wq : (xw & 1) ? p.Wv : p.Wk, D_ * HD * 2);
      const int ub = lane >> 3;
      const unsigned vB = (unsigned)(((ub & 3) + 8 * (ub >> 2)) * HD + 16 * (((lane >> 1) & 3) ^ (ub >> 1)) + 8 * (lane & 1)) * 2u, lbase = (unsigned)(size_t)lds;
#pragma unroll
      for (int q = 0; q < 16; ++q) {
        const int wq = (w & 3) + 4 * (q >> 3);
        const unsigned so = (unsigned)((32 * (wq >> 2) + 16 * ((wq >> 1) & 1) + 4 * (wq & 1) + (((q >> 2) & 1) * 8 + (q & 3)) * 64) * HD + j * 128) * 2u;
        const unsigned d = lbase + ((q >> 2) & 1) * 65536 + (q & 3) * 16384 + wq * 1024;
        dma16(rsW, vB, so, d); dma16(rsW, vB, so + 128, d + 8192);
      }
    } else {
#pragma unroll
      for (int q = 0; q < 4; ++q) ln_row(v[q]);
      float sc[16], sh[16];
      load_mod(rs, 1, lane, sc, cap); load_mod(rs, 0, lane, sh, cap);
      WST(rs, wk, 6);
#pragma unroll
      for (int i = 0; i < 16; ++i) sc[i] = rbf(1.0f + sc[i]);
#pragma unroll
      for (int q = 0; q < 4; ++q) {
#pragma unroll
        for (int i = 0; i < 16; ++i) v[q][i] = rbf(rbf(v[q][i] * sc[i]) + sh[i]);
        if (q < 2 || two) put_row(rs, (unsigned)lane * 16, WS_H + (unsigned)(r0 + (q & 1) + (q >> 1) * 8 * NLN) * 2048, v[q]);
      }
      WAIT_V(0);
    }
    BAR();
    if (tid == 0) stx4(rs, 0, F_LN + c * 128, 1u);
    WST(rs, wk, 1);
  }
  if (xw == 4) stage_gemm<SG_Q>(lds, rs, p.Wq, wk, 0, j, P_S1 + j, cap);
  else {
    const u16* nw = xw == 0 ? p.Wo + (size_t)(j >> 3) * 1024 * D_ + (j & 7) * 128 : xw == 1 ? p.w4 + (size_t)(j >> 3) * 896 * D_ + (j & 7) * 128
                  : xw == 3 ? p.w3 + j * 128 : (j < 8 ? p.w1 : p.w2) + (j & 7) * 128;
    const int nld = (xw >= 2 && j >= (xw == 3 ? 28 : 16)) ? 0 : xw == 3 ? FF : D_, nrows = xw == 1 ? 896 : 1024;
    if (xw & 1) stage_gemm<SG_V>(lds, rs, p.Wv, wk, xw >> 1, j, P_PJ + xw * 32 + j, cap, nw, nw + nrows, nld, p.wmod + (64 + wk) * 32);
    else stage_gemm<SG_K>(lds, rs, p.Wk, wk, xw >> 1, j, P_PJ + xw * 32 + j, cap, nw, nw + nrows, nld, p.wmod + (64 + wk) * 32);
    WST(rs, wk, 3);
    { ModLoad m; mod_load(p, 64 + wk, m); mod_finish(rs, (LAS float*)(lds + 65536), 64 + wk, m); }
    if (xw == 0) stage_gemm<SG_WO>(lds, rs, p.Wo, wk, j >> 3, j & 7, P_S2 + j, cap);
    else if (xw == 1) stage_gemm<SG_W4>(lds, rs, p.w4, wk, j >> 3, j & 7, P_S8 + j, cap);
    else if (j >= 28) stage_out(rs, p.out, wk, (xw - 2) * 4 + j - 28, cap);
    else if (xw == 3) stage_gemm<SG_W3>(lds, rs, p.w3, wk, 0, j, P_S7 + j, cap);
    else if (j < 8) stage_gemm<SG_W1>(lds, rs, p.w1, wk, 0, j, P_S4 + j, cap);
    else if (j < 16) stage_gemm<SG_W2>(lds, rs, p.w2, wk, 0, j - 8, P_S5 + j - 8, cap);
    else stage_mid(rs, p.x, wk, j - 16, cap);
  }
  if (cap == 0u && lane == 0) stx4(rs, 0, X_CTL, 1u);
}

#define ST_DECL
#define ST0()
#define ST(i)
#define ST_FLUSH()
constexpr int R_W = 0, R_MEM = 131072, R_QK = R_MEM + 16384, R_KT = R_QK + 8192, R_RED = R_MEM + 16384, R_LDS = 163840;
__device__ __forceinline__ void bar_lo(int w) { asm volatile("s_cmp_ge_u32 %0, 4\n\ts_cbranch_scc1 1f\n\ts_barrier\n\t1:" :: "s"(w) : "scc", "memory"); }
__device__ __forceinline__ void bar_hi(int w) { asm volatile("s_cmp_lt_u32 %0, 4\n\ts_cbranch_scc1 1f\n\ts_setprio 3\n\ts_barrier\n\t1:" :: "s"(w) : "scc", "memory"); }
__device__ __forceinline__ void issue4(__amdgpu_buffer_rsrc_t rs, unsigned vo, unsigned s0, unsigned s1, unsigned s2, unsigned s3, u32x4& a0, u32x4& a1, u32x4& a2, u32x4& a3) {
  asm volatile("buffer_load_dwordx4 v[240:243], %[vo], %[rs], %[s0] offen sc1\n\tbuffer_load_dwordx4 v[244:247], %[vo], %[rs], %[s1] offen sc1\n\t"
               "buffer_load_dwordx4 v[248:251], %[vo], %[rs], %[s2] offen sc1\n\tbuffer_load_dwordx4 v[252:255], %[vo], %[rs], %[s3] offen sc1"
               : "=&{v[240:243]}"(a0), "=&{v[244:247]}"(a1), "=&{v[248:251]}"(a2), "=&{v[252:255]}"(a3)
               : [vo] "v"(vo), [rs] "s"(rs), [s0] "s"(s0), [s1] "s"(s1), [s2] "s"(s2), [s3] "s"(s3) : "memory");
}
#define ARRIVE(R0, R1, R2, R3, CNT) \
  asm volatile("s_waitcnt vmcnt(" #CNT ")\n\t" \
               "1:\n\t" \
               "v_min3_u32 %[t], v" #R0 ", v" #R1 ", v" #R2 "\n\t" \
               "v_min_u32 %[t], %[t], v" #R3 "\n\t" \
               "v_cmp_eq_u32 vcc, 0, %[t]\n\t" \
               "s_cbranch_vccz 2f\n\t" \
               "s_mov_b64 exec, vcc\n\t" \
               "buffer_load_dwordx4 v[" #R0 ":" #R3 "], %[vo], %[rs], %[so] offen sc1\n\t" \
               "s_mov_b64 exec, -1\n\t" \
               "s_waitcnt vmcnt(0)\n\t" \
               "s_sub_u32 %[cap], %[cap], 1\n\t" \
               "s_cbranch_scc0 1b\n\t" \
               "s_mov_b32 %[cap], 0\n\t" \
               "2:" \
               : "+{v[" #R0 ":" #R3 "]}"(a), [t] "=&v"(t), [cap] "+s"(cap) : [vo] "v"(vo), [rs] "s"(rs), [so] "s"(so) : "vcc", "scc", "memory")
template <int I> __device__ __forceinline__ void arrive(__amdgpu_buffer_rsrc_t rs, unsigned vo, unsigned so, u32x4& a, unsigned& cap) {
  unsigned t;
  if (I == 0) ARRIVE(240, 241, 242, 243, 3);
  else if (I == 1) ARRIVE(244, 245, 246, 247, 2);
  else if (I == 2) ARRIVE(248, 249, 250, 251, 1);
  else ARRIVE(252, 253, 254, 255, 0);
}
__device__ __forceinline__ unsigned half_of(unsigned v) { unsigned r; asm volatile("v_lshrrev_b32 %0, 1, %1" : "=v"(r) : "v"(v)); return r; }
__device__ __forceinline__ void issue2(__amdgpu_buffer_rsrc_t rs, unsigned l8, unsigned so, int w, u32x4& a) {
  asm volatile("s_cmp_ge_u32 %[w], 4\n\ts_cbranch_scc1 1f\n\tv_lshrrev_b32 v255, 1, %[l8]\n\t"
               "buffer_load_dwordx2 v[252:253], v255, %[rs], %[so] offen sc1\n\tbuffer_load_dwordx2 v[254:255], v255, %[rs], %[so] offen offset:512 sc1\n\t1:"
               : "=&{v[252:255]}"(a) : [l8] "v"(l8), [rs] "s"(rs), [so] "s"(so), [w] "s"(w) : "scc", "memory");
}
__device__ __forceinline__ void arrive2(__amdgpu_buffer_rsrc_t rs, unsigned l8, unsigned so, u32x4& a, unsigned& cap) {
  unsigned t;
  asm volatile("s_waitcnt vmcnt(0)\n\t"
               "1:\n\t"
               "v_min3_u32 %[t], v252, v253, v254\n\t"
               "v_min_u32 %[t], %[t], v255\n\t"
               "v_cmp_eq_u32 vcc, 0, %[t]\n\t"
               "s_cbranch_vccz 2f\n\t"
               "v_lshrrev_b32 %[t], 1, %[l8]\n\t"
               "buffer_load_dwordx2 v[252:253], %[t], %[rs], %[so] offen sc1\n\t"
               "buffer_load_dwordx2 v[254:255], %[t], %[rs], %[so] offen offset:512 sc1\n\t"
               "s_waitcnt vmcnt(0)\n\t"
               "s_sub_u32 %[cap], %[cap], 1\n\t"
               "s_cbranch_scc0 1b\n\t"
               "s_mov_b32 %[cap], 0\n\t"
               "2:"
               : "+{v[252:255]}"(a), [t] "=&v"(t), [cap] "+s"(cap) : [l8] "v"(l8), [rs] "s"(rs), [so] "s"(so) : "vcc", "scc", "memory");
}
__device__ __forceinline__ unsigned ror8(unsigned v) { return __builtin_amdgcn_update_dpp(0u, v, 0x128, 0xF, 0xF, true); }
__device__ __forceinline__ void gather_mem(__amdgpu_buffer_rsrc_t rs, unsigned lbase, int s, int w, unsigned l16, unsigned& cap) {
  const unsigned so = X_MEM + s * RSTR + w * 2048;
  u32x4 a0, a1;
  asm volatile("buffer_load_dwordx4 v[248:251], %[vo], %[rs], %[so] offen sc1\n\tbuffer_load_dwordx4 v[252:255], %[vo], %[rs], %[so] offen offset:1024 sc1"
               : "=&{v[248:251]}"(a0), "=&{v[252:255]}"(a1) : [vo] "v"(l16), [rs] "s"(rs), [so] "s"(so) : "memory");
  LAS unsigned char* d = lad(l16, lbase + R_MEM + w * 2048);
  arrive<2>(rs, l16, so, a0, cap);
  *(LAS u32x4*)(d) = a0;
  arrive<3>(rs, l16, so + 1024, a1, cap);
  *(LAS u32x4*)(d + 1024) = a1;
}
__device__ __forceinline__ unsigned mem_off(int lane) { return (unsigned)((lane & 1) * 512 + (((lane >> 1) ^ ((lane & 1) * 16)) * 8)); }
__device__ __forceinline__ bf16x8 mem_frag(LAS unsigned char* mp, int chunk) { return cat8(trread(mp + chunk * 1024), trread(mp + chunk * 1024 + 256)); }
__device__ __forceinline__ bf16x16 w_frag(LAS unsigned char* p) {
  return cat16(cat8(trread(p), trread(p + 2048)), cat8(trread(p + 4096), trread(p + 6144)));
}
__device__ __forceinline__ bf16x16 l_frag(LAS unsigned char* p, int i) { return cat16(*(const LAS bf16x8*)(p + i * 2048), *(const LAS bf16x8*)(p + i * 2048 + 1024)); }
__device__ __forceinline__ void make_resident(LAS unsigned char* wb, unsigned lo, unsigned l16) {
#pragma unroll
  for (int c = 0; c < 2; ++c) {
    bf16x16 f[4];
#pragma unroll
    for (int t = 0; t < 4; ++t) f[t] = w_frag(wb + c * 8192 + (lo ^ (t << 5)));
    WAIT_L0(); SCHED();
#pragma unroll
    for (int t = 0; t < 4; ++t) {
      *(LAS bf16x8*)(wb + (c * 4 + t) * 2048 + l16) = __builtin_shufflevector(f[t], f[t], 0, 1, 2, 3, 4, 5, 6, 7);
      *(LAS bf16x8*)(wb + (c * 4 + t) * 2048 + 1024 + l16) = __builtin_shufflevector(f[t], f[t], 8, 9, 10, 11, 12, 13, 14, 15);
    }
    WAIT_L0(); SCHED();
  }
}
__device__ __forceinline__ f32x4 fold(f32x4 a, f32x4 b) {
  f32x4 r;
#pragma unroll
  for (int i = 0; i < 4; ++i) {
    const auto t = __builtin_amdgcn_permlane32_swap(f2u(a[i]), f2u(b[i]), false, false);
    r[i] = u2f(t[0]) + u2f(t[1]);
  }
  return r;
}

struct Sched { unsigned tau, prev; int ea, eb, d1; bool moved; };
__device__ __forceinline__ void retime(__amdgpu_buffer_rsrc_t rs, unsigned l16, int s, Sched& t, const MainP& p, unsigned& cap) {
  if (s > 0) {
    unsigned vo;
    asm volatile("v_lshlrev_b32 %0, 3, %1" : "=v"(vo) : "v"(l16));
    unsigned va;
    asm volatile("v_and_b32 %0, 0xF80, %1" : "=v"(va) : "v"(vo));
    wait_ge(rs, vo, X_TS + (unsigned)s * RSTR, 1u, cap);
    wait_ge(rs, va, X_TS + (unsigned)s * RSTR + 8192, 1u, cap);
    int vb = (int)(ldx4(rs, vo, X_TS + (unsigned)s * RSTR) - t.prev), vc = (int)(ldx4(rs, va, X_TS + (unsigned)s * RSTR + 8192) - t.prev);
#pragma unroll
    for (int o = 32; o >= 1; o >>= 1) { vb = max(vb, __shfl_xor(vb, o)); vc = max(vc, __shfl_xor(vc, o)); }
    const int tb = __builtin_amdgcn_readfirstlane(vb), ta = __builtin_amdgcn_readfirstlane(vc);
    const int late = tb + p.d3 - (int)(t.tau - t.prev);
    if (s > 2) {
      const int up = s < 5 ? 80 : 8, dn = s < 10 ? 40 : 8;
      if (!t.moved) t.ea = max(t.ea - dn, min(ta * 8, t.ea + up));
      if (ta + p.d3 <= t.d1) t.eb = max(t.eb - dn, min((tb - t.d1) * 8, t.eb + up));
    }
    t.moved = late > 0;
    if (late > 0) t.tau += late + min(p.d4, 8 * late);
  }
  t.d1 = (t.ea >> 3) + p.d1;
}
__device__ __forceinline__ void advance(Sched& t, const MainP& p) { t.prev = t.tau; t.tau += t.d1 + (t.eb >> 3) + p.d2; }

__device__ __forceinline__ void role_a(const MainP& p, LAS unsigned char* lds, int h) {
  const int tid = threadIdx.x, lane = tid & 63, w = __builtin_amdgcn_readfirstlane(tid >> 6);
  const int cg = w & 3, kh = w >> 2;
  const __amdgpu_buffer_rsrc_t rs = mk_rs(p.ws, WS_END);
  const unsigned lbase = (unsigned)(size_t)lds;
  unsigned l16 = lane * 16;
  asm volatile("" : "+v"(l16));
  const int sidx = (lane & 8) ? 0xEEEE : 0x4444;
  bf16x16 W[24];
  unsigned qoff;
  {
    const int fr = lane & 15, g = lane >> 4, tu = 4 * (g & 1) + (fr >> 2), ub = lane >> 3;
    const unsigned lo = (g >> 1) * 1024 + tu * 128 + ((tu >> 1) << 5) + (fr & 3) * 8;
    qoff = R_QK + ((fr >> 1) & 1) * 512 + (g >> 1) * 256 + (fr & 1) * 128 + (8 * (g & 1) + (fr >> 2)) * 8;
    const __amdgpu_buffer_rsrc_t rw = mk_rs(cg < 2 ? p.Wq : p.Wk, D_ * HD * 2);
    const unsigned vo = (unsigned)(ub * HD + 16 * (((lane >> 1) & 3) ^ (ub >> 1)) + 8 * (lane & 1)) * 2u;
    const unsigned so = (unsigned)(kh * 512 * HD + h * DH + (cg & 1) * 64) * 2u;
    auto stage = [&](int c) {
#pragma unroll
      for (int rho = 0; rho < 8; ++rho) dma16(rw, vo, so + (unsigned)((c * 64 + rho * 8) * HD) * 2u, lbase + R_W + w * 16384 + (c & 1) * 8192 + rho * 1024);
    };
    stage(0); stage(1);
#pragma unroll
    for (int c = 0; c < 6; ++c) {
      WAIT_V(8);
#pragma unroll
      for (int t = 0; t < 4; ++t) W[4 * c + t] = w_frag(lds + R_W + w * 16384 + (c & 1) * 8192 + (lo ^ (t << 5)));
      WAIT_L0(); SCHED();
      stage(c + 2);
    }
    WAIT_V(0);
    make_resident(lds + R_W + w * 16384, lo, l16);
  }
  WST(rs, 160 + (h >> 1), 4);
  unsigned cap = SPIN_CAP;
  wait_ge(rs, (l16 & 16) * 256, F_ST + (unsigned)(P_PJ + h) * 128, 1u, cap);
  dma16x(rs, l16, WS_K + h * 8192 + w * 1024, lbase + R_KT + w * 1024);
  Sched tt; tt.tau = rt32() - 100000; tt.prev = tt.tau; tt.ea = 195 * 8; tt.eb = 118 * 8; tt.d1 = 0; tt.moved = true;
  const int cu_ = h; (void)cu_;
  ST_DECL
  for (int s = 0; s < NSEG; ++s) {
    LAS unsigned char* lp = lad(l16, lbase + R_W + w * 16384);
    bf16x16 L[2];
    L[0] = l_frag(lp, 0); L[1] = l_frag(lp, 1);
    unsigned lz = l16;
    asm volatile("" : "+v"(lz));
    LAS unsigned char* mp = lad(mem_off((int)(lz >> 4)), lbase + R_MEM + kh * 8192);
    SCHED();
    if (w == 7) wait_until(tt.tau);
    BAR();
    ST0();
    gather_mem(rs, lbase, s, (w + h) & 7, l16, cap);
    ST(1);
    LBAR();
    ST(2);
    f32x4 c[4];
#pragma unroll
    for (int t = 0; t < 4; ++t) c[t] = f32x4{0.f, 0.f, 0.f, 0.f};
    bf16x8 al, af[2];
    u32x4 vt0, vt1;
    u32x4 vmv;
    const unsigned vso = X_VM + (unsigned)s * RSTR + h * 4096 + cg * 1024;
    al = mem_frag(mp, 6); af[0] = mem_frag(mp, 0); af[1] = mem_frag(mp, 1);
#pragma unroll
    for (int i = 0; i < 8; ++i) {
      c[i & 3] = smf(al, L[i & 1], c[i & 3], sidx);
      SCHED();
      if (i == 3) al = mem_frag(mp, 7);
      if (i < 6) L[i & 1] = l_frag(lp, i + 2);
      if (i == 6) {
        const unsigned vo = WS_VT + (unsigned)(s * NH + h) * 8192 + cg * 2048;
        asm volatile("s_cmp_ge_u32 %5, 4\n\ts_cbranch_scc1 1f\n\t"
                     "buffer_load_dwordx4 %0, %2, %3, %4 offen sc1\n\tbuffer_load_dwordx4 %1, %2, %3, %4 offen offset:1024 sc1\n\t1:"
                     : "=&v"(vt0), "=&v"(vt1) : "v"(l16), "s"(rs), "s"(vo), "s"(w) : "scc", "memory");
      }
      SCHED();
#pragma unroll
      for (int r = 3 * i; r < 3 * i + 3; ++r) {
        c[(r + 1) & 3] = smf(af[(r >> 2) & 1], W[(r & ~3) + ((r + 1) & 3)], c[(r + 1) & 3], sidx);
        SCHED();
        if ((r & 3) == 3 && (r >> 2) + 2 < 6) af[(r >> 2) & 1] = mem_frag(mp, (r >> 2) + 2);
        SCHED();
      }
    }
    {
      LAS unsigned char* qw = lad(half_of(l16), lbase + R_QK + cg * 2048 + kh * 512);
      const f32x4 f0 = fold(c[0], c[1]);
      *(LAS u32x2*)(qw) = u32x2{pack2(f0[0], f0[1]), pack2(f0[2], f0[3])};
      const f32x4 f1 = fold(c[2], c[3]);
      *(LAS u32x2*)(qw + 1024) = u32x2{pack2(f1[0], f1[1]), pack2(f1[2], f1[3])};

    }
    ST(3);
    if (w < 4) {
      bf16x8 k1[4], k2[4];
      LAS unsigned char* kb = lad(l16, lbase + R_KT);
#pragma unroll
      for (int ks = 0; ks < 2; ++ks) { k1[ks] = *(const LAS bf16x8*)(kb + ks * 1024); k2[ks] = *(const LAS bf16x8*)(kb + 4096 + ks * 1024); }
      LBAR();
      ST(4);
      issue2(rs, l16, vso, w, vmv);
      f32x4 t0 = {0.f, 0.f, 0.f, 0.f}, t1 = t0, t2 = t0;
      LAS unsigned char* qb = lad(qoff, lbase);
      bf16x8 qf[4], kf[4];
#pragma unroll
      for (int ks = 0; ks < 2; ++ks) {
        qf[ks] = cat8(trread(qb + ks * 1024), trread(qb + ks * 1024 + 32));
        kf[ks] = cat8(trread(qb + 4096 + ks * 1024), trread(qb + 4096 + ks * 1024 + 32));
      }
      SCHED();
#pragma unroll
      for (int ks = 0; ks < 4; ++ks) {
        t1 = mfma32(k1[ks], qf[ks], t1); t2 = mfma32(k2[ks], qf[ks], t2); t0 = mfma32(kf[ks], qf[ks], t0);
        SCHED();
        if (ks < 2) {
          qf[ks + 2] = cat8(trread(qb + (ks + 2) * 1024), trread(qb + (ks + 2) * 1024 + 32));
          k1[ks + 2] = *(const LAS bf16x8*)(kb + (ks + 2) * 1024); k2[ks + 2] = *(const LAS bf16x8*)(kb + 4096 + (ks + 2) * 1024);
          kf[ks + 2] = cat8(trread(qb + 4096 + (ks + 2) * 1024), trread(qb + 4096 + (ks + 2) * 1024 + 32));
        }
        SCHED();
      }
      ST(5);
      {
        const float sl = 0.08838834764831845f * 1.4426950408889634f;
        float z[4] = {t1[0], t1[1], t1[2], t1[3]}, y = t0[0], mx;
        {
          float u1 = t0[1], u2 = t0[2], u3 = t0[3];
#define M8(Z, B) "v_add_f32_dpp " Z ", " Z ", " Z " row_ror:8 row_mask:0xf bank_mask:0x3\n\tv_add_f32_dpp " Z ", " B ", " B " row_ror:8 row_mask:0xf bank_mask:0xc\n\t"
          asm("s_nop 7\n\t"
              "v_permlane32_swap_b32 %4, %6\n\tv_permlane32_swap_b32 %5, %7\n\t"
              M8("%0", "%9") "v_add_f32 %4, %4, %6\n\tv_add_f32 %5, %5, %7\n\t" M8("%1", "%10") M8("%2", "%11") M8("%4", "%5") M8("%3", "%12")
              "v_max3_f32 %8, %0, %1, %2\n\tv_max3_f32 %8, %8, %4, %3\n\ts_nop 1\n\t"
              "v_max_f32_dpp %8, %8, %8 row_ror:8 row_mask:0xf bank_mask:0xf\n\tv_mov_b32 %5, %8\n\ts_nop 1\n\t"
              "v_permlane16_swap_b32 %8, %5\n\tv_max_f32 %8, %8, %5\n\tv_mov_b32 %5, %8\n\ts_nop 1\n\t"
              "v_permlane32_swap_b32 %8, %5\n\tv_max_f32 %8, %8, %5"
              : "+v"(z[0]), "+v"(z[1]), "+v"(z[2]), "+v"(z[3]), "+v"(y), "+v"(u1), "+v"(u2), "+v"(u3), "=&v"(mx) : "v"(t2[0]), "v"(t2[1]), "v"(t2[2]), "v"(t2[3]));
#undef M8
        }
        const float nm = -mx * sl;
#pragma unroll
        for (int r = 0; r < 4; ++r) z[r] = __builtin_amdgcn_exp2f(fmaf(z[r], sl, nm));
        const unsigned p0 = pack2(z[0], z[1]), p1 = pack2(z[2], z[3]);
        const u32x4 pt = {p0, p1, ror8(p0), ror8(p1)};
        SCHED();
        asm volatile("s_waitcnt vmcnt(2)" : "+v"(vt0), "+v"(vt1) :: "memory");
        f32x4 o0 = {0.f, 0.f, 0.f, 0.f}, o1 = o0;
        o0 = mfma32(__builtin_bit_cast(bf16x8, vt0), __builtin_bit_cast(bf16x8, pt), o0);
        o1 = mfma32(__builtin_bit_cast(bf16x8, vt1), __builtin_bit_cast(bf16x8, pt), o1);
        y = __builtin_amdgcn_exp2f(fmaf(y, sl, nm));
        const unsigned pm1 = pack2(y, u2f(ror8(f2u(y))));
        const auto pms = __builtin_amdgcn_permlane32_swap(pm1, pm1, false, false);
        const u32x2 pm = {pms[0], pms[1]};
        float inv = __builtin_amdgcn_rcpf(gsum(dpp_add<0x128>(((z[0] + z[1]) + (z[2] + z[3])) + y)));
        o0 *= inv; o1 *= inv;
        unsigned ov;
        unsigned ot;
        asm volatile("v_and_b32 %0, 0x30, %5\n\tv_lshrrev_b32 %1, 2, %5\n\tv_and_b32 %1, 0xC0, %1\n\tv_or_b32 %0, %0, %1\n\t"
                     "v_and_b32 %1, 0x40, %5\n\tv_lshl_or_b32 %0, %1, 9, %0\n\tv_and_b32 %1, 0x80, %5\n\tv_lshl_or_b32 %0, %1, 24, %0"
                     : "=&v"(ov), "=&v"(ot), "+v"(o0), "+v"(o1), "+v"(inv) : "v"(l16));
        ST(6);
        arrive2(rs, l16, vso, vmv, cap);
        ST(7);
        const f32x4 n0 = mfma16n(u32x2{vmv[0], vmv[1]}, pm, f32x4{0.f, 0.f, 0.f, 0.f}), n1 = mfma16n(u32x2{vmv[2], vmv[3]}, pm, f32x4{0.f, 0.f, 0.f, 0.f});
        {
          const u32x4 v = {nz(pack2(fmaf(n0[0], inv, o0[0]), fmaf(n0[1], inv, o0[1]))), nz(pack2(fmaf(n0[2], inv, o0[2]), fmaf(n0[3], inv, o0[3]))),
                           nz(pack2(fmaf(n1[0], inv, o1[0]), fmaf(n1[1], inv, o1[1]))), nz(pack2(fmaf(n1[2], inv, o1[2]), fmaf(n1[3], inv, o1[3])))};
          stx16(rs, ov, X_OX + s * RSTR + h * 1024 + w * 256, v);
        }
        if (w == 3) { const unsigned t = max(rt32(), 1u); if (l16 == 0) stx4(rs, 0, X_TS + (unsigned)(s + 1) * RSTR + (unsigned)(64 + h) * 128, t); }
        ST(8);
      }
    } else {
      LBAR();
      LAS unsigned char* qb = lad(qoff, lbase + 4096 + (w - 4) * 1024);
      u32x4 kf = __builtin_bit_cast(u32x4, cat8(trread(qb), trread(qb + 32)));
#pragma unroll
      for (int i = 0; i < 4; ++i) kf[i] = pack2(dpp_add<0x128>(lo2f(kf[i])), dpp_add<0x128>(hi2f(kf[i])));
      stx16(rs, l16, WS_KM + (unsigned)(s * NH + h) * 4096 + (w - 4) * 1024, kf);
      WAIT_V(0);
    }
    LBAR();
    if (w == 4 && l16 == 0) stx4(rs, 0, F_CH + h * 128, (unsigned)s + 1u);
    ST(9);
    if (w == 7) { retime(rs, l16, s, tt, p, cap); advance(tt, p); }
    if (s + 1 < NSEG) {
      unsigned fo;
      asm volatile("v_and_b32 %0, 16, %1\n\tv_lshlrev_b32 %0, 8, %0" : "=v"(fo) : "v"(l16));
      wait_ge(rs, fo, F_ST + (unsigned)(P_PJ + ((s + 1) & 1) * 64 + h) * 128, (unsigned)s + 2u, cap);
      dma16x(rs, l16, WS_K + (unsigned)((s + 1) * NH + h) * 8192 + w * 1024, lbase + R_KT + w * 1024);
    }
  }
  ST_FLUSH();
  if (cap == 0u && l16 == 0) stx4(rs, 0, X_CTL, 1u);
}

__device__ __forceinline__ void role_vb(const MainP& p, LAS unsigned char* lds, int rg, int cb) {
  const int tid = threadIdx.x, lane = tid & 63, w = __builtin_amdgcn_readfirstlane(tid >> 6);
  const int kh = w >> 2, wr = (w + cb) & 7;
  const __amdgpu_buffer_rsrc_t rs = mk_rs(p.ws, WS_END);
  const unsigned lbase = (unsigned)(size_t)lds;
  unsigned l16 = lane * 16, l8 = lane * 8, moff = mem_off(lane), roff, soff = (unsigned)((lane >> 5) * 256 + (lane & 31) * 4);
  const int sidx = (lane & 8) ? 0xEEEE : 0x4444;
  bf16x16 WV[8], WB[16];
  {
    const int fr = lane & 15, g = lane >> 4, tu = 4 * (g & 1) + (fr >> 2), ub = lane >> 3;
    { const int sl = mem_slot(16 * (g >> 1) + fr); roff = R_RED + w * 512 + ((sl >> 5) * 32 + (sl & 15) * 2 + (g & 1)) * 8; }
    if (w == 0) {
      const int sl = (lane >> 1) & 15, col = cb * 32 + ((sl >> 2) & 1) * 16 + (sl >> 3) * 8 + (lane >> 5) * 4 + (sl & 1) * 2 + ((sl >> 1) & 1), r = rg * 4 + (lane & 1) * 2;
      stx4(rs, soff, X_MEM + (cb >> 1) * 1024 + rg * 512 + ((cb ^ rg) & 1) * 128, nz((unsigned)p.mem0[r * D_ + col] | ((unsigned)p.mem0[(r + 1) * D_ + col] << 16)));
    }
    const __amdgpu_buffer_rsrc_t rv = mk_rs(p.Wv, D_ * HD * 2), ro = mk_rs(p.Wo, D_ * HD * 2);
    const unsigned vv = (unsigned)(ub * HD + 16 * (((lane >> 1) & 3) ^ (ub >> 1)) + 8 * (lane & 1)) * 2u, sv = (unsigned)(8 * w * HD + cb * DH + rg * 64) * 2u;
#pragma unroll
    for (int c = 0; c < 16; ++c) dma16(rv, vv, sv + (unsigned)c * 64 * HD * 2, lbase + R_W + c * 8192 + w * 1024);
    WAIT_V(0); BAR();
    const unsigned lo = (g >> 1) * 1024 + tu * 128 + (((tu >> 1) ^ (w & 3)) << 5) + (fr & 3) * 8;
#pragma unroll
    for (int c = 0; c < 8; ++c) WV[c] = w_frag(lds + R_W + (kh * 8 + c) * 8192 + lo);
    LBAR();
    const int u = (lane >> 2) & 7;
    const unsigned vw = (unsigned)((16 * (u >> 2) + 8 * ((u >> 1) & 1) + 4 * (lane >> 5) + (u & 1)) * D_ + (lane & 3) * 8) * 2u;
    const unsigned sw = (unsigned)(wr * 512 * D_ + cb * 32) * 2u;
    const unsigned bo = (g >> 1) * 1024 + tu * 64 + (fr & 3) * 8;
    auto stageo = [&](int hd) {
#pragma unroll
      for (int q = 0; q < 8; ++q)
        dma16(ro, vw, sw + (unsigned)((hd * 128 + 64 * (q >> 2) + 32 * (q & 1) + 2 * ((q >> 1) & 1)) * D_) * 2u, lbase + R_W + w * 16384 + (hd & 1) * 8192 + q * 1024);
    };
    stageo(0); stageo(1);
#pragma unroll
    for (int hd = 0; hd < 4; ++hd) {
      if (hd < 3) wait_v<8>(); else wait_v<0>();
#pragma unroll
      for (int f = 0; f < 4; ++f) WB[hd * 4 + f] = w_frag(lds + R_W + w * 16384 + (hd & 1) * 8192 + (f >> 1) * 512 + (f & 1) * 32 + bo);
      WAIT_L0(); SCHED();
      if (hd < 2) stageo(hd + 2);
    }
  }
  WST(rs, 176 + rg * 8 + (cb >> 2), 4);
  asm volatile("" : "+v"(l16), "+v"(l8), "+v"(moff), "+v"(roff), "+v"(soff));
  unsigned cap = SPIN_CAP;
  Sched tt; tt.tau = rt32() - 100000; tt.prev = tt.tau; tt.ea = 195 * 8; tt.eb = 118 * 8; tt.d1 = 0; tt.moved = true;
  const int cu_ = 32 + (rg * 32 + cb) / 2; (void)cu_;
  ST_DECL
  for (int s = 0; s < NSEG; ++s) {
    if (w == 7) wait_until(tt.tau);
    BAR();
    ST0();
    gather_mem(rs, lbase, s, wr, l16, cap);
    ST(1);
    LBAR();
    ST(2);
    {
      f32x4 c0 = {0.f, 0.f, 0.f, 0.f};
      LAS unsigned char* mp = lad(moff, lbase + R_MEM + kh * 8192);
      bf16x8 af[8];
#pragma unroll
      for (int c = 0; c < 8; ++c) af[c] = mem_frag(mp, c);
      SCHED();
#pragma unroll
      for (int c = 0; c < 8; ++c) c0 = smf(af[c], WV[c], c0, sidx);
      const f32x4 f = fold(c0, c0);
      if (l16 < 512) stx8(rs, l8, X_VM + (unsigned)s * RSTR + cb * 4096 + (rg * 4 + (w & 3)) * 512 + kh * 256, u32x2{nz(pack2(f[0], f[1])), nz(pack2(f[2], f[3]))});
    }
    ST(3);
    if (w == 7) retime(rs, l16, s, tt, p, cap);
    if (s + 1 < NSEG) {
      u32x4 a[4];
      const unsigned so = X_OX + s * RSTR + rg * 32768 + wr * 4096;
      if (w == 7) wait_until(tt.tau + tt.d1);
      BAR();
      ST(4);
      issue4(rs, l16, so, so + 1024, so + 2048, so + 3072, a[0], a[1], a[2], a[3]);
      f32x4 c[4];
#pragma unroll
      for (int t = 0; t < 4; ++t) c[t] = f32x4{0.f, 0.f, 0.f, 0.f};
#pragma unroll
      for (int i = 0; i < 4; ++i) {
        if (i == 0) arrive<0>(rs, l16, so, a[0], cap); else if (i == 1) arrive<1>(rs, l16, so + 1024, a[1], cap);
        else if (i == 2) arrive<2>(rs, l16, so + 2048, a[2], cap); else arrive<3>(rs, l16, so + 3072, a[3], cap);
#pragma unroll
        for (int t = 0; t < 4; ++t) c[t] = smf(__builtin_bit_cast(bf16x8, a[i]), WB[i * 4 + t], c[t], sidx);
        SCHED();
      }
      ST(5);
      {
        const bool odd = (l16 & 256) != 0;
        const f32x4 f = fold(odd ? c[2] : c[0], odd ? c[3] : c[1]);
        const auto e0 = __builtin_amdgcn_permlane16_swap(f2u(f[0]), f2u(f[2]), false, false), e1 = __builtin_amdgcn_permlane16_swap(f2u(f[1]), f2u(f[3]), false, false);
        *(LAS f32x2*)(lad(roff, lbase)) = f32x2{u2f(e0[0]) + u2f(e0[1]), u2f(e1[0]) + u2f(e1[1])};
      }
      ST(6);
      LBAR();
      ST(7);
      if (w == 0) {
        LAS unsigned char* rp = lad(l8, lbase + R_RED);
        f32x2 v[8];
#pragma unroll
        for (int i = 0; i < 8; ++i) v[i] = *(const LAS f32x2*)(rp + i * 512);
        const f32x2 sum = ((v[0] + v[1]) + (v[2] + v[3])) + ((v[4] + v[5]) + (v[6] + v[7]));
        stx4(rs, soff, X_MEM + (s + 1) * RSTR + (cb >> 1) * 1024 + rg * 512 + ((cb ^ rg) & 1) * 128, nz(pack2(sum[0], sum[1])));
        const unsigned t = max(rt32(), 1u);
        if (l16 == 0) stx4(rs, 0, X_TS + (unsigned)(s + 1) * RSTR + (unsigned)(rg * 32 + cb) * 128, t);
      }
      ST(8);
    }
    advance(tt, p);
  }
  ST_FLUSH();
  if (cap == 0u && l16 == 0) stx4(rs, 0, X_CTL, 1u);
}

#if defined(__HIP_DEVICE_COMPILE__)
__attribute__((target("no-packed-fp32-ops")))
#endif
__global__ void __launch_bounds__(512, 2) k_main(MainP p) {
  extern __shared__ __attribute__((aligned(16))) unsigned char smem[];
  const int b = blockIdx.x, x = b & 7, j = b >> 3;
  if (x >= 3) worker(p, (LAS unsigned char*)smem, x - 3, j);
  else if (x == 0) role_a(p, (LAS unsigned char*)smem, j);
  else role_vb(p, (LAS unsigned char*)smem, x - 1, j);
}

extern "C" void kernel_launch(void* const* d_in, const int* in_sizes, int n_in, void* d_out, int out_size, void* d_ws, size_t ws_size,
                              hipStream_t stream) {
  unsigned char* ws = (unsigned char*)d_ws;
  (void)hipMemsetAsync(ws, 0, X_ZERO, stream);
  MainP p;
  p.x = (const u16*)d_in[0]; p.mem0 = (const u16*)d_in[1]; p.c = (const u16*)d_in[12]; p.wmod = (const u16*)d_in[13]; p.Wq = (const u16*)d_in[2]; p.Wk = (const u16*)d_in[3]; p.Wv = (const u16*)d_in[4]; p.Wo = (const u16*)d_in[5];
  p.w1 = (const u16*)d_in[8]; p.w2 = (const u16*)d_in[9]; p.w3 = (const u16*)d_in[10]; p.w4 = (const u16*)d_in[11];
  p.out = (u16*)d_out; p.ws = ws; p.d1 = 10; p.d2 = 10; p.d3 = 2; p.d4 = 100;
  (void)hipFuncSetAttribute((const void*)k_main, hipFuncAttributeMaxDynamicSharedMemorySize, R_LDS);
  k_main<<<256, 512, R_LDS, stream>>>(p);
}
```

```cpp
#include <hip/hip_runtime.h>
#include <hip/hip_bf16.h>
#include <cstdlib>
#pragma clang diagnostic ignored "-Winline-asm"

#define LAS __attribute__((address_space(3)))
#define GAS __attribute__((address_space(1)))
typedef unsigned short u16;
typedef __bf16 bf16x2 __attribute__((ext_vector_type(2)));
typedef __bf16 bf16x4 __attribute__((ext_vector_type(4)));
typedef __bf16 bf16x8 __attribute__((ext_vector_type(8)));
typedef __bf16 bf16x16 __attribute__((ext_vector_type(16)));
typedef short s16x4 __attribute__((ext_vector_type(4)));
typedef float f32x2 __attribute__((ext_vector_type(2)));
typedef float f32x4 __attribute__((ext_vector_type(4)));
typedef float f32x16 __attribute__((ext_vector_type(16)));
typedef unsigned u32x2 __attribute__((ext_vector_type(2)));
typedef unsigned u32x4 __attribute__((ext_vector_type(4)));

constexpr int S_ = 2048, D_ = 1024, NH = 32, DH = 128, HD = 4096, NSEG = 64, FF = 3584;

constexpr unsigned MB = 1u << 20;
constexpr unsigned F_LN = 0;
constexpr unsigned F_ST = 24576;
constexpr unsigned F_CH = F_ST + 81920;
constexpr unsigned X_CTL = F_CH + 4096;
constexpr unsigned X_MOD = X_CTL + 4096;
constexpr unsigned X_REC = X_MOD + 24576;
constexpr unsigned RSTR = 225280;
constexpr int ZA = 6;
constexpr unsigned X_MEM = X_REC;
constexpr unsigned X_OX = X_REC + 16384;
constexpr unsigned X_VM = X_REC + 81920;
constexpr unsigned X_TS = X_REC + 212992;
constexpr unsigned X_ZERO = X_REC + ZA * RSTR;
constexpr unsigned WS_H = 16 * MB;
constexpr unsigned WS_Q = 20 * MB;
constexpr unsigned WS_K = 36 * MB;
constexpr unsigned WS_VT = 52 * MB;
constexpr unsigned WS_O = 68 * MB;
constexpr unsigned WS_X1 = 84 * MB;
constexpr unsigned WS_H2 = 88 * MB;
constexpr unsigned WS_G = 92 * MB;
constexpr unsigned WS_H1S = 96 * MB;
constexpr unsigned WS_X2 = 101 * MB;
constexpr unsigned WS_ST5 = 106 * MB;
constexpr unsigned WS_U = 108 * MB;
constexpr unsigned WS_KM = 124 * MB;
constexpr unsigned WS_P2 = 132 * MB;
constexpr unsigned WS_P8 = 164 * MB;
constexpr unsigned WS_END = 198 * MB;

__device__ __forceinline__ float bf2f(unsigned short b) { return __builtin_bit_cast(float, (unsigned)b << 16); }
__device__ __forceinline__ unsigned f2u(float f) { return __builtin_bit_cast(unsigned, f); }
__device__ __forceinline__ float u2f(unsigned u) { return __builtin_bit_cast(float, u); }
__device__ __forceinline__ float lo2f(unsigned w) { return __builtin_bit_cast(float, w << 16); }
__device__ __forceinline__ float hi2f(unsigned w) { return __builtin_bit_cast(float, w & 0xFFFF0000u); }
__device__ __forceinline__ unsigned pack2(float a, float b) { bf16x2 v; v[0] = (__bf16)a; v[1] = (__bf16)b; return __builtin_bit_cast(unsigned, v); }
__device__ __forceinline__ float rbf(float a) { return (float)(__bf16)a; }
__device__ __forceinline__ unsigned nz(unsigned w) { return max(w, 1u); }
__device__ __forceinline__ float gelu_tanh(float x) {
  const float e = __builtin_amdgcn_exp2f(x * fmaf(x * x, -0.1029432397f, -2.302208198f));
  return x * __builtin_amdgcn_rcpf(1.0f + e);
}
template <int CTRL> __device__ __forceinline__ float dpp_add(float v) { return v + u2f(__builtin_amdgcn_update_dpp(0u, f2u(v), CTRL, 0xF, 0xF, true)); }
__device__ __forceinline__ float wave_sum(float v) {
  v = dpp_add<0xB1>(v); v = dpp_add<0x4E>(v); v = dpp_add<0x141>(v); v = dpp_add<0x140>(v);
  auto t = __builtin_amdgcn_permlane16_swap(f2u(v), f2u(v), false, false);
  v = u2f(t[0]) + u2f(t[1]);
  t = __builtin_amdgcn_permlane32_swap(f2u(v), f2u(v), false, false);
  return u2f(t[0]) + u2f(t[1]);
}
#define WAIT_V(n) asm volatile("s_waitcnt vmcnt(" #n ")" ::: "memory")
#define WAIT_L0() asm volatile("s_waitcnt lgkmcnt(0)" ::: "memory")
#define BAR() do { asm volatile("" ::: "memory"); __builtin_amdgcn_s_barrier(); asm volatile("" ::: "memory"); } while (0)
#define LBAR() do { WAIT_L0(); BAR(); } while (0)
template <int N> __device__ __forceinline__ void wait_v() { asm volatile("s_waitcnt vmcnt(%0)" :: "n"(N) : "memory"); }
__device__ __forceinline__ void dma16(__amdgpu_buffer_rsrc_t rs, unsigned voff, unsigned soff, unsigned ldsa) {
  asm volatile("s_mov_b32 m0, %0\n\ts_nop 3\n\tbuffer_load_dwordx4 %1, %2, %3 offen lds" :: "s"(ldsa), "v"(voff), "s"(rs), "s"(soff) : "m0", "memory");
}
__device__ __forceinline__ void dma16x(__amdgpu_buffer_rsrc_t rs, unsigned voff, unsigned soff, unsigned ldsa) {
  asm volatile("s_mov_b32 m0, %0\n\ts_nop 3\n\tbuffer_load_dwordx4 %1, %2, %3 offen sc1 lds" :: "s"(ldsa), "v"(voff), "s"(rs), "s"(soff) : "m0", "memory");
}
__device__ __forceinline__ u32x4 ldx16(__amdgpu_buffer_rsrc_t rs, unsigned voff, unsigned soff) { return __builtin_amdgcn_raw_buffer_load_b128(rs, voff, soff, 16); }
__device__ __forceinline__ u32x2 ldx8(__amdgpu_buffer_rsrc_t rs, unsigned voff, unsigned soff) { return __builtin_amdgcn_raw_buffer_load_b64(rs, voff, soff, 16); }
__device__ __forceinline__ unsigned ldx4(__amdgpu_buffer_rsrc_t rs, unsigned voff, unsigned soff) { return __builtin_amdgcn_raw_buffer_load_b32(rs, voff, soff, 16); }
__device__ __forceinline__ void stx16(__amdgpu_buffer_rsrc_t rs, unsigned voff, unsigned soff, u32x4 v) {
  __builtin_amdgcn_raw_buffer_store_b128(v, rs, voff, soff, 16);
  asm volatile("s_nop 1" :: "v"(v) : "memory");
}
__device__ __forceinline__ void stx8(__amdgpu_buffer_rsrc_t rs, unsigned voff, unsigned soff, u32x2 v) { __builtin_amdgcn_raw_buffer_store_b64(v, rs, voff, soff, 16); }
__device__ __forceinline__ void stx4(__amdgpu_buffer_rsrc_t rs, unsigned voff, unsigned soff, unsigned v) { __builtin_amdgcn_raw_buffer_store_b32(v, rs, voff, soff, 16); }
__device__ __forceinline__ void wait_ge(__amdgpu_buffer_rsrc_t rs, unsigned voff, unsigned soff, unsigned need, unsigned& cap) {
  unsigned t;
  asm volatile("s_nop 4\n\t"
               "1:\n\t"
               "buffer_load_dword %[t], %[vo], %[rs], %[so] offen sc1\n\t"
               "s_waitcnt vmcnt(0)\n\t"
               "v_cmp_gt_u32 vcc, %[need], %[t]\n\t"
               "s_cmp_eq_u64 vcc, 0\n\t"
               "s_cbranch_scc1 2f\n\t"
               "s_sleep 2\n\t"
               "s_sub_u32 %[cap], %[cap], 1\n\t"
               "s_cbranch_scc0 1b\n\t"
               "s_mov_b32 %[cap], 0\n\t"
               "2:"
               : [t] "=&v"(t), [cap] "+s"(cap) : [vo] "v"(voff), [rs] "s"(rs), [so] "s"(soff), [need] "s"(need) : "vcc", "scc", "memory");
}
__device__ __forceinline__ void wait_seen(__amdgpu_buffer_rsrc_t rs, unsigned voff, unsigned soff, unsigned need, unsigned& seen, unsigned& cap) {
  if (seen >= need) return;
  unsigned t;
  asm volatile("s_nop 4\n\t"
               "1:\n\t"
               "buffer_load_dword %[t], %[vo], %[rs], %[so] offen sc1\n\t"
               "s_waitcnt vmcnt(0)\n\t"
               "v_cmp_gt_u32 vcc, %[need], %[t]\n\t"
               "s_cbranch_vccz 2f\n\t"
               "s_sleep 1\n\t"
               "s_sub_u32 %[cap], %[cap], 1\n\t"
               "s_cbranch_scc0 1b\n\t"
               "s_mov_b32 %[cap], 0\n\t"
               "2:"
               : [t] "=&v"(t), [cap] "+s"(cap) : [vo] "v"(voff), [rs] "s"(rs), [so] "s"(soff), [need] "s"(need) : "vcc", "scc", "memory");
  seen = min((unsigned)__builtin_amdgcn_readlane(t, 0), (unsigned)__builtin_amdgcn_readlane(t, 1));
}
__device__ __forceinline__ __amdgpu_buffer_rsrc_t mk_rs(const void* p, unsigned bytes) { return __builtin_amdgcn_make_buffer_rsrc((void*)p, 0, (int)bytes, 0x00020000); }
#define SCHED() __builtin_amdgcn_sched_barrier(0)
__device__ __forceinline__ unsigned now32() { unsigned long long t; asm volatile("s_memtime %0\n\ts_waitcnt lgkmcnt(0)" : "=s"(t) :: "memory"); return (unsigned)t; }
__device__ __forceinline__ unsigned rt32() { unsigned long long t; asm volatile("s_memrealtime %0\n\ts_waitcnt lgkmcnt(0)" : "=s"(t) :: "memory"); return (unsigned)t; }
__device__ __forceinline__ void wait_until(unsigned t) { while ((int)(rt32() - t) < 0) {} }
template <int X = 0> __device__ __forceinline__ LAS unsigned char* lad(unsigned v, unsigned u) {
  unsigned a;
  if (X == 0) asm volatile("v_add_u32 %0, %1, %2" : "=v"(a) : "s"(u), "v"(v));
  else asm volatile("v_xor_b32 %0, %3, %2\n\tv_add_u32 %0, %1, %0" : "=&v"(a) : "s"(u), "v"(v), "n"(X));
  return (LAS unsigned char*)(size_t)a;
}
__device__ __forceinline__ bf16x4 trread(LAS unsigned char* p) { return __builtin_amdgcn_ds_read_tr16_b64_v4bf16((LAS bf16x4*)p); }
__device__ __forceinline__ bf16x8 cat8(bf16x4 a, bf16x4 b) { return __builtin_shufflevector(a, b, 0, 1, 2, 3, 4, 5, 6, 7); }
__device__ __forceinline__ bf16x16 cat16(bf16x8 a, bf16x8 b) { return __builtin_shufflevector(a, b, 0, 1, 2, 3, 4, 5, 6, 7, 8, 9, 10, 11, 12, 13, 14, 15); }
__device__ __forceinline__ f32x4 mfma32(bf16x8 a, bf16x8 b, f32x4 c) { return __builtin_amdgcn_mfma_f32_16x16x32_bf16(a, b, c, 0, 0, 0); }
__device__ __forceinline__ f32x4 mfma16(u32x2 a, u32x2 b, f32x4 c) {
  return mfma32(__builtin_bit_cast(bf16x8, u32x4{a[0], a[1], 0u, 0u}), __builtin_bit_cast(bf16x8, u32x4{b[0], b[1], 0u, 0u}), c);
}
__device__ __forceinline__ f32x4 mfma16n(u32x2 a, u32x2 b, f32x4 c) { return __builtin_amdgcn_mfma_f32_16x16x16bf16_1k(__builtin_bit_cast(s16x4, a), __builtin_bit_cast(s16x4, b), c, 0, 0, 0); }
__device__ __forceinline__ f32x4 smf(bf16x8 a, bf16x16 b, f32x4 c, int idx) { return __builtin_amdgcn_smfmac_f32_16x16x64_bf16(a, b, c, idx, 0, 0); }

__device__ __forceinline__ void ln_row(float (&x)[16]) {
  float s = 0.f;
#pragma unroll
  for (int i = 0; i < 16; ++i) s += x[i];
  const float mean = wave_sum(s) * (1.0f / 1024.0f);
  float q = 0.f;
#pragma unroll
  for (int i = 0; i < 16; ++i) { const float d = x[i] - mean; q += d * d; }
  const float var = wave_sum(q) * (1.0f / 1024.0f);
  const float m = rbf(mean), isd = __builtin_amdgcn_rcpf(rbf(__builtin_amdgcn_sqrtf(rbf(rbf(var) + 1e-5f))));
#pragma unroll
  for (int i = 0; i < 16; ++i) x[i] = rbf(rbf(x[i] - m) * isd);
}
__device__ __forceinline__ void load_row16(const u16* rowp, int lane, float (&x)[16]) {
  const u32x4 a = *(const u32x4*)(rowp + lane * 8), b = *(const u32x4*)(rowp + 512 + lane * 8);
#pragma unroll
  for (int i = 0; i < 4; ++i) { x[2 * i] = lo2f(a[i]); x[2 * i + 1] = hi2f(a[i]); x[8 + 2 * i] = lo2f(b[i]); x[9 + 2 * i] = hi2f(b[i]); }
}
__device__ __forceinline__ void store_row16(u16* rowp, int lane, const float (&x)[16]) {
  u32x4 a, b;
#pragma unroll
  for (int i = 0; i < 4; ++i) { a[i] = pack2(x[2 * i], x[2 * i + 1]); b[i] = pack2(x[8 + 2 * i], x[9 + 2 * i]); }
  *(u32x4*)(rowp + lane * 8) = a; *(u32x4*)(rowp + 512 + lane * 8) = b;
}
__host__ __device__ __forceinline__ int mem_slot(int dl) {
  return ((dl >> 2) & 1) * 32 + (((dl >> 5) & 1) * 2 + ((dl >> 3) & 1)) * 8 + (((dl >> 4) & 1) * 2 + (dl & 1)) * 2 + ((dl >> 1) & 1);
}

#define WST(rs, wk, i)
struct MainP { const u16 *x, *mem0, *Wq, *Wk, *Wv, *Wo, *w1, *w2, *w3, *w4, *c, *wmod; u16* out; unsigned char* ws; int d1, d2, d3, d4; };
constexpr unsigned SPIN_CAP = 1u << 18;

__device__ __forceinline__ float gmax(float v) {
  auto t = __builtin_amdgcn_permlane16_swap(f2u(v), f2u(v), false, false);
  v = fmaxf(u2f(t[0]), u2f(t[1]));
  t = __builtin_amdgcn_permlane32_swap(f2u(v), f2u(v), false, false);
  return fmaxf(u2f(t[0]), u2f(t[1]));
}
__device__ __forceinline__ float gsum(float v) {
  auto t = __builtin_amdgcn_permlane16_swap(f2u(v), f2u(v), false, false);
  v = u2f(t[0]) + u2f(t[1]);
  t = __builtin_amdgcn_permlane32_swap(f2u(v), f2u(v), false, false);
  return u2f(t[0]) + u2f(t[1]);
}
__device__ __forceinline__ void publish(__amdgpu_buffer_rsrc_t rs, unsigned word, unsigned val) {
  WAIT_V(0); BAR();
  if (threadIdx.x == 0) stx4(rs, 0, word, val);
}

constexpr int P_S2 = 0, P_S8 = 32, P_S7 = 64, P_S4 = 96, P_S5 = 104, P_S1 = 112, P_S3 = 144, P_PJ = 240;
constexpr int NLN = 160;

enum { SG_WO = 0, SG_W1 = 1, SG_W2 = 2, SG_W3 = 3, SG_W4 = 4, SG_K = 5, SG_V = 6, SG_Q = 7 };
constexpr int G_XCH = 131072, G_K = G_XCH + 16384, G_CTL = G_K + 8192;
__device__ __forceinline__ f32x16 mfma3232(bf16x8 a, bf16x8 b, f32x16 c) { return __builtin_amdgcn_mfma_f32_32x32x16_bf16(a, b, c, 0, 0, 0); }
__device__ __forceinline__ void widen32(const f32x16& a, u32x4 (&v)[2]) {
#pragma unroll
  for (int u = 0; u < 2; ++u) {
    const auto r0 = __builtin_amdgcn_permlane32_swap(pack2(a[8 * u], a[8 * u + 1]), pack2(a[8 * u + 4], a[8 * u + 5]), false, false);
    const auto r1 = __builtin_amdgcn_permlane32_swap(pack2(a[8 * u + 2], a[8 * u + 3]), pack2(a[8 * u + 6], a[8 * u + 7]), false, false);
    v[u] = u32x4{r0[0], r1[0], r0[1], r1[1]};
  }
}
__device__ __forceinline__ float hsum(float v) { const auto t = __builtin_amdgcn_permlane32_swap(f2u(v), f2u(v), false, false); return u2f(t[0]) + u2f(t[1]); }

__device__ __forceinline__ void attend(__amdgpu_buffer_rsrc_t rs, LAS unsigned char* qb, LAS unsigned char* kb, int seg, int h, int t, int eh, int lane, const u32x4 (&vv)[4],
                                       unsigned& chs, unsigned& cap) {
  const int fr = lane & 15, g = lane >> 4;
  const unsigned l16 = lane * 16, ub = (unsigned)(seg * NH + h);
  f32x4 s0 = {0.f, 0.f, 0.f, 0.f}, s1 = s0, s2 = s0;
  bf16x8 q[4];
#pragma unroll
  for (int ks = 0; ks < 4; ++ks) {
    q[ks] = *(const LAS bf16x8*)(qb + ks * 4096);
    s1 = mfma32(*(const LAS bf16x8*)(kb + ks * 1024), q[ks], s1); s2 = mfma32(*(const LAS bf16x8*)(kb + 4096 + ks * 1024), q[ks], s2);
  }
  const float scale = 0.08838834764831845f, L2E = 1.4426950408889634f;
  float a[4], b[4], c[4], mt = -3.0e38f, lt = 0.f;
#pragma unroll
  for (int r = 0; r < 4; ++r) {
    const int j = 4 * g + r;
    b[r] = (t == 1 || j <= fr) ? rbf(rbf(s1[r]) * scale) : -3.0e38f;
    c[r] = (t == 1 && j <= fr) ? rbf(rbf(s2[r]) * scale) : -3.0e38f;
    mt = fmaxf(mt, fmaxf(b[r], c[r]));
  }
  mt = gmax(mt);
#pragma unroll
  for (int r = 0; r < 4; ++r) { b[r] = __builtin_amdgcn_exp2f((b[r] - mt) * L2E); c[r] = __builtin_amdgcn_exp2f((c[r] - mt) * L2E); lt += b[r] + c[r]; }
  const u32x4 pt = {pack2(b[0], b[1]), pack2(b[2], b[3]), pack2(c[0], c[1]), pack2(c[2], c[3])};
  f32x4 o[4];
#pragma unroll
  for (int i = 0; i < 4; ++i) o[i] = mfma32(__builtin_bit_cast(bf16x8, vv[i]), __builtin_bit_cast(bf16x8, pt), f32x4{0.f, 0.f, 0.f, 0.f});
  lt = gsum(lt);
  const unsigned em = (unsigned)(fr >> 3) * 512 + (unsigned)g * 128 + (unsigned)(8 * ((fr >> 2) & 1) + (fr & 3)) * 8;
  asm volatile("" : "+v"(o[0]), "+v"(o[1]), "+v"(o[2]), "+v"(o[3]), "+v"(lt));
  wait_seen(rs, 0, F_CH + h * 128, (unsigned)seg + 1u, chs, cap);
  u32x2 vmf[4];
  for (;;) {
    asm volatile("" ::: "memory");
    unsigned mn = 0xFFFFFFFFu;
    bf16x8 kmf[4];
#pragma unroll
    for (int ks = 0; ks < 4; ++ks) kmf[ks] = __builtin_bit_cast(bf16x8, ldx16(rs, l16, WS_KM + ub * 4096 + ks * 1024));
#pragma unroll
    for (int i = 0; i < 4; ++i) vmf[i] = ldx8(rs, em, X_VM + (unsigned)seg * RSTR + h * 4096 + (2 * eh + (i >> 1)) * 1024 + (i & 1) * 32);
    s0 = f32x4{0.f, 0.f, 0.f, 0.f};
#pragma unroll
    for (int ks = 0; ks < 4; ++ks) s0 = mfma32(kmf[ks], q[ks], s0);
#pragma unroll
    for (int i = 0; i < 4; ++i) mn = min(mn, min(vmf[i][0], vmf[i][1]));
    if (__all(mn != 0u) || cap == 0u) break;
    cap = __builtin_amdgcn_readfirstlane(cap - 1u);
  }
  cap = __builtin_amdgcn_readfirstlane(cap);
#pragma unroll
  for (int r = 0; r < 4; ++r) a[r] = rbf(rbf(s0[r]) * scale);
  const float mx = fmaxf(mt, gmax(fmaxf(fmaxf(a[0], a[1]), fmaxf(a[2], a[3]))));
#pragma unroll
  for (int r = 0; r < 4; ++r) a[r] = __builtin_amdgcn_exp2f((a[r] - mx) * L2E);
  const u32x2 pm = {pack2(a[0], a[1]), pack2(a[2], a[3])};
  f32x4 om[4];
#pragma unroll
  for (int i = 0; i < 4; ++i) om[i] = mfma16n(vmf[i], pm, f32x4{0.f, 0.f, 0.f, 0.f});
  const float al = __builtin_amdgcn_exp2f((mt - mx) * L2E);
  const float inv = __builtin_amdgcn_rcpf(fmaf(al, lt, 0.5f * gsum((a[0] + a[1]) + (a[2] + a[3])))), ai = al * inv;
#pragma unroll
  for (int i = 0; i < 4; ++i) o[i] = o[i] * ai + om[i] * inv;
#pragma unroll
  for (int u = 0; u < 2; ++u)
    stx16(rs, l16, WS_O + (unsigned)seg * 262144 + (unsigned)((h * 4 + 2 * eh + u) * 2 + t) * 1024,
          u32x4{pack2(o[2 * u][0], o[2 * u][1]), pack2(o[2 * u][2], o[2 * u][3]), pack2(o[2 * u + 1][0], o[2 * u + 1][1]), pack2(o[2 * u + 1][2], o[2 * u + 1][3])});
}

template <int SG>
__device__ __forceinline__ void stage_gemm(LAS unsigned char* lds, __amdgpu_buffer_rsrc_t rs, const u16* Wg, int wk, int kq, int cb, int pid, unsigned& cap,
                                           const u16* side = nullptr, const u16* side2 = nullptr, int sld = 0, const u16* side3 = nullptr) {
  constexpr bool KV = (SG == SG_K || SG == SG_V), PROJ = KV || SG == SG_Q, ROWM = (SG == SG_W1 || PROJ);
  constexpr int KS = (SG == SG_W4) ? 28 : 32;
  constexpr int LDW = PROJ ? HD : (SG == SG_W3) ? FF : D_;
  constexpr int WROWS = (SG == SG_WO) ? HD : (SG == SG_W4 ? FF : D_);
  constexpr int NBLK = KV ? 32 : (SG == SG_WO || SG == SG_W1 || SG == SG_Q) ? 64 : 65;
  constexpr unsigned ABASE = PROJ ? WS_H : SG == SG_WO ? WS_O : SG == SG_W1 ? WS_G : SG == SG_W2 ? WS_H1S : SG == SG_W3 ? WS_X2 : WS_U;
  constexpr unsigned ASTR = SG == SG_WO ? 262144u : SG == SG_W4 ? 229376u : 65536u;
  constexpr int NPC = KS / 4;
  const unsigned myword = F_ST + (unsigned)pid * 128u;
  int tid = threadIdx.x; asm volatile("" : "+v"(tid));
  const int lane = tid & 63, w = __builtin_amdgcn_readfirstlane(tid >> 6), row = lane & 31, hf = lane >> 5;
  const int kh = w >> 2, wl = w & 3;
  const int koff = PROJ ? 0 : kq;
  const unsigned lbase = (unsigned)(size_t)lds, l16 = lane * 16;
  const int col0 = cb * 128 + wl * 32;
  bf16x8 Wf[KS];
  {
    const __amdgpu_buffer_rsrc_t rsW = mk_rs(Wg, (unsigned)WROWS * LDW * 2u);
    const int ub = lane >> 3, tu = 4 * hf + ((lane & 15) >> 2);
    const unsigned vB = (unsigned)(((ub & 3) + 8 * (ub >> 2)) * LDW + 16 * (((lane >> 1) & 3) ^ (ub >> 1)) + 8 * (lane & 1)) * 2u;
    const unsigned sB = (unsigned)((koff * KS * 32 + 32 * (w >> 2) + 16 * ((w >> 1) & 1) + 4 * (w & 1)) * LDW + cb * 128) * 2u;
    const int boff = kh * 65536 + (wl >> 1) * 8192 + tu * 128 + (((tu >> 1) ^ (2 * (wl & 1) + ((lane >> 4) & 1))) << 5) + (lane & 3) * 8;
#pragma unroll
    for (int ps = 0; ps < 2; ++ps) {
      BAR();
#pragma unroll
      for (int hh = 0; hh < 2; ++hh)
#pragma unroll
        for (int i = 0; i < 4; ++i)
          if (4 * ps + i < NPC && !(PROJ && ps == 0)) {
            const unsigned so = sB + (unsigned)((hh * NPC + 4 * ps + i) * 64 * LDW) * 2u, d = lbase + hh * 65536 + i * 16384 + w * 1024;
            dma16(rsW, vB, so, d); dma16(rsW, vB, so + 128, d + 8192);
          }
      WAIT_V(0); BAR();
#pragma unroll
      for (int i = 0; i < 4; ++i)
        if (4 * ps + i < NPC) {
#pragma unroll
          for (int k = 0; k < 4; ++k) { LAS unsigned char* q = lds + (boff + i * 16384 + k * 2048); Wf[(4 * ps + i) * 4 + k] = cat8(trread(q), trread(q + 1024)); }
        }
      WAIT_L0();
    }
    if (SG == SG_W1) {
      const u32x4 z = {0u, 0u, 0u, 0u};
      stx16(rs, (unsigned)tid * 16, WS_H1S + cb * 8192, z); stx16(rs, (unsigned)tid * 16, WS_H1S + 64 * 65536 + cb * 8192, z);
      WAIT_V(0);
    }
    if (tid < 8) *(LAS unsigned*)(lds + G_CTL + tid * 4) = 0u;
    LBAR();
  }
  const f32x16 zero16 = {0.f, 0.f, 0.f, 0.f, 0.f, 0.f, 0.f, 0.f, 0.f, 0.f, 0.f, 0.f, 0.f, 0.f, 0.f, 0.f};
  auto put16 = [&](LAS unsigned char* p, const f32x16& a) {
#pragma unroll
    for (int q = 0; q < 4; ++q) *(LAS f32x4*)(p + q * 1024) = f32x4{a[4 * q], a[4 * q + 1], a[4 * q + 2], a[4 * q + 3]};
  };
  if (SG == SG_W3) {
    const u32x4 one4 = {0x3F803F80u, 0x3F803F80u, 0x3F803F80u, 0x3F803F80u};
    f32x16 cs = zero16;
#pragma unroll
    for (int k = 0; k < KS; ++k) cs = mfma3232(Wf[k], __builtin_bit_cast(bf16x8, one4), cs);
    if (kh == 0) put16(lds + wl * 4096 + l16, cs);
    LBAR();
    if (kh == 1 && row == 0) {
#pragma unroll
      for (int q = 0; q < 4; ++q)
        *(LAS f32x4*)(lds + G_K + wl * 128 + hf * 64 + q * 16) = *(const LAS f32x4*)(lds + wl * 4096 + q * 1024 + l16) + f32x4{cs[4 * q], cs[4 * q + 1], cs[4 * q + 2], cs[4 * q + 3]};
    }
    LBAR();
  }
  auto blk = [&](int i) -> int { return KV ? 2 * i + kq : i; };
  const unsigned aoff = ROWM ? (unsigned)(row * 128 + ((hf ^ ((row >> 1) & 7)) << 4)) : (unsigned)((row >> 4) * 1024 + hf * 256 + (row & 15) * 16);
  const unsigned vA = ROWM ? (unsigned)((lane >> 3) * 2048 + (((lane & 7) ^ ((4 * wl + (lane >> 4)) & 7)) << 4)) : l16;
  auto piece = [&](int hb, int q, bool on) {
    const int i = hb >> 1, hh = hb & 1;
    const unsigned so = ABASE + (unsigned)blk(i) * ASTR + (unsigned)koff * (KS * 2048) + (ROWM ? wl * 16384 + hh * 1024 : (hh * KS + wl) * 1024);
    if (on) dma16x(rs, vA, so + q * (ROWM ? 128 : 4096), lbase + (i & 1) * 65536 + hh * 32768 + wl * 1024 + q * 4096);
  };
  auto fetch = [&](int hb) {
#pragma unroll
    for (int q = 0; q < NPC; ++q) piece(hb, q, true);
  };
  constexpr bool WEAVE = (SG == SG_W1 || SG == SG_W2 || SG == SG_W3);
  auto products = [&](int i, f32x16& acc, auto&& first, auto&& early, auto&& handover) {
    const unsigned ub = lbase + (i & 1) * 65536 + kh * 32768;
    LAS unsigned char* ab[4];
    ab[0] = lad(aoff, ub);
    if (ROWM) { ab[1] = lad<32>(aoff, ub); ab[2] = lad<64>(aoff, ub); ab[3] = lad<96>(aoff, ub); }
    constexpr int PD = 10;
    bf16x8 af[PD + 1];
    auto rd = [&](int k) { af[k % (PD + 1)] = ROWM ? *(const LAS bf16x8*)(ab[k & 3] + (k >> 2) * 4096) : *(const LAS bf16x8*)(ab[0] + (k >> 1) * 2048 + (k & 1) * 512); };
#pragma unroll
    for (int k = 0; k < PD; ++k) rd(k);
    SCHED();
#pragma unroll
    for (int k = 0; k < KS; ++k) {
      if (k + PD < KS) rd(k + PD);
      acc = SG == SG_V ? mfma3232(af[k % (PD + 1)], Wf[k], k ? acc : zero16) : mfma3232(Wf[k], af[k % (PD + 1)], k ? acc : zero16);
      SCHED();
      first(k);
      SCHED();
      if (k == KS - 10) { early(); SCHED(); }
      if (k == KS - 5) { handover(); SCHED(); }
    }
    asm volatile("" : "+v"(acc));
  };
  WST(rs, wk, KV ? 2 : 10);
#define SEG(x)
  if (kh == 0) {
    auto pvo = [&](int i) -> unsigned {
      const int b = blk(i);
      if (PROJ) return F_LN + (unsigned)((b % (NLN / 4)) * 4 + (lane & 3)) * 128u;
      if (SG == SG_WO) return F_ST + (unsigned)(P_S1 + kq * 8 + (lane & 7)) * 128u;
      if (SG == SG_W1) return F_ST + (unsigned)(P_S3 + (b % 3) * 32 + (lane & 31)) * 128u;
      if (SG == SG_W2) return F_ST + (unsigned)(P_S4 + (lane & 7)) * 128u;
      if (SG == SG_W3) return F_ST + (unsigned)(P_S5 + (lane & 7)) * 128u;
      return F_ST + (unsigned)(P_S7 + kq * 7 + min(lane & 7, 6)) * 128u;
    };
    auto need = [&](int i) -> unsigned { return PROJ ? 1u : (SG == SG_W2 && i == 64) ? 64u : (unsigned)i + 1u; };
    int R = 0, Rp = 0;
    for (int i = 0; i < NBLK; ++i) {
      if (R <= i) { wait_ge(rs, pvo(i), 0, need(i), cap); fetch(2 * i); fetch(2 * i + 1); }
      const unsigned pk = w == 0 ? (lane < 32 ? pvo(i + 1) : pvo(i + 2)) : 0xF0000000u;
      if (Rp <= i) { if (R <= i + 1) wait_v<NPC>(); else wait_v<2 * NPC>(); }
      BAR();
      SEG(1);
      f32x16 acc;
      unsigned peek;
      asm volatile("buffer_load_dword %0, %1, %2, 0 offen sc1" : "=v"(peek) : "v"(pk), "s"(rs) : "memory");
      u32x4 st[8];
      float mean = 0.f, q2 = 0.f;
      if (SG == SG_W3) {
#pragma unroll
        for (int q = 0; q < 8; ++q) st[q] = ldx16(rs, l16, WS_ST5 + (unsigned)i * 8192 + q * 1024);
      }
      products(i, acc, [&](int k) {
        if (SG == SG_W3) {
          if (k >= 18 && k < 26) { mean += u2f(st[k - 18][0]) + u2f(st[k - 18][2]); q2 += u2f(st[k - 18][1]) + u2f(st[k - 18][3]); asm volatile("" : "+v"(mean), "+v"(q2)); }
          if (k == 26) { mean = hsum(mean) * (1.0f / 1024.0f); q2 = hsum(q2); asm volatile("" : "+v"(mean), "+v"(q2)); }
          if (k == 28) { q2 = rbf(rbf(fmaxf(fmaf(-mean, mean, q2 * (1.0f / 1024.0f)), 0.f)) + 1e-5f); asm volatile("" : "+v"(q2)); }
          if (k == 29) { q2 = __builtin_amdgcn_rcpf(rbf(__builtin_amdgcn_sqrtf(q2))); mean = rbf(mean); asm volatile("" : "+v"(q2), "+v"(mean)); }
        }
      }, []() {}, [&]() {
        SEG(4);
        if (SG == SG_Q && R >= i + 2) fetch(2 * i + 3);
        WAIT_V(0);
        SEG(0);
        asm volatile("" : "+v"(peek));
        if (w == 0) {
          const unsigned long long ok = __ballot(peek >= (lane < 32 ? need(i + 1) : need(i + 2)));
          const bool r1 = (R >= i + 2 || (unsigned)ok == 0xFFFFFFFFu) && i + 1 < NBLK, r2 = r1 && (unsigned)(ok >> 32) == 0xFFFFFFFFu && i + 2 < NBLK;
          if (lane == 0) *(volatile LAS unsigned*)(lds + G_CTL + 4) = (unsigned)(i + 1 + (r1 ? 1 : 0) + (r2 ? 1 : 0));
        }
        LBAR();
      });
      SEG(2);
      const unsigned rv = *(const volatile LAS unsigned*)(lds + G_CTL + 4);
      if (SG == SG_W3) {
        const float m = mean;
        *(LAS float*)(lds + G_K + 1024 + wl * 256 + lane * 4) = q2;
#pragma unroll
        for (int q = 0; q < 4; ++q) {
          const f32x4 cs = *(const LAS f32x4*)(lds + G_K + wl * 128 + hf * 64 + q * 16);
#pragma unroll
          for (int e = 0; e < 4; ++e) acc[4 * q + e] = fmaf(-m, cs[e], acc[4 * q + e]);
        }
      }
      put16(lad(l16, lbase + G_XCH + wl * 4096), acc);
      asm volatile("" ::: "memory");
      if (lane == 0) *(volatile LAS unsigned*)(lds + G_CTL + 16 + wl * 4) = (unsigned)i + 1u;
      Rp = R; R = __builtin_amdgcn_readfirstlane(rv);
      SEG(3);
      if (R >= i + 2 && Rp < i + 2) { fetch(2 * i + 2); fetch(2 * i + 3); }
      if (R >= i + 3) fetch(2 * i + 4);
      if (KV) {
        if (wl < 2) dma16(mk_rs(side3, 0x7FFFFFFFu), (unsigned)((lane >> 2) * 6144 + (lane & 3) * 8) * 2u, (unsigned)((32 * i + 16 * wl) * 6144) * 2u, lbase + G_K);
        if (sld && 32 * i < (int)(side2 - side)) {
          const __amdgpu_buffer_rsrc_t rsN = mk_rs(side, 0x7FFFFFFFu);
#pragma unroll
          for (int q = 0; q < 2; ++q) dma16(rsN, (unsigned)((lane >> 4) * sld + (lane & 15) * 8) * 2u, (unsigned)((32 * i + 8 * wl + 4 * q) * sld) * 2u, lbase + G_K + wl * 2048 + q * 1024);
        }
      }
      SEG(5);
    }
    BAR();
  } else {
    bool pending = false;
    auto announce = [&](int i) {
      if (lane == 0) {
        const unsigned c = __hip_atomic_fetch_add((LAS unsigned*)(lds + G_CTL), 1u, __ATOMIC_RELAXED, __HIP_MEMORY_SCOPE_WORKGROUP);
        if ((c & 3u) == 3u) stx4(rs, 0, myword, (unsigned)blk(i) + 1u);
      }
    };
    unsigned kvs[2] = {0u, 0u}, chs = 0u;
    BAR();
    for (int i = 0; i < NBLK; ++i) {
      const int b = blk(i);
      if (SG == SG_Q) {
        if (i & 1) wait_seen(rs, (unsigned)(lane & 1) * 4096, F_ST + (unsigned)(P_PJ + 64 + cb) * 128, (unsigned)i + 1u, kvs[1], cap);
        else wait_seen(rs, (unsigned)(lane & 1) * 4096, F_ST + (unsigned)(P_PJ + cb) * 128, (unsigned)i + 1u, kvs[0], cap);
      }
      BAR();
      SEG(0);
      const unsigned rv = *(const volatile LAS unsigned*)(lds + G_CTL + 4);
      u32x4 res[2], vv[4];
      float isd;
      if (SG == SG_W2) {
#pragma unroll
        for (int u = 0; u < 2; ++u) {
          const int t = 32 * b - 2 + 16 * u + (lane >> 2);
          dma16x(rs, (t >= 0 && t < S_) ? (unsigned)(t * D_ + col0 + ((lane ^ (lane >> 3)) & 3) * 8) * 2u : 0xF0000000u, WS_H2, lbase + G_K + wl * 2048 + u * 1024);
        }
      }
      if (SG == SG_Q) {
        const unsigned ub = (unsigned)(b * NH + cb) * 8192;
        dma16x(rs, l16, WS_K + ub + wl * 2048, lbase + G_K + wl * 2048); dma16x(rs, l16, WS_K + ub + wl * 2048 + 1024, lbase + G_K + wl * 2048 + 1024);
        const unsigned ev = (unsigned)((lane >> 3) & 1) * 1024 + (unsigned)((lane >> 4) * 16 + 8 * ((lane >> 2) & 1) + (lane & 3)) * 16;
#pragma unroll
        for (int q = 0; q < 4; ++q) vv[q] = ldx16(rs, ev, WS_VT + ub + (2 * (wl >> 1) + (q >> 1)) * 2048 + (q & 1) * 64);
      }
      f32x16 acc;
      int R = 0;
      LAS unsigned char* xr = lad(l16, lbase + G_XCH + wl * 4096);
      f32x4 xa[4];
      unsigned xf;
      auto take = [&]() {
        asm volatile("" ::: "memory");
        xf = *(const volatile LAS unsigned*)(lds + G_CTL + 16 + wl * 4);
        asm volatile("" ::: "memory");
#pragma unroll
        for (int q = 0; q < 4; ++q) xa[q] = *(const LAS f32x4*)(xr + q * 1024);
        if (SG == SG_W3) isd = *(const LAS float*)(lds + G_K + 1024 + wl * 256 + lane * 4);
        asm volatile("" ::: "memory");
      };
      products(i, acc, [&](int k) {
        if (SG == SG_Q && k == 8 && i > 0) { wait_v<6>(); announce(i - 1); }
      }, [&]() {
        take();
      }, [&]() {
        SEG(5);
        R = __builtin_amdgcn_readfirstlane(rv);
        if (R >= i + 2) {
          WAIT_V(0);
          LBAR();
          if (pending && tid == 256) stx4(rs, 0, myword, (unsigned)blk(i - 1) + 1u);
        }
      });
      const bool more = R >= i + 2;
      SEG(1);
      while (__builtin_amdgcn_readfirstlane(xf) <= (unsigned)i) take();
#pragma unroll
      for (int q = 0; q < 16; ++q) { acc[q] += xa[q >> 2][q & 3]; if (SG == SG_W3) acc[q] *= isd; }
      SEG(2);
      const unsigned fo = (unsigned)((col0 >> 5) * 2048 + hf * 256), ro = (unsigned)((row >> 4) * 1024 + (row & 15) * 16);
      u32x4 v[2];
      if (SG == SG_W1 || SG == SG_W3) {
#pragma unroll
        for (int q = 0; q < 16; q += 2) {
          float g0 = gelu_tanh(acc[q]), g1 = gelu_tanh(acc[q + 1]);
          asm volatile("" : "+v"(g0), "+v"(g1));
          acc[q] = g0; acc[q + 1] = g1;
          piece(2 * i + 5, q >> 1, R >= i + 3 && i >= 2);
        }
      }
      if (SG != SG_V) widen32(acc, v);
      if (SG == SG_WO || SG == SG_W4) {
        LAS unsigned char* tw = lds + G_XCH + wl * 4096 + row * 64;
        *(LAS u32x4*)(tw + ((hf ^ (row >> 1)) & 3) * 16) = v[0]; *(LAS u32x4*)(tw + (((2 + hf) ^ (row >> 1)) & 3) * 16) = v[1];
        asm volatile("" ::: "memory");
        LAS unsigned char* tr = lds + G_XCH + wl * 4096 + (lane >> 2) * 64 + ((lane ^ (lane >> 3)) & 3) * 16;
        const unsigned so = (SG == SG_WO ? WS_P2 : WS_P8) + (unsigned)(b * 4 + kq) * 65536u, vo = (unsigned)((lane >> 2) * D_ + col0 + (lane & 3) * 8) * 2u;
        stx16(rs, vo, so, *(const LAS u32x4*)(tr)); stx16(rs, vo, so + 32768, *(const LAS u32x4*)(tr + 1024));
      } else if (SG == SG_K) {
        const unsigned so = WS_K + (unsigned)(b * NH + cb) * 8192 + wl * 1024, vo = (unsigned)((row >> 4) * 4096 + hf * 256 + (row & 15) * 16);
        stx16(rs, vo, so, v[0]); stx16(rs, vo, so + 512, v[1]);
      } else if (SG == SG_V) {
        const unsigned so = WS_VT + (unsigned)(b * NH + cb) * 8192 + wl * 2048, vo = ro + hf * 256;
#pragma unroll
        for (int u = 0; u < 2; ++u)
          stx16(rs, vo, so + u * 512, u32x4{pack2(acc[4 * u], acc[4 * u + 1]), pack2(acc[4 * u + 2], acc[4 * u + 3]), pack2(acc[8 + 4 * u], acc[9 + 4 * u]), pack2(acc[10 + 4 * u], acc[11 + 4 * u])});
      } else if (SG == SG_Q) {
        LAS unsigned char* qw = lds + G_XCH + wl * 4096 + (row >> 4) * 1024 + hf * 256 + (row & 15) * 16;
        *(LAS u32x4*)(qw) = v[0]; *(LAS u32x4*)(qw + 512) = v[1];
        WAIT_V(0);
        if (lane == 0) __hip_atomic_fetch_add((LAS unsigned*)(lds + G_CTL + 8), 1u, __ATOMIC_RELAXED, __HIP_MEMORY_SCOPE_WORKGROUP);
        while (__builtin_amdgcn_readfirstlane(*(const volatile LAS unsigned*)(lds + G_CTL + 8)) < 4u * (unsigned)i + 4u) {}
        asm volatile("" ::: "memory");
        attend(rs, lds + G_XCH + (wl & 1) * 1024 + l16, lds + G_K + l16, b, cb, wl & 1, wl >> 1, lane, vv, chs, cap);
      } else if (SG == SG_W1) {
        const int grp = cb >> 1, sh = (grp & 1) ? -(1 + (grp >> 1)) : (1 + (grp >> 1));
        const int rho = 32 * b + row + sh + 2;
        const unsigned vo = (rho >= 2 && rho < S_ + 2) ? (unsigned)(rho >> 5) * 65536u + fo + (unsigned)((rho >> 4) & 1) * 1024u + (unsigned)(rho & 15) * 16u : 0xF0000000u;
        stx16(rs, vo, WS_H1S, v[0]); stx16(rs, vo, WS_H1S + 512, v[1]);
      } else if (SG == SG_W2) {
        if (!more) WAIT_V(0);
        res[0] = *(const LAS u32x4*)(lds + G_K + wl * 2048 + row * 64 + ((hf ^ (row >> 1)) & 3) * 16);
        res[1] = *(const LAS u32x4*)(lds + G_K + wl * 2048 + row * 64 + (((2 + hf) ^ (row >> 1)) & 3) * 16);
        float zs = 0.f, q2 = 0.f;
#pragma unroll
        for (int q = 0; q < 8; ++q) {
          const unsigned x = pack2(lo2f(res[q >> 2][q & 3]) + lo2f(v[q >> 2][q & 3]), hi2f(res[q >> 2][q & 3]) + hi2f(v[q >> 2][q & 3]));
          v[q >> 2][q & 3] = x; zs += lo2f(x) + hi2f(x); q2 = fmaf(lo2f(x), lo2f(x), fmaf(hi2f(x), hi2f(x), q2));
          asm volatile("" : "+v"(zs), "+v"(q2));
          piece(2 * i + 5, q, R >= i + 3 && i >= 2);
        }
        stx16(rs, fo + ro, WS_X2 + (unsigned)b * 65536u, v[0]); stx16(rs, fo + ro, WS_X2 + (unsigned)b * 65536u + 512, v[1]);
        const float mw = hsum(zs);
        q2 = hsum(q2);
        if (hf == 0) stx8(rs, (unsigned)((cb * 2 + (wl >> 1)) * 512 + row * 16 + (wl & 1) * 8), WS_ST5 + (unsigned)b * 8192, u32x2{f2u(mw), f2u(q2)});
      } else {
        stx16(rs, fo + ro, WS_U + (unsigned)b * 229376u, v[0]); stx16(rs, fo + ro, WS_U + (unsigned)b * 229376u + 512, v[1]);
      }
      if (KV && 2 * i + ZA + (wl >> 1) <= NSEG && wk * 2 + (wl & 1) < 220) stx16(rs, l16, X_REC + (unsigned)(2 * i + ZA + (wl >> 1)) * RSTR + (unsigned)(wk * 2 + (wl & 1)) * 1024, u32x4{0u, 0u, 0u, 0u});
      SEG(3);
      pending = R >= i + 3 && i >= 2 && SG != SG_Q;
      if (SG == SG_Q ? i == NBLK - 1 : !pending) { WAIT_V(0); announce(i); }
      if (SG != SG_Q && R >= i + 3 && !(WEAVE && i >= 2)) fetch(2 * i + 5);
      if (!KV) WST(rs, wk, 16 + b);
      SEG(4);
      if (!more) BAR();
    }
  }
}

__device__ __forceinline__ void wait32(__amdgpu_buffer_rsrc_t rs, int first, int mine, int lane, unsigned need, unsigned& cap) {
  wait_ge(rs, 0, F_ST + (unsigned)(first + mine) * 128u, need, cap);
  wait_ge(rs, (unsigned)(lane & 31) * 128u, F_ST + (unsigned)first * 128u, need, cap);
}
__device__ __forceinline__ void sum_partials(__amdgpu_buffer_rsrc_t rs, unsigned l16, unsigned so, float (&a)[16]) {
  u32x4 pp[4][2];
#pragma unroll
  for (int k = 0; k < 4; ++k) { pp[k][0] = ldx16(rs, l16, so + k * 65536u); pp[k][1] = ldx16(rs, l16, so + k * 65536u + 1024); }
#pragma unroll
  for (int i = 0; i < 8; ++i) {
    f32x2 t = {0.f, 0.f};
#pragma unroll
    for (int k = 0; k < 4; ++k) t += f32x2{lo2f(pp[k][i >> 2][i & 3]), hi2f(pp[k][i >> 2][i & 3])};
    a[2 * i] = rbf(t[0]); a[2 * i + 1] = rbf(t[1]);
  }
}
__device__ __forceinline__ void put_row(__amdgpu_buffer_rsrc_t rs, unsigned l16, unsigned so, const float (&v)[16]) {
  stx16(rs, l16, so, u32x4{pack2(v[0], v[1]), pack2(v[2], v[3]), pack2(v[4], v[5]), pack2(v[6], v[7])});
  stx16(rs, l16, so + 1024, u32x4{pack2(v[8], v[9]), pack2(v[10], v[11]), pack2(v[12], v[13]), pack2(v[14], v[15])});
}
__device__ __forceinline__ void load_mod(__amdgpu_buffer_rsrc_t rs, int part, int lane, float (&v)[16], unsigned& cap) {
  for (;;) {
    asm volatile("" ::: "memory");
    u32x4 t[4];
#pragma unroll
    for (int i = 0; i < 4; ++i) t[i] = ldx16(rs, (unsigned)lane * 32, X_MOD + part * 4096 + (i >> 1) * 2048 + (i & 1) * 16);
    unsigned mn = 0xFFFFFFFFu;
#pragma unroll
    for (int i = 0; i < 16; ++i) { mn = min(mn, t[i >> 2][i & 3]); v[i] = hi2f(t[i >> 2][i & 3]); }
    if (__all(mn != 0u) || cap == 0u) break;
    cap = __builtin_amdgcn_readfirstlane(cap - 1u);
    __builtin_amdgcn_s_sleep(4);
  }
  cap = __builtin_amdgcn_readfirstlane(cap);
}
struct ModLoad { u32x4 w[8]; u16 c[8]; };
__device__ __forceinline__ void mod_load(const MainP& p, int grp, ModLoad& m) {
  const int kg = threadIdx.x >> 2, c8 = threadIdx.x & 3;
#pragma unroll
  for (int i = 0; i < 8; ++i) { m.w[i] = *(const u32x4*)(p.wmod + (size_t)(kg + 128 * i) * 6144 + grp * 32 + c8 * 8); m.c[i] = p.c[kg + 128 * i]; }
}
__device__ __forceinline__ void mod_finish(__amdgpu_buffer_rsrc_t rs, LAS float* red, int grp, const ModLoad& m) {
  const int tid = threadIdx.x, kg = tid >> 2, c8 = tid & 3;
  float acc[8] = {0.f, 0.f, 0.f, 0.f, 0.f, 0.f, 0.f, 0.f};
#pragma unroll
  for (int i = 0; i < 8; ++i) {
    const float v = bf2f(m.c[i]), sc = rbf(v / (1.0f + __expf(-v)));
#pragma unroll
    for (int j = 0; j < 4; ++j) { acc[2 * j] += sc * lo2f(m.w[i][j]); acc[2 * j + 1] += sc * hi2f(m.w[i][j]); }
  }
#pragma unroll
  for (int j = 0; j < 8; ++j) red[kg * 33 + c8 * 8 + j] = acc[j];
  LBAR();
  float t = 0.f;
#pragma unroll
  for (int i = 0; i < 8; ++i) t += red[((tid >> 5) * 8 + i) * 33 + (tid & 31)];
  t = hsum(t);
  LBAR();
  if ((tid & 63) < 32) red[(tid >> 6) * 33 + (tid & 31)] = t;
  LBAR();
  if (tid < 32) {
    float u = 0.f;
#pragma unroll
    for (int i = 0; i < 8; ++i) u += red[i * 33 + tid];
    stx4(rs, (unsigned)tid * 4, X_MOD + (unsigned)grp * 128, f2u(rbf(u)) | 1u);
  }
}

__device__ __forceinline__ void stage_mid(__amdgpu_buffer_rsrc_t rs, const u16* x, int wk, int cu, unsigned& cap) {
  const int tid = threadIdx.x, lane = tid & 63, w = __builtin_amdgcn_readfirstlane(tid >> 6);
  const int id = w * 12 + cu, grp = id >> 5, rl = id & 31;
  const unsigned l16 = lane * 16;
  float ga[16], sc[16], sh[16];
  load_mod(rs, 2, lane, ga, cap); load_mod(rs, 4, lane, sc, cap); load_mod(rs, 3, lane, sh, cap);
#pragma unroll
  for (int i = 0; i < 16; ++i) sc[i] = rbf(1.0f + sc[i]);
  for (int seg = grp; seg < NSEG; seg += 3) {
    const int row = seg * 32 + rl;
    float v[16], a[16];
    load_row16(x + (size_t)row * D_, lane, v);
    wait32(rs, P_S2, rl, lane, (unsigned)seg + 1u, cap);
    sum_partials(rs, l16, WS_P2 + (unsigned)seg * 262144u + (unsigned)rl * 2048, a);
#pragma unroll
    for (int i = 0; i < 16; ++i) v[i] = rbf(v[i] + rbf(ga[i] * a[i]));
    put_row(rs, l16, WS_X1 + (unsigned)row * 2048, v);
    ln_row(v);
#pragma unroll
    for (int i = 0; i < 16; ++i) v[i] = rbf(rbf(v[i] * sc[i]) + sh[i]);
    put_row(rs, l16, WS_H2 + (unsigned)row * 2048, v);
    ln_row(v);
    put_row(rs, l16, WS_G + (unsigned)row * 2048, v);
    WAIT_V(0);
    if (lane == 0) stx4(rs, 0, F_ST + (unsigned)(P_S3 + grp * 32 + rl) * 128, (unsigned)seg + 1u);
    WST(rs, wk, 16 + seg);
  }
}

__device__ __forceinline__ void stage_out(__amdgpu_buffer_rsrc_t rs, u16* out, int wk, int cu, unsigned& cap) {
  const int tid = threadIdx.x, lane = tid & 63, w = __builtin_amdgcn_readfirstlane(tid >> 6);
  const int grp = w >> 2, rl = cu * 4 + (w & 3);
  const unsigned l16 = lane * 16;
  float gb[16];
  load_mod(rs, 5, lane, gb, cap);
  for (int b = grp; b < 65; b += 2) {
    const int t = 32 * b - 2 + rl;
    if (t < 0 || t >= S_) continue;
    wait_ge(rs, (unsigned)(lane & 7) * 128u, F_ST + P_S5 * 128u, (unsigned)b + 1u, cap);
    const unsigned xo = (unsigned)((lane >> 2) * 2048 + (lane & 3) * 256), xs = WS_X2 + (unsigned)b * 65536u + (unsigned)((rl >> 4) * 1024 + (rl & 15) * 16);
    const u32x4 x2a = ldx16(rs, xo, xs), x2b = ldx16(rs, xo, xs + 32768);
    const u32x4 x1a = ldx16(rs, l16, WS_X1 + (unsigned)t * 2048), x1b = ldx16(rs, l16, WS_X1 + (unsigned)t * 2048 + 1024);
    wait32(rs, P_S8, rl, lane, (unsigned)b + 1u, cap);
    float a[16];
    sum_partials(rs, l16, WS_P8 + (unsigned)b * 262144u + (unsigned)rl * 2048, a);
#pragma unroll
    for (int i = 0; i < 4; ++i) {
      a[2 * i] = lo2f(x1a[i]) + rbf(gb[2 * i] * rbf(lo2f(x2a[i]) + a[2 * i])); a[2 * i + 1] = hi2f(x1a[i]) + rbf(gb[2 * i + 1] * rbf(hi2f(x2a[i]) + a[2 * i + 1]));
      a[8 + 2 * i] = lo2f(x1b[i]) + rbf(gb[8 + 2 * i] * rbf(lo2f(x2b[i]) + a[8 + 2 * i])); a[9 + 2 * i] = hi2f(x1b[i]) + rbf(gb[9 + 2 * i] * rbf(hi2f(x2b[i]) + a[9 + 2 * i]));
    }
    store_row16(out + (size_t)t * D_, lane, a);
    WST(rs, wk, 16 + b);
  }
}

__device__ __forceinline__ void worker(const MainP& p, LAS unsigned char* lds, int xw, int j) {
  const int tid = threadIdx.x, lane = tid & 63, w = __builtin_amdgcn_readfirstlane(tid >> 6);
  const __amdgpu_buffer_rsrc_t rs = mk_rs(p.ws, WS_END);
  unsigned cap = SPIN_CAP;
  const int wk = xw * 32 + j;
  WST(rs, wk, 0);
  {
    const int c = j * 5 + xw, r0 = c * 8 + 2 * (w & 3);
    const bool two = r0 + 8 * NLN < S_;
    float v[4][16];
    ModLoad m;
    if (c >= NLN - 64) mod_load(p, c - (NLN - 64), m);
    if (w < 4) {
#pragma unroll
      for (int q = 0; q < 4; ++q) load_row16(p.x + (size_t)(r0 + (q & 1) + (two ? (q >> 1) * 8 * NLN : 0)) * D_, lane, v[q]);
    }
    if (c >= NLN - 64) mod_finish(rs, (LAS float*)(lds + G_XCH), c - (NLN - 64), m);
    WST(rs, wk, 5);
    if (w >= 4) {
      const __amdgpu_buffer_rsrc_t rsW = mk_rs(xw == 4 ? p.Wq : (xw & 1) ? p.Wv : p.Wk, D_ * HD * 2);
      const int ub = lane >> 3;
      const unsigned vB = (unsigned)(((ub & 3) + 8 * (ub >> 2)) * HD + 16 * (((lane >> 1) & 3) ^ (ub >> 1)) + 8 * (lane & 1)) * 2u, lbase = (unsigned)(size_t)lds;
#pragma unroll
      for (int q = 0; q < 16; ++q) {
        const int wq = (w & 3) + 4 * (q >> 3);
        const unsigned so = (unsigned)((32 * (wq >> 2) + 16 * ((wq >> 1) & 1) + 4 * (wq & 1) + (((q >> 2) & 1) * 8 + (q & 3)) * 64) * HD + j * 128) * 2u;
        const unsigned d = lbase + ((q >> 2) & 1) * 65536 + (q & 3) * 16384 + wq * 1024;
        dma16(rsW, vB, so, d); dma16(rsW, vB, so + 128, d + 8192);
      }
    } else {
#pragma unroll
      for (int q = 0; q < 4; ++q) ln_row(v[q]);
      float sc[16], sh[16];
      load_mod(rs, 1, lane, sc, cap); load_mod(rs, 0, lane, sh, cap);
      WST(rs, wk, 6);
#pragma unroll
      for (int i = 0; i < 16; ++i) sc[i] = rbf(1.0f + sc[i]);
#pragma unroll
      for (int q = 0; q < 4; ++q) {
#pragma unroll
        for (int i = 0; i < 16; ++i) v[q][i] = rbf(rbf(v[q][i] * sc[i]) + sh[i]);
        if (q < 2 || two) put_row(rs, (unsigned)lane * 16, WS_H + (unsigned)(r0 + (q & 1) + (q >> 1) * 8 * NLN) * 2048, v[q]);
      }
      WAIT_V(0);
    }
    BAR();
    if (tid == 0) stx4(rs, 0, F_LN + c * 128, 1u);
    WST(rs, wk, 1);
  }
  if (xw == 4) stage_gemm<SG_Q>(lds, rs, p.Wq, wk, 0, j, P_S1 + j, cap);
  else {
    const u16* nw = xw == 0 ? p.Wo + (size_t)(j >> 3) * 1024 * D_ + (j & 7) * 128 : xw == 1 ? p.w4 + (size_t)(j >> 3) * 896 * D_ + (j & 7) * 128
                  : xw == 3 ? p.w3 + j * 128 : (j < 8 ? p.w1 : p.w2) + (j & 7) * 128;
    const int nld = (xw >= 2 && j >= (xw == 3 ? 28 : 16)) ? 0 : xw == 3 ? FF : D_, nrows = xw == 1 ? 896 : 1024;
    if (xw & 1) stage_gemm<SG_V>(lds, rs, p.Wv, wk, xw >> 1, j, P_PJ + xw * 32 + j, cap, nw, nw + nrows, nld, p.wmod + (64 + wk) * 32);
    else stage_gemm<SG_K>(lds, rs, p.Wk, wk, xw >> 1, j, P_PJ + xw * 32 + j, cap, nw, nw + nrows, nld, p.wmod + (64 + wk) * 32);
    WST(rs, wk, 3);
    { ModLoad m; mod_load(p, 64 + wk, m); mod_finish(rs, (LAS float*)(lds + 65536), 64 + wk, m); }
    if (xw == 0) stage_gemm<SG_WO>(lds, rs, p.Wo, wk, j >> 3, j & 7, P_S2 + j, cap);
    else if (xw == 1) stage_gemm<SG_W4>(lds, rs, p.w4, wk, j >> 3, j & 7, P_S8 + j, cap);
    else if (j >= 28) stage_out(rs, p.out, wk, (xw - 2) * 4 + j - 28, cap);
    else if (xw == 3) stage_gemm<SG_W3>(lds, rs, p.w3, wk, 0, j, P_S7 + j, cap);
    else if (j < 8) stage_gemm<SG_W1>(lds, rs, p.w1, wk, 0, j, P_S4 + j, cap);
    else if (j < 16) stage_gemm<SG_W2>(lds, rs, p.w2, wk, 0, j - 8, P_S5 + j - 8, cap);
    else stage_mid(rs, p.x, wk, j - 16, cap);
  }
  if (cap == 0u && lane == 0) stx4(rs, 0, X_CTL, 1u);
}

#define ST_DECL
#define ST0()
#define ST(i)
#define ST_FLUSH()
constexpr int R_W = 0, R_MEM = 131072, R_QK = R_MEM + 16384, R_KT = R_QK + 8192, R_RED = R_MEM + 16384, R_LDS = 163840;
__device__ __forceinline__ void bar_lo(int w) { asm volatile("s_cmp_ge_u32 %0, 4\n\ts_cbranch_scc1 1f\n\ts_barrier\n\t1:" :: "s"(w) : "scc", "memory"); }
__device__ __forceinline__ void bar_hi(int w) { asm volatile("s_cmp_lt_u32 %0, 4\n\ts_cbranch_scc1 1f\n\ts_setprio 3\n\ts_barrier\n\t1:" :: "s"(w) : "scc", "memory"); }
#define VMI 7
#define GAP "s_sleep 1\n\t"
__device__ __forceinline__ void issue4(__amdgpu_buffer_rsrc_t rs, unsigned vo, unsigned s0, unsigned s1, unsigned s2, unsigned s3, u32x4& a0, u32x4& a1, u32x4& a2, u32x4& a3) {
  asm volatile("buffer_load_dwordx4 v[240:243], %[vo], %[rs], %[s0] offen sc1\n\t" GAP "buffer_load_dwordx4 v[244:247], %[vo], %[rs], %[s1] offen sc1\n\t" GAP
               "buffer_load_dwordx4 v[248:251], %[vo], %[rs], %[s2] offen sc1\n\t" GAP "buffer_load_dwordx4 v[252:255], %[vo], %[rs], %[s3] offen sc1"
               : "=&{v[240:243]}"(a0), "=&{v[244:247]}"(a1), "=&{v[248:251]}"(a2), "=&{v[252:255]}"(a3)
               : [vo] "v"(vo), [rs] "s"(rs), [s0] "s"(s0), [s1] "s"(s1), [s2] "s"(s2), [s3] "s"(s3) : "memory");
}
#define ARRIVE(R0, R1, R2, R3, CNT) \
  asm volatile("s_waitcnt vmcnt(" #CNT ")\n\t" \
               "1:\n\t" \
               "v_min3_u32 %[t], v" #R0 ", v" #R1 ", v" #R2 "\n\t" \
               "v_min_u32 %[t], %[t], v" #R3 "\n\t" \
               "v_cmp_eq_u32 vcc, 0, %[t]\n\t" \
               "s_cbranch_vccz 2f\n\t" \
               "s_mov_b64 exec, vcc\n\t" \
               "buffer_load_dwordx4 v[" #R0 ":" #R3 "], %[vo], %[rs], %[so] offen sc1\n\t" \
               "s_mov_b64 exec, -1\n\t" \
               "s_waitcnt vmcnt(0)\n\t" \
               "s_sub_u32 %[cap], %[cap], 1\n\t" \
               "s_cbranch_scc0 1b\n\t" \
               "s_mov_b32 %[cap], 0\n\t" \
               "2:" \
               : "+{v[" #R0 ":" #R3 "]}"(a), [t] "=&v"(t), [cap] "+s"(cap) : [vo] "v"(vo), [rs] "s"(rs), [so] "s"(so) : "vcc", "scc", "memory")
template <int I> __device__ __forceinline__ void arrive(__amdgpu_buffer_rsrc_t rs, unsigned vo, unsigned so, u32x4& a, unsigned& cap) {
  unsigned t;
  if (I == 0) ARRIVE(240, 241, 242, 243, 3);
  else if (I == 1) ARRIVE(244, 245, 246, 247, 2);
  else if (I == 2) ARRIVE(248, 249, 250, 251, 1);
  else ARRIVE(252, 253, 254, 255, 0);
}
__device__ __forceinline__ unsigned half_of(unsigned v) { unsigned r; asm volatile("v_lshrrev_b32 %0, 1, %1" : "=v"(r) : "v"(v)); return r; }
__device__ __forceinline__ void issue2(__amdgpu_buffer_rsrc_t rs, unsigned l8, unsigned so, int w, u32x4& a) {
  asm volatile("s_cmp_ge_u32 %[w], 4\n\ts_cbranch_scc1 1f\n\tv_lshrrev_b32 v255, 1, %[l8]\n\t"
               "buffer_load_dwordx2 v[252:253], v255, %[rs], %[so] offen sc1\n\tbuffer_load_dwordx2 v[254:255], v255, %[rs], %[so] offen offset:512 sc1\n\t1:"
               : "=&{v[252:255]}"(a) : [l8] "v"(l8), [rs] "s"(rs), [so] "s"(so), [w] "s"(w) : "scc", "memory");
}
__device__ __forceinline__ void arrive2(__amdgpu_buffer_rsrc_t rs, unsigned l8, unsigned so, u32x4& a, unsigned& cap) {
  unsigned t;
  asm volatile("s_waitcnt vmcnt(0)\n\t"
               "1:\n\t"
               "v_min3_u32 %[t], v252, v253, v254\n\t"
               "v_min_u32 %[t], %[t], v255\n\t"
               "v_cmp_eq_u32 vcc, 0, %[t]\n\t"
               "s_cbranch_vccz 2f\n\t"
               "v_lshrrev_b32 %[t], 1, %[l8]\n\t"
               "buffer_load_dwordx2 v[252:253], %[t], %[rs], %[so] offen sc1\n\t"
               "buffer_load_dwordx2 v[254:255], %[t], %[rs], %[so] offen offset:512 sc1\n\t"
               "s_waitcnt vmcnt(0)\n\t"
               "s_sub_u32 %[cap], %[cap], 1\n\t"
               "s_cbranch_scc0 1b\n\t"
               "s_mov_b32 %[cap], 0\n\t"
               "2:"
               : "+{v[252:255]}"(a), [t] "=&v"(t), [cap] "+s"(cap) : [l8] "v"(l8), [rs] "s"(rs), [so] "s"(so) : "vcc", "scc", "memory");
}
__device__ __forceinline__ unsigned ror8(unsigned v) { return __builtin_amdgcn_update_dpp(0u, v, 0x128, 0xF, 0xF, true); }
__device__ __forceinline__ void gather_mem(__amdgpu_buffer_rsrc_t rs, unsigned lbase, int s, int w, unsigned l16, unsigned& cap) {
  const unsigned so = X_MEM + s * RSTR + w * 2048;
  u32x4 a0, a1;
  asm volatile("buffer_load_dwordx4 v[248:251], %[vo], %[rs], %[so] offen sc1\n\tbuffer_load_dwordx4 v[252:255], %[vo], %[rs], %[so] offen offset:1024 sc1"
               : "=&{v[248:251]}"(a0), "=&{v[252:255]}"(a1) : [vo] "v"(l16), [rs] "s"(rs), [so] "s"(so) : "memory");
  LAS unsigned char* d = lad(l16, lbase + R_MEM + w * 2048);
  arrive<2>(rs, l16, so, a0, cap);
  *(LAS u32x4*)(d) = a0;
  arrive<3>(rs, l16, so + 1024, a1, cap);
  *(LAS u32x4*)(d + 1024) = a1;
}
__device__ __forceinline__ unsigned mem_off(int lane) { return (unsigned)((lane & 1) * 512 + (((lane >> 1) ^ ((lane & 1) * 16)) * 8)); }
__device__ __forceinline__ bf16x8 mem_frag(LAS unsigned char* mp, int chunk) { return cat8(trread(mp + chunk * 1024), trread(mp + chunk * 1024 + 256)); }
__device__ __forceinline__ bf16x16 w_frag(LAS unsigned char* p) {
  return cat16(cat8(trread(p), trread(p + 2048)), cat8(trread(p + 4096), trread(p + 6144)));
}
__device__ __forceinline__ bf16x16 l_frag(LAS unsigned char* p, int i) { return cat16(*(const LAS bf16x8*)(p + i * 2048), *(const LAS bf16x8*)(p + i * 2048 + 1024)); }
__device__ __forceinline__ void make_resident(LAS unsigned char* wb, unsigned lo, unsigned l16) {
#pragma unroll
  for (int c = 0; c < 2; ++c) {
    bf16x16 f[4];
#pragma unroll
    for (int t = 0; t < 4; ++t) f[t] = w_frag(wb + c * 8192 + (lo ^ (t << 5)));
    WAIT_L0(); SCHED();
#pragma unroll
    for (int t = 0; t < 4; ++t) {
      *(LAS bf16x8*)(wb + (c * 4 + t) * 2048 + l16) = __builtin_shufflevector(f[t], f[t], 0, 1, 2, 3, 4, 5, 6, 7);
      *(LAS bf16x8*)(wb + (c * 4 + t) * 2048 + 1024 + l16) = __builtin_shufflevector(f[t], f[t], 8, 9, 10, 11, 12, 13, 14, 15);
    }
    WAIT_L0(); SCHED();
  }
}
__device__ __forceinline__ f32x4 fold(f32x4 a, f32x4 b) {
  f32x4 r;
#pragma unroll
  for (int i = 0; i < 4; ++i) {
    const auto t = __builtin_amdgcn_permlane32_swap(f2u(a[i]), f2u(b[i]), false, false);
    r[i] = u2f(t[0]) + u2f(t[1]);
  }
  return r;
}

struct Sched { unsigned tau, prev; int ea, eb, d1; bool moved; };
__device__ __forceinline__ void retime(__amdgpu_buffer_rsrc_t rs, unsigned l16, int s, Sched& t, const MainP& p, unsigned& cap) {
  if (s > 0) {
    unsigned vo;
    asm volatile("v_lshlrev_b32 %0, 3, %1" : "=v"(vo) : "v"(l16));
    unsigned va;
    asm volatile("v_and_b32 %0, 0xF80, %1" : "=v"(va) : "v"(vo));
    wait_ge(rs, vo, X_TS + (unsigned)s * RSTR, 1u, cap);
    wait_ge(rs, va, X_TS + (unsigned)s * RSTR + 8192, 1u, cap);
    int vb = (int)(ldx4(rs, vo, X_TS + (unsigned)s * RSTR) - t.prev), vc = (int)(ldx4(rs, va, X_TS + (unsigned)s * RSTR + 8192) - t.prev);
#pragma unroll
    for (int o = 32; o >= 1; o >>= 1) { vb = max(vb, __shfl_xor(vb, o)); vc = max(vc, __shfl_xor(vc, o)); }
    const int tb = __builtin_amdgcn_readfirstlane(vb), ta = __builtin_amdgcn_readfirstlane(vc);
    const int late = tb + p.d3 - (int)(t.tau - t.prev);
    if (s > 2) {
      const int up = s < 5 ? 80 : 8, dn = s < 10 ? 40 : 8;
      if (!t.moved) t.ea = max(t.ea - dn, min(ta * 8, t.ea + up));
      if (ta + p.d3 <= t.d1) t.eb = max(t.eb - dn, min((tb - t.d1) * 8, t.eb + up));
    }
    t.moved = late > 0;
    if (late > 0) t.tau += late + min(p.d4, 8 * late);
  }
  t.d1 = (t.ea >> 3) + p.d1;
}
__device__ __forceinline__ void advance(Sched& t, const MainP& p) { t.prev = t.tau; t.tau += t.d1 + (t.eb >> 3) + p.d2; }

__device__ __forceinline__ void role_a(const MainP& p, LAS unsigned char* lds, int h) {
  const int tid = threadIdx.x, lane = tid & 63, w = __builtin_amdgcn_readfirstlane(tid >> 6);
  const int cg = w & 3, kh = w >> 2;
  const __amdgpu_buffer_rsrc_t rs = mk_rs(p.ws, WS_END);
  const unsigned lbase = (unsigned)(size_t)lds;
  unsigned l16 = lane * 16;
  asm volatile("" : "+v"(l16));
  const int sidx = (lane & 8) ? 0xEEEE : 0x4444;
  bf16x16 W[24];
  unsigned qoff;
  {
    const int fr = lane & 15, g = lane >> 4, tu = 4 * (g & 1) + (fr >> 2), ub = lane >> 3;
    const unsigned lo = (g >> 1) * 1024 + tu * 128 + ((tu >> 1) << 5) + (fr & 3) * 8;
    qoff = R_QK + ((fr >> 1) & 1) * 512 + (g >> 1) * 256 + (fr & 1) * 128 + (8 * (g & 1) + (fr >> 2)) * 8;
    const __amdgpu_buffer_rsrc_t rw = mk_rs(cg < 2 ? p.Wq : p.Wk, D_ * HD * 2);
    const unsigned vo = (unsigned)(ub * HD + 16 * (((lane >> 1) & 3) ^ (ub >> 1)) + 8 * (lane & 1)) * 2u;
    const unsigned so = (unsigned)(kh * 512 * HD + h * DH + (cg & 1) * 64) * 2u;
    auto stage = [&](int c) {
#pragma unroll
      for (int rho = 0; rho < 8; ++rho) dma16(rw, vo, so + (unsigned)((c * 64 + rho * 8) * HD) * 2u, lbase + R_W + w * 16384 + (c & 1) * 8192 + rho * 1024);
    };
    stage(0); stage(1);
#pragma unroll
    for (int c = 0; c < 6; ++c) {
      WAIT_V(8);
#pragma unroll
      for (int t = 0; t < 4; ++t) W[4 * c + t] = w_frag(lds + R_W + w * 16384 + (c & 1) * 8192 + (lo ^ (t << 5)));
      WAIT_L0(); SCHED();
      stage(c + 2);
    }
    WAIT_V(0);
    make_resident(lds + R_W + w * 16384, lo, l16);
  }
  WST(rs, 160 + (h >> 1), 4);
  unsigned cap = SPIN_CAP;
  wait_ge(rs, (l16 & 16) * 256, F_ST + (unsigned)(P_PJ + h) * 128, 1u, cap);
  dma16x(rs, l16, WS_K + h * 8192 + w * 1024, lbase + R_KT + w * 1024);
  Sched tt; tt.tau = rt32() - 100000; tt.prev = tt.tau; tt.ea = 195 * 8; tt.eb = 118 * 8; tt.d1 = 0; tt.moved = true;
  const int cu_ = h; (void)cu_;
  ST_DECL
  for (int s = 0; s < NSEG; ++s) {
    LAS unsigned char* lp = lad(l16, lbase + R_W + w * 16384);
    bf16x16 L[2];
    L[0] = l_frag(lp, 0); L[1] = l_frag(lp, 1);
    unsigned lz = l16;
    asm volatile("" : "+v"(lz));
    LAS unsigned char* mp = lad(mem_off((int)(lz >> 4)), lbase + R_MEM + kh * 8192);
    SCHED();
    if (w == 7) wait_until(tt.tau);
    BAR();
    ST0();
    gather_mem(rs, lbase, s, (w + h) & 7, l16, cap);
    ST(1);
    LBAR();
    ST(2);
    f32x4 c[4];
#pragma unroll
    for (int t = 0; t < 4; ++t) c[t] = f32x4{0.f, 0.f, 0.f, 0.f};
    bf16x8 al, af[2];
    u32x4 vt0, vt1;
    u32x4 vmv;
    const unsigned vso = X_VM + (unsigned)s * RSTR + h * 4096 + cg * 1024;
    al = mem_frag(mp, 6); af[0] = mem_frag(mp, 0); af[1] = mem_frag(mp, 1);
#pragma unroll
    for (int i = 0; i < 8; ++i) {
      c[i & 3] = smf(al, L[i & 1], c[i & 3], sidx);
      SCHED();
      if (i == 3) al = mem_frag(mp, 7);
      if (i < 6) L[i & 1] = l_frag(lp, i + 2);
      if (i == 6) {
        const unsigned vo = WS_VT + (unsigned)(s * NH + h) * 8192 + cg * 2048;
        asm volatile("s_cmp_ge_u32 %5, 4\n\ts_cbranch_scc1 1f\n\t"
                     "buffer_load_dwordx4 %0, %2, %3, %4 offen sc1\n\tbuffer_load_dwordx4 %1, %2, %3, %4 offen offset:1024 sc1\n\t1:"
                     : "=&v"(vt0), "=&v"(vt1) : "v"(l16), "s"(rs), "s"(vo), "s"(w) : "scc", "memory");
      }
      if (i == VMI) issue2(rs, l16, vso, w, vmv);
      SCHED();
#pragma unroll
      for (int r = 3 * i; r < 3 * i + 3; ++r) {
        c[(r + 1) & 3] = smf(af[(r >> 2) & 1], W[(r & ~3) + ((r + 1) & 3)], c[(r + 1) & 3], sidx);
        SCHED();
        if ((r & 3) == 3 && (r >> 2) + 2 < 6) af[(r >> 2) & 1] = mem_frag(mp, (r >> 2) + 2);
        SCHED();
      }
    }
    {
      LAS unsigned char* qw = lad(half_of(l16), lbase + R_QK + cg * 2048 + kh * 512);
      const f32x4 f0 = fold(c[0], c[1]);
      *(LAS u32x2*)(qw) = u32x2{pack2(f0[0], f0[1]), pack2(f0[2], f0[3])};
      const f32x4 f1 = fold(c[2], c[3]);
      *(LAS u32x2*)(qw + 1024) = u32x2{pack2(f1[0], f1[1]), pack2(f1[2], f1[3])};
    }
    ST(3);
    if (w < 4) {
      bf16x8 k1[4], k2[4];
      LAS unsigned char* kb = lad(l16, lbase + R_KT);
#pragma unroll
      for (int ks = 0; ks < 2; ++ks) { k1[ks] = *(const LAS bf16x8*)(kb + ks * 1024); k2[ks] = *(const LAS bf16x8*)(kb + 4096 + ks * 1024); }
      unsigned ov;
      {
        unsigned ot;
        asm volatile("v_and_b32 %0, 0x30, %2\n\tv_lshrrev_b32 %1, 2, %2\n\tv_and_b32 %1, 0xC0, %1\n\tv_or_b32 %0, %0, %1\n\t"
                     "v_and_b32 %1, 0x40, %2\n\tv_lshl_or_b32 %0, %1, 9, %0\n\tv_and_b32 %1, 0x80, %2\n\tv_lshl_or_b32 %0, %1, 24, %0"
                     : "=&v"(ov), "=&v"(ot) : "v"(l16));
      }
      LBAR();
      ST(4);
      f32x4 t0 = {0.f, 0.f, 0.f, 0.f}, t1 = t0, t2 = t0;
      LAS unsigned char* qb = lad(qoff, lbase);
      bf16x8 qf[4], kf[4];
#pragma unroll
      for (int ks = 0; ks < 2; ++ks) {
        qf[ks] = cat8(trread(qb + ks * 1024), trread(qb + ks * 1024 + 32));
        kf[ks] = cat8(trread(qb + 4096 + ks * 1024), trread(qb + 4096 + ks * 1024 + 32));
      }
      SCHED();
#pragma unroll
      for (int ks = 0; ks < 4; ++ks) {
        t1 = mfma32(k1[ks], qf[ks], t1); t2 = mfma32(k2[ks], qf[ks], t2); t0 = mfma32(kf[ks], qf[ks], t0);
        SCHED();
        if (ks < 2) {
          qf[ks + 2] = cat8(trread(qb + (ks + 2) * 1024), trread(qb + (ks + 2) * 1024 + 32));
          k1[ks + 2] = *(const LAS bf16x8*)(kb + (ks + 2) * 1024); k2[ks + 2] = *(const LAS bf16x8*)(kb + 4096 + (ks + 2) * 1024);
          kf[ks + 2] = cat8(trread(qb + 4096 + (ks + 2) * 1024), trread(qb + 4096 + (ks + 2) * 1024 + 32));
        }
        SCHED();
      }
      ST(5);
      arrive2(rs, l16, vso, vmv, cap);
      asm volatile("" : "+v"(vt0), "+v"(vt1));
      {
        const float sl = 0.08838834764831845f * 1.4426950408889634f;
        float z[4] = {t1[0], t1[1], t1[2], t1[3]}, y = t0[0], mx;
        {
          float u1 = t0[1], u2 = t0[2], u3 = t0[3];
#define M8(Z, B) "v_add_f32_dpp " Z ", " Z ", " Z " row_ror:8 row_mask:0xf bank_mask:0x3\n\tv_add_f32_dpp " Z ", " B ", " B " row_ror:8 row_mask:0xf bank_mask:0xc\n\t"
          asm volatile("s_nop 3\n\t"
              "v_permlane32_swap_b32 %4, %6\n\tv_permlane32_swap_b32 %5, %7\n\t"
              M8("%0", "%9") "v_add_f32 %4, %4, %6\n\tv_add_f32 %5, %5, %7\n\t" M8("%1", "%10") M8("%2", "%11") M8("%4", "%5") M8("%3", "%12")
              "v_max3_f32 %8, %0, %1, %2\n\tv_max3_f32 %8, %8, %4, %3\n\ts_nop 1\n\t"
              "v_max_f32_dpp %8, %8, %8 row_ror:8 row_mask:0xf bank_mask:0xf\n\tv_mov_b32 %5, %8\n\ts_nop 1\n\t"
              "v_permlane16_swap_b32 %8, %5\n\tv_max_f32 %8, %8, %5\n\tv_mov_b32 %5, %8\n\ts_nop 1\n\t"
              "v_permlane32_swap_b32 %8, %5\n\tv_max_f32 %8, %8, %5"
              : "+v"(z[0]), "+v"(z[1]), "+v"(z[2]), "+v"(z[3]), "+v"(y), "+v"(u1), "+v"(u2), "+v"(u3), "=&v"(mx) : "v"(t2[0]), "v"(t2[1]), "v"(t2[2]), "v"(t2[3]));
#undef M8
        }
        const float nm = -mx * sl;
        y = __builtin_amdgcn_exp2f(fmaf(y, sl, nm));
#pragma unroll
        for (int r = 0; r < 4; ++r) z[r] = __builtin_amdgcn_exp2f(fmaf(z[r], sl, nm));
        const unsigned pm1 = pack2(y, u2f(ror8(f2u(y))));
        const auto pms = __builtin_amdgcn_permlane32_swap(pm1, pm1, false, false);
        const u32x2 pm = {pms[0], pms[1]};
        f32x4 n0 = mfma16n(u32x2{vmv[0], vmv[1]}, pm, f32x4{0.f, 0.f, 0.f, 0.f}), n1 = mfma16n(u32x2{vmv[2], vmv[3]}, pm, f32x4{0.f, 0.f, 0.f, 0.f});
        const unsigned p0 = pack2(z[0], z[1]), p1 = pack2(z[2], z[3]);
        const u32x4 pt = {p0, p1, ror8(p0), ror8(p1)};
        const f32x4 o0 = mfma32(__builtin_bit_cast(bf16x8, vt0), __builtin_bit_cast(bf16x8, pt), f32x4{0.f, 0.f, 0.f, 0.f});
        const f32x4 o1 = mfma32(__builtin_bit_cast(bf16x8, vt1), __builtin_bit_cast(bf16x8, pt), f32x4{0.f, 0.f, 0.f, 0.f});
        const float inv = __builtin_amdgcn_rcpf(gsum(dpp_add<0x128>(((z[0] + z[1]) + (z[2] + z[3])) + y)));
        n0 += o0; n1 += o1;
        {
          const u32x4 v = {nz(pack2(n0[0] * inv, n0[1] * inv)), nz(pack2(n0[2] * inv, n0[3] * inv)), nz(pack2(n1[0] * inv, n1[1] * inv)), nz(pack2(n1[2] * inv, n1[3] * inv))};
          stx16(rs, ov, X_OX + s * RSTR + h * 1024 + w * 256, v);
        }
        if (w == 3) { const unsigned t = max(rt32(), 1u); if (l16 == 0) stx4(rs, 0, X_TS + (unsigned)(s + 1) * RSTR + (unsigned)(64 + h) * 128, t); }
        ST(8);
      }
    } else {
      LBAR();
      LAS unsigned char* qb = lad(qoff, lbase + 4096 + (w - 4) * 1024);
      u32x4 kf = __builtin_bit_cast(u32x4, cat8(trread(qb), trread(qb + 32)));
#pragma unroll
      for (int i = 0; i < 4; ++i) kf[i] = pack2(dpp_add<0x128>(lo2f(kf[i])), dpp_add<0x128>(hi2f(kf[i])));
      stx16(rs, l16, WS_KM + (unsigned)(s * NH + h) * 4096 + (w - 4) * 1024, kf);
      WAIT_V(0);
    }
    LBAR();
    if (w == 4 && l16 == 0) stx4(rs, 0, F_CH + h * 128, (unsigned)s + 1u);
    ST(9);
    if (w == 7) { retime(rs, l16, s, tt, p, cap); advance(tt, p); }
    if (s + 1 < NSEG) {
      unsigned fo;
      asm volatile("v_and_b32 %0, 16, %1\n\tv_lshlrev_b32 %0, 8, %0" : "=v"(fo) : "v"(l16));
      wait_ge(rs, fo, F_ST + (unsigned)(P_PJ + ((s + 1) & 1) * 64 + h) * 128, (unsigned)s + 2u, cap);
      dma16x(rs, l16, WS_K + (unsigned)((s + 1) * NH + h) * 8192 + w * 1024, lbase + R_KT + w * 1024);
    }
  }
  ST_FLUSH();
  if (cap == 0u && l16 == 0) stx4(rs, 0, X_CTL, 1u);
}

__device__ __forceinline__ void role_vb(const MainP& p, LAS unsigned char* lds, int rg, int cb) {
  const int tid = threadIdx.x, lane = tid & 63, w = __builtin_amdgcn_readfirstlane(tid >> 6);
  const int kh = w >> 2, wr = (w + cb) & 7;
  const __amdgpu_buffer_rsrc_t rs = mk_rs(p.ws, WS_END);
  const unsigned lbase = (unsigned)(size_t)lds;
  unsigned l16 = lane * 16, l8 = lane * 8, moff = mem_off(lane), roff, soff = (unsigned)((lane >> 5) * 256 + (lane & 31) * 4);
  const int sidx = (lane & 8) ? 0xEEEE : 0x4444;
  bf16x16 WV[8], WB[16];
  {
    const int fr = lane & 15, g = lane >> 4, tu = 4 * (g & 1) + (fr >> 2), ub = lane >> 3;
    { const int sl = mem_slot(16 * (g >> 1) + fr); roff = R_RED + w * 512 + ((sl >> 5) * 32 + (sl & 15) * 2 + (g & 1)) * 8; }
    if (w == 0) {
      const int sl = (lane >> 1) & 15, col = cb * 32 + ((sl >> 2) & 1) * 16 + (sl >> 3) * 8 + (lane >> 5) * 4 + (sl & 1) * 2 + ((sl >> 1) & 1), r = rg * 4 + (lane & 1) * 2;
      stx4(rs, soff, X_MEM + (cb >> 1) * 1024 + rg * 512 + ((cb ^ rg) & 1) * 128, nz((unsigned)p.mem0[r * D_ + col] | ((unsigned)p.mem0[(r + 1) * D_ + col] << 16)));
    }
    const __amdgpu_buffer_rsrc_t rv = mk_rs(p.Wv, D_ * HD * 2), ro = mk_rs(p.Wo, D_ * HD * 2);
    const unsigned vv = (unsigned)(ub * HD + 16 * (((lane >> 1) & 3) ^ (ub >> 1)) + 8 * (lane & 1)) * 2u, sv = (unsigned)(8 * w * HD + cb * DH + rg * 64) * 2u;
#pragma unroll
    for (int c = 0; c < 16; ++c) dma16(rv, vv, sv + (unsigned)c * 64 * HD * 2, lbase + R_W + c * 8192 + w * 1024);
    WAIT_V(0); BAR();
    const unsigned lo = (g >> 1) * 1024 + tu * 128 + (((tu >> 1) ^ (w & 3)) << 5) + (fr & 3) * 8;
#pragma unroll
    for (int c = 0; c < 8; ++c) WV[c] = w_frag(lds + R_W + (kh * 8 + c) * 8192 + lo);
    LBAR();
    const int u = (lane >> 2) & 7;
    const unsigned vw = (unsigned)((16 * (u >> 2) + 8 * ((u >> 1) & 1) + 4 * (lane >> 5) + (u & 1)) * D_ + (lane & 3) * 8) * 2u;
    const unsigned sw = (unsigned)(wr * 512 * D_ + cb * 32) * 2u;
    const unsigned bo = (g >> 1) * 1024 + tu * 64 + (fr & 3) * 8;
    auto stageo = [&](int hd) {
#pragma unroll
      for (int q = 0; q < 8; ++q)
        dma16(ro, vw, sw + (unsigned)((hd * 128 + 64 * (q >> 2) + 32 * (q & 1) + 2 * ((q >> 1) & 1)) * D_) * 2u, lbase + R_W + w * 16384 + (hd & 1) * 8192 + q * 1024);
    };
    stageo(0); stageo(1);
#pragma unroll
    for (int hd = 0; hd < 4; ++hd) {
      if (hd < 3) wait_v<8>(); else wait_v<0>();
#pragma unroll
      for (int f = 0; f < 4; ++f) WB[hd * 4 + f] = w_frag(lds + R_W + w * 16384 + (hd & 1) * 8192 + (f >> 1) * 512 + (f & 1) * 32 + bo);
      WAIT_L0(); SCHED();
      if (hd < 2) stageo(hd + 2);
    }
  }
  WST(rs, 176 + rg * 8 + (cb >> 2), 4);
  asm volatile("" : "+v"(l16), "+v"(l8), "+v"(moff), "+v"(roff), "+v"(soff));
  unsigned cap = SPIN_CAP;
  Sched tt; tt.tau = rt32() - 100000; tt.prev = tt.tau; tt.ea = 195 * 8; tt.eb = 118 * 8; tt.d1 = 0; tt.moved = true;
  const int cu_ = 32 + (rg * 32 + cb) / 2; (void)cu_;
  ST_DECL
  for (int s = 0; s < NSEG; ++s) {
    if (w == 7) wait_until(tt.tau);
    BAR();
    ST0();
    gather_mem(rs, lbase, s, wr, l16, cap);
    ST(1);
    LBAR();
    ST(2);
    {
      f32x4 c0 = {0.f, 0.f, 0.f, 0.f};
      LAS unsigned char* mp = lad(moff, lbase + R_MEM + kh * 8192);
      bf16x8 af[8];
#pragma unroll
      for (int c = 0; c < 8; ++c) af[c] = mem_frag(mp, c);
      SCHED();
#pragma unroll
      for (int c = 0; c < 8; ++c) c0 = smf(af[c], WV[c], c0, sidx);
      const f32x4 f = fold(c0, c0);
      if (l16 < 512) stx8(rs, l8, X_VM + (unsigned)s * RSTR + cb * 4096 + (rg * 4 + (w & 3)) * 512 + kh * 256, u32x2{nz(pack2(f[0], f[1])), nz(pack2(f[2], f[3]))});
    }
    ST(3);
    if (w == 7) retime(rs, l16, s, tt, p, cap);
    if (s + 1 < NSEG) {
      u32x4 a[4];
      const unsigned so = X_OX + s * RSTR + rg * 32768 + wr * 4096;
      if (w == 7) wait_until(tt.tau + tt.d1);
      BAR();
      ST(4);
      issue4(rs, l16, so, so + 1024, so + 2048, so + 3072, a[0], a[1], a[2], a[3]);
      f32x4 c[4];
#pragma unroll
      for (int t = 0; t < 4; ++t) c[t] = f32x4{0.f, 0.f, 0.f, 0.f};
#pragma unroll
      for (int i = 0; i < 4; ++i) {
        if (i == 0) arrive<0>(rs, l16, so, a[0], cap); else if (i == 1) arrive<1>(rs, l16, so + 1024, a[1], cap);
        else if (i == 2) arrive<2>(rs, l16, so + 2048, a[2], cap); else arrive<3>(rs, l16, so + 3072, a[3], cap);
#pragma unroll
        for (int t = 0; t < 4; ++t) c[t] = smf(__builtin_bit_cast(bf16x8, a[i]), WB[i * 4 + t], c[t], sidx);
        SCHED();
      }
      ST(5);
      {
        const bool odd = (l16 & 256) != 0;
        const f32x4 f = fold(odd ? c[2] : c[0], odd ? c[3] : c[1]);
        const auto e0 = __builtin_amdgcn_permlane16_swap(f2u(f[0]), f2u(f[2]), false, false), e1 = __builtin_amdgcn_permlane16_swap(f2u(f[1]), f2u(f[3]), false, false);
        *(LAS f32x2*)(lad(roff, lbase)) = f32x2{u2f(e0[0]) + u2f(e0[1]), u2f(e1[0]) + u2f(e1[1])};
      }
      ST(6);
      LBAR();
      ST(7);
      if (w == 0) {
        LAS unsigned char* rp = lad(l8, lbase + R_RED);
        f32x2 v[8];
#pragma unroll
        for (int i = 0; i < 8; ++i) v[i] = *(const LAS f32x2*)(rp + i * 512);
        const f32x2 sum = ((v[0] + v[1]) + (v[2] + v[3])) + ((v[4] + v[5]) + (v[6] + v[7]));
        stx4(rs, soff, X_MEM + (s + 1) * RSTR + (cb >> 1) * 1024 + rg * 512 + ((cb ^ rg) & 1) * 128, nz(pack2(sum[0], sum[1])));
        const unsigned t = max(rt32(), 1u);
        if (l16 == 0) stx4(rs, 0, X_TS + (unsigned)(s + 1) * RSTR + (unsigned)(rg * 32 + cb) * 128, t);
      }
      ST(8);
    }
    advance(tt, p);
  }
  ST_FLUSH();
  if (cap == 0u && l16 == 0) stx4(rs, 0, X_CTL, 1u);
}

#if defined(__HIP_DEVICE_COMPILE__)
__attribute__((target("no-packed-fp32-ops")))
#endif
__global__ void __launch_bounds__(512, 2) k_main(MainP p) {
  extern __shared__ __attribute__((aligned(16))) unsigned char smem[];
  const int b = blockIdx.x, x = b & 7, j = b >> 3;
  if (x >= 3) worker(p, (LAS unsigned char*)smem, x - 3, j);
  else if (x == 0) role_a(p, (LAS unsigned char*)smem, j);
  else role_vb(p, (LAS unsigned char*)smem, x - 1, j);
}

extern "C" void kernel_launch(void* const* d_in, const int* in_sizes, int n_in, void* d_out, int out_size, void* d_ws, size_t ws_size,
                              hipStream_t stream) {
  unsigned char* ws = (unsigned char*)d_ws;
  (void)hipMemsetAsync(ws, 0, X_ZERO, stream);
  MainP p;
  p.x = (const u16*)d_in[0]; p.mem0 = (const u16*)d_in[1]; p.c = (const u16*)d_in[12]; p.wmod = (const u16*)d_in[13]; p.Wq = (const u16*)d_in[2]; p.Wk = (const u16*)d_in[3]; p.Wv = (const u16*)d_in[4]; p.Wo = (const u16*)d_in[5];
  p.w1 = (const u16*)d_in[8]; p.w2 = (const u16*)d_in[9]; p.w3 = (const u16*)d_in[10]; p.w4 = (const u16*)d_in[11];
  p.out = (u16*)d_out; p.ws = ws; p.d1 = 10; p.d2 = 10; p.d3 = 2; p.d4 = 100;
  (void)hipFuncSetAttribute((const void*)k_main, hipFuncAttributeMaxDynamicSharedMemorySize, R_LDS);
  k_main<<<256, 512, R_LDS, stream>>>(p);
}
```

```cpp
#include <hip/hip_runtime.h>
#include <hip/hip_bf16.h>
#include <cstdlib>
#pragma clang diagnostic ignored "-Winline-asm"

#define LAS __attribute__((address_space(3)))
#define GAS __attribute__((address_space(1)))
typedef unsigned short u16;
typedef __bf16 bf16x2 __attribute__((ext_vector_type(2)));
typedef __bf16 bf16x4 __attribute__((ext_vector_type(4)));
typedef __bf16 bf16x8 __attribute__((ext_vector_type(8)));
typedef __bf16 bf16x16 __attribute__((ext_vector_type(16)));
typedef short s16x4 __attribute__((ext_vector_type(4)));
typedef float f32x2 __attribute__((ext_vector_type(2)));
typedef float f32x4 __attribute__((ext_vector_type(4)));
typedef float f32x16 __attribute__((ext_vector_type(16)));
typedef unsigned u32x2 __attribute__((ext_vector_type(2)));
typedef unsigned u32x4 __attribute__((ext_vector_type(4)));

constexpr int S_ = 2048, D_ = 1024, NH = 32, DH = 128, HD = 4096, NSEG = 64, FF = 3584;

constexpr unsigned MB = 1u << 20;
constexpr unsigned F_LN = 0;
constexpr unsigned F_ST = 24576;
constexpr unsigned F_CH = F_ST + 98304;
constexpr unsigned X_CTL = F_CH + 4096;
constexpr unsigned X_MOD = X_CTL + 4096;
constexpr unsigned X_REC = X_MOD + 24576;
constexpr unsigned RSTR = 225280;
constexpr int ZA = 8;
constexpr unsigned X_MEM = X_REC;
constexpr unsigned X_OX = X_REC + 16384;
constexpr unsigned X_VM = X_REC + 81920;
constexpr unsigned X_TS = X_REC + 212992;
constexpr unsigned X_ZERO = X_REC + ZA * RSTR;
constexpr unsigned WS_H = 16 * MB;
constexpr unsigned WS_Q = 20 * MB;
constexpr unsigned WS_K = 36 * MB;
constexpr unsigned WS_VT = 52 * MB;
constexpr unsigned WS_O = 68 * MB;
constexpr unsigned WS_X1 = 84 * MB;
constexpr unsigned WS_H2 = 88 * MB;
constexpr unsigned WS_G = 92 * MB;
constexpr unsigned WS_H1S = 96 * MB;
constexpr unsigned WS_X2 = 101 * MB;
constexpr unsigned WS_ST5 = 106 * MB;
constexpr unsigned WS_U = 108 * MB;
constexpr unsigned WS_KM = 124 * MB;
constexpr unsigned WS_P2 = 132 * MB;
constexpr unsigned WS_P8 = 164 * MB;
constexpr unsigned WS_END = 198 * MB;

__device__ __forceinline__ float bf2f(unsigned short b) { return __builtin_bit_cast(float, (unsigned)b << 16); }
__device__ __forceinline__ unsigned f2u(float f) { return __builtin_bit_cast(unsigned, f); }
__device__ __forceinline__ float u2f(unsigned u) { return __builtin_bit_cast(float, u); }
__device__ __forceinline__ float lo2f(unsigned w) { return __builtin_bit_cast(float, w << 16); }
__device__ __forceinline__ float hi2f(unsigned w) { return __builtin_bit_cast(float, w & 0xFFFF0000u); }
__device__ __forceinline__ unsigned pack2(float a, float b) { bf16x2 v; v[0] = (__bf16)a; v[1] = (__bf16)b; return __builtin_bit_cast(unsigned, v); }
__device__ __forceinline__ float rbf(float a) { return (float)(__bf16)a; }
__device__ __forceinline__ unsigned nz(unsigned w) { return max(w, 1u); }
__device__ __forceinline__ float gelu_tanh(float x) {
  const float e = __builtin_amdgcn_exp2f(x * fmaf(x * x, -0.1029432397f, -2.302208198f));
  return x * __builtin_amdgcn_rcpf(1.0f + e);
}
template <int CTRL> __device__ __forceinline__ float dpp_add(float v) { return v + u2f(__builtin_amdgcn_update_dpp(0u, f2u(v), CTRL, 0xF, 0xF, true)); }
__device__ __forceinline__ float wave_sum(float v) {
  v = dpp_add<0xB1>(v); v = dpp_add<0x4E>(v); v = dpp_add<0x141>(v); v = dpp_add<0x140>(v);
  auto t = __builtin_amdgcn_permlane16_swap(f2u(v), f2u(v), false, false);
  v = u2f(t[0]) + u2f(t[1]);
  t = __builtin_amdgcn_permlane32_swap(f2u(v), f2u(v), false, false);
  return u2f(t[0]) + u2f(t[1]);
}
#define WAIT_V(n) asm volatile("s_waitcnt vmcnt(" #n ")" ::: "memory")
#define WAIT_L0() asm volatile("s_waitcnt lgkmcnt(0)" ::: "memory")
#define BAR() do { asm volatile("" ::: "memory"); __builtin_amdgcn_s_barrier(); asm volatile("" ::: "memory"); } while (0)
#define LBAR() do { WAIT_L0(); BAR(); } while (0)
template <int N> __device__ __forceinline__ void wait_v() { asm volatile("s_waitcnt vmcnt(%0)" :: "n"(N) : "memory"); }
__device__ __forceinline__ void dma16(__amdgpu_buffer_rsrc_t rs, unsigned voff, unsigned soff, unsigned ldsa) {
  asm volatile("s_mov_b32 m0, %0\n\ts_nop 3\n\tbuffer_load_dwordx4 %1, %2, %3 offen lds" :: "s"(ldsa), "v"(voff), "s"(rs), "s"(soff) : "m0", "memory");
}
__device__ __forceinline__ void dma16x(__amdgpu_buffer_rsrc_t rs, unsigned voff, unsigned soff, unsigned ldsa) {
  asm volatile("s_mov_b32 m0, %0\n\ts_nop 3\n\tbuffer_load_dwordx4 %1, %2, %3 offen sc1 lds" :: "s"(ldsa), "v"(voff), "s"(rs), "s"(soff) : "m0", "memory");
}
__device__ __forceinline__ u32x4 ldx16(__amdgpu_buffer_rsrc_t rs, unsigned voff, unsigned soff) { return __builtin_amdgcn_raw_buffer_load_b128(rs, voff, soff, 16); }
__device__ __forceinline__ u32x2 ldx8(__amdgpu_buffer_rsrc_t rs, unsigned voff, unsigned soff) { return __builtin_amdgcn_raw_buffer_load_b64(rs, voff, soff, 16); }
__device__ __forceinline__ unsigned ldx4(__amdgpu_buffer_rsrc_t rs, unsigned voff, unsigned soff) { return __builtin_amdgcn_raw_buffer_load_b32(rs, voff, soff, 16); }
__device__ __forceinline__ void stx16(__amdgpu_buffer_rsrc_t rs, unsigned voff, unsigned soff, u32x4 v) {
  __builtin_amdgcn_raw_buffer_store_b128(v, rs, voff, soff, 16);
  asm volatile("s_nop 1" :: "v"(v) : "memory");
}
__device__ __forceinline__ void stx8(__amdgpu_buffer_rsrc_t rs, unsigned voff, unsigned soff, u32x2 v) { __builtin_amdgcn_raw_buffer_store_b64(v, rs, voff, soff, 16); }
__device__ __forceinline__ void stx4(__amdgpu_buffer_rsrc_t rs, unsigned voff, unsigned soff, unsigned v) { __builtin_amdgcn_raw_buffer_store_b32(v, rs, voff, soff, 16); }
__device__ __forceinline__ void wait_ge(__amdgpu_buffer_rsrc_t rs, unsigned voff, unsigned soff, unsigned need, unsigned& cap) {
  unsigned t;
  asm volatile("s_nop 4\n\t"
               "1:\n\t"
               "buffer_load_dword %[t], %[vo], %[rs], %[so] offen sc1\n\t"
               "s_waitcnt vmcnt(0)\n\t"
               "v_cmp_gt_u32 vcc, %[need], %[t]\n\t"
               "s_cmp_eq_u64 vcc, 0\n\t"
               "s_cbranch_scc1 2f\n\t"
               "s_sleep 2\n\t"
               "s_sub_u32 %[cap], %[cap], 1\n\t"
               "s_cbranch_scc0 1b\n\t"
               "s_mov_b32 %[cap], 0\n\t"
               "2:"
               : [t] "=&v"(t), [cap] "+s"(cap) : [vo] "v"(voff), [rs] "s"(rs), [so] "s"(soff), [need] "s"(need) : "vcc", "scc", "memory");
}
__device__ __forceinline__ void wait_seen(__amdgpu_buffer_rsrc_t rs, unsigned voff, unsigned soff, unsigned need, unsigned& seen, unsigned& cap) {
  if (seen >= need) return;
  unsigned t;
  asm volatile("s_nop 4\n\t"
               "1:\n\t"
               "buffer_load_dword %[t], %[vo], %[rs], %[so] offen sc1\n\t"
               "s_waitcnt vmcnt(0)\n\t"
               "v_cmp_gt_u32 vcc, %[need], %[t]\n\t"
               "s_cbranch_vccz 2f\n\t"
               "s_sleep 1\n\t"
               "s_sub_u32 %[cap], %[cap], 1\n\t"
               "s_cbranch_scc0 1b\n\t"
               "s_mov_b32 %[cap], 0\n\t"
               "2:"
               : [t] "=&v"(t), [cap] "+s"(cap) : [vo] "v"(voff), [rs] "s"(rs), [so] "s"(soff), [need] "s"(need) : "vcc", "scc", "memory");
  seen = min((unsigned)__builtin_amdgcn_readlane(t, 0), (unsigned)__builtin_amdgcn_readlane(t, 1));
}
__device__ __forceinline__ __amdgpu_buffer_rsrc_t mk_rs(const void* p, unsigned bytes) { return __builtin_amdgcn_make_buffer_rsrc((void*)p, 0, (int)bytes, 0x00020000); }
#define SCHED() __builtin_amdgcn_sched_barrier(0)
__device__ __forceinline__ unsigned now32() { unsigned long long t; asm volatile("s_memtime %0\n\ts_waitcnt lgkmcnt(0)" : "=s"(t) :: "memory"); return (unsigned)t; }
__device__ __forceinline__ unsigned rt32() { unsigned long long t; asm volatile("s_memrealtime %0\n\ts_waitcnt lgkmcnt(0)" : "=s"(t) :: "memory"); return (unsigned)t; }
__device__ __forceinline__ void wait_until(unsigned t) { while ((int)(rt32() - t) < 0) {} }
template <int X = 0> __device__ __forceinline__ LAS unsigned char* lad(unsigned v, unsigned u) {
  unsigned a;
  if (X == 0) asm volatile("v_add_u32 %0, %1, %2" : "=v"(a) : "s"(u), "v"(v));
  else asm volatile("v_xor_b32 %0, %3, %2\n\tv_add_u32 %0, %1, %0" : "=&v"(a) : "s"(u), "v"(v), "n"(X));
  return (LAS unsigned char*)(size_t)a;
}
__device__ __forceinline__ bf16x4 trread(LAS unsigned char* p) { return __builtin_amdgcn_ds_read_tr16_b64_v4bf16((LAS bf16x4*)p); }
__device__ __forceinline__ bf16x8 cat8(bf16x4 a, bf16x4 b) { return __builtin_shufflevector(a, b, 0, 1, 2, 3, 4, 5, 6, 7); }
__device__ __forceinline__ bf16x16 cat16(bf16x8 a, bf16x8 b) { return __builtin_shufflevector(a, b, 0, 1, 2, 3, 4, 5, 6, 7, 8, 9, 10, 11, 12, 13, 14, 15); }
__device__ __forceinline__ f32x4 mfma32(bf16x8 a, bf16x8 b, f32x4 c) { return __builtin_amdgcn_mfma_f32_16x16x32_bf16(a, b, c, 0, 0, 0); }
__device__ __forceinline__ f32x4 mfma16(u32x2 a, u32x2 b, f32x4 c) {
  return mfma32(__builtin_bit_cast(bf16x8, u32x4{a[0], a[1], 0u, 0u}), __builtin_bit_cast(bf16x8, u32x4{b[0], b[1], 0u, 0u}), c);
}
__device__ __forceinline__ f32x4 mfma16n(u32x2 a, u32x2 b, f32x4 c) { return __builtin_amdgcn_mfma_f32_16x16x16bf16_1k(__builtin_bit_cast(s16x4, a), __builtin_bit_cast(s16x4, b), c, 0, 0, 0); }
__device__ __forceinline__ f32x4 smf(bf16x8 a, bf16x16 b, f32x4 c, int idx) { return __builtin_amdgcn_smfmac_f32_16x16x64_bf16(a, b, c, idx, 0, 0); }

__device__ __forceinline__ void ln_row(float (&x)[16]) {
  float s = 0.f;
#pragma unroll
  for (int i = 0; i < 16; ++i) s += x[i];
  const float mean = wave_sum(s) * (1.0f / 1024.0f);
  float q = 0.f;
#pragma unroll
  for (int i = 0; i < 16; ++i) { const float d = x[i] - mean; q += d * d; }
  const float var = wave_sum(q) * (1.0f / 1024.0f);
  const float m = rbf(mean), isd = __builtin_amdgcn_rcpf(rbf(__builtin_amdgcn_sqrtf(rbf(rbf(var) + 1e-5f))));
#pragma unroll
  for (int i = 0; i < 16; ++i) x[i] = rbf(rbf(x[i] - m) * isd);
}
__device__ __forceinline__ void load_row16(const u16* rowp, int lane, float (&x)[16]) {
  const u32x4 a = *(const u32x4*)(rowp + lane * 8), b = *(const u32x4*)(rowp + 512 + lane * 8);
#pragma unroll
  for (int i = 0; i < 4; ++i) { x[2 * i] = lo2f(a[i]); x[2 * i + 1] = hi2f(a[i]); x[8 + 2 * i] = lo2f(b[i]); x[9 + 2 * i] = hi2f(b[i]); }
}
__device__ __forceinline__ void store_row16(u16* rowp, int lane, const float (&x)[16]) {
  u32x4 a, b;
#pragma unroll
  for (int i = 0; i < 4; ++i) { a[i] = pack2(x[2 * i], x[2 * i + 1]); b[i] = pack2(x[8 + 2 * i], x[9 + 2 * i]); }
  *(u32x4*)(rowp + lane * 8) = a; *(u32x4*)(rowp + 512 + lane * 8) = b;
}
__host__ __device__ __forceinline__ int mem_slot(int dl) { return ((dl >> 3) & 1) * 32 + (dl >> 4) * 8 + (dl & 4) + (dl & 1) * 2 + ((dl >> 1) & 1); }

#define WST(rs, wk, i)
struct MainP { const u16 *x, *mem0, *Wq, *Wk, *Wv, *Wo, *w1, *w2, *w3, *w4, *c, *wmod; u16* out; unsigned char* ws; int d1, d2, d3, d4; };
constexpr unsigned SPIN_CAP = 1u << 18;

__device__ __forceinline__ float gmax(float v) {
  auto t = __builtin_amdgcn_permlane16_swap(f2u(v), f2u(v), false, false);
  v = fmaxf(u2f(t[0]), u2f(t[1]));
  t = __builtin_amdgcn_permlane32_swap(f2u(v), f2u(v), false, false);
  return fmaxf(u2f(t[0]), u2f(t[1]));
}
__device__ __forceinline__ float gsum(float v) {
  auto t = __builtin_amdgcn_permlane16_swap(f2u(v), f2u(v), false, false);
  v = u2f(t[0]) + u2f(t[1]);
  t = __builtin_amdgcn_permlane32_swap(f2u(v), f2u(v), false, false);
  return u2f(t[0]) + u2f(t[1]);
}
__device__ __forceinline__ void publish(__amdgpu_buffer_rsrc_t rs, unsigned word, unsigned val) {
  WAIT_V(0); BAR();
  if (threadIdx.x == 0) stx4(rs, 0, word, val);
}

constexpr int QB0 = 24;
constexpr int P_S2 = 0, P_S8 = 32, P_S7 = 64, P_S4 = 96, P_S5 = 104, P_S3 = 112, P_PJ = 208, P_S1 = 336;
constexpr int NLN = 160;

enum { SG_WO = 0, SG_W1 = 1, SG_W2 = 2, SG_W3 = 3, SG_W4 = 4, SG_PJ = 5 };
constexpr int G_XCH = 131072, G_K = G_XCH + 16384, G_CTL = G_K + 8192;
__device__ __forceinline__ f32x16 mfma3232(bf16x8 a, bf16x8 b, f32x16 c) { return __builtin_amdgcn_mfma_f32_32x32x16_bf16(a, b, c, 0, 0, 0); }
__device__ __forceinline__ void widen32(const f32x16& a, u32x4 (&v)[2]) {
#pragma unroll
  for (int u = 0; u < 2; ++u) {
    const auto r0 = __builtin_amdgcn_permlane32_swap(pack2(a[8 * u], a[8 * u + 1]), pack2(a[8 * u + 4], a[8 * u + 5]), false, false);
    const auto r1 = __builtin_amdgcn_permlane32_swap(pack2(a[8 * u + 2], a[8 * u + 3]), pack2(a[8 * u + 6], a[8 * u + 7]), false, false);
    v[u] = u32x4{r0[0], r1[0], r0[1], r1[1]};
  }
}
__device__ __forceinline__ float hsum(float v) { const auto t = __builtin_amdgcn_permlane32_swap(f2u(v), f2u(v), false, false); return u2f(t[0]) + u2f(t[1]); }

__device__ __forceinline__ void attend(__amdgpu_buffer_rsrc_t rs, int seg, int h, int t, int lane, unsigned& chs, unsigned& cap) {
  const int fr = lane & 15, g = lane >> 4;
  const unsigned l16 = lane * 16, ub = (unsigned)(seg * NH + h);
  wait_seen(rs, 0, F_CH + h * 128, (unsigned)max(seg, 1), chs, cap);
  wait_ge(rs, (lane & 1) && seg >= QB0 ? 96u * 128u : (unsigned)(seg % 3) * 4096u, F_ST + (unsigned)(P_PJ + h) * 128, (unsigned)seg + 1u, cap);
  f32x4 s0 = {0.f, 0.f, 0.f, 0.f}, s1 = s0, s2 = s0;
  u32x4 q[4], kmf[4];
  u32x2 vmf[8];
  const unsigned em = (unsigned)(fr >> 3) * 512 + (unsigned)g * 128 + (unsigned)(8 * ((fr >> 2) & 1) + (fr & 3)) * 8;
  auto mem_load = [&](unsigned off) {
#pragma unroll
    for (int ks = 0; ks < 4; ++ks) kmf[ks] = ldx16(rs, l16 + off, WS_KM + ub * 4096 + ks * 1024);
#pragma unroll
    for (int i = 0; i < 8; ++i) vmf[i] = ldx8(rs, em + off, X_VM + (unsigned)seg * RSTR + h * 4096 + (i >> 1) * 1024 + (i & 1) * 32);
  };
  const bool have = chs > (unsigned)seg;
  f32x4 o[8];
  float mt = -3.0e38f, lt = 0.f;
  const float scale = 0.08838834764831845f, L2E = 1.4426950408889634f;
  {
    u32x4 k0[4], k1[4], vv[8];
#pragma unroll
    for (int ks = 0; ks < 4; ++ks) {
      q[ks] = ldx16(rs, l16, WS_Q + ub * 8192 + t * 4096 + ks * 1024);
      k0[ks] = ldx16(rs, l16, WS_K + ub * 8192 + ks * 1024); k1[ks] = ldx16(rs, t ? l16 : 0xF0000000u, WS_K + ub * 8192 + 4096 + ks * 1024);
    }
    const unsigned ev = (unsigned)(fr >> 3) * 1024 + (unsigned)(g * 16 + 8 * ((fr >> 2) & 1) + (fr & 3)) * 16;
#pragma unroll
    for (int i = 0; i < 8; ++i) vv[i] = ldx16(rs, ev, WS_VT + ub * 8192 + (i >> 1) * 2048 + (i & 1) * 64);
#pragma unroll
    for (int ks = 0; ks < 4; ++ks) {
      s1 = mfma32(__builtin_bit_cast(bf16x8, k0[ks]), __builtin_bit_cast(bf16x8, q[ks]), s1); s2 = mfma32(__builtin_bit_cast(bf16x8, k1[ks]), __builtin_bit_cast(bf16x8, q[ks]), s2);
    }
    SCHED();
    mem_load(have ? 0u : 0xF0000000u);
    SCHED();
    float b[4], c[4];
#pragma unroll
    for (int r = 0; r < 4; ++r) {
      const int j = 4 * g + r;
      b[r] = (t == 1 || j <= fr) ? rbf(rbf(s1[r]) * scale) : -3.0e38f;
      c[r] = (t == 1 && j <= fr) ? rbf(rbf(s2[r]) * scale) : -3.0e38f;
      mt = fmaxf(mt, fmaxf(b[r], c[r]));
    }
    mt = gmax(mt);
#pragma unroll
    for (int r = 0; r < 4; ++r) { b[r] = __builtin_amdgcn_exp2f((b[r] - mt) * L2E); c[r] = __builtin_amdgcn_exp2f((c[r] - mt) * L2E); lt += b[r] + c[r]; }
    const u32x4 pt = {pack2(b[0], b[1]), pack2(b[2], b[3]), pack2(c[0], c[1]), pack2(c[2], c[3])};
#pragma unroll
    for (int i = 0; i < 8; ++i) o[i] = mfma32(__builtin_bit_cast(bf16x8, vv[i]), __builtin_bit_cast(bf16x8, pt), f32x4{0.f, 0.f, 0.f, 0.f});
    lt = gsum(lt);
  }
  asm volatile("" : "+v"(o[0]), "+v"(o[1]), "+v"(o[2]), "+v"(o[3]), "+v"(o[4]), "+v"(o[5]), "+v"(o[6]), "+v"(o[7]), "+v"(lt));
  if (!have) { wait_seen(rs, 0, F_CH + h * 128, (unsigned)seg + 1u, chs, cap); mem_load(0u); }
#pragma unroll
  for (int ks = 0; ks < 4; ++ks) s0 = mfma32(__builtin_bit_cast(bf16x8, kmf[ks]), __builtin_bit_cast(bf16x8, q[ks]), s0);
  float a[4];
#pragma unroll
  for (int r = 0; r < 4; ++r) a[r] = rbf(rbf(s0[r]) * scale);
  const float mx = fmaxf(mt, gmax(fmaxf(fmaxf(a[0], a[1]), fmaxf(a[2], a[3]))));
#pragma unroll
  for (int r = 0; r < 4; ++r) a[r] = __builtin_amdgcn_exp2f((a[r] - mx) * L2E);
  const u32x2 pm = {pack2(a[0], a[1]), pack2(a[2], a[3])};
  const float al = __builtin_amdgcn_exp2f((mt - mx) * L2E);
  const float inv = __builtin_amdgcn_rcpf(fmaf(al, lt, 0.5f * gsum((a[0] + a[1]) + (a[2] + a[3])))), ai = al * inv;
#pragma unroll
  for (int u = 0; u < 4; ++u) {
    const f32x4 x = o[2 * u] * ai + mfma16n(vmf[2 * u], pm, f32x4{0.f, 0.f, 0.f, 0.f}) * inv, y = o[2 * u + 1] * ai + mfma16n(vmf[2 * u + 1], pm, f32x4{0.f, 0.f, 0.f, 0.f}) * inv;
    stx16(rs, l16, WS_O + (unsigned)seg * 262144 + (unsigned)((h * 4 + u) * 2 + t) * 1024, u32x4{pack2(x[0], x[1]), pack2(x[2], x[3]), pack2(y[0], y[1]), pack2(y[2], y[3])});
  }
  WAIT_V(0);
  if (lane == 0) stx4(rs, 0, F_ST + (unsigned)(P_S1 + (seg & 3) * 64 + h * 2 + t) * 128, (unsigned)seg + 1u);
}

template <int SG>
__device__ __forceinline__ void stage_gemm(LAS unsigned char* lds, __amdgpu_buffer_rsrc_t rs, const u16* Wg, int wk, int kq, int cb, int pid, unsigned& cap,
                                           unsigned dst = 0, int bstr = 1, int nb = 0, const u16* side = nullptr, int srows = 0, int sld = 0, const u16* side3 = nullptr) {
  constexpr bool PROJ = SG == SG_PJ, ROWM = (SG == SG_W1 || PROJ);
  constexpr int KS = (SG == SG_W4) ? 28 : 32;
  constexpr int LDW = PROJ ? HD : (SG == SG_W3) ? FF : D_;
  constexpr int WROWS = (SG == SG_WO) ? HD : (SG == SG_W4 ? FF : D_);
  const int NBLK = PROJ ? nb : (SG == SG_WO || SG == SG_W1) ? 64 : 65;
  constexpr unsigned ABASE = PROJ ? WS_H : SG == SG_WO ? WS_O : SG == SG_W1 ? WS_G : SG == SG_W2 ? WS_H1S : SG == SG_W3 ? WS_X2 : WS_U;
  constexpr unsigned ASTR = SG == SG_WO ? 262144u : SG == SG_W4 ? 229376u : 65536u;
  constexpr int NPC = KS / 4;
  const unsigned myword = F_ST + (unsigned)pid * 128u;
  int tid = threadIdx.x; asm volatile("" : "+v"(tid));
  const int lane = tid & 63, w = __builtin_amdgcn_readfirstlane(tid >> 6), row = lane & 31, hf = lane >> 5;
  const int kh = w >> 2, wl = w & 3;
  const int koff = PROJ ? 0 : kq;
  const unsigned lbase = (unsigned)(size_t)lds, l16 = lane * 16;
  const int col0 = cb * 128 + wl * 32;
  bf16x8 Wf[KS];
  {
    const __amdgpu_buffer_rsrc_t rsW = mk_rs(Wg, (unsigned)WROWS * LDW * 2u);
    const int ub = lane >> 3, tu = 4 * hf + ((lane & 15) >> 2);
    const unsigned vB = (unsigned)(((ub & 3) + 8 * (ub >> 2)) * LDW + 16 * (((lane >> 1) & 3) ^ (ub >> 1)) + 8 * (lane & 1)) * 2u;
    const unsigned sB = (unsigned)((koff * KS * 32 + 32 * (w >> 2) + 16 * ((w >> 1) & 1) + 4 * (w & 1)) * LDW + cb * 128) * 2u;
    const int boff = kh * 65536 + (wl >> 1) * 8192 + tu * 128 + (((tu >> 1) ^ (2 * (wl & 1) + ((lane >> 4) & 1))) << 5) + (lane & 3) * 8;
#pragma unroll
    for (int ps = 0; ps < 2; ++ps) {
      BAR();
#pragma unroll
      for (int hh = 0; hh < 2; ++hh)
#pragma unroll
        for (int i = 0; i < 4; ++i)
          if (4 * ps + i < NPC && !(PROJ && ps == 0)) {
            const unsigned so = sB + (unsigned)((hh * NPC + 4 * ps + i) * 64 * LDW) * 2u, d = lbase + hh * 65536 + i * 16384 + w * 1024;
            dma16(rsW, vB, so, d); dma16(rsW, vB, so + 128, d + 8192);
          }
      WAIT_V(0); BAR();
#pragma unroll
      for (int i = 0; i < 4; ++i)
        if (4 * ps + i < NPC) {
#pragma unroll
          for (int k = 0; k < 4; ++k) { LAS unsigned char* q = lds + (boff + i * 16384 + k * 2048); Wf[(4 * ps + i) * 4 + k] = cat8(trread(q), trread(q + 1024)); }
        }
      WAIT_L0();
    }
    if (SG == SG_W1) {
      const u32x4 z = {0u, 0u, 0u, 0u};
      stx16(rs, (unsigned)tid * 16, WS_H1S + cb * 8192, z); stx16(rs, (unsigned)tid * 16, WS_H1S + 64 * 65536 + cb * 8192, z);
      WAIT_V(0);
    }
    if (tid < 8) *(LAS unsigned*)(lds + G_CTL + tid * 4) = 0u;
    LBAR();
  }
  const f32x16 zero16 = {0.f, 0.f, 0.f, 0.f, 0.f, 0.f, 0.f, 0.f, 0.f, 0.f, 0.f, 0.f, 0.f, 0.f, 0.f, 0.f};
  auto put16 = [&](LAS unsigned char* p, const f32x16& a) {
#pragma unroll
    for (int q = 0; q < 4; ++q) *(LAS f32x4*)(p + q * 1024) = f32x4{a[4 * q], a[4 * q + 1], a[4 * q + 2], a[4 * q + 3]};
  };
  if (SG == SG_W3) {
    const u32x4 one4 = {0x3F803F80u, 0x3F803F80u, 0x3F803F80u, 0x3F803F80u};
    f32x16 cs = zero16;
#pragma unroll
    for (int k = 0; k < KS; ++k) cs = mfma3232(Wf[k], __builtin_bit_cast(bf16x8, one4), cs);
    if (kh == 0) put16(lds + wl * 4096 + l16, cs);
    LBAR();
    if (kh == 1 && row == 0) {
#pragma unroll
      for (int q = 0; q < 4; ++q)
        *(LAS f32x4*)(lds + G_K + wl * 128 + hf * 64 + q * 16) = *(const LAS f32x4*)(lds + wl * 4096 + q * 1024 + l16) + f32x4{cs[4 * q], cs[4 * q + 1], cs[4 * q + 2], cs[4 * q + 3]};
    }
    LBAR();
  }
  auto blk = [&](int i) -> int { return PROJ ? bstr * i + kq : i; };
  const unsigned aoff = ROWM ? (unsigned)(row * 128 + ((hf ^ ((row >> 1) & 7)) << 4)) : (unsigned)((row >> 4) * 1024 + hf * 256 + (row & 15) * 16);
  const unsigned vA = ROWM ? (unsigned)((lane >> 3) * 2048 + (((lane & 7) ^ ((4 * wl + (lane >> 4)) & 7)) << 4)) : l16;
  auto piece = [&](int hb, int q, bool on) {
    const int i = hb >> 1, hh = hb & 1;
    const unsigned so = ABASE + (unsigned)blk(i) * ASTR + (unsigned)koff * (KS * 2048) + (ROWM ? wl * 16384 + hh * 1024 : (hh * KS + wl) * 1024);
    if (on) dma16x(rs, vA, so + q * (ROWM ? 128 : 4096), lbase + (i & 1) * 65536 + hh * 32768 + wl * 1024 + q * 4096);
  };
  auto fetch = [&](int hb) {
#pragma unroll
    for (int q = 0; q < NPC; ++q) piece(hb, q, true);
  };
  constexpr bool WEAVE = (SG == SG_W1 || SG == SG_W2 || SG == SG_W3);
  auto products = [&](int i, f32x16& acc, auto&& first, auto&& early, auto&& handover) {
    const unsigned ub = lbase + (i & 1) * 65536 + kh * 32768;
    LAS unsigned char* ab[4];
    ab[0] = lad(aoff, ub);
    if (ROWM) { ab[1] = lad<32>(aoff, ub); ab[2] = lad<64>(aoff, ub); ab[3] = lad<96>(aoff, ub); }
    constexpr int PD = 10;
    bf16x8 af[PD + 1];
    auto rd = [&](int k) { af[k % (PD + 1)] = ROWM ? *(const LAS bf16x8*)(ab[k & 3] + (k >> 2) * 4096) : *(const LAS bf16x8*)(ab[0] + (k >> 1) * 2048 + (k & 1) * 512); };
#pragma unroll
    for (int k = 0; k < PD; ++k) rd(k);
    SCHED();
#pragma unroll
    for (int k = 0; k < KS; ++k) {
      if (k + PD < KS) rd(k + PD);
      acc = mfma3232(Wf[k], af[k % (PD + 1)], k ? acc : zero16);
      SCHED();
      first(k);
      SCHED();
      if (k == KS - 10) { early(); SCHED(); }
      if (k == KS - 5) { handover(); SCHED(); }
    }
    asm volatile("" : "+v"(acc));
  };
  WST(rs, wk, PROJ ? 2 : 10);
#define SEG(x)
  auto announce = [&](int i) {
    if (lane == 0) {
      const unsigned c = __hip_atomic_fetch_add((LAS unsigned*)(lds + G_CTL), 1u, __ATOMIC_RELAXED, __HIP_MEMORY_SCOPE_WORKGROUP);
      if ((c & 3u) == 3u) stx4(rs, 0, myword, (unsigned)blk(i) + 1u);
    }
  };
  if (kh == 0) {
    auto pvo = [&](int i) -> unsigned {
      const int b = blk(i);
      if (PROJ) return F_LN + (unsigned)((b % (NLN / 4)) * 4 + (lane & 3)) * 128u;
      if (SG == SG_WO) return F_ST + (unsigned)(P_S1 + (b & 3) * 64 + kq * 16 + (lane & 15)) * 128u;
      if (SG == SG_W1) return F_ST + (unsigned)(P_S3 + (b % 3) * 32 + (lane & 31)) * 128u;
      if (SG == SG_W2) return F_ST + (unsigned)(P_S4 + (lane & 7)) * 128u;
      if (SG == SG_W3) return F_ST + (unsigned)(P_S5 + (lane & 7)) * 128u;
      return F_ST + (unsigned)(P_S7 + kq * 7 + min(lane & 7, 6)) * 128u;
    };
    auto need = [&](int i) -> unsigned { return PROJ ? 1u : (SG == SG_W2 && i == 64) ? 64u : (unsigned)i + 1u; };
    int R = 0, Rp = 0;
    for (int i = 0; i < NBLK; ++i) {
      if (R <= i) { wait_ge(rs, pvo(i), 0, need(i), cap); fetch(2 * i); fetch(2 * i + 1); }
      const unsigned pk = w == 0 ? (lane < 32 ? pvo(i + 1) : pvo(i + 2)) : 0xF0000000u;
      if (Rp <= i) { if (R <= i + 1) wait_v<NPC>(); else wait_v<2 * NPC>(); }
      BAR();
      SEG(1);
      f32x16 acc;
      unsigned peek;
      asm volatile("buffer_load_dword %0, %1, %2, 0 offen sc1" : "=v"(peek) : "v"(pk), "s"(rs) : "memory");
      u32x4 st[8];
      float mean = 0.f, q2 = 0.f;
      if (SG == SG_W3) {
#pragma unroll
        for (int q = 0; q < 8; ++q) st[q] = ldx16(rs, l16, WS_ST5 + (unsigned)i * 8192 + q * 1024);
      }
      products(i, acc, [&](int k) {
        if (SG == SG_W3) {
          if (k >= 18 && k < 26) { mean += u2f(st[k - 18][0]) + u2f(st[k - 18][2]); q2 += u2f(st[k - 18][1]) + u2f(st[k - 18][3]); asm volatile("" : "+v"(mean), "+v"(q2)); }
          if (k == 26) { mean = hsum(mean) * (1.0f / 1024.0f); q2 = hsum(q2); asm volatile("" : "+v"(mean), "+v"(q2)); }
          if (k == 28) { q2 = rbf(rbf(fmaxf(fmaf(-mean, mean, q2 * (1.0f / 1024.0f)), 0.f)) + 1e-5f); asm volatile("" : "+v"(q2)); }
          if (k == 29) { q2 = __builtin_amdgcn_rcpf(rbf(__builtin_amdgcn_sqrtf(q2))); mean = rbf(mean); asm volatile("" : "+v"(q2), "+v"(mean)); }
        }
      }, []() {}, [&]() {
        SEG(4);
        WAIT_V(0);
        SEG(0);
        asm volatile("" : "+v"(peek));
        if (w == 0) {
          const unsigned long long ok = __ballot(peek >= (lane < 32 ? need(i + 1) : need(i + 2)));
          const bool r1 = (R >= i + 2 || (unsigned)ok == 0xFFFFFFFFu) && i + 1 < NBLK, r2 = r1 && (unsigned)(ok >> 32) == 0xFFFFFFFFu && i + 2 < NBLK;
          if (lane == 0) *(volatile LAS unsigned*)(lds + G_CTL + 4) = (unsigned)(i + 1 + (r1 ? 1 : 0) + (r2 ? 1 : 0));
        }
        LBAR();
      });
      SEG(2);
      const unsigned rv = *(const volatile LAS unsigned*)(lds + G_CTL + 4);
      if (SG == SG_W3) {
        const float m = mean;
        *(LAS float*)(lds + G_K + 1024 + wl * 256 + lane * 4) = q2;
#pragma unroll
        for (int q = 0; q < 4; ++q) {
          const f32x4 cs = *(const LAS f32x4*)(lds + G_K + wl * 128 + hf * 64 + q * 16);
#pragma unroll
          for (int e = 0; e < 4; ++e) acc[4 * q + e] = fmaf(-m, cs[e], acc[4 * q + e]);
        }
      }
      put16(lad(l16, lbase + G_XCH + wl * 4096), acc);
      asm volatile("" ::: "memory");
      if (lane == 0) *(volatile LAS unsigned*)(lds + G_CTL + 16 + wl * 4) = (unsigned)i + 1u;
      Rp = R; R = __builtin_amdgcn_readfirstlane(rv);
      SEG(3);
      if (R >= i + 2 && Rp < i + 2) { fetch(2 * i + 2); fetch(2 * i + 3); }
      if (R >= i + 3) fetch(2 * i + 4);
      if (PROJ && i < 16) {
        if (side3) dma16(mk_rs(side3, 0x7FFFFFFFu), (unsigned)((lane >> 2) * 6144 + (lane & 3) * 8) * 2u, (unsigned)((64 * i + 16 * wl) * 6144) * 2u, lbase + G_K);
        if (64 * i < srows) {
          const __amdgpu_buffer_rsrc_t rsN = mk_rs(side, 0x7FFFFFFFu);
#pragma unroll
          for (int q = 0; q < 4; ++q) dma16(rsN, (unsigned)((lane >> 4) * sld + (lane & 15) * 8) * 2u, (unsigned)((64 * i + 16 * wl + 4 * q) * sld) * 2u, lbase + G_K + wl * 2048);
        }
      }
      SEG(5);
    }
    BAR();
  } else {
    bool pending = false;
    BAR();
    for (int i = 0; i < NBLK; ++i) {
      const int b = blk(i);
      BAR();
      SEG(0);
      const unsigned rv = *(const volatile LAS unsigned*)(lds + G_CTL + 4);
      u32x4 res[2];
      float isd;
      if (SG == SG_W2) {
#pragma unroll
        for (int u = 0; u < 2; ++u) {
          const int t = 32 * b - 2 + 16 * u + (lane >> 2);
          dma16x(rs, (t >= 0 && t < S_) ? (unsigned)(t * D_ + col0 + ((lane ^ (lane >> 3)) & 3) * 8) * 2u : 0xF0000000u, WS_H2, lbase + G_K + wl * 2048 + u * 1024);
        }
      }
      f32x16 acc;
      int R = 0;
      LAS unsigned char* xr = lad(l16, lbase + G_XCH + wl * 4096);
      f32x4 xa[4];
      unsigned xf;
      auto take = [&]() {
        asm volatile("" ::: "memory");
        xf = *(const volatile LAS unsigned*)(lds + G_CTL + 16 + wl * 4);
        asm volatile("" ::: "memory");
#pragma unroll
        for (int q = 0; q < 4; ++q) xa[q] = *(const LAS f32x4*)(xr + q * 1024);
        if (SG == SG_W3) isd = *(const LAS float*)(lds + G_K + 1024 + wl * 256 + lane * 4);
        asm volatile("" ::: "memory");
      };
      products(i, acc, [&](int k) {
      }, [&]() {
        take();
      }, [&]() {
        SEG(5);
        R = __builtin_amdgcn_readfirstlane(rv);
        if (R >= i + 2) {
          WAIT_V(0);
          LBAR();
          if (pending && tid == 256) stx4(rs, 0, myword, (unsigned)blk(i - 1) + 1u);
        }
      });
      const bool more = R >= i + 2;
      SEG(1);
      while (__builtin_amdgcn_readfirstlane(xf) <= (unsigned)i) take();
#pragma unroll
      for (int q = 0; q < 16; ++q) { acc[q] += xa[q >> 2][q & 3]; if (SG == SG_W3) acc[q] *= isd; }
      SEG(2);
      const unsigned fo = (unsigned)((col0 >> 5) * 2048 + hf * 256), ro = (unsigned)((row >> 4) * 1024 + (row & 15) * 16);
      u32x4 v[2];
      if (SG == SG_W1 || SG == SG_W3) {
#pragma unroll
        for (int q = 0; q < 16; q += 2) {
          float g0 = gelu_tanh(acc[q]), g1 = gelu_tanh(acc[q + 1]);
          asm volatile("" : "+v"(g0), "+v"(g1));
          acc[q] = g0; acc[q + 1] = g1;
          piece(2 * i + 5, q >> 1, R >= i + 3 && i >= 2);
        }
      }
      widen32(acc, v);
      if (SG == SG_WO || SG == SG_W4) {
        LAS unsigned char* tw = lds + G_XCH + wl * 4096 + row * 64;
        *(LAS u32x4*)(tw + ((hf ^ (row >> 1)) & 3) * 16) = v[0]; *(LAS u32x4*)(tw + (((2 + hf) ^ (row >> 1)) & 3) * 16) = v[1];
        asm volatile("" ::: "memory");
        LAS unsigned char* tr = lds + G_XCH + wl * 4096 + (lane >> 2) * 64 + ((lane ^ (lane >> 3)) & 3) * 16;
        const unsigned so = (SG == SG_WO ? WS_P2 : WS_P8) + (unsigned)(b * 4 + kq) * 65536u, vo = (unsigned)((lane >> 2) * D_ + col0 + (lane & 3) * 8) * 2u;
        stx16(rs, vo, so, *(const LAS u32x4*)(tr)); stx16(rs, vo, so + 32768, *(const LAS u32x4*)(tr + 1024));
      } else if (PROJ) {
        const unsigned so = dst + (unsigned)(b * NH + cb) * 8192 + wl * 1024, vo = (unsigned)((row >> 4) * 4096 + hf * 256 + (row & 15) * 16);
        stx16(rs, vo, so, v[0]); stx16(rs, vo, so + 512, v[1]);
      } else if (SG == SG_W1) {
        const int grp = cb >> 1, sh = (grp & 1) ? -(1 + (grp >> 1)) : (1 + (grp >> 1));
        const int rho = 32 * b + row + sh + 2;
        const unsigned vo = (rho >= 2 && rho < S_ + 2) ? (unsigned)(rho >> 5) * 65536u + fo + (unsigned)((rho >> 4) & 1) * 1024u + (unsigned)(rho & 15) * 16u : 0xF0000000u;
        stx16(rs, vo, WS_H1S, v[0]); stx16(rs, vo, WS_H1S + 512, v[1]);
      } else if (SG == SG_W2) {
        if (!more) WAIT_V(0);
        res[0] = *(const LAS u32x4*)(lds + G_K + wl * 2048 + row * 64 + ((hf ^ (row >> 1)) & 3) * 16);
        res[1] = *(const LAS u32x4*)(lds + G_K + wl * 2048 + row * 64 + (((2 + hf) ^ (row >> 1)) & 3) * 16);
        float zs = 0.f, q2 = 0.f;
#pragma unroll
        for (int q = 0; q < 8; ++q) {
          const unsigned x = pack2(lo2f(res[q >> 2][q & 3]) + lo2f(v[q >> 2][q & 3]), hi2f(res[q >> 2][q & 3]) + hi2f(v[q >> 2][q & 3]));
          v[q >> 2][q & 3] = x; zs += lo2f(x) + hi2f(x); q2 = fmaf(lo2f(x), lo2f(x), fmaf(hi2f(x), hi2f(x), q2));
          asm volatile("" : "+v"(zs), "+v"(q2));
          piece(2 * i + 5, q, R >= i + 3 && i >= 2);
        }
        stx16(rs, fo + ro, WS_X2 + (unsigned)b * 65536u, v[0]); stx16(rs, fo + ro, WS_X2 + (unsigned)b * 65536u + 512, v[1]);
        const float mw = hsum(zs);
        q2 = hsum(q2);
        if (hf == 0) stx8(rs, (unsigned)((cb * 2 + (wl >> 1)) * 512 + row * 16 + (wl & 1) * 8), WS_ST5 + (unsigned)b * 8192, u32x2{f2u(mw), f2u(q2)});
      } else {
        stx16(rs, fo + ro, WS_U + (unsigned)b * 229376u, v[0]); stx16(rs, fo + ro, WS_U + (unsigned)b * 229376u + 512, v[1]);
      }
      if (PROJ && bstr == 3) {
#pragma unroll
        for (int q = 0; q < 2; ++q) {
          const int pc = (wk * 4 + wl) * 2 + q;
          if (pc < 660 && 3 * i + ZA + pc / 220 <= NSEG) stx16(rs, l16, X_REC + (unsigned)(3 * i + ZA) * RSTR + (unsigned)pc * 1024, u32x4{0u, 0u, 0u, 0u});
        }
      }
      SEG(3);
      pending = R >= i + 3 && i >= 2;
      if (!pending) { WAIT_V(0); announce(i); }
      if (R >= i + 3 && !(WEAVE && i >= 2)) fetch(2 * i + 5);
      if (!PROJ) WST(rs, wk, 16 + b);
      SEG(4);
      if (!more) BAR();
    }
  }
}

__device__ __forceinline__ void wait32(__amdgpu_buffer_rsrc_t rs, int first, int mine, int lane, unsigned need, unsigned& cap) {
  wait_ge(rs, 0, F_ST + (unsigned)(first + mine) * 128u, need, cap);
  wait_ge(rs, (unsigned)(lane & 31) * 128u, F_ST + (unsigned)first * 128u, need, cap);
}
__device__ __forceinline__ void sum_partials(__amdgpu_buffer_rsrc_t rs, unsigned l16, unsigned so, float (&a)[16]) {
  u32x4 pp[4][2];
#pragma unroll
  for (int k = 0; k < 4; ++k) { pp[k][0] = ldx16(rs, l16, so + k * 65536u); pp[k][1] = ldx16(rs, l16, so + k * 65536u + 1024); }
#pragma unroll
  for (int i = 0; i < 8; ++i) {
    f32x2 t = {0.f, 0.f};
#pragma unroll
    for (int k = 0; k < 4; ++k) t += f32x2{lo2f(pp[k][i >> 2][i & 3]), hi2f(pp[k][i >> 2][i & 3])};
    a[2 * i] = rbf(t[0]); a[2 * i + 1] = rbf(t[1]);
  }
}
__device__ __forceinline__ void put_row(__amdgpu_buffer_rsrc_t rs, unsigned l16, unsigned so, const float (&v)[16]) {
  stx16(rs, l16, so, u32x4{pack2(v[0], v[1]), pack2(v[2], v[3]), pack2(v[4], v[5]), pack2(v[6], v[7])});
  stx16(rs, l16, so + 1024, u32x4{pack2(v[8], v[9]), pack2(v[10], v[11]), pack2(v[12], v[13]), pack2(v[14], v[15])});
}
__device__ __forceinline__ void load_mod(__amdgpu_buffer_rsrc_t rs, int part, int lane, float (&v)[16], unsigned& cap) {
  for (;;) {
    asm volatile("" ::: "memory");
    u32x4 t[4];
#pragma unroll
    for (int i = 0; i < 4; ++i) t[i] = ldx16(rs, (unsigned)lane * 32, X_MOD + part * 4096 + (i >> 1) * 2048 + (i & 1) * 16);
    unsigned mn = 0xFFFFFFFFu;
#pragma unroll
    for (int i = 0; i < 16; ++i) { mn = min(mn, t[i >> 2][i & 3]); v[i] = hi2f(t[i >> 2][i & 3]); }
    if (__all(mn != 0u) || cap == 0u) break;
    cap = __builtin_amdgcn_readfirstlane(cap - 1u);
    __builtin_amdgcn_s_sleep(4);
  }
  cap = __builtin_amdgcn_readfirstlane(cap);
}
struct ModLoad { u32x4 w[8]; u16 c[8]; };
__device__ __forceinline__ void mod_load(const u16* wmod, const u16* cv, int grp, ModLoad& m) {
  const int kg = threadIdx.x >> 2, c8 = threadIdx.x & 3;
#pragma unroll
  for (int i = 0; i < 8; ++i) { m.w[i] = *(const u32x4*)(wmod + (size_t)(kg + 128 * i) * 6144 + grp * 32 + c8 * 8); m.c[i] = cv[kg + 128 * i]; }
}
__device__ __forceinline__ void mod_finish(__amdgpu_buffer_rsrc_t rs, LAS float* red, int grp, const ModLoad& m) {
  const int tid = threadIdx.x, kg = tid >> 2, c8 = tid & 3;
  float acc[8] = {0.f, 0.f, 0.f, 0.f, 0.f, 0.f, 0.f, 0.f};
#pragma unroll
  for (int i = 0; i < 8; ++i) {
    const float v = bf2f(m.c[i]), sc = rbf(v / (1.0f + __expf(-v)));
#pragma unroll
    for (int j = 0; j < 4; ++j) { acc[2 * j] += sc * lo2f(m.w[i][j]); acc[2 * j + 1] += sc * hi2f(m.w[i][j]); }
  }
#pragma unroll
  for (int j = 0; j < 8; ++j) red[kg * 33 + c8 * 8 + j] = acc[j];
  LBAR();
  float t = 0.f;
#pragma unroll
  for (int i = 0; i < 8; ++i) t += red[((tid >> 5) * 8 + i) * 33 + (tid & 31)];
  t = hsum(t);
  LBAR();
  if ((tid & 63) < 32) red[(tid >> 6) * 33 + (tid & 31)] = t;
  LBAR();
  if (tid < 32) {
    float u = 0.f;
#pragma unroll
    for (int i = 0; i < 8; ++i) u += red[i * 33 + tid];
    stx4(rs, (unsigned)tid * 4, X_MOD + (unsigned)grp * 128, f2u(rbf(u)) | 1u);
  }
}

__device__ __forceinline__ void stage_mid(__amdgpu_buffer_rsrc_t rs, const u16* x, int wk, int cu, unsigned& cap) {
  const int tid = threadIdx.x, lane = tid & 63, w = __builtin_amdgcn_readfirstlane(tid >> 6);
  const int id = w * 12 + cu, grp = id >> 5, rl = id & 31;
  const unsigned l16 = lane * 16;
  float ga[16], sc[16], sh[16];
  load_mod(rs, 2, lane, ga, cap); load_mod(rs, 4, lane, sc, cap); load_mod(rs, 3, lane, sh, cap);
#pragma unroll
  for (int i = 0; i < 16; ++i) sc[i] = rbf(1.0f + sc[i]);
  for (int seg = grp; seg < NSEG; seg += 3) {
    const int row = seg * 32 + rl;
    float v[16], a[16];
    load_row16(x + (size_t)row * D_, lane, v);
    wait32(rs, P_S2, rl, lane, (unsigned)seg + 1u, cap);
    sum_partials(rs, l16, WS_P2 + (unsigned)seg * 262144u + (unsigned)rl * 2048, a);
#pragma unroll
    for (int i = 0; i < 16; ++i) v[i] = rbf(v[i] + rbf(ga[i] * a[i]));
    put_row(rs, l16, WS_X1 + (unsigned)row * 2048, v);
    ln_row(v);
#pragma unroll
    for (int i = 0; i < 16; ++i) v[i] = rbf(rbf(v[i] * sc[i]) + sh[i]);
    put_row(rs, l16, WS_H2 + (unsigned)row * 2048, v);
    ln_row(v);
    put_row(rs, l16, WS_G + (unsigned)row * 2048, v);
    WAIT_V(0);
    if (lane == 0) stx4(rs, 0, F_ST + (unsigned)(P_S3 + grp * 32 + rl) * 128, (unsigned)seg + 1u);
    WST(rs, wk, 16 + seg);
  }
}

__device__ __forceinline__ void stage_out(__amdgpu_buffer_rsrc_t rs, u16* out, int wk, int cu, unsigned& cap) {
  const int tid = threadIdx.x, lane = tid & 63, w = __builtin_amdgcn_readfirstlane(tid >> 6);
  const int grp = w >> 2, rl = cu * 4 + (w & 3);
  const unsigned l16 = lane * 16;
  float gb[16];
  load_mod(rs, 5, lane, gb, cap);
  for (int b = grp; b < 65; b += 2) {
    const int t = 32 * b - 2 + rl;
    if (t < 0 || t >= S_) continue;
    wait_ge(rs, (unsigned)(lane & 7) * 128u, F_ST + P_S5 * 128u, (unsigned)b + 1u, cap);
    const unsigned xo = (unsigned)((lane >> 2) * 2048 + (lane & 3) * 256), xs = WS_X2 + (unsigned)b * 65536u + (unsigned)((rl >> 4) * 1024 + (rl & 15) * 16);
    const u32x4 x2a = ldx16(rs, xo, xs), x2b = ldx16(rs, xo, xs + 32768);
    const u32x4 x1a = ldx16(rs, l16, WS_X1 + (unsigned)t * 2048), x1b = ldx16(rs, l16, WS_X1 + (unsigned)t * 2048 + 1024);
    wait32(rs, P_S8, rl, lane, (unsigned)b + 1u, cap);
    float a[16];
    sum_partials(rs, l16, WS_P8 + (unsigned)b * 262144u + (unsigned)rl * 2048, a);
#pragma unroll
    for (int i = 0; i < 4; ++i) {
      a[2 * i] = lo2f(x1a[i]) + rbf(gb[2 * i] * rbf(lo2f(x2a[i]) + a[2 * i])); a[2 * i + 1] = hi2f(x1a[i]) + rbf(gb[2 * i + 1] * rbf(hi2f(x2a[i]) + a[2 * i + 1]));
      a[8 + 2 * i] = lo2f(x1b[i]) + rbf(gb[8 + 2 * i] * rbf(lo2f(x2b[i]) + a[8 + 2 * i])); a[9 + 2 * i] = hi2f(x1b[i]) + rbf(gb[9 + 2 * i] * rbf(hi2f(x2b[i]) + a[9 + 2 * i]));
    }
    store_row16(out + (size_t)t * D_, lane, a);
    WST(rs, wk, 16 + b);
  }
}

__device__ __forceinline__ void worker(const MainP& p, LAS unsigned char* lds, int xw, int j) {
  const int tid = threadIdx.x, lane = tid & 63, w = __builtin_amdgcn_readfirstlane(tid >> 6);
  const __amdgpu_buffer_rsrc_t rs = mk_rs(p.ws, WS_END);
  unsigned cap = SPIN_CAP;
  const int wk = xw * 32 + j;
  WST(rs, wk, 0);
  {
    const int c = j * 5 + xw, r0 = c * 8 + 2 * (w & 3);
    const bool two = r0 + 8 * NLN < S_;
    float v[4][16];
    ModLoad m;
    if (c >= NLN - 64) mod_load(p.wmod, p.c, c - (NLN - 64), m);
    if (w < 4) {
#pragma unroll
      for (int q = 0; q < 4; ++q) load_row16(p.x + (size_t)(r0 + (q & 1) + (two ? (q >> 1) * 8 * NLN : 0)) * D_, lane, v[q]);
    }
    if (c >= NLN - 64) mod_finish(rs, (LAS float*)(lds + G_XCH), c - (NLN - 64), m);
    WST(rs, wk, 5);
    if (w >= 4) {
      const __amdgpu_buffer_rsrc_t rsW = mk_rs(xw < 3 ? p.Wk : p.Wq, D_ * HD * 2);
      const int ub = lane >> 3;
      const unsigned vB = (unsigned)(((ub & 3) + 8 * (ub >> 2)) * HD + 16 * (((lane >> 1) & 3) ^ (ub >> 1)) + 8 * (lane & 1)) * 2u, lbase = (unsigned)(size_t)lds;
#pragma unroll
      for (int q = 0; q < 16; ++q) {
        const int wq = (w & 3) + 4 * (q >> 3);
        const unsigned so = (unsigned)((32 * (wq >> 2) + 16 * ((wq >> 1) & 1) + 4 * (wq & 1) + (((q >> 2) & 1) * 8 + (q & 3)) * 64) * HD + j * 128) * 2u;
        const unsigned d = lbase + ((q >> 2) & 1) * 65536 + (q & 3) * 16384 + wq * 1024;
        dma16(rsW, vB, so, d); dma16(rsW, vB, so + 128, d + 8192);
      }
    } else {
#pragma unroll
      for (int q = 0; q < 4; ++q) ln_row(v[q]);
      float sc[16], sh[16];
      load_mod(rs, 1, lane, sc, cap); load_mod(rs, 0, lane, sh, cap);
      WST(rs, wk, 6);
#pragma unroll
      for (int i = 0; i < 16; ++i) sc[i] = rbf(1.0f + sc[i]);
#pragma unroll
      for (int q = 0; q < 4; ++q) {
#pragma unroll
        for (int i = 0; i < 16; ++i) v[q][i] = rbf(rbf(v[q][i] * sc[i]) + sh[i]);
        if (q < 2 || two) put_row(rs, (unsigned)lane * 16, WS_H + (unsigned)(r0 + (q & 1) + (q >> 1) * 8 * NLN) * 2048, v[q]);
      }
      WAIT_V(0);
    }
    BAR();
    if (tid == 0) stx4(rs, 0, F_LN + c * 128, 1u);
    WST(rs, wk, 1);
  }
  {
    const u16* nw = xw == 0 ? p.Wo + (size_t)(j >> 3) * 1024 * D_ + (j & 7) * 128 : xw == 3 ? p.w4 + (size_t)(j >> 3) * 896 * D_ + (j & 7) * 128
                  : xw == 2 ? p.w3 + j * 128 : (j < 8 ? p.w1 : p.w2) + (j & 7) * 128;
    const int nrows = (xw == 4 || (xw == 1 && j >= 16) || (xw == 2 && j >= 28)) ? 0 : xw == 3 ? 896 : 1024;
    const int mg = xw < 3 ? 64 + wk : 160 + j;
    stage_gemm<SG_PJ>(lds, rs, xw < 3 ? p.Wk : p.Wq, wk, xw < 3 ? xw : xw == 3 ? QB0 : 0, j, P_PJ + min(xw, 3) * 32 + j + (xw == 4 ? 400 : 0), cap, xw < 3 ? WS_K : WS_Q, xw < 3 ? 3 : 1,
                      xw == 0 ? 22 : xw < 3 ? 21 : xw == 3 ? NSEG - QB0 : QB0, nw, nrows, xw == 2 ? FF : D_, xw == 3 ? nullptr : p.wmod + mg * 32);
    WST(rs, wk, 3);
  }
  typedef __attribute__((address_space(4))) const MainP* KArg;
  KArg kp = (KArg)__builtin_amdgcn_kernarg_segment_ptr();
  asm volatile("" : "+s"(kp));
  if (xw != 3) { const int mg = xw < 3 ? 64 + wk : 160 + j; ModLoad m; mod_load(kp->wmod, kp->c, mg, m); mod_finish(rs, (LAS float*)(lds + 65536), mg, m); }
  if (xw == 0) stage_gemm<SG_WO>(lds, rs, kp->Wo, wk, j >> 3, j & 7, P_S2 + j, cap);
  else if (xw == 3) stage_gemm<SG_W4>(lds, rs, kp->w4, wk, j >> 3, j & 7, P_S8 + j, cap);
  else if (xw == 4) {
    unsigned chs = 0u;
    for (int sg = w >> 1; sg < NSEG; sg += 4) { attend(rs, sg, j, w & 1, lane, chs, cap); WST(rs, wk, 16 + sg); }
  }
  else if (j >= 28) stage_out(rs, kp->out, wk, (xw - 1) * 4 + j - 28, cap);
  else if (xw == 2) stage_gemm<SG_W3>(lds, rs, kp->w3, wk, 0, j, P_S7 + j, cap);
  else if (j < 8) stage_gemm<SG_W1>(lds, rs, kp->w1, wk, 0, j, P_S4 + j, cap);
  else if (j < 16) stage_gemm<SG_W2>(lds, rs, kp->w2, wk, 0, j - 8, P_S5 + j - 8, cap);
  else stage_mid(rs, kp->x, wk, j - 16, cap);
  if (cap == 0u && lane == 0) stx4(rs, 0, X_CTL, 1u);
}

#define ST_DECL
#define ST0()
#define ST(i)
#define ST_FLUSH()
constexpr int R_W = 0, R_MEM = 131072, R_QK = R_MEM + 16384, R_KT = R_QK + 8192, R_RED = R_MEM + 16384, R_LDS = 163840;
__device__ __forceinline__ void bar_lo(int w) { asm volatile("s_cmp_ge_u32 %0, 4\n\ts_cbranch_scc1 1f\n\ts_barrier\n\t1:" :: "s"(w) : "scc", "memory"); }
__device__ __forceinline__ void bar_hi(int w) { asm volatile("s_cmp_lt_u32 %0, 4\n\ts_cbranch_scc1 1f\n\ts_setprio 3\n\ts_barrier\n\t1:" :: "s"(w) : "scc", "memory"); }
#define EXPV 0
#define VMI 7
#define GAP "s_sleep 1\n\t"
__device__ __forceinline__ void issue4(__amdgpu_buffer_rsrc_t rs, unsigned vo, unsigned s0, unsigned s1, unsigned s2, unsigned s3, u32x4& a0, u32x4& a1, u32x4& a2, u32x4& a3) {
  asm volatile("buffer_load_dwordx4 v[240:243], %[vo], %[rs], %[s0] offen sc1\n\t" GAP "buffer_load_dwordx4 v[244:247], %[vo], %[rs], %[s1] offen sc1\n\t" GAP
               "buffer_load_dwordx4 v[248:251], %[vo], %[rs], %[s2] offen sc1\n\t" GAP "buffer_load_dwordx4 v[252:255], %[vo], %[rs], %[s3] offen sc1"
               : "=&{v[240:243]}"(a0), "=&{v[244:247]}"(a1), "=&{v[248:251]}"(a2), "=&{v[252:255]}"(a3)
               : [vo] "v"(vo), [rs] "s"(rs), [s0] "s"(s0), [s1] "s"(s1), [s2] "s"(s2), [s3] "s"(s3) : "memory");
}
#define ARRIVE(R0, R1, R2, R3, CNT) \
  asm volatile("s_waitcnt vmcnt(" #CNT ")\n\t" \
               "1:\n\t" \
               "v_min3_u32 %[t], v" #R0 ", v" #R1 ", v" #R2 "\n\t" \
               "v_min_u32 %[t], %[t], v" #R3 "\n\t" \
               "v_cmp_eq_u32 vcc, 0, %[t]\n\t" \
               "s_cbranch_vccz 2f\n\t" \
               "s_mov_b64 exec, vcc\n\t" \
               "buffer_load_dwordx4 v[" #R0 ":" #R3 "], %[vo], %[rs], %[so] offen sc1\n\t" \
               "s_mov_b64 exec, -1\n\t" \
               "s_waitcnt vmcnt(0)\n\t" \
               "s_sub_u32 %[cap], %[cap], 1\n\t" \
               "s_cbranch_scc0 1b\n\t" \
               "s_mov_b32 %[cap], 0\n\t" \
               "2:" \
               : "+{v[" #R0 ":" #R3 "]}"(a), [t] "=&v"(t), [cap] "+s"(cap) : [vo] "v"(vo), [rs] "s"(rs), [so] "s"(so) : "vcc", "scc", "memory")
template <int I> __device__ __forceinline__ void arrive(__amdgpu_buffer_rsrc_t rs, unsigned vo, unsigned so, u32x4& a, unsigned& cap) {
  unsigned t;
  if (I == 0) ARRIVE(240, 241, 242, 243, 3);
  else if (I == 1) ARRIVE(244, 245, 246, 247, 2);
  else if (I == 2) ARRIVE(248, 249, 250, 251, 1);
  else ARRIVE(252, 253, 254, 255, 0);
}
__device__ __forceinline__ unsigned half_of(unsigned v) { unsigned r; asm volatile("v_lshrrev_b32 %0, 1, %1" : "=v"(r) : "v"(v)); return r; }
__device__ __forceinline__ void issue2(__amdgpu_buffer_rsrc_t rs, unsigned l8, unsigned so, int w, u32x4& a) {
  asm volatile("s_cmp_ge_u32 %[w], 4\n\ts_cbranch_scc1 1f\n\tv_lshrrev_b32 v255, 1, %[l8]\n\t"
               "buffer_load_dwordx2 v[252:253], v255, %[rs], %[so] offen sc1\n\tbuffer_load_dwordx2 v[254:255], v255, %[rs], %[so] offen offset:512 sc1\n\t1:"
               : "=&{v[252:255]}"(a) : [l8] "v"(l8), [rs] "s"(rs), [so] "s"(so), [w] "s"(w) : "scc", "memory");
}
__device__ __forceinline__ void arrive2(__amdgpu_buffer_rsrc_t rs, unsigned l8, unsigned so, u32x4& a, unsigned& cap) {
  unsigned t;
  asm volatile("s_waitcnt vmcnt(0)\n\t"
               "1:\n\t"
               "v_min3_u32 %[t], v252, v253, v254\n\t"
               "v_min_u32 %[t], %[t], v255\n\t"
               "v_cmp_eq_u32 vcc, 0, %[t]\n\t"
               "s_cbranch_vccz 2f\n\t"
               "v_lshrrev_b32 %[t], 1, %[l8]\n\t"
               "buffer_load_dwordx2 v[252:253], %[t], %[rs], %[so] offen sc1\n\t"
               "buffer_load_dwordx2 v[254:255], %[t], %[rs], %[so] offen offset:512 sc1\n\t"
               "s_waitcnt vmcnt(0)\n\t"
               "s_sub_u32 %[cap], %[cap], 1\n\t"
               "s_cbranch_scc0 1b\n\t"
               "s_mov_b32 %[cap], 0\n\t"
               "2:"
               : "+{v[252:255]}"(a), [t] "=&v"(t), [cap] "+s"(cap) : [l8] "v"(l8), [rs] "s"(rs), [so] "s"(so) : "vcc", "scc", "memory");
}
__device__ __forceinline__ unsigned ror8(unsigned v) { return __builtin_amdgcn_update_dpp(0u, v, 0x128, 0xF, 0xF, true); }
__device__ __forceinline__ void gather_mem(__amdgpu_buffer_rsrc_t rs, unsigned lbase, int s, int w, unsigned l16, unsigned& cap) {
  const unsigned so = X_MEM + s * RSTR + w * 2048;
  u32x4 a0, a1;
  asm volatile("buffer_load_dwordx4 v[248:251], %[vo], %[rs], %[so] offen sc1\n\tbuffer_load_dwordx4 v[252:255], %[vo], %[rs], %[so] offen offset:1024 sc1"
               : "=&{v[248:251]}"(a0), "=&{v[252:255]}"(a1) : [vo] "v"(l16), [rs] "s"(rs), [so] "s"(so) : "memory");
  LAS unsigned char* d = lad(l16, lbase + R_MEM + w * 2048);
  arrive<2>(rs, l16, so, a0, cap);
  *(LAS u32x4*)(d) = a0;
  arrive<3>(rs, l16, so + 1024, a1, cap);
  *(LAS u32x4*)(d + 1024) = a1;
}
__device__ __forceinline__ unsigned mem_off(int lane) { return (unsigned)((lane & 1) * 512 + (((lane >> 1) ^ ((lane & 1) * 16)) * 8)); }
__device__ __forceinline__ bf16x8 mem_frag(LAS unsigned char* mp, int chunk) { return cat8(trread(mp + chunk * 1024), trread(mp + chunk * 1024 + 256)); }
__device__ __forceinline__ bf16x16 w_frag(LAS unsigned char* p) {
  return cat16(cat8(trread(p), trread(p + 2048)), cat8(trread(p + 4096), trread(p + 6144)));
}
__device__ __forceinline__ bf16x16 l_frag(LAS unsigned char* p, int i) { return cat16(*(const LAS bf16x8*)(p + i * 2048), *(const LAS bf16x8*)(p + i * 2048 + 1024)); }
__device__ __forceinline__ void make_resident(LAS unsigned char* wb, unsigned lo, unsigned l16) {
#pragma unroll
  for (int c = 0; c < 2; ++c) {
    bf16x16 f[4];
#pragma unroll
    for (int t = 0; t < 4; ++t) f[t] = w_frag(wb + c * 8192 + (lo ^ (t << 5)));
    WAIT_L0(); SCHED();
#pragma unroll
    for (int t = 0; t < 4; ++t) {
      *(LAS bf16x8*)(wb + (c * 4 + t) * 2048 + l16) = __builtin_shufflevector(f[t], f[t], 0, 1, 2, 3, 4, 5, 6, 7);
      *(LAS bf16x8*)(wb + (c * 4 + t) * 2048 + 1024 + l16) = __builtin_shufflevector(f[t], f[t], 8, 9, 10, 11, 12, 13, 14, 15);
    }
    WAIT_L0(); SCHED();
  }
}
__device__ __forceinline__ f32x4 fold(f32x4 a, f32x4 b) {
  f32x4 r;
#pragma unroll
  for (int i = 0; i < 4; ++i) {
    const auto t = __builtin_amdgcn_permlane32_swap(f2u(a[i]), f2u(b[i]), false, false);
    r[i] = u2f(t[0]) + u2f(t[1]);
  }
  return r;
}

struct Sched { unsigned tau, prev; int ea, eb, d1; bool moved; };
template <int CTRL> __device__ __forceinline__ int dpp_max(int v) { return max(v, (int)__builtin_amdgcn_update_dpp((unsigned)v, (unsigned)v, CTRL, 0xF, 0xF, false)); }
__device__ __forceinline__ int wave_max(int v) {
  v = dpp_max<0xB1>(v); v = dpp_max<0x4E>(v); v = dpp_max<0x141>(v); v = dpp_max<0x140>(v);
  return max(max(__builtin_amdgcn_readlane(v, 0), __builtin_amdgcn_readlane(v, 16)), max(__builtin_amdgcn_readlane(v, 32), __builtin_amdgcn_readlane(v, 48)));
}
__device__ __forceinline__ void times_issue(__amdgpu_buffer_rsrc_t rs, unsigned l16, int s, unsigned& xb, unsigned& xa) {
  asm volatile("v_lshlrev_b32 %0, 3, %2\n\tv_and_b32 %1, 0xF80, %0\n\tbuffer_load_dword %0, %0, %3, %4 offen sc1\n\tbuffer_load_dword %1, %1, %3, %5 offen sc1"
               : "=&v"(xb), "=&v"(xa) : "v"(l16), "s"(rs), "s"(X_TS + (unsigned)s * RSTR), "s"(X_TS + (unsigned)s * RSTR + 8192) : "memory");
}
__device__ __forceinline__ void retime(__amdgpu_buffer_rsrc_t rs, unsigned l16, int s, unsigned xb, unsigned xa, Sched& t, const MainP& p, unsigned& cap) {
  if (s > 0) {
    while (!__all(min(xa, xb) != 0u) && cap != 0u) {
      times_issue(rs, l16, s, xb, xa);
      asm volatile("s_waitcnt vmcnt(0)" : "+v"(xb), "+v"(xa) :: "memory");
      cap = __builtin_amdgcn_readfirstlane(cap - 1u);
    }
    cap = __builtin_amdgcn_readfirstlane(cap);
    const int tb = wave_max((int)(xb - t.prev)), ta = wave_max((int)(xa - t.prev));
    const int late = tb + p.d3 - (int)(t.tau - t.prev);
    if (s > 2) {
      const int up = s < 5 ? 80 : 8, dn = s < 10 ? 40 : 8;
      if (!t.moved) t.ea = max(t.ea - dn, min(ta * 8, t.ea + up));
      if (ta + p.d3 <= t.d1) t.eb = max(t.eb - dn, min((tb - t.d1) * 8, t.eb + up));
    }
    t.moved = late > 0;
    if (late > 0) t.tau += late + min(p.d4, 8 * late);
  }
  t.d1 = (t.ea >> 3) + p.d1;
}
__device__ __forceinline__ void advance(Sched& t, const MainP& p) { t.prev = t.tau; t.tau += t.d1 + (t.eb >> 3) + p.d2; }

__device__ __forceinline__ void role_a(const MainP& p, LAS unsigned char* lds, int h) {
  const int tid = threadIdx.x, lane = tid & 63, w = __builtin_amdgcn_readfirstlane(tid >> 6);
  const int cg = w & 3, kh = w >> 2;
  const __amdgpu_buffer_rsrc_t rs = mk_rs(p.ws, WS_END);
  const unsigned lbase = (unsigned)(size_t)lds;
  unsigned l16 = lane * 16;
  asm volatile("" : "+v"(l16));
  const int sidx = (lane & 8) ? 0xEEEE : 0x4444;
  bf16x16 W[24];
  unsigned qoff;
  {
    const int fr = lane & 15, g = lane >> 4, tu = 4 * (g & 1) + (fr >> 2), ub = lane >> 3;
    const unsigned lo = (g >> 1) * 1024 + tu * 128 + ((tu >> 1) << 5) + (fr & 3) * 8;
    qoff = R_QK + ((fr >> 1) & 1) * 512 + (g >> 1) * 256 + (fr & 1) * 128 + (8 * (g & 1) + (fr >> 2)) * 8;
    const __amdgpu_buffer_rsrc_t rw = mk_rs(cg < 2 ? p.Wq : p.Wk, D_ * HD * 2);
    const unsigned vo = (unsigned)(((ub & 3) + 8 * (ub >> 2)) * HD + 16 * (((lane >> 1) & 3) ^ (ub >> 1)) + 8 * (lane & 1)) * 2u;
    const unsigned so = (unsigned)(kh * 512 * HD + h * DH + (cg & 1) * 64) * 2u;
    auto stage = [&](int c) {
#pragma unroll
      for (int rho = 0; rho < 8; ++rho) dma16(rw, vo, so + (unsigned)((c * 64 + 32 * (rho >> 2) + 16 * (rho & 1) + 4 * ((rho >> 1) & 1)) * HD) * 2u, lbase + R_W + w * 16384 + (c & 1) * 8192 + rho * 1024);
    };
    stage(0); stage(1);
#pragma unroll
    for (int c = 0; c < 6; ++c) {
      WAIT_V(8);
#pragma unroll
      for (int t = 0; t < 4; ++t) W[4 * c + t] = w_frag(lds + R_W + w * 16384 + (c & 1) * 8192 + (lo ^ (t << 5)));
      WAIT_L0(); SCHED();
      stage(c + 2);
    }
    WAIT_V(0);
    make_resident(lds + R_W + w * 16384, lo, l16);
  }
  WST(rs, 160 + (h >> 1), 4);
  unsigned cap = SPIN_CAP;
  wait_ge(rs, 0, F_ST + (unsigned)(P_PJ + h) * 128, 1u, cap);
  dma16x(rs, l16, WS_K + h * 8192 + w * 1024, lbase + R_KT + w * 1024);
  Sched tt; tt.tau = rt32() - 100000; tt.prev = tt.tau; tt.ea = 195 * 8; tt.eb = 118 * 8; tt.d1 = 0; tt.moved = true;
  const int cu_ = h; (void)cu_;
  ST_DECL
  for (int s = 0; s < NSEG; ++s) {
    LAS unsigned char* lp = lad(l16, lbase + R_W + w * 16384);
    bf16x16 L[2];
    L[0] = l_frag(lp, 0); L[1] = l_frag(lp, 1);
    unsigned lz = l16;
    asm volatile("" : "+v"(lz));
    LAS unsigned char* mp = lad(mem_off((int)(lz >> 4)), lbase + R_MEM + kh * 8192);
    SCHED();
    if (w == 7) wait_until(tt.tau);
    BAR();
    ST0();
    gather_mem(rs, lbase, s, (w + h) & 7, l16, cap);
    ST(1);
    LBAR();
    ST(2);
    f32x4 c[4];
#pragma unroll
    for (int t = 0; t < 4; ++t) c[t] = f32x4{0.f, 0.f, 0.f, 0.f};
    bf16x8 al, af[2];
    u32x4 vt0, vt1;
    u32x4 vmv;
    const unsigned vso = X_VM + (unsigned)s * RSTR + h * 4096 + cg * 1024;
    al = mem_frag(mp, 6); af[0] = mem_frag(mp, 0); af[1] = mem_frag(mp, 1);
#pragma unroll
    for (int i = 0; i < 8; ++i) {
      c[i & 3] = smf(al, L[i & 1], c[i & 3], sidx);
      SCHED();
      if (i == 3) al = mem_frag(mp, 7);
      if (i < 6) L[i & 1] = l_frag(lp, i + 2);
      if (i == 6) {
        const unsigned vo = WS_VT + (unsigned)(s * NH + h) * 8192 + cg * 2048;
        asm volatile("s_cmp_ge_u32 %5, 4\n\ts_cbranch_scc1 1f\n\t"
                     "buffer_load_dwordx4 %0, %2, %3, %4 offen sc1\n\tbuffer_load_dwordx4 %1, %2, %3, %4 offen offset:1024 sc1\n\t1:"
                     : "=&v"(vt0), "=&v"(vt1) : "v"(l16), "s"(rs), "s"(vo), "s"(w) : "scc", "memory");
      }
      if (i == VMI) issue2(rs, l16, vso, w, vmv);
      SCHED();
#pragma unroll
      for (int r = 3 * i; r < 3 * i + 3; ++r) {
        c[(r + 1) & 3] = smf(af[(r >> 2) & 1], W[(r & ~3) + ((r + 1) & 3)], c[(r + 1) & 3], sidx);
        SCHED();
        if ((r & 3) == 3 && (r >> 2) + 2 < 6) af[(r >> 2) & 1] = mem_frag(mp, (r >> 2) + 2);
        SCHED();
      }
    }
    {
      LAS unsigned char* qw = lad(half_of(l16), lbase + R_QK + cg * 2048 + kh * 512);
      const f32x4 f0 = fold(c[0], c[1]);
      *(LAS u32x2*)(qw) = u32x2{pack2(f0[0], f0[1]), pack2(f0[2], f0[3])};
      const f32x4 f1 = fold(c[2], c[3]);
      *(LAS u32x2*)(qw + 1024) = u32x2{pack2(f1[0], f1[1]), pack2(f1[2], f1[3])};
    }
    ST(3);
    if (w < 4) {
      bf16x8 k1[4], k2[4];
      LAS unsigned char* kb = lad(l16, lbase + R_KT);
#pragma unroll
      for (int ks = 0; ks < 2; ++ks) { k1[ks] = *(const LAS bf16x8*)(kb + ks * 1024); k2[ks] = *(const LAS bf16x8*)(kb + 4096 + ks * 1024); }
      unsigned ov;
      {
        unsigned ot;
        asm volatile("v_and_b32 %0, 0x30, %2\n\tv_lshrrev_b32 %1, 2, %2\n\tv_and_b32 %1, 0xC0, %1\n\tv_or_b32 %0, %0, %1\n\t"
                     "v_and_b32 %1, 0x40, %2\n\tv_lshl_or_b32 %0, %1, 9, %0\n\tv_and_b32 %1, 0x80, %2\n\tv_lshl_or_b32 %0, %1, 24, %0"
                     : "=&v"(ov), "=&v"(ot) : "v"(l16));
      }
      LBAR();
      ST(4);
      f32x4 t0 = {0.f, 0.f, 0.f, 0.f}, t1 = t0, t2 = t0;
      LAS unsigned char* qb = lad(qoff, lbase);
      bf16x8 qf[4], kf[4];
#pragma unroll
      for (int ks = 0; ks < 2; ++ks) {
        qf[ks] = cat8(trread(qb + ks * 1024), trread(qb + ks * 1024 + 32));
        kf[ks] = cat8(trread(qb + 4096 + ks * 1024), trread(qb + 4096 + ks * 1024 + 32));
      }
      SCHED();
#pragma unroll
      for (int ks = 0; ks < 4; ++ks) {
        t1 = mfma32(k1[ks], qf[ks], t1); t2 = mfma32(k2[ks], qf[ks], t2); t0 = mfma32(kf[ks], qf[ks], t0);
        SCHED();
        if (ks < 2) {
          qf[ks + 2] = cat8(trread(qb + (ks + 2) * 1024), trread(qb + (ks + 2) * 1024 + 32));
          k1[ks + 2] = *(const LAS bf16x8*)(kb + (ks + 2) * 1024); k2[ks + 2] = *(const LAS bf16x8*)(kb + 4096 + (ks + 2) * 1024);
          kf[ks + 2] = cat8(trread(qb + 4096 + (ks + 2) * 1024), trread(qb + 4096 + (ks + 2) * 1024 + 32));
        }
        SCHED();
      }
      ST(5);
      arrive2(rs, l16, vso, vmv, cap);
      asm volatile("" : "+v"(vt0), "+v"(vt1));
      {
        const float sl = 0.08838834764831845f * 1.4426950408889634f;
        float z[4] = {t1[0], t1[1], t1[2], t1[3]}, y = t0[0], mx;
        {
          float u1 = t0[1], u2 = t0[2], u3 = t0[3];
#define M8(Z, B) "v_add_f32_dpp " Z ", " Z ", " Z " row_ror:8 row_mask:0xf bank_mask:0x3\n\tv_add_f32_dpp " Z ", " B ", " B " row_ror:8 row_mask:0xf bank_mask:0xc\n\t"
          asm volatile("s_nop 3\n\t"
              "v_permlane32_swap_b32 %4, %6\n\tv_permlane32_swap_b32 %5, %7\n\t"
              M8("%0", "%9") "v_add_f32 %4, %4, %6\n\tv_add_f32 %5, %5, %7\n\t" M8("%1", "%10") M8("%2", "%11") M8("%4", "%5") M8("%3", "%12")
              "v_max3_f32 %8, %0, %1, %2\n\tv_max3_f32 %8, %8, %4, %3\n\ts_nop 1\n\t"
              "v_max_f32_dpp %8, %8, %8 row_ror:8 row_mask:0xf bank_mask:0xf\n\tv_mov_b32 %5, %8\n\ts_nop 1\n\t"
              "v_permlane16_swap_b32 %8, %5\n\tv_max_f32 %8, %8, %5\n\tv_mov_b32 %5, %8\n\ts_nop 1\n\t"
              "v_permlane32_swap_b32 %8, %5\n\tv_max_f32 %8, %8, %5"
              : "+v"(z[0]), "+v"(z[1]), "+v"(z[2]), "+v"(z[3]), "+v"(y), "+v"(u1), "+v"(u2), "+v"(u3), "=&v"(mx) : "v"(t2[0]), "v"(t2[1]), "v"(t2[2]), "v"(t2[3]));
#undef M8
        }
        const float nm = -mx * sl;
        y = __builtin_amdgcn_exp2f(fmaf(y, sl, nm));
#pragma unroll
        for (int r = 0; r < 4; ++r) z[r] = __builtin_amdgcn_exp2f(fmaf(z[r], sl, nm));
        const unsigned pm1 = pack2(y, u2f(ror8(f2u(y))));
        const auto pms = __builtin_amdgcn_permlane32_swap(pm1, pm1, false, false);
        const u32x2 pm = {pms[0], pms[1]};
        f32x4 n0 = mfma16n(u32x2{vmv[0], vmv[1]}, pm, f32x4{0.f, 0.f, 0.f, 0.f}), n1 = mfma16n(u32x2{vmv[2], vmv[3]}, pm, f32x4{0.f, 0.f, 0.f, 0.f});
        const unsigned p0 = pack2(z[0], z[1]), p1 = pack2(z[2], z[3]);
        const u32x4 pt = {p0, p1, ror8(p0), ror8(p1)};
        const f32x4 o0 = mfma32(__builtin_bit_cast(bf16x8, vt0), __builtin_bit_cast(bf16x8, pt), f32x4{0.f, 0.f, 0.f, 0.f});
        const f32x4 o1 = mfma32(__builtin_bit_cast(bf16x8, vt1), __builtin_bit_cast(bf16x8, pt), f32x4{0.f, 0.f, 0.f, 0.f});
        const float inv = __builtin_amdgcn_rcpf(gsum(dpp_add<0x128>(((z[0] + z[1]) + (z[2] + z[3])) + y)));
        n0 += o0; n1 += o1;
        {
          const u32x4 v = {nz(pack2(n0[0] * inv, n0[1] * inv)), nz(pack2(n0[2] * inv, n0[3] * inv)), nz(pack2(n1[0] * inv, n1[1] * inv)), nz(pack2(n1[2] * inv, n1[3] * inv))};
          stx16(rs, ov, X_OX + s * RSTR + h * 1024 + w * 256, v);
        }
        if (w == 3) { const unsigned t = max(rt32(), 1u); if (l16 == 0) stx4(rs, 0, X_TS + (unsigned)(s + 1) * RSTR + (unsigned)(64 + h) * 128, t); }
        ST(8);
      }
    } else {
      LBAR();
      LAS unsigned char* qb = lad(qoff, lbase + 4096 + (w - 4) * 1024);
      u32x4 kf = __builtin_bit_cast(u32x4, cat8(trread(qb), trread(qb + 32)));
#pragma unroll
      for (int i = 0; i < 4; ++i) kf[i] = pack2(dpp_add<0x128>(lo2f(kf[i])), dpp_add<0x128>(hi2f(kf[i])));
      stx16(rs, l16, WS_KM + (unsigned)(s * NH + h) * 4096 + (w - 4) * 1024, kf);
      WAIT_V(0);
    }
    LBAR();
    if (w == 4 && l16 == 0) stx4(rs, 0, F_CH + h * 128, (unsigned)s + 1u);
    ST(9);
    if (w == 7) {
      unsigned xb, xa;
      times_issue(rs, l16, s, xb, xa);
      asm volatile("s_waitcnt vmcnt(0)" : "+v"(xb), "+v"(xa) :: "memory");
      retime(rs, l16, s, xb, xa, tt, p, cap); advance(tt, p);
    }
    if (s + 1 < NSEG) {
      unsigned fo;
      asm volatile("v_mov_b32 %0, 0" : "=v"(fo));
      wait_ge(rs, fo, F_ST + (unsigned)(P_PJ + ((s + 1) % 3) * 32 + h) * 128, (unsigned)s + 2u, cap);
      dma16x(rs, l16, WS_K + (unsigned)((s + 1) * NH + h) * 8192 + w * 1024, lbase + R_KT + w * 1024);
    }
  }
  ST_FLUSH();
  if (cap == 0u && l16 == 0) stx4(rs, 0, X_CTL, 1u);
}

__device__ __forceinline__ void role_vb(const MainP& p, LAS unsigned char* lds, int rg, int cb) {
  const int tid = threadIdx.x, lane = tid & 63, w = __builtin_amdgcn_readfirstlane(tid >> 6);
  const int kh = w >> 2, wr = (w + cb) & 7;
  const __amdgpu_buffer_rsrc_t rs = mk_rs(p.ws, WS_END);
  const unsigned lbase = (unsigned)(size_t)lds;
  unsigned l16 = lane * 16, l8 = lane * 8, moff = mem_off(lane), roff, soff = (unsigned)((lane >> 5) * 256 + (lane & 31) * 4);
  const int sidx = (lane & 8) ? 0xEEEE : 0x4444;
  bf16x16 WV[8], WB[16];
  {
    const int fr = lane & 15, g = lane >> 4, tu = 4 * (g & 1) + (fr >> 2), ub = lane >> 3;
    { const int sl = mem_slot(16 * (g >> 1) + fr); roff = R_RED + w * 512 + ((sl >> 5) * 32 + (sl & 15) * 2 + (g & 1)) * 8; }
    const __amdgpu_buffer_rsrc_t rv = mk_rs(p.Wv, D_ * HD * 2), ro = mk_rs(p.Wo, D_ * HD * 2);
    const unsigned vv = (unsigned)(((ub & 3) + 8 * (ub >> 2)) * HD + 16 * (((lane >> 1) & 3) ^ (ub >> 1)) + 8 * (lane & 1)) * 2u;
    const unsigned sv = (unsigned)((32 * (w >> 2) + 16 * (w & 1) + 4 * ((w >> 1) & 1)) * HD + cb * DH + rg * 64) * 2u;
#pragma unroll
    for (int c = 0; c < 16; ++c) dma16(rv, vv, sv + (unsigned)c * 64 * HD * 2, lbase + R_W + c * 8192 + w * 1024);
    WAIT_V(0); BAR();
    const unsigned lo = (g >> 1) * 1024 + tu * 128 + (((tu >> 1) ^ (w & 3)) << 5) + (fr & 3) * 8;
#pragma unroll
    for (int c = 0; c < 8; ++c) WV[c] = w_frag(lds + R_W + (kh * 8 + c) * 8192 + lo);
    LBAR();
    const int u = (lane >> 2) & 7;
    const unsigned vw = (unsigned)((16 * (u >> 2) + 8 * ((u >> 1) & 1) + 4 * (lane >> 5) + (u & 1)) * D_ + (lane & 3) * 8) * 2u;
    const unsigned sw = (unsigned)(wr * 512 * D_ + cb * 32) * 2u;
    const unsigned bo = (g >> 1) * 1024 + tu * 64 + (fr & 3) * 8;
    auto stageo = [&](int hd) {
#pragma unroll
      for (int q = 0; q < 8; ++q)
        dma16(ro, vw, sw + (unsigned)((hd * 128 + 64 * (q >> 2) + 32 * (q & 1) + 2 * ((q >> 1) & 1)) * D_) * 2u, lbase + R_W + w * 16384 + (hd & 1) * 8192 + q * 1024);
    };
    stageo(0); stageo(1);
#pragma unroll
    for (int hd = 0; hd < 4; ++hd) {
      if (hd < 3) wait_v<8>(); else wait_v<0>();
#pragma unroll
      for (int f = 0; f < 4; ++f) WB[hd * 4 + f] = w_frag(lds + R_W + w * 16384 + (hd & 1) * 8192 + (f >> 1) * 512 + (f & 1) * 32 + bo);
      WAIT_L0(); SCHED();
      if (hd < 2) stageo(hd + 2);
    }
  }
  WST(rs, 176 + rg * 8 + (cb >> 2), 4);
  unsigned hsrc = (unsigned)((lane >> 3) * 2048 + (((lane & 7) ^ (lane >> 3)) << 4)), hoff = (unsigned)((lane & 15) * 128 + (((lane >> 4) ^ (lane & 7)) << 4));
  asm volatile("" : "+v"(l16), "+v"(l8), "+v"(moff), "+v"(roff), "+v"(soff), "+v"(hsrc), "+v"(hoff));
  unsigned cap = SPIN_CAP;
  auto h_piece = [&](int b, int q) { dma16x(rs, hsrc, WS_H + (unsigned)b * 65536u + (w & 3) * 16384 + (2 * q + kh) * 128, lbase + R_W + (b & 1) * 65536 + (q * 8 + w) * 1024); };
  auto v_tok = [&](int b, int nb) {
    LAS unsigned char* a0 = lad(hoff, lbase + R_W + (b & 1) * 65536 + kh * 32768);
    LAS unsigned char* a1 = lad<64>(hoff, lbase + R_W + (b & 1) * 65536 + kh * 32768);
    f32x4 x0 = {0.f, 0.f, 0.f, 0.f}, x1 = x0;
    bf16x8 af[4][2];
    auto rd = [&](int k) { LAS unsigned char* q = ((k & 1) ? a1 : a0) + (k >> 1) * 4096; af[k & 3][0] = *(const LAS bf16x8*)(q); af[k & 3][1] = *(const LAS bf16x8*)(q + 2048); };
    rd(0); rd(1); rd(2);
#pragma unroll
    for (int k = 0; k < ((EXPV & 2) ? 8 : 16); ++k) {
      if (k + 3 < 16) rd(k + 3);
      const bf16x8 wf = (k & 1) ? __builtin_shufflevector(WV[k >> 1], WV[k >> 1], 8, 9, 10, 11, 12, 13, 14, 15) : __builtin_shufflevector(WV[k >> 1], WV[k >> 1], 0, 1, 2, 3, 4, 5, 6, 7);
      x0 = mfma32(af[k & 3][0], wf, x0); x1 = mfma32(af[k & 3][1], wf, x1);
      SCHED();
      if (k < 8 && !(EXPV & 1)) { h_piece(nb, k); SCHED(); }
    }
    LAS unsigned char* xv = lad(l16, lbase + R_RED + 4096 + (w & 3) * 2048);
    if (kh) { *(LAS f32x4*)(xv) = x0; *(LAS f32x4*)(xv + 1024) = x1; }
    LBAR();
    if (!kh && b < NSEG) {
      x0 += *(const LAS f32x4*)(xv); x1 += *(const LAS f32x4*)(xv + 1024);
      stx16(rs, l16, WS_VT + (unsigned)(b * NH + cb) * 8192 + (rg * 4 + w) * 1024, u32x4{pack2(x0[0], x0[1]), pack2(x0[2], x0[3]), pack2(x1[0], x1[1]), pack2(x1[2], x1[3])});
    }
  };
  BAR();
  wait_ge(rs, l16 * 8, F_LN, 1u, cap);
#pragma unroll
  for (int q = 0; q < 8; ++q) h_piece(0, q);
  WAIT_V(0); BAR();
  v_tok(0, 1);
  WAIT_V(0); BAR();
  v_tok(1, 2);
  wait_ge(rs, l16 * 8, F_LN + 8192, 1u, cap); wait_ge(rs, (l16 & 0x1F0) * 8, F_LN + 16384, 1u, cap);
  WAIT_V(0); BAR();
  if (w == 0) {
    const int ln = l16 >> 4, sl = (ln >> 1) & 15, col = cb * 32 + (sl >> 3) * 16 + (ln >> 5) * 8 + (sl & 4) + (sl & 1) * 2 + ((sl >> 1) & 1), r = rg * 4 + (ln & 1) * 2;
    stx4(rs, soff, X_MEM + (cb >> 1) * 1024 + rg * 512 + ((cb ^ rg) & 1) * 128, nz((unsigned)p.mem0[r * D_ + col] | ((unsigned)p.mem0[(r + 1) * D_ + col] << 16)));
  }
  Sched tt; tt.tau = rt32() - 100000; tt.prev = tt.tau; tt.ea = 195 * 8; tt.eb = 118 * 8; tt.d1 = 0; tt.moved = true;
  const int cu_ = 32 + (rg * 32 + cb) / 2; (void)cu_;
  ST_DECL
  for (int s = 0; s < NSEG; ++s) {
    if (w == 7) wait_until(tt.tau);
    BAR();
    ST0();
    gather_mem(rs, lbase, s, wr, l16, cap);
    ST(1);
    LBAR();
    ST(2);
    {
      f32x4 c0 = {0.f, 0.f, 0.f, 0.f};
      LAS unsigned char* mp = lad(moff, lbase + R_MEM + kh * 8192);
      bf16x8 af[8];
#pragma unroll
      for (int c = 0; c < 8; ++c) af[c] = mem_frag(mp, c);
      SCHED();
#pragma unroll
      for (int c = 0; c < 8; ++c) c0 = smf(af[c], WV[c], c0, sidx);
      const f32x4 f = fold(c0, c0);
      if (l16 < 512) stx8(rs, l8, X_VM + (unsigned)s * RSTR + cb * 4096 + (rg * 4 + (w & 3)) * 512 + kh * 256, u32x2{nz(pack2(f[0], f[1])), nz(pack2(f[2], f[3]))});
    }
    ST(3);
    {
      unsigned xb, xa;
      times_issue(rs, w == 7 ? l16 : 0x1E000000u, s, xb, xa);
      v_tok(s + 2, min(s + 3, NSEG - 1));
      if (EXPV & 1) WAIT_V(0);
      asm volatile("s_waitcnt vmcnt(8)" : "+v"(xb), "+v"(xa) :: "memory");
      if (w == 7) retime(rs, l16, s, xb, xa, tt, p, cap);
    }
    if (s + 1 < NSEG) {
      u32x4 a[4];
      const unsigned so = X_OX + s * RSTR + rg * 32768 + wr * 4096;
      if (w == 7) wait_until(tt.tau + tt.d1);
      BAR();
      ST(4);
      issue4(rs, l16, so, so + 1024, so + 2048, so + 3072, a[0], a[1], a[2], a[3]);
      f32x4 c[4];
#pragma unroll
      for (int t = 0; t < 4; ++t) c[t] = f32x4{0.f, 0.f, 0.f, 0.f};
#pragma unroll
      for (int i = 0; i < 4; ++i) {
        if (i == 0) arrive<0>(rs, l16, so, a[0], cap); else if (i == 1) arrive<1>(rs, l16, so + 1024, a[1], cap);
        else if (i == 2) arrive<2>(rs, l16, so + 2048, a[2], cap); else arrive<3>(rs, l16, so + 3072, a[3], cap);
#pragma unroll
        for (int t = 0; t < 4; ++t) c[t] = smf(__builtin_bit_cast(bf16x8, a[i]), WB[i * 4 + t], c[t], sidx);
        SCHED();
      }
      ST(5);
      {
        const bool odd = (l16 & 256) != 0;
        const f32x4 f = fold(odd ? c[2] : c[0], odd ? c[3] : c[1]);
        const auto e0 = __builtin_amdgcn_permlane16_swap(f2u(f[0]), f2u(f[2]), false, false), e1 = __builtin_amdgcn_permlane16_swap(f2u(f[1]), f2u(f[3]), false, false);
        *(LAS f32x2*)(lad(roff, lbase)) = f32x2{u2f(e0[0]) + u2f(e0[1]), u2f(e1[0]) + u2f(e1[1])};
      }
      ST(6);
      LBAR();
      ST(7);
      if (w == 0) {
        LAS unsigned char* rp = lad(l8, lbase + R_RED);
        f32x2 v[8];
#pragma unroll
        for (int i = 0; i < 8; ++i) v[i] = *(const LAS f32x2*)(rp + i * 512);
        const f32x2 sum = ((v[0] + v[1]) + (v[2] + v[3])) + ((v[4] + v[5]) + (v[6] + v[7]));
        stx4(rs, soff, X_MEM + (s + 1) * RSTR + (cb >> 1) * 1024 + rg * 512 + ((cb ^ rg) & 1) * 128, nz(pack2(sum[0], sum[1])));
        const unsigned t = max(rt32(), 1u);
        if (l16 == 0) stx4(rs, 0, X_TS + (unsigned)(s + 1) * RSTR + (unsigned)(rg * 32 + cb) * 128, t);
      }
      ST(8);
    }
    advance(tt, p);
  }
  ST_FLUSH();
  if (cap == 0u && l16 == 0) stx4(rs, 0, X_CTL, 1u);
}

#if defined(__HIP_DEVICE_COMPILE__)
__attribute__((target("no-packed-fp32-ops")))
#endif
__global__ void __launch_bounds__(512, 2) k_main(MainP p) {
  extern __shared__ __attribute__((aligned(16))) unsigned char smem[];
  const int b = blockIdx.x, x = b & 7, j = b >> 3;
  if (x >= 3) worker(p, (LAS unsigned char*)smem, x - 3, j);
  else if (x == 0) role_a(p, (LAS unsigned char*)smem, j);
  else role_vb(p, (LAS unsigned char*)smem, x - 1, j);
}

extern "C" void kernel_launch(void* const* d_in, const int* in_sizes, int n_in, void* d_out, int out_size, void* d_ws, size_t ws_size,
                              hipStream_t stream) {
  unsigned char* ws = (unsigned char*)d_ws;
  (void)hipMemsetAsync(ws, 0, X_ZERO, stream);
  MainP p;
  p.x = (const u16*)d_in[0]; p.mem0 = (const u16*)d_in[1]; p.c = (const u16*)d_in[12]; p.wmod = (const u16*)d_in[13]; p.Wq = (const u16*)d_in[2]; p.Wk = (const u16*)d_in[3]; p.Wv = (const u16*)d_in[4]; p.Wo = (const u16*)d_in[5];
  p.w1 = (const u16*)d_in[8]; p.w2 = (const u16*)d_in[9]; p.w3 = (const u16*)d_in[10]; p.w4 = (const u16*)d_in[11];
  p.out = (u16*)d_out; p.ws = ws; p.d1 = 10; p.d2 = 10; p.d3 = 2; p.d4 = 100;
  (void)hipFuncSetAttribute((const void*)k_main, hipFuncAttributeMaxDynamicSharedMemorySize, R_LDS);
  k_main<<<256, 512, R_LDS, stream>>>(p);
}
```

```cpp
#include <hip/hip_runtime.h>
#include <hip/hip_bf16.h>
#include <cstdlib>
#include <type_traits>
#pragma clang diagnostic ignored "-Winline-asm"

#define LAS __attribute__((address_space(3)))
#define GAS __attribute__((address_space(1)))
typedef unsigned short u16;
typedef __bf16 bf16x2 __attribute__((ext_vector_type(2)));
typedef __bf16 bf16x4 __attribute__((ext_vector_type(4)));
typedef __bf16 bf16x8 __attribute__((ext_vector_type(8)));
typedef __bf16 bf16x16 __attribute__((ext_vector_type(16)));
typedef short s16x4 __attribute__((ext_vector_type(4)));
typedef float f32x2 __attribute__((ext_vector_type(2)));
typedef float f32x4 __attribute__((ext_vector_type(4)));
typedef float f32x16 __attribute__((ext_vector_type(16)));
typedef unsigned u32x2 __attribute__((ext_vector_type(2)));
typedef unsigned u32x4 __attribute__((ext_vector_type(4)));

constexpr int S_ = 2048, D_ = 1024, NH = 32, DH = 128, HD = 4096, NSEG = 64, FF = 3584;

constexpr unsigned MB = 1u << 20;
constexpr unsigned F_LN = 0;
constexpr unsigned F_ST = 24576;
constexpr unsigned F_CH = F_ST + 98304;
constexpr unsigned X_CTL = F_CH + 4096;
constexpr unsigned X_MOD = X_CTL + 4096;
constexpr unsigned X_REC = X_MOD + 24576;
constexpr unsigned RSTR = 225280;
constexpr int ZA = 8;
constexpr unsigned X_MEM = X_REC;
constexpr unsigned X_OX = X_REC + 16384;
constexpr unsigned X_VM = X_REC + 81920;
constexpr unsigned X_TS = X_REC + 212992;
constexpr unsigned X_ZERO = X_REC + ZA * RSTR;
constexpr unsigned WS_H = 16 * MB;
constexpr unsigned WS_Q = 20 * MB;
constexpr unsigned WS_K = 36 * MB;
constexpr unsigned WS_VT = 52 * MB;
constexpr unsigned WS_O = 68 * MB;
constexpr unsigned WS_X1 = 84 * MB;
constexpr unsigned WS_H2 = 88 * MB;
constexpr unsigned WS_G = 92 * MB;
constexpr unsigned WS_H1S = 96 * MB;
constexpr unsigned WS_X2 = 101 * MB;
constexpr unsigned WS_ST5 = 106 * MB;
constexpr unsigned WS_U = 108 * MB;
constexpr unsigned WS_KM = 124 * MB;
constexpr unsigned WS_P2 = 132 * MB;
constexpr unsigned WS_P8 = 164 * MB;
constexpr unsigned WS_END = 198 * MB;

__device__ __forceinline__ float bf2f(unsigned short b) { return __builtin_bit_cast(float, (unsigned)b << 16); }
__device__ __forceinline__ unsigned f2u(float f) { return __builtin_bit_cast(unsigned, f); }
__device__ __forceinline__ float u2f(unsigned u) { return __builtin_bit_cast(float, u); }
__device__ __forceinline__ float lo2f(unsigned w) { return __builtin_bit_cast(float, w << 16); }
__device__ __forceinline__ float hi2f(unsigned w) { return __builtin_bit_cast(float, w & 0xFFFF0000u); }
__device__ __forceinline__ unsigned pack2(float a, float b) { bf16x2 v; v[0] = (__bf16)a; v[1] = (__bf16)b; return __builtin_bit_cast(unsigned, v); }
__device__ __forceinline__ float rbf(float a) { return (float)(__bf16)a; }
__device__ __forceinline__ unsigned nz(unsigned w) { return max(w, 1u); }
__device__ __forceinline__ float gelu_tanh(float x) {
  const float e = __builtin_amdgcn_exp2f(x * fmaf(x * x, -0.1029432397f, -2.302208198f));
  return x * __builtin_amdgcn_rcpf(1.0f + e);
}
template <int CTRL> __device__ __forceinline__ float dpp_add(float v) { return v + u2f(__builtin_amdgcn_update_dpp(0u, f2u(v), CTRL, 0xF, 0xF, true)); }
__device__ __forceinline__ float wave_sum(float v) {
  v = dpp_add<0xB1>(v); v = dpp_add<0x4E>(v); v = dpp_add<0x141>(v); v = dpp_add<0x140>(v);
  auto t = __builtin_amdgcn_permlane16_swap(f2u(v), f2u(v), false, false);
  v = u2f(t[0]) + u2f(t[1]);
  t = __builtin_amdgcn_permlane32_swap(f2u(v), f2u(v), false, false);
  return u2f(t[0]) + u2f(t[1]);
}
#define WAIT_V(n) asm volatile("s_waitcnt vmcnt(" #n ")" ::: "memory")
#define WAIT_L0() asm volatile("s_waitcnt lgkmcnt(0)" ::: "memory")
#define BAR() do { asm volatile("" ::: "memory"); __builtin_amdgcn_s_barrier(); asm volatile("" ::: "memory"); } while (0)
#define LBAR() do { WAIT_L0(); BAR(); } while (0)
template <int N> __device__ __forceinline__ void wait_v() { asm volatile("s_waitcnt vmcnt(%0)" :: "n"(N) : "memory"); }
__device__ __forceinline__ void dma16(__amdgpu_buffer_rsrc_t rs, unsigned voff, unsigned soff, unsigned ldsa) {
  asm volatile("s_mov_b32 m0, %0\n\ts_nop 3\n\tbuffer_load_dwordx4 %1, %2, %3 offen lds" :: "s"(ldsa), "v"(voff), "s"(rs), "s"(soff) : "m0", "memory");
}
__device__ __forceinline__ void dma16x(__amdgpu_buffer_rsrc_t rs, unsigned voff, unsigned soff, unsigned ldsa) {
  asm volatile("s_mov_b32 m0, %0\n\ts_nop 3\n\tbuffer_load_dwordx4 %1, %2, %3 offen sc1 lds" :: "s"(ldsa), "v"(voff), "s"(rs), "s"(soff) : "m0", "memory");
}
__device__ __forceinline__ u32x4 ldx16(__amdgpu_buffer_rsrc_t rs, unsigned voff, unsigned soff) { return __builtin_amdgcn_raw_buffer_load_b128(rs, voff, soff, 16); }
__device__ __forceinline__ u32x2 ldx8(__amdgpu_buffer_rsrc_t rs, unsigned voff, unsigned soff) { return __builtin_amdgcn_raw_buffer_load_b64(rs, voff, soff, 16); }
__device__ __forceinline__ unsigned ldx4(__amdgpu_buffer_rsrc_t rs, unsigned voff, unsigned soff) { return __builtin_amdgcn_raw_buffer_load_b32(rs, voff, soff, 16); }
__device__ __forceinline__ void stx16(__amdgpu_buffer_rsrc_t rs, unsigned voff, unsigned soff, u32x4 v) {
  __builtin_amdgcn_raw_buffer_store_b128(v, rs, voff, soff, 16);
  asm volatile("s_nop 1" :: "v"(v) : "memory");
}
__device__ __forceinline__ void stx8(__amdgpu_buffer_rsrc_t rs, unsigned voff, unsigned soff, u32x2 v) { __builtin_amdgcn_raw_buffer_store_b64(v, rs, voff, soff, 16); }
__device__ __forceinline__ void stx4(__amdgpu_buffer_rsrc_t rs, unsigned voff, unsigned soff, unsigned v) { __builtin_amdgcn_raw_buffer_store_b32(v, rs, voff, soff, 16); }
__device__ __forceinline__ void wait_ge(__amdgpu_buffer_rsrc_t rs, unsigned voff, unsigned soff, unsigned need, unsigned& cap) {
  unsigned t;
  asm volatile("s_nop 4\n\t"
               "1:\n\t"
               "buffer_load_dword %[t], %[vo], %[rs], %[so] offen sc1\n\t"
               "s_waitcnt vmcnt(0)\n\t"
               "v_cmp_gt_u32 vcc, %[need], %[t]\n\t"
               "s_cmp_eq_u64 vcc, 0\n\t"
               "s_cbranch_scc1 2f\n\t"
               "s_sleep 2\n\t"
               "s_sub_u32 %[cap], %[cap], 1\n\t"
               "s_cbranch_scc0 1b\n\t"
               "s_mov_b32 %[cap], 0\n\t"
               "2:"
               : [t] "=&v"(t), [cap] "+s"(cap) : [vo] "v"(voff), [rs] "s"(rs), [so] "s"(soff), [need] "s"(need) : "vcc", "scc", "memory");
}
__device__ __forceinline__ void wait_seen(__amdgpu_buffer_rsrc_t rs, unsigned voff, unsigned soff, unsigned need, unsigned& seen, unsigned& cap) {
  if (seen >= need) return;
  unsigned t;
  asm volatile("s_nop 4\n\t"
               "1:\n\t"
               "buffer_load_dword %[t], %[vo], %[rs], %[so] offen sc1\n\t"
               "s_waitcnt vmcnt(0)\n\t"
               "v_cmp_gt_u32 vcc, %[need], %[t]\n\t"
               "s_cbranch_vccz 2f\n\t"
               "s_sleep 1\n\t"
               "s_sub_u32 %[cap], %[cap], 1\n\t"
               "s_cbranch_scc0 1b\n\t"
               "s_mov_b32 %[cap], 0\n\t"
               "2:"
               : [t] "=&v"(t), [cap] "+s"(cap) : [vo] "v"(voff), [rs] "s"(rs), [so] "s"(soff), [need] "s"(need) : "vcc", "scc", "memory");
  seen = min((unsigned)__builtin_amdgcn_readlane(t, 0), (unsigned)__builtin_amdgcn_readlane(t, 1));
}
__device__ __forceinline__ __amdgpu_buffer_rsrc_t mk_rs(const void* p, unsigned bytes) { return __builtin_amdgcn_make_buffer_rsrc((void*)p, 0, (int)bytes, 0x00020000); }
#define SCHED() __builtin_amdgcn_sched_barrier(0)
__device__ __forceinline__ unsigned now32() { unsigned long long t; asm volatile("s_memtime %0\n\ts_waitcnt lgkmcnt(0)" : "=s"(t) :: "memory"); return (unsigned)t; }
__device__ __forceinline__ unsigned rt32() { unsigned long long t; asm volatile("s_memrealtime %0\n\ts_waitcnt lgkmcnt(0)" : "=s"(t) :: "memory"); return (unsigned)t; }
__device__ __forceinline__ void wait_until(unsigned t) { while ((int)(rt32() - t) < 0) {} }
template <int X = 0> __device__ __forceinline__ LAS unsigned char* lad(unsigned v, unsigned u) {
  unsigned a;
  if (X == 0) asm volatile("v_add_u32 %0, %1, %2" : "=v"(a) : "s"(u), "v"(v));
  else asm volatile("v_xor_b32 %0, %3, %2\n\tv_add_u32 %0, %1, %0" : "=&v"(a) : "s"(u), "v"(v), "n"(X));
  return (LAS unsigned char*)(size_t)a;
}
__device__ __forceinline__ bf16x4 trread(LAS unsigned char* p) { return __builtin_amdgcn_ds_read_tr16_b64_v4bf16((LAS bf16x4*)p); }
__device__ __forceinline__ bf16x8 cat8(bf16x4 a, bf16x4 b) { return __builtin_shufflevector(a, b, 0, 1, 2, 3, 4, 5, 6, 7); }
__device__ __forceinline__ bf16x16 cat16(bf16x8 a, bf16x8 b) { return __builtin_shufflevector(a, b, 0, 1, 2, 3, 4, 5, 6, 7, 8, 9, 10, 11, 12, 13, 14, 15); }
__device__ __forceinline__ f32x4 mfma32(bf16x8 a, bf16x8 b, f32x4 c) { return __builtin_amdgcn_mfma_f32_16x16x32_bf16(a, b, c, 0, 0, 0); }
__device__ __forceinline__ f32x4 mfma16(u32x2 a, u32x2 b, f32x4 c) {
  return mfma32(__builtin_bit_cast(bf16x8, u32x4{a[0], a[1], 0u, 0u}), __builtin_bit_cast(bf16x8, u32x4{b[0], b[1], 0u, 0u}), c);
}
__device__ __forceinline__ f32x4 mfma16n(u32x2 a, u32x2 b, f32x4 c) { return __builtin_amdgcn_mfma_f32_16x16x16bf16_1k(__builtin_bit_cast(s16x4, a), __builtin_bit_cast(s16x4, b), c, 0, 0, 0); }
__device__ __forceinline__ f32x4 smf(bf16x8 a, bf16x16 b, f32x4 c, int idx) { return __builtin_amdgcn_smfmac_f32_16x16x64_bf16(a, b, c, idx, 0, 0); }

__device__ __forceinline__ void ln_row(float (&x)[16]) {
  float s = 0.f;
#pragma unroll
  for (int i = 0; i < 16; ++i) s += x[i];
  const float mean = wave_sum(s) * (1.0f / 1024.0f);
  float q = 0.f;
#pragma unroll
  for (int i = 0; i < 16; ++i) { const float d = x[i] - mean; q += d * d; }
  const float var = wave_sum(q) * (1.0f / 1024.0f);
  const float m = rbf(mean), isd = __builtin_amdgcn_rcpf(rbf(__builtin_amdgcn_sqrtf(rbf(rbf(var) + 1e-5f))));
#pragma unroll
  for (int i = 0; i < 16; ++i) x[i] = rbf(rbf(x[i] - m) * isd);
}
__device__ __forceinline__ void load_row16(const u16* rowp, int lane, float (&x)[16]) {
  const u32x4 a = *(const u32x4*)(rowp + lane * 8), b = *(const u32x4*)(rowp + 512 + lane * 8);
#pragma unroll
  for (int i = 0; i < 4; ++i) { x[2 * i] = lo2f(a[i]); x[2 * i + 1] = hi2f(a[i]); x[8 + 2 * i] = lo2f(b[i]); x[9 + 2 * i] = hi2f(b[i]); }
}
__device__ __forceinline__ void store_row16(u16* rowp, int lane, const float (&x)[16]) {
  u32x4 a, b;
#pragma unroll
  for (int i = 0; i < 4; ++i) { a[i] = pack2(x[2 * i], x[2 * i + 1]); b[i] = pack2(x[8 + 2 * i], x[9 + 2 * i]); }
  *(u32x4*)(rowp + lane * 8) = a; *(u32x4*)(rowp + 512 + lane * 8) = b;
}
__host__ __device__ __forceinline__ int mem_slot(int dl) { return ((dl >> 3) & 1) * 32 + (dl >> 4) * 8 + (dl & 4) + (dl & 1) * 2 + ((dl >> 1) & 1); }

#define WST(rs, wk, i)
struct MainP { const u16 *x, *mem0, *Wq, *Wk, *Wv, *Wo, *w1, *w2, *w3, *w4, *c, *wmod; u16* out; unsigned char* ws; int d1, d2, d3, d4; };
constexpr unsigned SPIN_CAP = 1u << 18;

__device__ __forceinline__ float gmax(float v) {
  auto t = __builtin_amdgcn_permlane16_swap(f2u(v), f2u(v), false, false);
  v = fmaxf(u2f(t[0]), u2f(t[1]));
  t = __builtin_amdgcn_permlane32_swap(f2u(v), f2u(v), false, false);
  return fmaxf(u2f(t[0]), u2f(t[1]));
}
__device__ __forceinline__ float gsum(float v) {
  auto t = __builtin_amdgcn_permlane16_swap(f2u(v), f2u(v), false, false);
  v = u2f(t[0]) + u2f(t[1]);
  t = __builtin_amdgcn_permlane32_swap(f2u(v), f2u(v), false, false);
  return u2f(t[0]) + u2f(t[1]);
}
__device__ __forceinline__ void publish(__amdgpu_buffer_rsrc_t rs, unsigned word, unsigned val) {
  WAIT_V(0); BAR();
  if (threadIdx.x == 0) stx4(rs, 0, word, val);
}

constexpr int QB0 = 24;
constexpr int P_S2 = 0, P_S8 = 32, P_S7 = 64, P_S4 = 96, P_S5 = 104, P_S3 = 112, P_PJ = 208, P_S1 = 336;
constexpr int NLN = 160;

enum { SG_WO = 0, SG_W1 = 1, SG_W2 = 2, SG_W3 = 3, SG_W4 = 4, SG_PJ = 5 };
constexpr int G_XCH = 131072, G_K = G_XCH + 16384, G_CTL = G_K + 8192;
__device__ __forceinline__ f32x16 mfma3232(bf16x8 a, bf16x8 b, f32x16 c) { return __builtin_amdgcn_mfma_f32_32x32x16_bf16(a, b, c, 0, 0, 0); }
__device__ __forceinline__ void widen32(const f32x16& a, u32x4 (&v)[2]) {
#pragma unroll
  for (int u = 0; u < 2; ++u) {
    const auto r0 = __builtin_amdgcn_permlane32_swap(pack2(a[8 * u], a[8 * u + 1]), pack2(a[8 * u + 4], a[8 * u + 5]), false, false);
    const auto r1 = __builtin_amdgcn_permlane32_swap(pack2(a[8 * u + 2], a[8 * u + 3]), pack2(a[8 * u + 6], a[8 * u + 7]), false, false);
    v[u] = u32x4{r0[0], r1[0], r0[1], r1[1]};
  }
}
__device__ __forceinline__ float hsum(float v) { const auto t = __builtin_amdgcn_permlane32_swap(f2u(v), f2u(v), false, false); return u2f(t[0]) + u2f(t[1]); }

__device__ __forceinline__ void attend(__amdgpu_buffer_rsrc_t rs, int seg, int h, int t, int lane, unsigned& chs, unsigned& cap) {
  const int fr = lane & 15, g = lane >> 4;
  const unsigned l16 = lane * 16, ub = (unsigned)(seg * NH + h);
  wait_seen(rs, 0, F_CH + h * 128, (unsigned)max(seg, 1), chs, cap);
  wait_ge(rs, (lane & 1) && seg >= QB0 ? 96u * 128u : (unsigned)(seg % 3) * 4096u, F_ST + (unsigned)(P_PJ + h) * 128, (unsigned)seg + 1u, cap);
  f32x4 s0 = {0.f, 0.f, 0.f, 0.f}, s1 = s0, s2 = s0;
  u32x4 q[4], kmf[4];
  u32x2 vmf[8];
  const unsigned em = (unsigned)(fr >> 3) * 512 + (unsigned)g * 128 + (unsigned)(8 * ((fr >> 2) & 1) + (fr & 3)) * 8;
  auto mem_load = [&](unsigned off) {
#pragma unroll
    for (int ks = 0; ks < 4; ++ks) kmf[ks] = ldx16(rs, l16 + off, WS_KM + ub * 4096 + ks * 1024);
#pragma unroll
    for (int i = 0; i < 8; ++i) vmf[i] = ldx8(rs, em + off, X_VM + (unsigned)seg * RSTR + h * 4096 + (i >> 1) * 1024 + (i & 1) * 32);
  };
  const bool have = chs > (unsigned)seg;
  f32x4 o[8];
  float mt = -3.0e38f, lt = 0.f;
  const float scale = 0.08838834764831845f, L2E = 1.4426950408889634f;
  {
    u32x4 k0[4], k1[4], vv[8];
#pragma unroll
    for (int ks = 0; ks < 4; ++ks) {
      q[ks] = ldx16(rs, l16, WS_Q + ub * 8192 + t * 4096 + ks * 1024);
      k0[ks] = ldx16(rs, l16, WS_K + ub * 8192 + ks * 1024); k1[ks] = ldx16(rs, t ? l16 : 0xF0000000u, WS_K + ub * 8192 + 4096 + ks * 1024);
    }
    const unsigned ev = (unsigned)(fr >> 3) * 1024 + (unsigned)(g * 16 + 8 * ((fr >> 2) & 1) + (fr & 3)) * 16;
#pragma unroll
    for (int i = 0; i < 8; ++i) vv[i] = ldx16(rs, ev, WS_VT + ub * 8192 + (i >> 1) * 2048 + (i & 1) * 64);
#pragma unroll
    for (int ks = 0; ks < 4; ++ks) {
      s1 = mfma32(__builtin_bit_cast(bf16x8, k0[ks]), __builtin_bit_cast(bf16x8, q[ks]), s1); s2 = mfma32(__builtin_bit_cast(bf16x8, k1[ks]), __builtin_bit_cast(bf16x8, q[ks]), s2);
    }
    SCHED();
    mem_load(have ? 0u : 0xF0000000u);
    SCHED();
    float b[4], c[4];
#pragma unroll
    for (int r = 0; r < 4; ++r) {
      const int j = 4 * g + r;
      b[r] = (t == 1 || j <= fr) ? rbf(rbf(s1[r]) * scale) : -3.0e38f;
      c[r] = (t == 1 && j <= fr) ? rbf(rbf(s2[r]) * scale) : -3.0e38f;
      mt = fmaxf(mt, fmaxf(b[r], c[r]));
    }
    mt = gmax(mt);
#pragma unroll
    for (int r = 0; r < 4; ++r) { b[r] = __builtin_amdgcn_exp2f((b[r] - mt) * L2E); c[r] = __builtin_amdgcn_exp2f((c[r] - mt) * L2E); lt += b[r] + c[r]; }
    const u32x4 pt = {pack2(b[0], b[1]), pack2(b[2], b[3]), pack2(c[0], c[1]), pack2(c[2], c[3])};
#pragma unroll
    for (int i = 0; i < 8; ++i) o[i] = mfma32(__builtin_bit_cast(bf16x8, vv[i]), __builtin_bit_cast(bf16x8, pt), f32x4{0.f, 0.f, 0.f, 0.f});
    lt = gsum(lt);
  }
  asm volatile("" : "+v"(o[0]), "+v"(o[1]), "+v"(o[2]), "+v"(o[3]), "+v"(o[4]), "+v"(o[5]), "+v"(o[6]), "+v"(o[7]), "+v"(lt));
  if (!have) { wait_seen(rs, 0, F_CH + h * 128, (unsigned)seg + 1u, chs, cap); mem_load(0u); }
#pragma unroll
  for (int ks = 0; ks < 4; ++ks) s0 = mfma32(__builtin_bit_cast(bf16x8, kmf[ks]), __builtin_bit_cast(bf16x8, q[ks]), s0);
  float a[4];
#pragma unroll
  for (int r = 0; r < 4; ++r) a[r] = rbf(rbf(s0[r]) * scale);
  const float mx = fmaxf(mt, gmax(fmaxf(fmaxf(a[0], a[1]), fmaxf(a[2], a[3]))));
#pragma unroll
  for (int r = 0; r < 4; ++r) a[r] = __builtin_amdgcn_exp2f((a[r] - mx) * L2E);
  const u32x2 pm = {pack2(a[0], a[1]), pack2(a[2], a[3])};
  const float al = __builtin_amdgcn_exp2f((mt - mx) * L2E);
  const float inv = __builtin_amdgcn_rcpf(fmaf(al, lt, 0.5f * gsum((a[0] + a[1]) + (a[2] + a[3])))), ai = al * inv;
#pragma unroll
  for (int u = 0; u < 4; ++u) {
    const f32x4 x = o[2 * u] * ai + mfma16n(vmf[2 * u], pm, f32x4{0.f, 0.f, 0.f, 0.f}) * inv, y = o[2 * u + 1] * ai + mfma16n(vmf[2 * u + 1], pm, f32x4{0.f, 0.f, 0.f, 0.f}) * inv;
    stx16(rs, l16, WS_O + (unsigned)seg * 262144 + (unsigned)((h * 4 + u) * 2 + t) * 1024, u32x4{pack2(x[0], x[1]), pack2(x[2], x[3]), pack2(y[0], y[1]), pack2(y[2], y[3])});
  }
  WAIT_V(0);
  if (lane == 0) stx4(rs, 0, F_ST + (unsigned)(P_S1 + (seg & 3) * 64 + h * 2 + t) * 128, (unsigned)seg + 1u);
}

template <int SG>
__device__ __forceinline__ void stage_gemm(LAS unsigned char* lds, __amdgpu_buffer_rsrc_t rs, const u16* Wg, int wk, int kq, int cb, int pid, unsigned& cap,
                                           unsigned dst = 0, int bstr = 1, int nb = 0, const u16* side = nullptr, int srows = 0, int sld = 0, const u16* side3 = nullptr) {
  constexpr bool PROJ = SG == SG_PJ, ROWM = (SG == SG_W1 || PROJ);
  constexpr int KS = (SG == SG_W4) ? 28 : 32;
  constexpr int LDW = PROJ ? HD : (SG == SG_W3) ? FF : D_;
  constexpr int WROWS = (SG == SG_WO) ? HD : (SG == SG_W4 ? FF : D_);
  const int NBLK = PROJ ? nb : (SG == SG_WO || SG == SG_W1) ? 64 : 65;
  constexpr unsigned ABASE = PROJ ? WS_H : SG == SG_WO ? WS_O : SG == SG_W1 ? WS_G : SG == SG_W2 ? WS_H1S : SG == SG_W3 ? WS_X2 : WS_U;
  constexpr unsigned ASTR = SG == SG_WO ? 262144u : SG == SG_W4 ? 229376u : 65536u;
  constexpr int NPC = KS / 4;
  const unsigned myword = F_ST + (unsigned)pid * 128u;
  int tid = threadIdx.x; asm volatile("" : "+v"(tid));
  const int lane = tid & 63, w = __builtin_amdgcn_readfirstlane(tid >> 6), row = lane & 31, hf = lane >> 5;
  const int kh = w >> 2, wl = w & 3;
  const int koff = PROJ ? 0 : kq;
  const unsigned lbase = (unsigned)(size_t)lds, l16 = lane * 16;
  const int col0 = cb * 128 + wl * 32;
  bf16x8 Wf[KS];
  {
    const __amdgpu_buffer_rsrc_t rsW = mk_rs(Wg, (unsigned)WROWS * LDW * 2u);
    const int ub = lane >> 3, tu = 4 * hf + ((lane & 15) >> 2);
    const unsigned vB = (unsigned)(((ub & 3) + 8 * (ub >> 2)) * LDW + 16 * (((lane >> 1) & 3) ^ (ub >> 1)) + 8 * (lane & 1)) * 2u;
    const unsigned sB = (unsigned)((koff * KS * 32 + 32 * (w >> 2) + 16 * ((w >> 1) & 1) + 4 * (w & 1)) * LDW + cb * 128) * 2u;
    const int boff = kh * 65536 + (wl >> 1) * 8192 + tu * 128 + (((tu >> 1) ^ (2 * (wl & 1) + ((lane >> 4) & 1))) << 5) + (lane & 3) * 8;
#pragma unroll
    for (int ps = 0; ps < 2; ++ps) {
      BAR();
#pragma unroll
      for (int hh = 0; hh < 2; ++hh)
#pragma unroll
        for (int i = 0; i < 4; ++i)
          if (4 * ps + i < NPC && !(PROJ && ps == 0)) {
            const unsigned so = sB + (unsigned)((hh * NPC + 4 * ps + i) * 64 * LDW) * 2u, d = lbase + hh * 65536 + i * 16384 + w * 1024;
            dma16(rsW, vB, so, d); dma16(rsW, vB, so + 128, d + 8192);
          }
      WAIT_V(0); BAR();
#pragma unroll
      for (int i = 0; i < 4; ++i)
        if (4 * ps + i < NPC) {
#pragma unroll
          for (int k = 0; k < 4; ++k) { LAS unsigned char* q = lds + (boff + i * 16384 + k * 2048); Wf[(4 * ps + i) * 4 + k] = cat8(trread(q), trread(q + 1024)); }
        }
      WAIT_L0();
    }
    if (SG == SG_W1) {
      const u32x4 z = {0u, 0u, 0u, 0u};
      stx16(rs, (unsigned)tid * 16, WS_H1S + cb * 8192, z); stx16(rs, (unsigned)tid * 16, WS_H1S + 64 * 65536 + cb * 8192, z);
      WAIT_V(0);
    }
    if (tid < 8) *(LAS unsigned*)(lds + G_CTL + tid * 4) = 0u;
    LBAR();
  }
  const f32x16 zero16 = {0.f, 0.f, 0.f, 0.f, 0.f, 0.f, 0.f, 0.f, 0.f, 0.f, 0.f, 0.f, 0.f, 0.f, 0.f, 0.f};
  auto put16 = [&](LAS unsigned char* p, const f32x16& a) {
#pragma unroll
    for (int q = 0; q < 4; ++q) *(LAS f32x4*)(p + q * 1024) = f32x4{a[4 * q], a[4 * q + 1], a[4 * q + 2], a[4 * q + 3]};
  };
  if (SG == SG_W3) {
    const u32x4 one4 = {0x3F803F80u, 0x3F803F80u, 0x3F803F80u, 0x3F803F80u};
    f32x16 cs = zero16;
#pragma unroll
    for (int k = 0; k < KS; ++k) cs = mfma3232(Wf[k], __builtin_bit_cast(bf16x8, one4), cs);
    if (kh == 0) put16(lds + wl * 4096 + l16, cs);
    LBAR();
    if (kh == 1 && row == 0) {
#pragma unroll
      for (int q = 0; q < 4; ++q)
        *(LAS f32x4*)(lds + G_K + wl * 128 + hf * 64 + q * 16) = *(const LAS f32x4*)(lds + wl * 4096 + q * 1024 + l16) + f32x4{cs[4 * q], cs[4 * q + 1], cs[4 * q + 2], cs[4 * q + 3]};
    }
    LBAR();
  }
  auto blk = [&](int i) -> int { return PROJ ? bstr * i + kq : i; };
  const unsigned aoff = ROWM ? (unsigned)(row * 128 + ((hf ^ ((row >> 1) & 7)) << 4)) : (unsigned)((row >> 4) * 1024 + hf * 256 + (row & 15) * 16);
  const unsigned vA = ROWM ? (unsigned)((lane >> 3) * 2048 + (((lane & 7) ^ ((4 * wl + (lane >> 4)) & 7)) << 4)) : l16;
  auto piece = [&](int hb, int q, bool on) {
    const int i = hb >> 1, hh = hb & 1;
    const unsigned so = ABASE + (unsigned)blk(i) * ASTR + (unsigned)koff * (KS * 2048) + (ROWM ? wl * 16384 + hh * 1024 : (hh * KS + wl) * 1024);
    if (on) dma16x(rs, vA, so + q * (ROWM ? 128 : 4096), lbase + (i & 1) * 65536 + hh * 32768 + wl * 1024 + q * 4096);
  };
  auto fetch = [&](int hb) {
#pragma unroll
    for (int q = 0; q < NPC; ++q) piece(hb, q, true);
  };
  constexpr bool WEAVE = (SG == SG_W1 || SG == SG_W2 || SG == SG_W3);
  auto products = [&](int i, f32x16& acc, auto&& first, auto&& early, auto&& handover) {
    const unsigned ub = lbase + (i & 1) * 65536 + kh * 32768;
    LAS unsigned char* ab[4];
    ab[0] = lad(aoff, ub);
    if (ROWM) { ab[1] = lad<32>(aoff, ub); ab[2] = lad<64>(aoff, ub); ab[3] = lad<96>(aoff, ub); }
    constexpr int PD = 10;
    bf16x8 af[PD + 1];
    auto rd = [&](int k) { af[k % (PD + 1)] = ROWM ? *(const LAS bf16x8*)(ab[k & 3] + (k >> 2) * 4096) : *(const LAS bf16x8*)(ab[0] + (k >> 1) * 2048 + (k & 1) * 512); };
#pragma unroll
    for (int k = 0; k < PD; ++k) rd(k);
    SCHED();
#pragma unroll
    for (int k = 0; k < KS; ++k) {
      if (k + PD < KS) rd(k + PD);
      acc = mfma3232(Wf[k], af[k % (PD + 1)], k ? acc : zero16);
      SCHED();
      first(k);
      SCHED();
      if (k == KS - 10) { early(); SCHED(); }
      if (k == KS - 5) { handover(); SCHED(); }
    }
    asm volatile("" : "+v"(acc));
  };
  WST(rs, wk, PROJ ? 2 : 10);
#define SEG(x)
  auto announce = [&](int i) {
    if (lane == 0) {
      const unsigned c = __hip_atomic_fetch_add((LAS unsigned*)(lds + G_CTL), 1u, __ATOMIC_RELAXED, __HIP_MEMORY_SCOPE_WORKGROUP);
      if ((c & 3u) == 3u) stx4(rs, 0, myword, (unsigned)blk(i) + 1u);
    }
  };
  if (kh == 0) {
    auto pvo = [&](int i) -> unsigned {
      const int b = blk(i);
      if (PROJ) return F_LN + (unsigned)((b % (NLN / 4)) * 4 + (lane & 3)) * 128u;
      if (SG == SG_WO) return F_ST + (unsigned)(P_S1 + (b & 3) * 64 + kq * 16 + (lane & 15)) * 128u;
      if (SG == SG_W1) return F_ST + (unsigned)(P_S3 + (b % 3) * 32 + (lane & 31)) * 128u;
      if (SG == SG_W2) return F_ST + (unsigned)(P_S4 + (lane & 7)) * 128u;
      if (SG == SG_W3) return F_ST + (unsigned)(P_S5 + (lane & 7)) * 128u;
      return F_ST + (unsigned)(P_S7 + kq * 7 + min(lane & 7, 6)) * 128u;
    };
    auto need = [&](int i) -> unsigned { return PROJ ? 1u : (SG == SG_W2 && i == 64) ? 64u : (unsigned)i + 1u; };
    int R = 0, Rp = 0;
    for (int i = 0; i < NBLK; ++i) {
      if (R <= i) { wait_ge(rs, pvo(i), 0, need(i), cap); fetch(2 * i); fetch(2 * i + 1); }
      const unsigned pk = w == 0 ? (lane < 32 ? pvo(i + 1) : pvo(i + 2)) : 0xF0000000u;
      if (Rp <= i) { if (R <= i + 1) wait_v<NPC>(); else wait_v<2 * NPC>(); }
      BAR();
      SEG(1);
      f32x16 acc;
      unsigned peek;
      asm volatile("buffer_load_dword %0, %1, %2, 0 offen sc1" : "=v"(peek) : "v"(pk), "s"(rs) : "memory");
      u32x4 st[8];
      float mean = 0.f, q2 = 0.f;
      if (SG == SG_W3) {
#pragma unroll
        for (int q = 0; q < 8; ++q) st[q] = ldx16(rs, l16, WS_ST5 + (unsigned)i * 8192 + q * 1024);
      }
      products(i, acc, [&](int k) {
        if (SG == SG_W3) {
          if (k >= 18 && k < 26) { mean += u2f(st[k - 18][0]) + u2f(st[k - 18][2]); q2 += u2f(st[k - 18][1]) + u2f(st[k - 18][3]); asm volatile("" : "+v"(mean), "+v"(q2)); }
          if (k == 26) { mean = hsum(mean) * (1.0f / 1024.0f); q2 = hsum(q2); asm volatile("" : "+v"(mean), "+v"(q2)); }
          if (k == 28) { q2 = rbf(rbf(fmaxf(fmaf(-mean, mean, q2 * (1.0f / 1024.0f)), 0.f)) + 1e-5f); asm volatile("" : "+v"(q2)); }
          if (k == 29) { q2 = __builtin_amdgcn_rcpf(rbf(__builtin_amdgcn_sqrtf(q2))); mean = rbf(mean); asm volatile("" : "+v"(q2), "+v"(mean)); }
        }
      }, []() {}, [&]() {
        SEG(4);
        WAIT_V(0);
        SEG(0);
        asm volatile("" : "+v"(peek));
        if (w == 0) {
          const unsigned long long ok = __ballot(peek >= (lane < 32 ? need(i + 1) : need(i + 2)));
          const bool r1 = (R >= i + 2 || (unsigned)ok == 0xFFFFFFFFu) && i + 1 < NBLK, r2 = r1 && (unsigned)(ok >> 32) == 0xFFFFFFFFu && i + 2 < NBLK;
          if (lane == 0) *(volatile LAS unsigned*)(lds + G_CTL + 4) = (unsigned)(i + 1 + (r1 ? 1 : 0) + (r2 ? 1 : 0));
        }
        LBAR();
      });
      SEG(2);
      const unsigned rv = *(const volatile LAS unsigned*)(lds + G_CTL + 4);
      if (SG == SG_W3) {
        const float m = mean;
        *(LAS float*)(lds + G_K + 1024 + wl * 256 + lane * 4) = q2;
#pragma unroll
        for (int q = 0; q < 4; ++q) {
          const f32x4 cs = *(const LAS f32x4*)(lds + G_K + wl * 128 + hf * 64 + q * 16);
#pragma unroll
          for (int e = 0; e < 4; ++e) acc[4 * q + e] = fmaf(-m, cs[e], acc[4 * q + e]);
        }
      }
      put16(lad(l16, lbase + G_XCH + wl * 4096), acc);
      asm volatile("" ::: "memory");
      if (lane == 0) *(volatile LAS unsigned*)(lds + G_CTL + 16 + wl * 4) = (unsigned)i + 1u;
      Rp = R; R = __builtin_amdgcn_readfirstlane(rv);
      SEG(3);
      if (R >= i + 2 && Rp < i + 2) { fetch(2 * i + 2); fetch(2 * i + 3); }
      if (R >= i + 3) fetch(2 * i + 4);
      if (PROJ && i < 16) {
        if (side3) dma16(mk_rs(side3, 0x7FFFFFFFu), (unsigned)((lane >> 2) * 6144 + (lane & 3) * 8) * 2u, (unsigned)((64 * i + 16 * wl) * 6144) * 2u, lbase + G_K);
        if (64 * i < srows) {
          const __amdgpu_buffer_rsrc_t rsN = mk_rs(side, 0x7FFFFFFFu);
#pragma unroll
          for (int q = 0; q < 4; ++q) dma16(rsN, (unsigned)((lane >> 4) * sld + (lane & 15) * 8) * 2u, (unsigned)((64 * i + 16 * wl + 4 * q) * sld) * 2u, lbase + G_K + wl * 2048);
        }
      }
      SEG(5);
    }
    BAR();
  } else {
    bool pending = false;
    BAR();
    for (int i = 0; i < NBLK; ++i) {
      const int b = blk(i);
      BAR();
      SEG(0);
      const unsigned rv = *(const volatile LAS unsigned*)(lds + G_CTL + 4);
      u32x4 res[2];
      float isd;
      if (SG == SG_W2) {
#pragma unroll
        for (int u = 0; u < 2; ++u) {
          const int t = 32 * b - 2 + 16 * u + (lane >> 2);
          dma16x(rs, (t >= 0 && t < S_) ? (unsigned)(t * D_ + col0 + ((lane ^ (lane >> 3)) & 3) * 8) * 2u : 0xF0000000u, WS_H2, lbase + G_K + wl * 2048 + u * 1024);
        }
      }
      f32x16 acc;
      int R = 0;
      LAS unsigned char* xr = lad(l16, lbase + G_XCH + wl * 4096);
      f32x4 xa[4];
      unsigned xf;
      auto take = [&]() {
        asm volatile("" ::: "memory");
        xf = *(const volatile LAS unsigned*)(lds + G_CTL + 16 + wl * 4);
        asm volatile("" ::: "memory");
#pragma unroll
        for (int q = 0; q < 4; ++q) xa[q] = *(const LAS f32x4*)(xr + q * 1024);
        if (SG == SG_W3) isd = *(const LAS float*)(lds + G_K + 1024 + wl * 256 + lane * 4);
        asm volatile("" ::: "memory");
      };
      products(i, acc, [&](int k) {
      }, [&]() {
        take();
      }, [&]() {
        SEG(5);
        R = __builtin_amdgcn_readfirstlane(rv);
        if (R >= i + 2) {
          WAIT_V(0);
          LBAR();
          if (pending && tid == 256) stx4(rs, 0, myword, (unsigned)blk(i - 1) + 1u);
        }
      });
      const bool more = R >= i + 2;
      SEG(1);
      while (__builtin_amdgcn_readfirstlane(xf) <= (unsigned)i) take();
#pragma unroll
      for (int q = 0; q < 16; ++q) { acc[q] += xa[q >> 2][q & 3]; if (SG == SG_W3) acc[q] *= isd; }
      SEG(2);
      const unsigned fo = (unsigned)((col0 >> 5) * 2048 + hf * 256), ro = (unsigned)((row >> 4) * 1024 + (row & 15) * 16);
      u32x4 v[2];
      if (SG == SG_W1 || SG == SG_W3) {
#pragma unroll
        for (int q = 0; q < 16; q += 2) {
          float g0 = gelu_tanh(acc[q]), g1 = gelu_tanh(acc[q + 1]);
          asm volatile("" : "+v"(g0), "+v"(g1));
          acc[q] = g0; acc[q + 1] = g1;
          piece(2 * i + 5, q >> 1, R >= i + 3 && i >= 2);
        }
      }
      widen32(acc, v);
      if (SG == SG_WO || SG == SG_W4) {
        LAS unsigned char* tw = lds + G_XCH + wl * 4096 + row * 64;
        *(LAS u32x4*)(tw + ((hf ^ (row >> 1)) & 3) * 16) = v[0]; *(LAS u32x4*)(tw + (((2 + hf) ^ (row >> 1)) & 3) * 16) = v[1];
        asm volatile("" ::: "memory");
        LAS unsigned char* tr = lds + G_XCH + wl * 4096 + (lane >> 2) * 64 + ((lane ^ (lane >> 3)) & 3) * 16;
        const unsigned so = (SG == SG_WO ? WS_P2 : WS_P8) + (unsigned)(b * 4 + kq) * 65536u, vo = (unsigned)((lane >> 2) * D_ + col0 + (lane & 3) * 8) * 2u;
        stx16(rs, vo, so, *(const LAS u32x4*)(tr)); stx16(rs, vo, so + 32768, *(const LAS u32x4*)(tr + 1024));
      } else if (PROJ) {
        const unsigned so = dst + (unsigned)(b * NH + cb) * 8192 + wl * 1024, vo = (unsigned)((row >> 4) * 4096 + hf * 256 + (row & 15) * 16);
        stx16(rs, vo, so, v[0]); stx16(rs, vo, so + 512, v[1]);
      } else if (SG == SG_W1) {
        const int grp = cb >> 1, sh = (grp & 1) ? -(1 + (grp >> 1)) : (1 + (grp >> 1));
        const int rho = 32 * b + row + sh + 2;
        const unsigned vo = (rho >= 2 && rho < S_ + 2) ? (unsigned)(rho >> 5) * 65536u + fo + (unsigned)((rho >> 4) & 1) * 1024u + (unsigned)(rho & 15) * 16u : 0xF0000000u;
        stx16(rs, vo, WS_H1S, v[0]); stx16(rs, vo, WS_H1S + 512, v[1]);
      } else if (SG == SG_W2) {
        if (!more) WAIT_V(0);
        res[0] = *(const LAS u32x4*)(lds + G_K + wl * 2048 + row * 64 + ((hf ^ (row >> 1)) & 3) * 16);
        res[1] = *(const LAS u32x4*)(lds + G_K + wl * 2048 + row * 64 + (((2 + hf) ^ (row >> 1)) & 3) * 16);
        float zs = 0.f, q2 = 0.f;
#pragma unroll
        for (int q = 0; q < 8; ++q) {
          const unsigned x = pack2(lo2f(res[q >> 2][q & 3]) + lo2f(v[q >> 2][q & 3]), hi2f(res[q >> 2][q & 3]) + hi2f(v[q >> 2][q & 3]));
          v[q >> 2][q & 3] = x; zs += lo2f(x) + hi2f(x); q2 = fmaf(lo2f(x), lo2f(x), fmaf(hi2f(x), hi2f(x), q2));
          asm volatile("" : "+v"(zs), "+v"(q2));
          piece(2 * i + 5, q, R >= i + 3 && i >= 2);
        }
        stx16(rs, fo + ro, WS_X2 + (unsigned)b * 65536u, v[0]); stx16(rs, fo + ro, WS_X2 + (unsigned)b * 65536u + 512, v[1]);
        const float mw = hsum(zs);
        q2 = hsum(q2);
        if (hf == 0) stx8(rs, (unsigned)((cb * 2 + (wl >> 1)) * 512 + row * 16 + (wl & 1) * 8), WS_ST5 + (unsigned)b * 8192, u32x2{f2u(mw), f2u(q2)});
      } else {
        stx16(rs, fo + ro, WS_U + (unsigned)b * 229376u, v[0]); stx16(rs, fo + ro, WS_U + (unsigned)b * 229376u + 512, v[1]);
      }
      if (PROJ && bstr == 3) {
#pragma unroll
        for (int q = 0; q < 2; ++q) {
          const int pc = (wk * 4 + wl) * 2 + q;
          if (pc < 660 && 3 * i + ZA + pc / 220 <= NSEG) stx16(rs, l16, X_REC + (unsigned)(3 * i + ZA) * RSTR + (unsigned)pc * 1024, u32x4{0u, 0u, 0u, 0u});
        }
      }
      SEG(3);
      pending = R >= i + 3 && i >= 2;
      if (!pending) { WAIT_V(0); announce(i); }
      if (R >= i + 3 && !(WEAVE && i >= 2)) fetch(2 * i + 5);
      if (!PROJ) WST(rs, wk, 16 + b);
      SEG(4);
      if (!more) BAR();
    }
  }
}

__device__ __forceinline__ void wait32(__amdgpu_buffer_rsrc_t rs, int first, int mine, int lane, unsigned need, unsigned& cap) {
  wait_ge(rs, 0, F_ST + (unsigned)(first + mine) * 128u, need, cap);
  wait_ge(rs, (unsigned)(lane & 31) * 128u, F_ST + (unsigned)first * 128u, need, cap);
}
__device__ __forceinline__ void sum_partials(__amdgpu_buffer_rsrc_t rs, unsigned l16, unsigned so, float (&a)[16]) {
  u32x4 pp[4][2];
#pragma unroll
  for (int k = 0; k < 4; ++k) { pp[k][0] = ldx16(rs, l16, so + k * 65536u); pp[k][1] = ldx16(rs, l16, so + k * 65536u + 1024); }
#pragma unroll
  for (int i = 0; i < 8; ++i) {
    f32x2 t = {0.f, 0.f};
#pragma unroll
    for (int k = 0; k < 4; ++k) t += f32x2{lo2f(pp[k][i >> 2][i & 3]), hi2f(pp[k][i >> 2][i & 3])};
    a[2 * i] = rbf(t[0]); a[2 * i + 1] = rbf(t[1]);
  }
}
__device__ __forceinline__ void put_row(__amdgpu_buffer_rsrc_t rs, unsigned l16, unsigned so, const float (&v)[16]) {
  stx16(rs, l16, so, u32x4{pack2(v[0], v[1]), pack2(v[2], v[3]), pack2(v[4], v[5]), pack2(v[6], v[7])});
  stx16(rs, l16, so + 1024, u32x4{pack2(v[8], v[9]), pack2(v[10], v[11]), pack2(v[12], v[13]), pack2(v[14], v[15])});
}
__device__ __forceinline__ void load_mod(__amdgpu_buffer_rsrc_t rs, int part, int lane, float (&v)[16], unsigned& cap) {
  for (;;) {
    asm volatile("" ::: "memory");
    u32x4 t[4];
#pragma unroll
    for (int i = 0; i < 4; ++i) t[i] = ldx16(rs, (unsigned)lane * 32, X_MOD + part * 4096 + (i >> 1) * 2048 + (i & 1) * 16);
    unsigned mn = 0xFFFFFFFFu;
#pragma unroll
    for (int i = 0; i < 16; ++i) { mn = min(mn, t[i >> 2][i & 3]); v[i] = hi2f(t[i >> 2][i & 3]); }
    if (__all(mn != 0u) || cap == 0u) break;
    cap = __builtin_amdgcn_readfirstlane(cap - 1u);
    __builtin_amdgcn_s_sleep(4);
  }
  cap = __builtin_amdgcn_readfirstlane(cap);
}
struct ModLoad { u32x4 w[8]; u16 c[8]; };
__device__ __forceinline__ void mod_load(const u16* wmod, const u16* cv, int grp, ModLoad& m) {
  const int kg = threadIdx.x >> 2, c8 = threadIdx.x & 3;
#pragma unroll
  for (int i = 0; i < 8; ++i) { m.w[i] = *(const u32x4*)(wmod + (size_t)(kg + 128 * i) * 6144 + grp * 32 + c8 * 8); m.c[i] = cv[kg + 128 * i]; }
}
__device__ __forceinline__ void mod_finish(__amdgpu_buffer_rsrc_t rs, LAS float* red, int grp, const ModLoad& m) {
  const int tid = threadIdx.x, kg = tid >> 2, c8 = tid & 3;
  float acc[8] = {0.f, 0.f, 0.f, 0.f, 0.f, 0.f, 0.f, 0.f};
#pragma unroll
  for (int i = 0; i < 8; ++i) {
    const float v = bf2f(m.c[i]), sc = rbf(v / (1.0f + __expf(-v)));
#pragma unroll
    for (int j = 0; j < 4; ++j) { acc[2 * j] += sc * lo2f(m.w[i][j]); acc[2 * j + 1] += sc * hi2f(m.w[i][j]); }
  }
#pragma unroll
  for (int j = 0; j < 8; ++j) red[kg * 33 + c8 * 8 + j] = acc[j];
  LBAR();
  float t = 0.f;
#pragma unroll
  for (int i = 0; i < 8; ++i) t += red[((tid >> 5) * 8 + i) * 33 + (tid & 31)];
  t = hsum(t);
  LBAR();
  if ((tid & 63) < 32) red[(tid >> 6) * 33 + (tid & 31)] = t;
  LBAR();
  if (tid < 32) {
    float u = 0.f;
#pragma unroll
    for (int i = 0; i < 8; ++i) u += red[i * 33 + tid];
    stx4(rs, (unsigned)tid * 4, X_MOD + (unsigned)grp * 128, f2u(rbf(u)) | 1u);
  }
}

__device__ __forceinline__ void stage_mid(__amdgpu_buffer_rsrc_t rs, const u16* x, int wk, int cu, unsigned& cap) {
  const int tid = threadIdx.x, lane = tid & 63, w = __builtin_amdgcn_readfirstlane(tid >> 6);
  const int id = w * 12 + cu, grp = id >> 5, rl = id & 31;
  const unsigned l16 = lane * 16;
  float ga[16], sc[16], sh[16];
  load_mod(rs, 2, lane, ga, cap); load_mod(rs, 4, lane, sc, cap); load_mod(rs, 3, lane, sh, cap);
#pragma unroll
  for (int i = 0; i < 16; ++i) sc[i] = rbf(1.0f + sc[i]);
  for (int seg = grp; seg < NSEG; seg += 3) {
    const int row = seg * 32 + rl;
    float v[16], a[16];
    load_row16(x + (size_t)row * D_, lane, v);
    wait32(rs, P_S2, rl, lane, (unsigned)seg + 1u, cap);
    sum_partials(rs, l16, WS_P2 + (unsigned)seg * 262144u + (unsigned)rl * 2048, a);
#pragma unroll
    for (int i = 0; i < 16; ++i) v[i] = rbf(v[i] + rbf(ga[i] * a[i]));
    put_row(rs, l16, WS_X1 + (unsigned)row * 2048, v);
    ln_row(v);
#pragma unroll
    for (int i = 0; i < 16; ++i) v[i] = rbf(rbf(v[i] * sc[i]) + sh[i]);
    put_row(rs, l16, WS_H2 + (unsigned)row * 2048, v);
    ln_row(v);
    put_row(rs, l16, WS_G + (unsigned)row * 2048, v);
    WAIT_V(0);
    if (lane == 0) stx4(rs, 0, F_ST + (unsigned)(P_S3 + grp * 32 + rl) * 128, (unsigned)seg + 1u);
    WST(rs, wk, 16 + seg);
  }
}

__device__ __forceinline__ void stage_out(__amdgpu_buffer_rsrc_t rs, u16* out, int wk, int cu, unsigned& cap) {
  const int tid = threadIdx.x, lane = tid & 63, w = __builtin_amdgcn_readfirstlane(tid >> 6);
  const int grp = w >> 2, rl = cu * 4 + (w & 3);
  const unsigned l16 = lane * 16;
  float gb[16];
  load_mod(rs, 5, lane, gb, cap);
  for (int b = grp; b < 65; b += 2) {
    const int t = 32 * b - 2 + rl;
    if (t < 0 || t >= S_) continue;
    wait_ge(rs, (unsigned)(lane & 7) * 128u, F_ST + P_S5 * 128u, (unsigned)b + 1u, cap);
    const unsigned xo = (unsigned)((lane >> 2) * 2048 + (lane & 3) * 256), xs = WS_X2 + (unsigned)b * 65536u + (unsigned)((rl >> 4) * 1024 + (rl & 15) * 16);
    const u32x4 x2a = ldx16(rs, xo, xs), x2b = ldx16(rs, xo, xs + 32768);
    const u32x4 x1a = ldx16(rs, l16, WS_X1 + (unsigned)t * 2048), x1b = ldx16(rs, l16, WS_X1 + (unsigned)t * 2048 + 1024);
    wait32(rs, P_S8, rl, lane, (unsigned)b + 1u, cap);
    float a[16];
    sum_partials(rs, l16, WS_P8 + (unsigned)b * 262144u + (unsigned)rl * 2048, a);
#pragma unroll
    for (int i = 0; i < 4; ++i) {
      a[2 * i] = lo2f(x1a[i]) + rbf(gb[2 * i] * rbf(lo2f(x2a[i]) + a[2 * i])); a[2 * i + 1] = hi2f(x1a[i]) + rbf(gb[2 * i + 1] * rbf(hi2f(x2a[i]) + a[2 * i + 1]));
      a[8 + 2 * i] = lo2f(x1b[i]) + rbf(gb[8 + 2 * i] * rbf(lo2f(x2b[i]) + a[8 + 2 * i])); a[9 + 2 * i] = hi2f(x1b[i]) + rbf(gb[9 + 2 * i] * rbf(hi2f(x2b[i]) + a[9 + 2 * i]));
    }
    store_row16(out + (size_t)t * D_, lane, a);
    WST(rs, wk, 16 + b);
  }
}

__device__ __forceinline__ void worker(const MainP& p, LAS unsigned char* lds, int xw, int j) {
  const int tid = threadIdx.x, lane = tid & 63, w = __builtin_amdgcn_readfirstlane(tid >> 6);
  const __amdgpu_buffer_rsrc_t rs = mk_rs(p.ws, WS_END);
  unsigned cap = SPIN_CAP;
  const int wk = xw * 32 + j;
  WST(rs, wk, 0);
  {
    const int c = j * 5 + xw, r0 = c * 8 + 2 * (w & 3);
    const bool two = r0 + 8 * NLN < S_;
    float v[4][16];
    ModLoad m;
    if (c >= NLN - 64) mod_load(p.wmod, p.c, c - (NLN - 64), m);
    if (w < 4) {
#pragma unroll
      for (int q = 0; q < 4; ++q) load_row16(p.x + (size_t)(r0 + (q & 1) + (two ? (q >> 1) * 8 * NLN : 0)) * D_, lane, v[q]);
    }
    if (c >= NLN - 64) mod_finish(rs, (LAS float*)(lds + G_XCH), c - (NLN - 64), m);
    WST(rs, wk, 5);
    if (w >= 4) {
      if (c < 128) {
        const __amdgpu_buffer_rsrc_t rsO = mk_rs(p.Wo, D_ * HD * 2);
#pragma unroll
        for (int q = 0; q < 16; ++q) dma16(rsO, (unsigned)lane * 16, (unsigned)((q * 128 + c) * 4 + (w & 3)) * 1024u, (unsigned)(size_t)lds + G_K + (w & 3) * 1024);
      }
      const __amdgpu_buffer_rsrc_t rsW = mk_rs(xw < 3 ? p.Wk : p.Wq, D_ * HD * 2);
      const int ub = lane >> 3;
      const unsigned vB = (unsigned)(((ub & 3) + 8 * (ub >> 2)) * HD + 16 * (((lane >> 1) & 3) ^ (ub >> 1)) + 8 * (lane & 1)) * 2u, lbase = (unsigned)(size_t)lds;
#pragma unroll
      for (int q = 0; q < 16; ++q) {
        const int wq = (w & 3) + 4 * (q >> 3);
        const unsigned so = (unsigned)((32 * (wq >> 2) + 16 * ((wq >> 1) & 1) + 4 * (wq & 1) + (((q >> 2) & 1) * 8 + (q & 3)) * 64) * HD + j * 128) * 2u;
        const unsigned d = lbase + ((q >> 2) & 1) * 65536 + (q & 3) * 16384 + wq * 1024;
        dma16(rsW, vB, so, d); dma16(rsW, vB, so + 128, d + 8192);
      }
    } else {
#pragma unroll
      for (int q = 0; q < 4; ++q) ln_row(v[q]);
      float sc[16], sh[16];
      load_mod(rs, 1, lane, sc, cap); load_mod(rs, 0, lane, sh, cap);
      WST(rs, wk, 6);
#pragma unroll
      for (int i = 0; i < 16; ++i) sc[i] = rbf(1.0f + sc[i]);
#pragma unroll
      for (int q = 0; q < 4; ++q) {
#pragma unroll
        for (int i = 0; i < 16; ++i) v[q][i] = rbf(rbf(v[q][i] * sc[i]) + sh[i]);
        if (q < 2 || two) put_row(rs, (unsigned)lane * 16, WS_H + (unsigned)(r0 + (q & 1) + (q >> 1) * 8 * NLN) * 2048, v[q]);
      }
      WAIT_V(0);
    }
    BAR();
    if (tid == 0) stx4(rs, 0, F_LN + c * 128, 1u);
    WST(rs, wk, 1);
  }
  {
    const u16* nw = xw == 0 ? p.Wo + (size_t)(j >> 3) * 1024 * D_ + (j & 7) * 128 : xw == 3 ? p.w4 + (size_t)(j >> 3) * 896 * D_ + (j & 7) * 128
                  : xw == 2 ? p.w3 + j * 128 : (j < 8 ? p.w1 : p.w2) + (j & 7) * 128;
    const int nrows = (xw == 4 || (xw == 1 && j >= 16) || (xw == 2 && j >= 28)) ? 0 : xw == 3 ? 896 : 1024;
    const int mg = xw < 3 ? 64 + wk : 160 + j;
    stage_gemm<SG_PJ>(lds, rs, xw < 3 ? p.Wk : p.Wq, wk, xw < 3 ? xw : xw == 3 ? QB0 : 0, j, P_PJ + min(xw, 3) * 32 + j + (xw == 4 ? 400 : 0), cap, xw < 3 ? WS_K : WS_Q, xw < 3 ? 3 : 1,
                      xw == 0 ? 22 : xw < 3 ? 21 : xw == 3 ? NSEG - QB0 : QB0, nw, nrows, xw == 2 ? FF : D_, xw == 3 ? nullptr : p.wmod + mg * 32);
    WST(rs, wk, 3);
  }
  typedef __attribute__((address_space(4))) const MainP* KArg;
  KArg kp = (KArg)__builtin_amdgcn_kernarg_segment_ptr();
  asm volatile("" : "+s"(kp));
  if (xw != 3) { const int mg = xw < 3 ? 64 + wk : 160 + j; ModLoad m; mod_load(kp->wmod, kp->c, mg, m); mod_finish(rs, (LAS float*)(lds + 65536), mg, m); }
  if (xw == 0) stage_gemm<SG_WO>(lds, rs, kp->Wo, wk, j >> 3, j & 7, P_S2 + j, cap);
  else if (xw == 3) stage_gemm<SG_W4>(lds, rs, kp->w4, wk, j >> 3, j & 7, P_S8 + j, cap);
  else if (xw == 4) {
    unsigned chs = 0u;
    for (int sg = w >> 1; sg < NSEG; sg += 4) { attend(rs, sg, j, w & 1, lane, chs, cap); WST(rs, wk, 16 + sg); }
  }
  else if (j >= 28) stage_out(rs, kp->out, wk, (xw - 1) * 4 + j - 28, cap);
  else if (xw == 2) stage_gemm<SG_W3>(lds, rs, kp->w3, wk, 0, j, P_S7 + j, cap);
  else if (j < 8) stage_gemm<SG_W1>(lds, rs, kp->w1, wk, 0, j, P_S4 + j, cap);
  else if (j < 16) stage_gemm<SG_W2>(lds, rs, kp->w2, wk, 0, j - 8, P_S5 + j - 8, cap);
  else stage_mid(rs, kp->x, wk, j - 16, cap);
  if (cap == 0u && lane == 0) stx4(rs, 0, X_CTL, 1u);
}

#define ST_DECL
#define ST0()
#define ST(i)
#define ST_FLUSH()
constexpr int R_W = 0, R_MEM = 131072, R_QK = R_MEM + 16384, R_KT = R_QK + 8192, R_RED = R_MEM + 16384, R_LDS = 163840;
__device__ __forceinline__ void bar_lo(int w) { asm volatile("s_cmp_ge_u32 %0, 4\n\ts_cbranch_scc1 1f\n\ts_barrier\n\t1:" :: "s"(w) : "scc", "memory"); }
__device__ __forceinline__ void bar_hi(int w) { asm volatile("s_cmp_lt_u32 %0, 4\n\ts_cbranch_scc1 1f\n\ts_setprio 3\n\ts_barrier\n\t1:" :: "s"(w) : "scc", "memory"); }
#define ISSUE(R) asm volatile("buffer_load_dwordx4 v[" R "], %[vo], %[rs], %[so] offen sc1" : "=&{v[" R "]}"(a) : [vo] "v"(vo), [rs] "s"(rs), [so] "s"(so) : "memory")
template <int I> __device__ __forceinline__ void issue(__amdgpu_buffer_rsrc_t rs, unsigned vo, unsigned so, u32x4& a) {
  if (I == 0) ISSUE("240:243"); else if (I == 1) ISSUE("244:247"); else if (I == 2) ISSUE("248:251"); else ISSUE("252:255");
}
#define ARRIVE(R0, R1, R2, R3, CNT) \
  asm volatile("s_waitcnt vmcnt(" #CNT ")\n\t" \
               "1:\n\t" \
               "v_min3_u32 %[t], v" #R0 ", v" #R1 ", v" #R2 "\n\t" \
               "v_min_u32 %[t], %[t], v" #R3 "\n\t" \
               "v_cmp_eq_u32 vcc, 0, %[t]\n\t" \
               "s_cbranch_vccz 2f\n\t" \
               "s_mov_b64 exec, vcc\n\t" \
               "buffer_load_dwordx4 v[" #R0 ":" #R3 "], %[vo], %[rs], %[so] offen sc1\n\t" \
               "s_mov_b64 exec, -1\n\t" \
               "s_waitcnt vmcnt(0)\n\t" \
               "s_sub_u32 %[cap], %[cap], 1\n\t" \
               "s_cbranch_scc0 1b\n\t" \
               "s_mov_b32 %[cap], 0\n\t" \
               "2:" \
               : "+{v[" #R0 ":" #R3 "]}"(a), [t] "=&v"(t), [cap] "+s"(cap) : [vo] "v"(vo), [rs] "s"(rs), [so] "s"(so) : "vcc", "scc", "memory")
template <int I> __device__ __forceinline__ void arrive(__amdgpu_buffer_rsrc_t rs, unsigned vo, unsigned so, u32x4& a, unsigned& cap) {
  unsigned t;
  if (I == 0) ARRIVE(240, 241, 242, 243, 3);
  else if (I == 1) ARRIVE(244, 245, 246, 247, 2);
  else if (I == 2) ARRIVE(248, 249, 250, 251, 1);
  else ARRIVE(252, 253, 254, 255, 0);
}
__device__ __forceinline__ unsigned half_of(unsigned v) { unsigned r; asm volatile("v_lshrrev_b32 %0, 1, %1" : "=v"(r) : "v"(v)); return r; }
__device__ __forceinline__ void issue2(__amdgpu_buffer_rsrc_t rs, unsigned l8, unsigned so, int w, u32x4& a) {
  asm volatile("s_cmp_ge_u32 %[w], 4\n\ts_cbranch_scc1 1f\n\tv_lshrrev_b32 v255, 1, %[l8]\n\t"
               "buffer_load_dwordx2 v[252:253], v255, %[rs], %[so] offen sc1\n\tbuffer_load_dwordx2 v[254:255], v255, %[rs], %[so] offen offset:512 sc1\n\t1:"
               : "=&{v[252:255]}"(a) : [l8] "v"(l8), [rs] "s"(rs), [so] "s"(so), [w] "s"(w) : "scc", "memory");
}
__device__ __forceinline__ void arrive2(__amdgpu_buffer_rsrc_t rs, unsigned l8, unsigned so, u32x4& a, unsigned& cap) {
  unsigned t;
  asm volatile("s_waitcnt vmcnt(0)\n\t"
               "1:\n\t"
               "v_min3_u32 %[t], v252, v253, v254\n\t"
               "v_min_u32 %[t], %[t], v255\n\t"
               "v_cmp_eq_u32 vcc, 0, %[t]\n\t"
               "s_cbranch_vccz 2f\n\t"
               "v_lshrrev_b32 %[t], 1, %[l8]\n\t"
               "buffer_load_dwordx2 v[252:253], %[t], %[rs], %[so] offen sc1\n\t"
               "buffer_load_dwordx2 v[254:255], %[t], %[rs], %[so] offen offset:512 sc1\n\t"
               "s_waitcnt vmcnt(0)\n\t"
               "s_sub_u32 %[cap], %[cap], 1\n\t"
               "s_cbranch_scc0 1b\n\t"
               "s_mov_b32 %[cap], 0\n\t"
               "2:"
               : "+{v[252:255]}"(a), [t] "=&v"(t), [cap] "+s"(cap) : [l8] "v"(l8), [rs] "s"(rs), [so] "s"(so) : "vcc", "scc", "memory");
}
__device__ __forceinline__ unsigned ror8(unsigned v) { return __builtin_amdgcn_update_dpp(0u, v, 0x128, 0xF, 0xF, true); }
template <class F>
__device__ __forceinline__ void gather_mem(__amdgpu_buffer_rsrc_t rs, unsigned lbase, int s, int w, unsigned l16, unsigned& cap, F&& meanwhile) {
  const unsigned so = X_MEM + s * RSTR + w * 2048;
  u32x4 a0, a1;
  issue<2>(rs, l16, so, a0); issue<3>(rs, l16, so + 1024, a1);
  meanwhile();
  LAS unsigned char* d = lad(l16, lbase + R_MEM + w * 2048);
  arrive<2>(rs, l16, so, a0, cap);
  *(LAS u32x4*)(d) = a0;
  arrive<3>(rs, l16, so + 1024, a1, cap);
  *(LAS u32x4*)(d + 1024) = a1;
}
__device__ __forceinline__ unsigned mem_off(int lane) { return (unsigned)((lane & 1) * 512 + (((lane >> 1) ^ ((lane & 1) * 16)) * 8)); }
__device__ __forceinline__ bf16x8 mem_frag(LAS unsigned char* mp, int chunk) { return cat8(trread(mp + chunk * 1024), trread(mp + chunk * 1024 + 256)); }
__device__ __forceinline__ bf16x16 w_frag(LAS unsigned char* p) {
  return cat16(cat8(trread(p), trread(p + 2048)), cat8(trread(p + 4096), trread(p + 6144)));
}
__device__ __forceinline__ bf16x16 l_frag(LAS unsigned char* p, int i) { return cat16(*(const LAS bf16x8*)(p + i * 2048), *(const LAS bf16x8*)(p + i * 2048 + 1024)); }
__device__ __forceinline__ void make_resident(LAS unsigned char* wb, unsigned lo, unsigned l16) {
#pragma unroll
  for (int c = 0; c < 2; ++c) {
    bf16x16 f[4];
#pragma unroll
    for (int t = 0; t < 4; ++t) f[t] = w_frag(wb + c * 8192 + (lo ^ (t << 5)));
    WAIT_L0(); SCHED();
#pragma unroll
    for (int t = 0; t < 4; ++t) {
      *(LAS bf16x8*)(wb + (c * 4 + t) * 2048 + l16) = __builtin_shufflevector(f[t], f[t], 0, 1, 2, 3, 4, 5, 6, 7);
      *(LAS bf16x8*)(wb + (c * 4 + t) * 2048 + 1024 + l16) = __builtin_shufflevector(f[t], f[t], 8, 9, 10, 11, 12, 13, 14, 15);
    }
    WAIT_L0(); SCHED();
  }
}
__device__ __forceinline__ f32x4 fold(f32x4 a, f32x4 b) {
  f32x4 r;
#pragma unroll
  for (int i = 0; i < 4; ++i) {
    const auto t = __builtin_amdgcn_permlane32_swap(f2u(a[i]), f2u(b[i]), false, false);
    r[i] = u2f(t[0]) + u2f(t[1]);
  }
  return r;
}

struct Sched { unsigned tau, prev; int ea, eb, d1; bool moved; };
template <int CTRL> __device__ __forceinline__ int dpp_max(int v) { return max(v, (int)__builtin_amdgcn_update_dpp((unsigned)v, (unsigned)v, CTRL, 0xF, 0xF, false)); }
__device__ __forceinline__ int wave_max(int v) {
  v = dpp_max<0xB1>(v); v = dpp_max<0x4E>(v); v = dpp_max<0x141>(v); v = dpp_max<0x140>(v);
  return max(max(__builtin_amdgcn_readlane(v, 0), __builtin_amdgcn_readlane(v, 16)), max(__builtin_amdgcn_readlane(v, 32), __builtin_amdgcn_readlane(v, 48)));
}
__device__ __forceinline__ void times_issue(__amdgpu_buffer_rsrc_t rs, unsigned l16, int s, unsigned& xb, unsigned& xa) {
  asm volatile("v_lshlrev_b32 %0, 3, %2\n\tv_and_b32 %1, 0xF80, %0\n\tbuffer_load_dword %0, %0, %3, %4 offen sc1\n\tbuffer_load_dword %1, %1, %3, %5 offen sc1"
               : "=&v"(xb), "=&v"(xa) : "v"(l16), "s"(rs), "s"(X_TS + (unsigned)s * RSTR), "s"(X_TS + (unsigned)s * RSTR + 8192) : "memory");
}
__device__ __forceinline__ void retime(__amdgpu_buffer_rsrc_t rs, unsigned l16, int s, unsigned xb, unsigned xa, Sched& t, const MainP& p, unsigned& cap) {
  if (s > 0) {
    while (!__all(min(xa, xb) != 0u) && cap != 0u) {
      times_issue(rs, l16, s, xb, xa);
      asm volatile("s_waitcnt vmcnt(0)" : "+v"(xb), "+v"(xa) :: "memory");
      cap = __builtin_amdgcn_readfirstlane(cap - 1u);
    }
    cap = __builtin_amdgcn_readfirstlane(cap);
    const int tb = wave_max((int)(xb - t.prev)), ta = wave_max((int)(xa - t.prev));
    const int late = tb + p.d3 - (int)(t.tau - t.prev);
    if (s > 2) {
      const int up = s < 5 ? 80 : 8, dn = s < 10 ? 40 : 8;
      if (!t.moved) t.ea = max(t.ea - dn, min(ta * 8, t.ea + up));
      if (ta + p.d3 <= t.d1) t.eb = max(t.eb - dn, min((tb - t.d1) * 8, t.eb + up));
    }
    t.moved = late > 0;
    if (late > 0) t.tau += late + min(p.d4, 8 * late);
  }
  t.d1 = (t.ea >> 3) + p.d1;
}
__device__ __forceinline__ void advance(Sched& t, const MainP& p) { t.prev = t.tau; t.tau += t.d1 + (t.eb >> 3) + p.d2; }

__device__ __forceinline__ void role_a(const MainP& p, LAS unsigned char* lds, int h) {
  const int tid = threadIdx.x, lane = tid & 63, w = __builtin_amdgcn_readfirstlane(tid >> 6);
  const int cg = w & 3, kh = w >> 2;
  const __amdgpu_buffer_rsrc_t rs = mk_rs(p.ws, WS_END);
  const unsigned lbase = (unsigned)(size_t)lds;
  unsigned l16 = lane * 16;
  asm volatile("" : "+v"(l16));
  const int sidx = (lane & 8) ? 0xEEEE : 0x4444;
  bf16x16 W[24];
  unsigned qoff;
  {
    const int fr = lane & 15, g = lane >> 4, tu = 4 * (g & 1) + (fr >> 2), ub = lane >> 3;
    const unsigned lo = (g >> 1) * 1024 + tu * 128 + ((tu >> 1) << 5) + (fr & 3) * 8;
    qoff = R_QK + ((fr >> 1) & 1) * 512 + (g >> 1) * 256 + (fr & 1) * 128 + (8 * (g & 1) + (fr >> 2)) * 8;
    const __amdgpu_buffer_rsrc_t rw = mk_rs(cg < 2 ? p.Wq : p.Wk, D_ * HD * 2);
    const unsigned vo = (unsigned)(((ub & 3) + 8 * (ub >> 2)) * HD + 16 * (((lane >> 1) & 3) ^ (ub >> 1)) + 8 * (lane & 1)) * 2u;
    const unsigned so = (unsigned)(kh * 512 * HD + h * DH + (cg & 1) * 64) * 2u;
    auto stage = [&](int c) {
#pragma unroll
      for (int rho = 0; rho < 8; ++rho) dma16(rw, vo, so + (unsigned)((c * 64 + 32 * (rho >> 2) + 16 * (rho & 1) + 4 * ((rho >> 1) & 1)) * HD) * 2u, lbase + R_W + w * 16384 + (c & 1) * 8192 + rho * 1024);
    };
    stage(0); stage(1);
#pragma unroll
    for (int c = 0; c < 6; ++c) {
      WAIT_V(8);
#pragma unroll
      for (int t = 0; t < 4; ++t) W[4 * c + t] = w_frag(lds + R_W + w * 16384 + (c & 1) * 8192 + (lo ^ (t << 5)));
      WAIT_L0(); SCHED();
      stage(c + 2);
    }
    WAIT_V(0);
    make_resident(lds + R_W + w * 16384, lo, l16);
  }
  WST(rs, 160 + (h >> 1), 4);
  unsigned cap = SPIN_CAP;
  wait_ge(rs, 0, F_ST + (unsigned)(P_PJ + h) * 128, 1u, cap);
  dma16x(rs, l16, WS_K + h * 8192 + w * 1024, lbase + R_KT + w * 1024);
  Sched tt; tt.tau = rt32() - 100000; tt.prev = tt.tau; tt.ea = 195 * 8; tt.eb = 118 * 8; tt.d1 = 0; tt.moved = true;
  const int cu_ = h; (void)cu_;
  ST_DECL
  for (int s = 0; s < NSEG; ++s) {
    LAS unsigned char* lp = lad(l16, lbase + R_W + w * 16384);
    bf16x16 L[2];
    L[0] = l_frag(lp, 0); L[1] = l_frag(lp, 1);
    unsigned lz = l16;
    asm volatile("" : "+v"(lz));
    LAS unsigned char* mp = lad(mem_off((int)(lz >> 4)), lbase + R_MEM + kh * 8192);
    SCHED();
    if (w == 7) wait_until(tt.tau);
    BAR();
    ST0();
    gather_mem(rs, lbase, s, (w + h) & 7, l16, cap, []() {});
    ST(1);
    LBAR();
    ST(2);
    f32x4 c[4];
#pragma unroll
    for (int t = 0; t < 4; ++t) c[t] = f32x4{0.f, 0.f, 0.f, 0.f};
    bf16x8 al, af[2];
    u32x4 vt0, vt1;
    u32x4 vmv;
    const unsigned vso = X_VM + (unsigned)s * RSTR + h * 4096 + cg * 1024;
    al = mem_frag(mp, 6); af[0] = mem_frag(mp, 0); af[1] = mem_frag(mp, 1);
#pragma unroll
    for (int i = 0; i < 8; ++i) {
      c[i & 3] = smf(al, L[i & 1], c[i & 3], sidx);
      SCHED();
      if (i == 3) al = mem_frag(mp, 7);
      if (i < 6) L[i & 1] = l_frag(lp, i + 2);
      if (i == 6) {
        const unsigned vo = WS_VT + (unsigned)(s * NH + h) * 8192 + cg * 2048;
        asm volatile("s_cmp_ge_u32 %5, 4\n\ts_cbranch_scc1 1f\n\t"
                     "buffer_load_dwordx4 %0, %2, %3, %4 offen sc1\n\tbuffer_load_dwordx4 %1, %2, %3, %4 offen offset:1024 sc1\n\t1:"
                     : "=&v"(vt0), "=&v"(vt1) : "v"(l16), "s"(rs), "s"(vo), "s"(w) : "scc", "memory");
      }
      SCHED();
#pragma unroll
      for (int r = 3 * i; r < 3 * i + 3; ++r) {
        c[(r + 1) & 3] = smf(af[(r >> 2) & 1], W[(r & ~3) + ((r + 1) & 3)], c[(r + 1) & 3], sidx);
        SCHED();
        if ((r & 3) == 3 && (r >> 2) + 2 < 6) af[(r >> 2) & 1] = mem_frag(mp, (r >> 2) + 2);
        SCHED();
      }
    }
    {
      LAS unsigned char* qw = lad(half_of(l16), lbase + R_QK + cg * 2048 + kh * 512);
      const f32x4 f0 = fold(c[0], c[1]);
      *(LAS u32x2*)(qw) = u32x2{pack2(f0[0], f0[1]), pack2(f0[2], f0[3])};
      const f32x4 f1 = fold(c[2], c[3]);
      *(LAS u32x2*)(qw + 1024) = u32x2{pack2(f1[0], f1[1]), pack2(f1[2], f1[3])};
    }
    ST(3);
    if (w < 4) {
      bf16x8 k1[4], k2[4];
      LAS unsigned char* kb = lad(l16, lbase + R_KT);
#pragma unroll
      for (int ks = 0; ks < 2; ++ks) { k1[ks] = *(const LAS bf16x8*)(kb + ks * 1024); k2[ks] = *(const LAS bf16x8*)(kb + 4096 + ks * 1024); }
      unsigned ov;
      {
        unsigned ot;
        asm volatile("v_and_b32 %0, 0x30, %2\n\tv_lshrrev_b32 %1, 2, %2\n\tv_and_b32 %1, 0xC0, %1\n\tv_or_b32 %0, %0, %1\n\t"
                     "v_and_b32 %1, 0x40, %2\n\tv_lshl_or_b32 %0, %1, 9, %0\n\tv_and_b32 %1, 0x80, %2\n\tv_lshl_or_b32 %0, %1, 24, %0"
                     : "=&v"(ov), "=&v"(ot) : "v"(l16));
      }
      LBAR();
      ST(4);
      issue2(rs, l16, vso, w, vmv);
      f32x4 t0 = {0.f, 0.f, 0.f, 0.f}, t1 = t0, t2 = t0;
      LAS unsigned char* qb = lad(qoff, lbase);
      bf16x8 qf[4], kf[4];
#pragma unroll
      for (int ks = 0; ks < 2; ++ks) {
        qf[ks] = cat8(trread(qb + ks * 1024), trread(qb + ks * 1024 + 32));
        kf[ks] = cat8(trread(qb + 4096 + ks * 1024), trread(qb + 4096 + ks * 1024 + 32));
      }
      SCHED();
#pragma unroll
      for (int ks = 0; ks < 4; ++ks) {
        t1 = mfma32(k1[ks], qf[ks], t1); t2 = mfma32(k2[ks], qf[ks], t2); t0 = mfma32(kf[ks], qf[ks], t0);
        SCHED();
        if (ks < 2) {
          qf[ks + 2] = cat8(trread(qb + (ks + 2) * 1024), trread(qb + (ks + 2) * 1024 + 32));
          k1[ks + 2] = *(const LAS bf16x8*)(kb + (ks + 2) * 1024); k2[ks + 2] = *(const LAS bf16x8*)(kb + 4096 + (ks + 2) * 1024);
          kf[ks + 2] = cat8(trread(qb + 4096 + (ks + 2) * 1024), trread(qb + 4096 + (ks + 2) * 1024 + 32));
        }
        SCHED();
      }
      ST(5);
      asm volatile("s_waitcnt vmcnt(2)" : "+v"(vt0), "+v"(vt1) :: "memory");
      {
        const float sl = 0.08838834764831845f * 1.4426950408889634f;
        float z[4] = {t1[0], t1[1], t1[2], t1[3]}, y = t0[0], mx;
        {
          float u1 = t0[1], u2 = t0[2], u3 = t0[3];
#define M8(Z, B) "v_add_f32_dpp " Z ", " Z ", " Z " row_ror:8 row_mask:0xf bank_mask:0x3\n\tv_add_f32_dpp " Z ", " B ", " B " row_ror:8 row_mask:0xf bank_mask:0xc\n\t"
          asm volatile("s_nop 7\n\t"
              "v_permlane32_swap_b32 %4, %6\n\tv_permlane32_swap_b32 %5, %7\n\t"
              M8("%0", "%9") "v_add_f32 %4, %4, %6\n\tv_add_f32 %5, %5, %7\n\t" M8("%1", "%10") M8("%2", "%11") M8("%4", "%5") M8("%3", "%12")
              "v_max3_f32 %8, %0, %1, %2\n\tv_max3_f32 %8, %8, %4, %3\n\ts_nop 1\n\t"
              "v_max_f32_dpp %8, %8, %8 row_ror:8 row_mask:0xf bank_mask:0xf\n\tv_mov_b32 %5, %8\n\ts_nop 1\n\t"
              "v_permlane16_swap_b32 %8, %5\n\tv_max_f32 %8, %8, %5\n\tv_mov_b32 %5, %8\n\ts_nop 1\n\t"
              "v_permlane32_swap_b32 %8, %5\n\tv_max_f32 %8, %8, %5"
              : "+v"(z[0]), "+v"(z[1]), "+v"(z[2]), "+v"(z[3]), "+v"(y), "+v"(u1), "+v"(u2), "+v"(u3), "=&v"(mx) : "v"(t2[0]), "v"(t2[1]), "v"(t2[2]), "v"(t2[3]));
#undef M8
        }
        const float nm = -mx * sl;
        y = __builtin_amdgcn_exp2f(fmaf(y, sl, nm));
#pragma unroll
        for (int r = 0; r < 4; ++r) z[r] = __builtin_amdgcn_exp2f(fmaf(z[r], sl, nm));
        const unsigned pm1 = pack2(y, u2f(ror8(f2u(y))));
        const auto pms = __builtin_amdgcn_permlane32_swap(pm1, pm1, false, false);
        const u32x2 pm = {pms[0], pms[1]};
        const unsigned p0 = pack2(z[0], z[1]), p1 = pack2(z[2], z[3]);
        const u32x4 pt = {p0, p1, ror8(p0), ror8(p1)};
        f32x4 o0 = mfma32(__builtin_bit_cast(bf16x8, vt0), __builtin_bit_cast(bf16x8, pt), f32x4{0.f, 0.f, 0.f, 0.f});
        f32x4 o1 = mfma32(__builtin_bit_cast(bf16x8, vt1), __builtin_bit_cast(bf16x8, pt), f32x4{0.f, 0.f, 0.f, 0.f});
        float inv = __builtin_amdgcn_rcpf(gsum(dpp_add<0x128>(((z[0] + z[1]) + (z[2] + z[3])) + y)));
        asm volatile("" : "+v"(inv));
        arrive2(rs, l16, vso, vmv, cap);
        const f32x4 n0 = mfma16n(u32x2{vmv[0], vmv[1]}, pm, f32x4{0.f, 0.f, 0.f, 0.f}), n1 = mfma16n(u32x2{vmv[2], vmv[3]}, pm, f32x4{0.f, 0.f, 0.f, 0.f});
        SCHED();
        o0 *= inv; o1 *= inv;
        {
          const u32x4 v = {nz(pack2(fmaf(n0[0], inv, o0[0]), fmaf(n0[1], inv, o0[1]))), nz(pack2(fmaf(n0[2], inv, o0[2]), fmaf(n0[3], inv, o0[3]))),
                           nz(pack2(fmaf(n1[0], inv, o1[0]), fmaf(n1[1], inv, o1[1]))), nz(pack2(fmaf(n1[2], inv, o1[2]), fmaf(n1[3], inv, o1[3])))};
          stx16(rs, ov, X_OX + s * RSTR + h * 1024 + w * 256, v);
        }
        if (w == 3) { const unsigned t = max(rt32(), 1u); if (l16 == 0) stx4(rs, 0, X_TS + (unsigned)(s + 1) * RSTR + (unsigned)(64 + h) * 128, t); }
        ST(8);
      }
    } else {
      LBAR();
      LAS unsigned char* qb = lad(qoff, lbase + 4096 + (w - 4) * 1024);
      u32x4 kf = __builtin_bit_cast(u32x4, cat8(trread(qb), trread(qb + 32)));
#pragma unroll
      for (int i = 0; i < 4; ++i) kf[i] = pack2(dpp_add<0x128>(lo2f(kf[i])), dpp_add<0x128>(hi2f(kf[i])));
      stx16(rs, l16, WS_KM + (unsigned)(s * NH + h) * 4096 + (w - 4) * 1024, kf);
      WAIT_V(0);
    }
    LBAR();
    if (w == 4 && l16 == 0) stx4(rs, 0, F_CH + h * 128, (unsigned)s + 1u);
    ST(9);
    if (w == 7) {
      unsigned xb, xa;
      times_issue(rs, l16, s, xb, xa);
      asm volatile("s_waitcnt vmcnt(0)" : "+v"(xb), "+v"(xa) :: "memory");
      retime(rs, l16, s, xb, xa, tt, p, cap); advance(tt, p);
    }
    if (s + 1 < NSEG) {
      unsigned fo;
      asm volatile("v_mov_b32 %0, 0" : "=v"(fo));
      wait_ge(rs, fo, F_ST + (unsigned)(P_PJ + ((s + 1) % 3) * 32 + h) * 128, (unsigned)s + 2u, cap);
      dma16x(rs, l16, WS_K + (unsigned)((s + 1) * NH + h) * 8192 + w * 1024, lbase + R_KT + w * 1024);
    }
  }
  ST_FLUSH();
  if (cap == 0u && l16 == 0) stx4(rs, 0, X_CTL, 1u);
}

__device__ __forceinline__ void role_vb(const MainP& p, LAS unsigned char* lds, int rg, int cb) {
  const int tid = threadIdx.x, lane = tid & 63, w = __builtin_amdgcn_readfirstlane(tid >> 6);
  const int kh = w >> 2, wr = (w + cb) & 7;
  const __amdgpu_buffer_rsrc_t rs = mk_rs(p.ws, WS_END);
  const unsigned lbase = (unsigned)(size_t)lds;
  unsigned l16 = lane * 16, moff = mem_off(lane), roff, soff = (unsigned)((lane >> 5) * 256 + (lane & 31) * 4);
  const int sidx = (lane & 8) ? 0xEEEE : 0x4444;
  bf16x16 WV[8], WB[16];
  {
    const int fr = lane & 15, g = lane >> 4, tu = 4 * (g & 1) + (fr >> 2), ub = lane >> 3;
    { const int sl = mem_slot(16 * (g >> 1) + fr); roff = R_RED + w * 512 + ((sl >> 5) * 32 + (sl & 15) * 2 + (g & 1)) * 8; }
    const __amdgpu_buffer_rsrc_t rv = mk_rs(p.Wv, D_ * HD * 2), ro = mk_rs(p.Wo, D_ * HD * 2);
    const unsigned vv = (unsigned)(((ub & 3) + 8 * (ub >> 2)) * HD + 16 * (((lane >> 1) & 3) ^ (ub >> 1)) + 8 * (lane & 1)) * 2u;
    const unsigned sv = (unsigned)((32 * (w >> 2) + 16 * (w & 1) + 4 * ((w >> 1) & 1)) * HD + cb * DH + rg * 64) * 2u;
#pragma unroll
    for (int c = 0; c < 16; ++c) dma16(rv, vv, sv + (unsigned)c * 64 * HD * 2, lbase + R_W + c * 8192 + w * 1024);
    WAIT_V(0); BAR();
    const unsigned lo = (g >> 1) * 1024 + tu * 128 + (((tu >> 1) ^ (w & 3)) << 5) + (fr & 3) * 8;
#pragma unroll
    for (int c = 0; c < 8; ++c) WV[c] = w_frag(lds + R_W + (kh * 8 + c) * 8192 + lo);
    LBAR();
    const int u = (lane >> 2) & 7;
    const unsigned vw = (unsigned)((16 * (u >> 2) + 8 * ((u >> 1) & 1) + 4 * (lane >> 5) + (u & 1)) * D_ + (lane & 3) * 8) * 2u;
    const unsigned sw = (unsigned)(wr * 512 * D_ + cb * 32) * 2u;
    const unsigned bo = (g >> 1) * 1024 + tu * 64 + (fr & 3) * 8;
    auto stageo = [&](int hd) {
#pragma unroll
      for (int q = 0; q < 8; ++q)
        dma16(ro, vw, sw + (unsigned)((hd * 128 + 64 * (q >> 2) + 32 * (q & 1) + 2 * ((q >> 1) & 1)) * D_) * 2u, lbase + R_W + w * 16384 + (hd & 1) * 8192 + q * 1024);
    };
    stageo(0); stageo(1);
#pragma unroll
    for (int hd = 0; hd < 4; ++hd) {
      if (hd < 3) wait_v<8>(); else wait_v<0>();
#pragma unroll
      for (int f = 0; f < 4; ++f) WB[hd * 4 + f] = w_frag(lds + R_W + w * 16384 + (hd & 1) * 8192 + (f >> 1) * 512 + (f & 1) * 32 + bo);
      WAIT_L0(); SCHED();
      if (hd < 2) stageo(hd + 2);
    }
  }
  WST(rs, 176 + rg * 8 + (cb >> 2), 4);
  unsigned hsrc = (unsigned)((lane >> 3) * 2048 + (((lane & 7) ^ (lane >> 3)) << 4)), hoff = (unsigned)((lane & 15) * 128 + (((lane >> 4) ^ (lane & 7)) << 4));
  asm volatile("" : "+v"(l16), "+v"(moff), "+v"(roff), "+v"(soff), "+v"(hsrc), "+v"(hoff));
  unsigned cap = SPIN_CAP;
  auto h_piece = [&](int b, int q) { dma16x(rs, hsrc, WS_H + (unsigned)b * 65536u + (w & 3) * 16384 + (2 * q + kh) * 128, lbase + R_W + (b & 1) * 65536 + (q * 8 + w) * 1024); };
  auto h_fetch = [&](int b) {
#pragma unroll
    for (int q = 0; q < 8; ++q) h_piece(b, q);
  };
  f32x4 x0, x1;
  auto v_part = [&](int b, auto part, auto&& each) {
    constexpr int K0 = decltype(part)::value * 8;
    LAS unsigned char* a0 = lad(hoff, lbase + R_W + (b & 1) * 65536 + kh * 32768);
    LAS unsigned char* a1 = lad<64>(hoff, lbase + R_W + (b & 1) * 65536 + kh * 32768);
    bf16x8 af[3][2];
    auto rd = [&](int k) { LAS unsigned char* q = ((k & 1) ? a1 : a0) + (k >> 1) * 4096; af[k % 3][0] = *(const LAS bf16x8*)(q); af[k % 3][1] = *(const LAS bf16x8*)(q + 2048); };
    rd(K0); rd(K0 + 1);
    SCHED();
#pragma unroll
    for (int k = K0; k < K0 + 8; ++k) {
      if (k + 2 < K0 + 8) rd(k + 2);
      const bf16x8 wf = (k & 1) ? __builtin_shufflevector(WV[k >> 1], WV[k >> 1], 8, 9, 10, 11, 12, 13, 14, 15) : __builtin_shufflevector(WV[k >> 1], WV[k >> 1], 0, 1, 2, 3, 4, 5, 6, 7);
      x0 = mfma32(af[k % 3][0], wf, k ? x0 : f32x4{0.f, 0.f, 0.f, 0.f}); x1 = mfma32(af[k % 3][1], wf, k ? x1 : f32x4{0.f, 0.f, 0.f, 0.f});
      asm volatile("" : "+v"(x0), "+v"(x1));
      SCHED();
      each(k);
      SCHED();
    }
    if (K0) {
      LAS unsigned char* xv = lad(l16, lbase + R_RED + 4096 + (w & 3) * 2048);
      asm volatile("s_nop 7\n\ts_cmp_lt_u32 %3, 4\n\ts_cbranch_scc1 1f\n\tds_write_b128 %0, %1\n\tds_write_b128 %0, %2 offset:1024\n\t1:" :: "v"(xv), "v"(x0), "v"(x1), "s"(w) : "scc", "memory");
    }
  };
  auto v_out = [&](int b) {
    if (!kh && b < NSEG) {
      LAS unsigned char* xv = lad(l16, lbase + R_RED + 4096 + (w & 3) * 2048);
      x0 += *(const LAS f32x4*)(xv); x1 += *(const LAS f32x4*)(xv + 1024);
      stx16(rs, l16, WS_VT + (unsigned)(b * NH + cb) * 8192 + (rg * 4 + w) * 1024, u32x4{pack2(x0[0], x0[1]), pack2(x0[2], x0[3]), pack2(x1[0], x1[1]), pack2(x1[2], x1[3])});
    }
  };
  typedef std::integral_constant<int, 0> P0; typedef std::integral_constant<int, 1> P1;
  BAR();
  wait_ge(rs, l16 * 8, F_LN, 1u, cap);
  h_fetch(0); h_fetch(1);
  WAIT_V(8); BAR();
  v_part(0, P0{}, [](int) {}); v_part(0, P1{}, [](int) {});
  WAIT_V(0); LBAR();
  v_out(0); h_fetch(2);
  LBAR();
  v_part(1, P0{}, [](int) {}); v_part(1, P1{}, [](int) {});
  WAIT_V(0); LBAR();
  v_out(1); h_fetch(3);
  v_part(2, P0{}, [](int) {});
  wait_ge(rs, l16 * 8, F_LN + 8192, 1u, cap); wait_ge(rs, (l16 & 0x1F0) * 8, F_LN + 16384, 1u, cap);
  WAIT_V(0); BAR();
  if (w == 0) {
    const int ln = l16 >> 4, sl = (ln >> 1) & 15, col = cb * 32 + (sl >> 3) * 16 + (ln >> 5) * 8 + (sl & 4) + (sl & 1) * 2 + ((sl >> 1) & 1), r = rg * 4 + (ln & 1) * 2;
    stx4(rs, soff, X_MEM + (cb >> 1) * 1024 + rg * 512 + ((cb ^ rg) & 1) * 128, nz((unsigned)p.mem0[r * D_ + col] | ((unsigned)p.mem0[(r + 1) * D_ + col] << 16)));
  }
  Sched tt; tt.tau = rt32() - 100000; tt.prev = tt.tau; tt.ea = 195 * 8; tt.eb = 118 * 8; tt.d1 = 0; tt.moved = true;
  const int cu_ = 32 + (rg * 32 + cb) / 2; (void)cu_;
  ST_DECL
  for (int s = 0; s < NSEG; ++s) {
    if (w == 7) wait_until(tt.tau);
    BAR();
    ST0();
    gather_mem(rs, lbase, s, wr, l16, cap, [&]() { v_part(s + 2, P1{}, [](int) {}); });
    ST(1);
    LBAR();
    ST(2);
    {
      f32x4 c0 = {0.f, 0.f, 0.f, 0.f};
      LAS unsigned char* mp = lad(moff, lbase + R_MEM + kh * 8192);
      bf16x8 af[6];
#pragma unroll
      for (int c = 0; c < 6; ++c) af[c] = mem_frag(mp, c);
      SCHED();
#pragma unroll
      for (int c = 0; c < 8; ++c) { c0 = smf(af[c % 6], WV[c], c0, sidx); SCHED(); if (c < 2) { af[c] = mem_frag(mp, 6 + c); SCHED(); } }
      const f32x4 f = fold(c0, c0);
      if (l16 < 512) stx8(rs, half_of(l16), X_VM + (unsigned)s * RSTR + cb * 4096 + (rg * 4 + (w & 3)) * 512 + kh * 256, u32x2{nz(pack2(f[0], f[1])), nz(pack2(f[2], f[3]))});
    }
    ST(3);
    {
      unsigned xb, xa;
      times_issue(rs, w == 7 ? l16 : 0x1E000000u, s, xb, xa);
      h_fetch(min(s + 4, NSEG - 2 + (s & 1)));
      v_out(s + 2);
      asm volatile("s_waitcnt vmcnt(8)" : "+v"(xb), "+v"(xa) :: "memory");
      if (w == 7) retime(rs, l16, s, xb, xa, tt, p, cap);
    }
    if (s + 1 < NSEG) {
      u32x4 a[4];
      const unsigned so = X_OX + s * RSTR + rg * 32768 + wr * 4096;
      if (w == 7) wait_until(tt.tau + tt.d1);
      BAR();
      ST(4);
      issue<0>(rs, l16, so, a[0]);
      v_part(s + 3, P0{}, [&](int k) {
        if (k == 0) issue<1>(rs, l16, so + 1024, a[1]);
        if (k == 2) issue<2>(rs, l16, so + 2048, a[2]);
        if (k == 4) issue<3>(rs, l16, so + 3072, a[3]);
      });
      f32x4 c[4];
#pragma unroll
      for (int t = 0; t < 4; ++t) c[t] = f32x4{0.f, 0.f, 0.f, 0.f};
#pragma unroll
      for (int i = 0; i < 4; ++i) {
        if (i == 0) arrive<0>(rs, l16, so, a[0], cap); else if (i == 1) arrive<1>(rs, l16, so + 1024, a[1], cap);
        else if (i == 2) arrive<2>(rs, l16, so + 2048, a[2], cap); else arrive<3>(rs, l16, so + 3072, a[3], cap);
#pragma unroll
        for (int t = 0; t < 4; ++t) c[t] = smf(__builtin_bit_cast(bf16x8, a[i]), WB[i * 4 + t], c[t], sidx);
        SCHED();
      }
      ST(5);
      {
        const bool odd = (l16 & 256) != 0;
        const f32x4 f = fold(odd ? c[2] : c[0], odd ? c[3] : c[1]);
        const auto e0 = __builtin_amdgcn_permlane16_swap(f2u(f[0]), f2u(f[2]), false, false), e1 = __builtin_amdgcn_permlane16_swap(f2u(f[1]), f2u(f[3]), false, false);
        *(LAS f32x2*)(lad(roff, lbase)) = f32x2{u2f(e0[0]) + u2f(e0[1]), u2f(e1[0]) + u2f(e1[1])};
      }
      ST(6);
      LBAR();
      ST(7);
      if (w == 0) {
        LAS unsigned char* rp = lad(half_of(l16), lbase + R_RED);
        f32x2 v[8];
#pragma unroll
        for (int i = 0; i < 8; ++i) v[i] = *(const LAS f32x2*)(rp + i * 512);
        const f32x2 sum = ((v[0] + v[1]) + (v[2] + v[3])) + ((v[4] + v[5]) + (v[6] + v[7]));
        stx4(rs, soff, X_MEM + (s + 1) * RSTR + (cb >> 1) * 1024 + rg * 512 + ((cb ^ rg) & 1) * 128, nz(pack2(sum[0], sum[1])));
        const unsigned t = max(rt32(), 1u);
        if (l16 == 0) stx4(rs, 0, X_TS + (unsigned)(s + 1) * RSTR + (unsigned)(rg * 32 + cb) * 128, t);
      }
      ST(8);
    }
    advance(tt, p);
  }
  ST_FLUSH();
  if (cap == 0u && l16 == 0) stx4(rs, 0, X_CTL, 1u);
}

#if defined(__HIP_DEVICE_COMPILE__)
__attribute__((target("no-packed-fp32-ops")))
#endif
__global__ void __launch_bounds__(512, 2) k_main(MainP p) {
  extern __shared__ __attribute__((aligned(16))) unsigned char smem[];
  const int b = blockIdx.x, x = b & 7, j = b >> 3;
  if (x >= 3) worker(p, (LAS unsigned char*)smem, x - 3, j);
  else if (x == 0) role_a(p, (LAS unsigned char*)smem, j);
  else role_vb(p, (LAS unsigned char*)smem, x - 1, j);
}

extern "C" void kernel_launch(void* const* d_in, const int* in_sizes, int n_in, void* d_out, int out_size, void* d_ws, size_t ws_size,
                              hipStream_t stream) {
  unsigned char* ws = (unsigned char*)d_ws;
  (void)hipMemsetAsync(ws, 0, X_ZERO, stream);
  MainP p;
  p.x = (const u16*)d_in[0]; p.mem0 = (const u16*)d_in[1]; p.c = (const u16*)d_in[12]; p.wmod = (const u16*)d_in[13]; p.Wq = (const u16*)d_in[2]; p.Wk = (const u16*)d_in[3]; p.Wv = (const u16*)d_in[4]; p.Wo = (const u16*)d_in[5];
  p.w1 = (const u16*)d_in[8]; p.w2 = (const u16*)d_in[9]; p.w3 = (const u16*)d_in[10]; p.w4 = (const u16*)d_in[11];
  p.out = (u16*)d_out; p.ws = ws; p.d1 = 8; p.d2 = 8; p.d3 = 2; p.d4 = 100;
  (void)hipFuncSetAttribute((const void*)k_main, hipFuncAttributeMaxDynamicSharedMemorySize, R_LDS);
  k_main<<<256, 512, R_LDS, stream>>>(p);
}
```

```cpp
#include <hip/hip_runtime.h>
#include <hip/hip_bf16.h>
#include <cstdlib>
#include <type_traits>
#pragma clang diagnostic ignored "-Winline-asm"

#define LAS __attribute__((address_space(3)))
#define GAS __attribute__((address_space(1)))
typedef unsigned short u16;
typedef __bf16 bf16x2 __attribute__((ext_vector_type(2)));
typedef __bf16 bf16x4 __attribute__((ext_vector_type(4)));
typedef __bf16 bf16x8 __attribute__((ext_vector_type(8)));
typedef __bf16 bf16x16 __attribute__((ext_vector_type(16)));
typedef short s16x4 __attribute__((ext_vector_type(4)));
typedef float f32x2 __attribute__((ext_vector_type(2)));
typedef float f32x4 __attribute__((ext_vector_type(4)));
typedef float f32x16 __attribute__((ext_vector_type(16)));
typedef unsigned u32x2 __attribute__((ext_vector_type(2)));
typedef unsigned u32x4 __attribute__((ext_vector_type(4)));

constexpr int S_ = 2048, D_ = 1024, NH = 32, DH = 128, HD = 4096, NSEG = 64, FF = 3584;

constexpr unsigned MB = 1u << 20;
constexpr unsigned F_LN = 0;
constexpr unsigned F_ST = 24576;
constexpr unsigned F_CH = F_ST + 98304;
constexpr unsigned X_CTL = F_CH + 4096;
constexpr unsigned X_MOD = X_CTL + 4096;
constexpr unsigned X_REC = X_MOD + 24576;
constexpr unsigned RSTR = 225280;
constexpr int ZA = 8;
constexpr unsigned X_MEM = X_REC;
constexpr unsigned X_OX = X_REC + 16384;
constexpr unsigned X_VM = X_REC + 81920;
constexpr unsigned X_TS = X_REC + 212992;
constexpr unsigned X_ZERO = X_REC + ZA * RSTR;
constexpr unsigned WS_H = 16 * MB;
constexpr unsigned WS_Q = 20 * MB;
constexpr unsigned WS_K = 36 * MB;
constexpr unsigned WS_VT = 52 * MB;
constexpr unsigned WS_O = 68 * MB;
constexpr unsigned WS_X1 = 84 * MB;
constexpr unsigned WS_H2 = 88 * MB;
constexpr unsigned WS_G = 92 * MB;
constexpr unsigned WS_H1S = 96 * MB;
constexpr unsigned WS_X2 = 101 * MB;
constexpr unsigned WS_ST5 = 106 * MB;
constexpr unsigned WS_U = 108 * MB;
constexpr unsigned WS_KM = 124 * MB;
constexpr unsigned WS_P2 = 132 * MB;
constexpr unsigned WS_P8 = 164 * MB;
constexpr unsigned WS_END = 198 * MB;

__device__ __forceinline__ float bf2f(unsigned short b) { return __builtin_bit_cast(float, (unsigned)b << 16); }
__device__ __forceinline__ unsigned f2u(float f) { return __builtin_bit_cast(unsigned, f); }
__device__ __forceinline__ float u2f(unsigned u) { return __builtin_bit_cast(float, u); }
__device__ __forceinline__ float lo2f(unsigned w) { return __builtin_bit_cast(float, w << 16); }
__device__ __forceinline__ float hi2f(unsigned w) { return __builtin_bit_cast(float, w & 0xFFFF0000u); }
__device__ __forceinline__ unsigned pack2(float a, float b) { bf16x2 v; v[0] = (__bf16)a; v[1] = (__bf16)b; return __builtin_bit_cast(unsigned, v); }
__device__ __forceinline__ float rbf(float a) { return (float)(__bf16)a; }
__device__ __forceinline__ unsigned nz(unsigned w) { return max(w, 1u); }
__device__ __forceinline__ float gelu_tanh(float x) {
  const float e = __builtin_amdgcn_exp2f(x * fmaf(x * x, -0.1029432397f, -2.302208198f));
  return x * __builtin_amdgcn_rcpf(1.0f + e);
}
template <int CTRL> __device__ __forceinline__ float dpp_add(float v) { return v + u2f(__builtin_amdgcn_update_dpp(0u, f2u(v), CTRL, 0xF, 0xF, true)); }
__device__ __forceinline__ float wave_sum(float v) {
  v = dpp_add<0xB1>(v); v = dpp_add<0x4E>(v); v = dpp_add<0x141>(v); v = dpp_add<0x140>(v);
  auto t = __builtin_amdgcn_permlane16_swap(f2u(v), f2u(v), false, false);
  v = u2f(t[0]) + u2f(t[1]);
  t = __builtin_amdgcn_permlane32_swap(f2u(v), f2u(v), false, false);
  return u2f(t[0]) + u2f(t[1]);
}
#define WAIT_V(n) asm volatile("s_waitcnt vmcnt(" #n ")" ::: "memory")
#define WAIT_L0() asm volatile("s_waitcnt lgkmcnt(0)" ::: "memory")
#define BAR() do { asm volatile("" ::: "memory"); __builtin_amdgcn_s_barrier(); asm volatile("" ::: "memory"); } while (0)
#define LBAR() do { WAIT_L0(); BAR(); } while (0)
template <int N> __device__ __forceinline__ void wait_v() { asm volatile("s_waitcnt vmcnt(%0)" :: "n"(N) : "memory"); }
__device__ __forceinline__ void dma16(__amdgpu_buffer_rsrc_t rs, unsigned voff, unsigned soff, unsigned ldsa) {
  asm volatile("s_mov_b32 m0, %0\n\ts_nop 3\n\tbuffer_load_dwordx4 %1, %2, %3 offen lds" :: "s"(ldsa), "v"(voff), "s"(rs), "s"(soff) : "m0", "memory");
}
__device__ __forceinline__ void dma16x(__amdgpu_buffer_rsrc_t rs, unsigned voff, unsigned soff, unsigned ldsa) {
  asm volatile("s_mov_b32 m0, %0\n\ts_nop 3\n\tbuffer_load_dwordx4 %1, %2, %3 offen sc1 lds" :: "s"(ldsa), "v"(voff), "s"(rs), "s"(soff) : "m0", "memory");
}
__device__ __forceinline__ u32x4 ldx16(__amdgpu_buffer_rsrc_t rs, unsigned voff, unsigned soff) { return __builtin_amdgcn_raw_buffer_load_b128(rs, voff, soff, 16); }
__device__ __forceinline__ u32x2 ldx8(__amdgpu_buffer_rsrc_t rs, unsigned voff, unsigned soff) { return __builtin_amdgcn_raw_buffer_load_b64(rs, voff, soff, 16); }
__device__ __forceinline__ unsigned ldx4(__amdgpu_buffer_rsrc_t rs, unsigned voff, unsigned soff) { return __builtin_amdgcn_raw_buffer_load_b32(rs, voff, soff, 16); }
__device__ __forceinline__ void stx16(__amdgpu_buffer_rsrc_t rs, unsigned voff, unsigned soff, u32x4 v) {
  __builtin_amdgcn_raw_buffer_store_b128(v, rs, voff, soff, 16);
  asm volatile("s_nop 1" :: "v"(v) : "memory");
}
__device__ __forceinline__ void stx8(__amdgpu_buffer_rsrc_t rs, unsigned voff, unsigned soff, u32x2 v) { __builtin_amdgcn_raw_buffer_store_b64(v, rs, voff, soff, 16); }
__device__ __forceinline__ void stx4(__amdgpu_buffer_rsrc_t rs, unsigned voff, unsigned soff, unsigned v) { __builtin_amdgcn_raw_buffer_store_b32(v, rs, voff, soff, 16); }
__device__ __forceinline__ void wait_ge(__amdgpu_buffer_rsrc_t rs, unsigned voff, unsigned soff, unsigned need, unsigned& cap) {
  unsigned t;
  asm volatile("s_nop 4\n\t"
               "1:\n\t"
               "buffer_load_dword %[t], %[vo], %[rs], %[so] offen sc1\n\t"
               "s_waitcnt vmcnt(0)\n\t"
               "v_cmp_gt_u32 vcc, %[need], %[t]\n\t"
               "s_cmp_eq_u64 vcc, 0\n\t"
               "s_cbranch_scc1 2f\n\t"
               "s_sleep 2\n\t"
               "s_sub_u32 %[cap], %[cap], 1\n\t"
               "s_cbranch_scc0 1b\n\t"
               "s_mov_b32 %[cap], 0\n\t"
               "2:"
               : [t] "=&v"(t), [cap] "+s"(cap) : [vo] "v"(voff), [rs] "s"(rs), [so] "s"(soff), [need] "s"(need) : "vcc", "scc", "memory");
}
__device__ __forceinline__ void wait_seen(__amdgpu_buffer_rsrc_t rs, unsigned voff, unsigned soff, unsigned need, unsigned& seen, unsigned& cap) {
  if (seen >= need) return;
  unsigned t;
  asm volatile("s_nop 4\n\t"
               "1:\n\t"
               "buffer_load_dword %[t], %[vo], %[rs], %[so] offen sc1\n\t"
               "s_waitcnt vmcnt(0)\n\t"
               "v_cmp_gt_u32 vcc, %[need], %[t]\n\t"
               "s_cbranch_vccz 2f\n\t"
               "s_sleep 1\n\t"
               "s_sub_u32 %[cap], %[cap], 1\n\t"
               "s_cbranch_scc0 1b\n\t"
               "s_mov_b32 %[cap], 0\n\t"
               "2:"
               : [t] "=&v"(t), [cap] "+s"(cap) : [vo] "v"(voff), [rs] "s"(rs), [so] "s"(soff), [need] "s"(need) : "vcc", "scc", "memory");
  seen = min((unsigned)__builtin_amdgcn_readlane(t, 0), (unsigned)__builtin_amdgcn_readlane(t, 1));
}
__device__ __forceinline__ __amdgpu_buffer_rsrc_t mk_rs(const void* p, unsigned bytes) { return __builtin_amdgcn_make_buffer_rsrc((void*)p, 0, (int)bytes, 0x00020000); }
#define SCHED() __builtin_amdgcn_sched_barrier(0)
__device__ __forceinline__ unsigned now32() { unsigned long long t; asm volatile("s_memtime %0\n\ts_waitcnt lgkmcnt(0)" : "=s"(t) :: "memory"); return (unsigned)t; }
__device__ __forceinline__ unsigned rt32() { unsigned long long t; asm volatile("s_memrealtime %0\n\ts_waitcnt lgkmcnt(0)" : "=s"(t) :: "memory"); return (unsigned)t; }
__device__ __forceinline__ void wait_until(unsigned t) { while ((int)(rt32() - t) < 0) {} }
template <int X = 0> __device__ __forceinline__ LAS unsigned char* lad(unsigned v, unsigned u) {
  unsigned a;
  if (X == 0) asm volatile("v_add_u32 %0, %1, %2" : "=v"(a) : "s"(u), "v"(v));
  else asm volatile("v_xor_b32 %0, %3, %2\n\tv_add_u32 %0, %1, %0" : "=&v"(a) : "s"(u), "v"(v), "n"(X));
  return (LAS unsigned char*)(size_t)a;
}
__device__ __forceinline__ bf16x4 trread(LAS unsigned char* p) { return __builtin_amdgcn_ds_read_tr16_b64_v4bf16((LAS bf16x4*)p); }
__device__ __forceinline__ bf16x8 cat8(bf16x4 a, bf16x4 b) { return __builtin_shufflevector(a, b, 0, 1, 2, 3, 4, 5, 6, 7); }
__device__ __forceinline__ bf16x16 cat16(bf16x8 a, bf16x8 b) { return __builtin_shufflevector(a, b, 0, 1, 2, 3, 4, 5, 6, 7, 8, 9, 10, 11, 12, 13, 14, 15); }
__device__ __forceinline__ f32x4 mfma32(bf16x8 a, bf16x8 b, f32x4 c) { return __builtin_amdgcn_mfma_f32_16x16x32_bf16(a, b, c, 0, 0, 0); }
__device__ __forceinline__ f32x4 mfma16(u32x2 a, u32x2 b, f32x4 c) {
  return mfma32(__builtin_bit_cast(bf16x8, u32x4{a[0], a[1], 0u, 0u}), __builtin_bit_cast(bf16x8, u32x4{b[0], b[1], 0u, 0u}), c);
}
__device__ __forceinline__ f32x4 mfma16n(u32x2 a, u32x2 b, f32x4 c) { return __builtin_amdgcn_mfma_f32_16x16x16bf16_1k(__builtin_bit_cast(s16x4, a), __builtin_bit_cast(s16x4, b), c, 0, 0, 0); }
__device__ __forceinline__ f32x4 smf(bf16x8 a, bf16x16 b, f32x4 c, int idx) { return __builtin_amdgcn_smfmac_f32_16x16x64_bf16(a, b, c, idx, 0, 0); }

__device__ __forceinline__ void ln_row(float (&x)[16]) {
  float s = 0.f;
#pragma unroll
  for (int i = 0; i < 16; ++i) s += x[i];
  const float mean = wave_sum(s) * (1.0f / 1024.0f);
  float q = 0.f;
#pragma unroll
  for (int i = 0; i < 16; ++i) { const float d = x[i] - mean; q += d * d; }
  const float var = wave_sum(q) * (1.0f / 1024.0f);
  const float m = rbf(mean), isd = __builtin_amdgcn_rcpf(rbf(__builtin_amdgcn_sqrtf(rbf(rbf(var) + 1e-5f))));
#pragma unroll
  for (int i = 0; i < 16; ++i) x[i] = rbf(rbf(x[i] - m) * isd);
}
__device__ __forceinline__ void load_row16(const u16* rowp, int lane, float (&x)[16]) {
  const u32x4 a = *(const u32x4*)(rowp + lane * 8), b = *(const u32x4*)(rowp + 512 + lane * 8);
#pragma unroll
  for (int i = 0; i < 4; ++i) { x[2 * i] = lo2f(a[i]); x[2 * i + 1] = hi2f(a[i]); x[8 + 2 * i] = lo2f(b[i]); x[9 + 2 * i] = hi2f(b[i]); }
}
__device__ __forceinline__ void store_row16(u16* rowp, int lane, const float (&x)[16]) {
  u32x4 a, b;
#pragma unroll
  for (int i = 0; i < 4; ++i) { a[i] = pack2(x[2 * i], x[2 * i + 1]); b[i] = pack2(x[8 + 2 * i], x[9 + 2 * i]); }
  *(u32x4*)(rowp + lane * 8) = a; *(u32x4*)(rowp + 512 + lane * 8) = b;
}
__host__ __device__ __forceinline__ int mem_slot(int dl) { return ((dl >> 3) & 1) * 32 + (dl >> 4) * 8 + (dl & 4) + (dl & 1) * 2 + ((dl >> 1) & 1); }

#define WST(rs, wk, i)
struct MainP { const u16 *x, *mem0, *Wq, *Wk, *Wv, *Wo, *w1, *w2, *w3, *w4, *c, *wmod; u16* out; unsigned char* ws; int d1, d2, d3, d4; };
constexpr unsigned SPIN_CAP = 1u << 18;

__device__ __forceinline__ float gmax(float v) {
  auto t = __builtin_amdgcn_permlane16_swap(f2u(v), f2u(v), false, false);
  v = fmaxf(u2f(t[0]), u2f(t[1]));
  t = __builtin_amdgcn_permlane32_swap(f2u(v), f2u(v), false, false);
  return fmaxf(u2f(t[0]), u2f(t[1]));
}
__device__ __forceinline__ float gsum(float v) {
  auto t = __builtin_amdgcn_permlane16_swap(f2u(v), f2u(v), false, false);
  v = u2f(t[0]) + u2f(t[1]);
  t = __builtin_amdgcn_permlane32_swap(f2u(v), f2u(v), false, false);
  return u2f(t[0]) + u2f(t[1]);
}
__device__ __forceinline__ void publish(__amdgpu_buffer_rsrc_t rs, unsigned word, unsigned val) {
  WAIT_V(0); BAR();
  if (threadIdx.x == 0) stx4(rs, 0, word, val);
}

constexpr int QB0 = 20;
constexpr int P_S2 = 0, P_S8 = 32, P_S7 = 64, P_S4 = 96, P_S5 = 104, P_S3 = 112, P_PJ = 208, P_S1 = 336;
constexpr int NLN = 160;

enum { SG_WO = 0, SG_W1 = 1, SG_W2 = 2, SG_W3 = 3, SG_W4 = 4, SG_PJ = 5 };
constexpr int G_XCH = 131072, G_K = G_XCH + 16384, G_CTL = G_K + 8192;
__device__ __forceinline__ f32x16 mfma3232(bf16x8 a, bf16x8 b, f32x16 c) { return __builtin_amdgcn_mfma_f32_32x32x16_bf16(a, b, c, 0, 0, 0); }
__device__ __forceinline__ void widen32(const f32x16& a, u32x4 (&v)[2]) {
#pragma unroll
  for (int u = 0; u < 2; ++u) {
    const auto r0 = __builtin_amdgcn_permlane32_swap(pack2(a[8 * u], a[8 * u + 1]), pack2(a[8 * u + 4], a[8 * u + 5]), false, false);
    const auto r1 = __builtin_amdgcn_permlane32_swap(pack2(a[8 * u + 2], a[8 * u + 3]), pack2(a[8 * u + 6], a[8 * u + 7]), false, false);
    v[u] = u32x4{r0[0], r1[0], r0[1], r1[1]};
  }
}
__device__ __forceinline__ unsigned umin4v(u32x4 v) { return min(min(v[0], v[1]), min(v[2], v[3])); }
__device__ __forceinline__ float hsum(float v) { const auto t = __builtin_amdgcn_permlane32_swap(f2u(v), f2u(v), false, false); return u2f(t[0]) + u2f(t[1]); }

__device__ __forceinline__ void attend(__amdgpu_buffer_rsrc_t rs, int seg, int h, int t, int lane, unsigned& chs, unsigned& cap) {
  const int fr = lane & 15, g = lane >> 4;
  const unsigned l16 = lane * 16, ub = (unsigned)(seg * NH + h);
  wait_seen(rs, 0, F_CH + h * 128, (unsigned)max(seg, 1), chs, cap);
  wait_ge(rs, (lane & 1) && seg >= QB0 ? 96u * 128u : (unsigned)(seg % 3) * 4096u, F_ST + (unsigned)(P_PJ + h) * 128, (unsigned)seg + 1u, cap);
  f32x4 s0 = {0.f, 0.f, 0.f, 0.f}, s1 = s0, s2 = s0;
  u32x4 q[4], kmf[4];
  u32x2 vmf[8];
  const unsigned em = (unsigned)(fr >> 3) * 512 + (unsigned)g * 128 + (unsigned)(8 * ((fr >> 2) & 1) + (fr & 3)) * 8;
  auto mem_load = [&](unsigned off) {
#pragma unroll
    for (int ks = 0; ks < 4; ++ks) kmf[ks] = ldx16(rs, l16 + off, WS_KM + ub * 4096 + ks * 1024);
#pragma unroll
    for (int i = 0; i < 8; ++i) vmf[i] = ldx8(rs, em + off, X_VM + (unsigned)seg * RSTR + h * 4096 + (i >> 1) * 1024 + (i & 1) * 32);
  };
  const bool have = chs > (unsigned)seg;
  f32x4 o[8];
  float mt = -3.0e38f, lt = 0.f;
  const float scale = 0.08838834764831845f, L2E = 1.4426950408889634f;
  {
    u32x4 k0[4], k1[4], vv[8];
#pragma unroll
    for (int ks = 0; ks < 4; ++ks) {
      q[ks] = ldx16(rs, l16, WS_Q + ub * 8192 + t * 4096 + ks * 1024);
      k0[ks] = ldx16(rs, l16, WS_K + ub * 8192 + ks * 1024); k1[ks] = ldx16(rs, t ? l16 : 0xF0000000u, WS_K + ub * 8192 + 4096 + ks * 1024);
    }
    const unsigned ev = (unsigned)(fr >> 3) * 1024 + (unsigned)(g * 16 + 8 * ((fr >> 2) & 1) + (fr & 3)) * 16;
#pragma unroll
    for (int i = 0; i < 8; ++i) vv[i] = ldx16(rs, ev, WS_VT + ub * 8192 + (i >> 1) * 2048 + (i & 1) * 64);
#pragma unroll
    for (int ks = 0; ks < 4; ++ks) {
      s1 = mfma32(__builtin_bit_cast(bf16x8, k0[ks]), __builtin_bit_cast(bf16x8, q[ks]), s1); s2 = mfma32(__builtin_bit_cast(bf16x8, k1[ks]), __builtin_bit_cast(bf16x8, q[ks]), s2);
    }
    SCHED();
    mem_load(have ? 0u : 0xF0000000u);
    SCHED();
    float b[4], c[4];
#pragma unroll
    for (int r = 0; r < 4; ++r) {
      const int j = 4 * g + r;
      b[r] = (t == 1 || j <= fr) ? rbf(rbf(s1[r]) * scale) : -3.0e38f;
      c[r] = (t == 1 && j <= fr) ? rbf(rbf(s2[r]) * scale) : -3.0e38f;
      mt = fmaxf(mt, fmaxf(b[r], c[r]));
    }
    mt = gmax(mt);
#pragma unroll
    for (int r = 0; r < 4; ++r) { b[r] = __builtin_amdgcn_exp2f((b[r] - mt) * L2E); c[r] = __builtin_amdgcn_exp2f((c[r] - mt) * L2E); lt += b[r] + c[r]; }
    const u32x4 pt = {pack2(b[0], b[1]), pack2(b[2], b[3]), pack2(c[0], c[1]), pack2(c[2], c[3])};
#pragma unroll
    for (int i = 0; i < 8; ++i) o[i] = mfma32(__builtin_bit_cast(bf16x8, vv[i]), __builtin_bit_cast(bf16x8, pt), f32x4{0.f, 0.f, 0.f, 0.f});
    lt = gsum(lt);
  }
  asm volatile("" : "+v"(o[0]), "+v"(o[1]), "+v"(o[2]), "+v"(o[3]), "+v"(o[4]), "+v"(o[5]), "+v"(o[6]), "+v"(o[7]), "+v"(lt));
  if (!have) { wait_seen(rs, 0, F_CH + h * 128, (unsigned)seg + 1u, chs, cap); mem_load(0u); }
#pragma unroll
  for (int ks = 0; ks < 4; ++ks) s0 = mfma32(__builtin_bit_cast(bf16x8, kmf[ks]), __builtin_bit_cast(bf16x8, q[ks]), s0);
  float a[4];
#pragma unroll
  for (int r = 0; r < 4; ++r) a[r] = rbf(rbf(s0[r]) * scale);
  const float mx = fmaxf(mt, gmax(fmaxf(fmaxf(a[0], a[1]), fmaxf(a[2], a[3]))));
#pragma unroll
  for (int r = 0; r < 4; ++r) a[r] = __builtin_amdgcn_exp2f((a[r] - mx) * L2E);
  const u32x2 pm = {pack2(a[0], a[1]), pack2(a[2], a[3])};
  const float al = __builtin_amdgcn_exp2f((mt - mx) * L2E);
  const float inv = __builtin_amdgcn_rcpf(fmaf(al, lt, 0.5f * gsum((a[0] + a[1]) + (a[2] + a[3])))), ai = al * inv;
#pragma unroll
  for (int u = 0; u < 4; ++u) {
    const f32x4 x = o[2 * u] * ai + mfma16n(vmf[2 * u], pm, f32x4{0.f, 0.f, 0.f, 0.f}) * inv, y = o[2 * u + 1] * ai + mfma16n(vmf[2 * u + 1], pm, f32x4{0.f, 0.f, 0.f, 0.f}) * inv;
    stx16(rs, l16, WS_O + (unsigned)seg * 262144 + (unsigned)((h * 4 + u) * 2 + t) * 1024, u32x4{pack2(x[0], x[1]), pack2(x[2], x[3]), pack2(y[0], y[1]), pack2(y[2], y[3])});
  }
  WAIT_V(0);
  if (lane == 0) stx4(rs, 0, F_ST + (unsigned)(P_S1 + (seg & 3) * 64 + h * 2 + t) * 128, (unsigned)seg + 1u);
}

template <int SG>
__device__ __forceinline__ void stage_gemm(LAS unsigned char* lds, __amdgpu_buffer_rsrc_t rs, const u16* Wg, int wk, int kq, int cb, int pid, unsigned& cap,
                                           unsigned dst = 0, int bstr = 1, int nb = 0, const u16* side = nullptr, int srows = 0, int sld = 0, const u16* side3 = nullptr) {
  constexpr bool PROJ = SG == SG_PJ, ROWM = (SG == SG_W1 || PROJ);
  constexpr int KS = (SG == SG_W4) ? 28 : 32;
  constexpr int LDW = PROJ ? HD : (SG == SG_W3) ? FF : D_;
  constexpr int WROWS = (SG == SG_WO) ? HD : (SG == SG_W4 ? FF : D_);
  const int NBLK = PROJ ? nb : (SG == SG_WO || SG == SG_W1) ? 64 : 65;
  constexpr unsigned ABASE = PROJ ? WS_H : SG == SG_WO ? WS_O : SG == SG_W1 ? WS_G : SG == SG_W2 ? WS_H1S : SG == SG_W3 ? WS_X2 : WS_U;
  constexpr unsigned ASTR = SG == SG_WO ? 262144u : SG == SG_W4 ? 229376u : 65536u;
  constexpr int NPC = KS / 4;
  const unsigned myword = F_ST + (unsigned)pid * 128u;
  int tid = threadIdx.x; asm volatile("" : "+v"(tid));
  const int lane = tid & 63, w = __builtin_amdgcn_readfirstlane(tid >> 6), row = lane & 31, hf = lane >> 5;
  const int kh = w >> 2, wl = w & 3;
  const int koff = PROJ ? 0 : kq;
  const unsigned lbase = (unsigned)(size_t)lds, l16 = lane * 16;
  const int col0 = cb * 128 + wl * 32;
  bf16x8 Wf[KS];
  {
    const __amdgpu_buffer_rsrc_t rsW = mk_rs(Wg, (unsigned)WROWS * LDW * 2u);
    const int ub = lane >> 3, tu = 4 * hf + ((lane & 15) >> 2);
    const unsigned vB = (unsigned)(((ub & 3) + 8 * (ub >> 2)) * LDW + 16 * (((lane >> 1) & 3) ^ (ub >> 1)) + 8 * (lane & 1)) * 2u;
    const unsigned sB = (unsigned)((koff * KS * 32 + 32 * (w >> 2) + 16 * ((w >> 1) & 1) + 4 * (w & 1)) * LDW + cb * 128) * 2u;
    const int boff = kh * 65536 + (wl >> 1) * 8192 + tu * 128 + (((tu >> 1) ^ (2 * (wl & 1) + ((lane >> 4) & 1))) << 5) + (lane & 3) * 8;
#pragma unroll
    for (int ps = 0; ps < 2; ++ps) {
      BAR();
#pragma unroll
      for (int hh = 0; hh < 2; ++hh)
#pragma unroll
        for (int i = 0; i < 4; ++i)
          if (4 * ps + i < NPC && !(PROJ && ps == 0)) {
            const unsigned so = sB + (unsigned)((hh * NPC + 4 * ps + i) * 64 * LDW) * 2u, d = lbase + hh * 65536 + i * 16384 + w * 1024;
            dma16(rsW, vB, so, d); dma16(rsW, vB, so + 128, d + 8192);
          }
      WAIT_V(0); BAR();
#pragma unroll
      for (int i = 0; i < 4; ++i)
        if (4 * ps + i < NPC) {
#pragma unroll
          for (int k = 0; k < 4; ++k) { LAS unsigned char* q = lds + (boff + i * 16384 + k * 2048); Wf[(4 * ps + i) * 4 + k] = cat8(trread(q), trread(q + 1024)); }
        }
      WAIT_L0();
    }
    if (SG == SG_W1) {
      const u32x4 z = {0u, 0u, 0u, 0u};
      stx16(rs, (unsigned)tid * 16, WS_H1S + cb * 8192, z); stx16(rs, (unsigned)tid * 16, WS_H1S + 64 * 65536 + cb * 8192, z);
      WAIT_V(0);
    }
    if (tid < 8) *(LAS unsigned*)(lds + G_CTL + tid * 4) = 0u;
    LBAR();
  }
  const f32x16 zero16 = {0.f, 0.f, 0.f, 0.f, 0.f, 0.f, 0.f, 0.f, 0.f, 0.f, 0.f, 0.f, 0.f, 0.f, 0.f, 0.f};
  auto put16 = [&](LAS unsigned char* p, const f32x16& a) {
#pragma unroll
    for (int q = 0; q < 4; ++q) *(LAS f32x4*)(p + q * 1024) = f32x4{a[4 * q], a[4 * q + 1], a[4 * q + 2], a[4 * q + 3]};
  };
  if (SG == SG_W3) {
    const u32x4 one4 = {0x3F803F80u, 0x3F803F80u, 0x3F803F80u, 0x3F803F80u};
    f32x16 cs = zero16;
#pragma unroll
    for (int k = 0; k < KS; ++k) cs = mfma3232(Wf[k], __builtin_bit_cast(bf16x8, one4), cs);
    if (kh == 0) put16(lds + wl * 4096 + l16, cs);
    LBAR();
    if (kh == 1 && row == 0) {
#pragma unroll
      for (int q = 0; q < 4; ++q)
        *(LAS f32x4*)(lds + G_K + wl * 128 + hf * 64 + q * 16) = *(const LAS f32x4*)(lds + wl * 4096 + q * 1024 + l16) + f32x4{cs[4 * q], cs[4 * q + 1], cs[4 * q + 2], cs[4 * q + 3]};
    }
    LBAR();
  }
  auto blk = [&](int i) -> int { return PROJ ? bstr * i + kq : i; };
  const unsigned aoff = ROWM ? (unsigned)(row * 128 + ((hf ^ ((row >> 1) & 7)) << 4)) : (unsigned)((row >> 4) * 1024 + hf * 256 + (row & 15) * 16);
  const unsigned vA = ROWM ? (unsigned)((lane >> 3) * 2048 + (((lane & 7) ^ ((4 * wl + (lane >> 4)) & 7)) << 4)) : l16;
  auto piece = [&](int hb, int q, bool on) {
    const int i = hb >> 1, hh = hb & 1;
    const unsigned so = ABASE + (unsigned)blk(i) * ASTR + (unsigned)koff * (KS * 2048) + (ROWM ? wl * 16384 + hh * 1024 : (hh * KS + wl) * 1024);
    if (on) dma16x(rs, vA, so + q * (ROWM ? 128 : 4096), lbase + (i & 1) * 65536 + hh * 32768 + wl * 1024 + q * 4096);
  };
  auto fetch = [&](int hb) {
#pragma unroll
    for (int q = 0; q < NPC; ++q) piece(hb, q, true);
  };
  constexpr bool WEAVE = (SG == SG_W1 || SG == SG_W2 || SG == SG_W3);
  auto products = [&](int i, f32x16& acc, auto&& first, auto&& early, auto&& handover, bool soon) {
    const unsigned ub = lbase + (i & 1) * 65536 + kh * 32768;
    LAS unsigned char* ab[4];
    ab[0] = lad(aoff, ub);
    if (ROWM) { ab[1] = lad<32>(aoff, ub); ab[2] = lad<64>(aoff, ub); ab[3] = lad<96>(aoff, ub); }
    constexpr int PD = 10;
    bf16x8 af[PD + 1];
    auto rd = [&](int k) { af[k % (PD + 1)] = ROWM ? *(const LAS bf16x8*)(ab[k & 3] + (k >> 2) * 4096) : *(const LAS bf16x8*)(ab[0] + (k >> 1) * 2048 + (k & 1) * 512); };
#pragma unroll
    for (int k = 0; k < PD; ++k) rd(k);
    SCHED();
#pragma unroll
    for (int k = 0; k < KS; ++k) {
      if (k + PD < KS) rd(k + PD);
      acc = mfma3232(Wf[k], af[k % (PD + 1)], k ? acc : zero16);
      SCHED();
      first(k);
      SCHED();
      if (k == KS - 10) { early(); SCHED(); }
      if (k == 9) { if (soon) handover(); SCHED(); }
      if (k == KS - 5) { if (!soon) handover(); SCHED(); }
    }
    asm volatile("" : "+v"(acc));
  };
  WST(rs, wk, PROJ ? 2 : 10);
#define SEG(x)
  auto announce = [&](int i) {
    if (lane == 0) {
      const unsigned c = __hip_atomic_fetch_add((LAS unsigned*)(lds + G_CTL), 1u, __ATOMIC_RELAXED, __HIP_MEMORY_SCOPE_WORKGROUP);
      if ((c & 3u) == 3u) stx4(rs, 0, myword, (unsigned)blk(i) + 1u);
    }
  };
  if (kh == 0) {
    auto pvo = [&](int i) -> unsigned {
      const int b = blk(i);
      if (PROJ) return F_LN + (unsigned)((b % (NLN / 4)) * 4 + (lane & 3)) * 128u;
      if (SG == SG_WO) return F_ST + (unsigned)(P_S1 + (b & 3) * 64 + kq * 16 + (lane & 15)) * 128u;
      if (SG == SG_W1) return F_ST + (unsigned)(P_S3 + (b % 3) * 32 + (lane & 31)) * 128u;
      if (SG == SG_W2) return F_ST + (unsigned)(P_S4 + (lane & 7)) * 128u;
      if (SG == SG_W3) return F_ST + (unsigned)(P_S5 + (lane & 7)) * 128u;
      return F_ST + (unsigned)(P_S7 + kq * 7 + min(lane & 7, 6)) * 128u;
    };
    auto need = [&](int i) -> unsigned { return PROJ ? 1u : (SG == SG_W2 && i == 64) ? 64u : (unsigned)i + 1u; };
    unsigned nl = 0;
    int R = 0, Rp = 0;
    for (int i = 0; i < NBLK; ++i) {
      if (R <= i) { wait_ge(rs, pvo(i), 0, need(i), cap); SEG(6); fetch(2 * i); fetch(2 * i + 1); SEG(7); }
      const unsigned pk = w == 0 ? (lane < 32 ? pvo(i + 1) : pvo(i + 2)) : 0xF0000000u;
      if (Rp <= i) { if (R <= i + 1) wait_v<NPC>(); else wait_v<2 * NPC>(); }
      if (R >= i + 2 || Rp > i) BAR();
      else {
        nl += 4;
        if (lane == 0) (void)__hip_atomic_fetch_add((LAS unsigned*)(lds + G_CTL + 8), 1u, __ATOMIC_RELAXED, __HIP_MEMORY_SCOPE_WORKGROUP);
        while (__builtin_amdgcn_readfirstlane(*(const volatile LAS unsigned*)(lds + G_CTL + 8)) < nl) {}
        asm volatile("" ::: "memory");
      }
      SEG(1);
      f32x16 acc;
      unsigned peek;
      asm volatile("buffer_load_dword %0, %1, %2, 0 offen sc1" : "=v"(peek) : "v"(pk), "s"(rs) : "memory");
      u32x4 st[8];
      float mean = 0.f, q2 = 0.f;
      if (SG == SG_W3) {
#pragma unroll
        for (int q = 0; q < 8; ++q) st[q] = ldx16(rs, l16, WS_ST5 + (unsigned)i * 8192 + q * 1024);
      }
      products(i, acc, [&](int k) {
        if (SG == SG_W3) {
          if (k >= 18 && k < 26) { mean += u2f(st[k - 18][0]) + u2f(st[k - 18][2]); q2 += u2f(st[k - 18][1]) + u2f(st[k - 18][3]); asm volatile("" : "+v"(mean), "+v"(q2)); }
          if (k == 26) { mean = hsum(mean) * (1.0f / 1024.0f); q2 = hsum(q2); asm volatile("" : "+v"(mean), "+v"(q2)); }
          if (k == 28) { q2 = rbf(rbf(fmaxf(fmaf(-mean, mean, q2 * (1.0f / 1024.0f)), 0.f)) + 1e-5f); asm volatile("" : "+v"(q2)); }
          if (k == 29) { q2 = __builtin_amdgcn_rcpf(rbf(__builtin_amdgcn_sqrtf(q2))); mean = rbf(mean); asm volatile("" : "+v"(q2), "+v"(mean)); }
        }
      }, []() {}, [&]() {
        SEG(4);
        WAIT_V(0);
        SEG(0);
        asm volatile("" : "+v"(peek));
        if (w == 0) {
          const unsigned long long ok = __ballot(peek >= (lane < 32 ? need(i + 1) : need(i + 2)));
          const bool r1 = (R >= i + 2 || (unsigned)ok == 0xFFFFFFFFu) && i + 1 < NBLK, r2 = r1 && (unsigned)(ok >> 32) == 0xFFFFFFFFu && i + 2 < NBLK;
          if (lane == 0) *(volatile LAS unsigned*)(lds + G_CTL + 4) = (unsigned)(i + 1 + (r1 ? 1 : 0) + (r2 ? 1 : 0));
        }
        LBAR();
        if (PROJ && i < 16) {
          if (side3) dma16(mk_rs(side3, 0x7FFFFFFFu), (unsigned)((lane >> 2) * 6144 + (lane & 3) * 8) * 2u, (unsigned)((64 * i + 16 * wl) * 6144) * 2u, lbase + G_K);
          if (64 * i < srows) {
            const __amdgpu_buffer_rsrc_t rsN = mk_rs(side, 0x7FFFFFFFu);
#pragma unroll
            for (int q = 0; q < 4; ++q) dma16(rsN, (unsigned)((lane >> 4) * sld + (lane & 15) * 8) * 2u, (unsigned)((64 * i + 16 * wl + 4 * q) * sld) * 2u, lbase + G_K + wl * 2048);
          }
        }
      }, Rp <= i);
      SEG(2);
      const unsigned rv = *(const volatile LAS unsigned*)(lds + G_CTL + 4);
      if (SG == SG_W3) {
        const float m = mean;
        *(LAS float*)(lds + G_K + 1024 + wl * 256 + lane * 4) = q2;
#pragma unroll
        for (int q = 0; q < 4; ++q) {
          const f32x4 cs = *(const LAS f32x4*)(lds + G_K + wl * 128 + hf * 64 + q * 16);
#pragma unroll
          for (int e = 0; e < 4; ++e) acc[4 * q + e] = fmaf(-m, cs[e], acc[4 * q + e]);
        }
      }
      put16(lad(l16, lbase + G_XCH + wl * 4096), acc);
      asm volatile("" ::: "memory");
      if (lane == 0) *(volatile LAS unsigned*)(lds + G_CTL + 16 + wl * 4) = (unsigned)i + 1u;
      const bool soon = Rp <= i;
      Rp = R; R = __builtin_amdgcn_readfirstlane(rv);
      SEG(3);
      if (R >= i + 2 && Rp < i + 2) { fetch(2 * i + 2); fetch(2 * i + 3); }
      if (R >= i + 3) {
        if (soon) { while (__builtin_amdgcn_readfirstlane(umin4v(*(const volatile LAS u32x4*)(lds + G_CTL + 16))) <= (unsigned)i) {} asm volatile("" ::: "memory"); }
        fetch(2 * i + 4);
      }
      SEG(5);
    }
  } else {
    bool pending = false;
    int Rp = 0;
    for (int i = 0; i < NBLK; ++i) {
      const int b = blk(i);
      BAR();
      SEG(0);
      const unsigned rv = *(const volatile LAS unsigned*)(lds + G_CTL + 4);
      u32x4 res[2];
      float isd;
      if (SG == SG_W2) {
#pragma unroll
        for (int u = 0; u < 2; ++u) {
          const int t = 32 * b - 2 + 16 * u + (lane >> 2);
          dma16x(rs, (t >= 0 && t < S_) ? (unsigned)(t * D_ + col0 + ((lane ^ (lane >> 3)) & 3) * 8) * 2u : 0xF0000000u, WS_H2, lbase + G_K + wl * 2048 + u * 1024);
        }
      }
      f32x16 acc;
      int R = 0;
      LAS unsigned char* xr = lad(l16, lbase + G_XCH + wl * 4096);
      f32x4 xa[4];
      unsigned xf;
      auto take = [&]() {
        asm volatile("" ::: "memory");
        xf = *(const volatile LAS unsigned*)(lds + G_CTL + 16 + wl * 4);
        asm volatile("" ::: "memory");
#pragma unroll
        for (int q = 0; q < 4; ++q) xa[q] = *(const LAS f32x4*)(xr + q * 1024);
        if (SG == SG_W3) isd = *(const LAS float*)(lds + G_K + 1024 + wl * 256 + lane * 4);
        asm volatile("" ::: "memory");
      };
      products(i, acc, [&](int k) {
      }, [&]() {
        take();
      }, [&]() {
        SEG(5);
        R = __builtin_amdgcn_readfirstlane(rv);
        if (R >= i + 3 || Rp >= i + 2) {
          WAIT_V(0);
          LBAR();
          if (pending && tid == 256) stx4(rs, 0, myword, (unsigned)blk(i - 1) + 1u);
        }
      }, false);
      const bool more = R >= i + 3 || Rp >= i + 2;
      Rp = R;
      SEG(1);
      while (__builtin_amdgcn_readfirstlane(xf) <= (unsigned)i) take();
#pragma unroll
      for (int q = 0; q < 16; ++q) { acc[q] += xa[q >> 2][q & 3]; if (SG == SG_W3) acc[q] *= isd; }
      SEG(2);
      const unsigned fo = (unsigned)((col0 >> 5) * 2048 + hf * 256), ro = (unsigned)((row >> 4) * 1024 + (row & 15) * 16);
      u32x4 v[2];
      if (SG == SG_W1 || SG == SG_W3) {
#pragma unroll
        for (int q = 0; q < 16; q += 2) {
          float g0 = gelu_tanh(acc[q]), g1 = gelu_tanh(acc[q + 1]);
          asm volatile("" : "+v"(g0), "+v"(g1));
          acc[q] = g0; acc[q + 1] = g1;
          piece(2 * i + 5, q >> 1, R >= i + 3 && i >= 2);
        }
      }
      widen32(acc, v);
      if (SG == SG_WO || SG == SG_W4) {
        LAS unsigned char* tw = lds + G_XCH + wl * 4096 + row * 64;
        *(LAS u32x4*)(tw + ((hf ^ (row >> 1)) & 3) * 16) = v[0]; *(LAS u32x4*)(tw + (((2 + hf) ^ (row >> 1)) & 3) * 16) = v[1];
        asm volatile("" ::: "memory");
        LAS unsigned char* tr = lds + G_XCH + wl * 4096 + (lane >> 2) * 64 + ((lane ^ (lane >> 3)) & 3) * 16;
        const unsigned so = (SG == SG_WO ? WS_P2 : WS_P8) + (unsigned)(b * 4 + kq) * 65536u, vo = (unsigned)((lane >> 2) * D_ + col0 + (lane & 3) * 8) * 2u;
        stx16(rs, vo, so, *(const LAS u32x4*)(tr)); stx16(rs, vo, so + 32768, *(const LAS u32x4*)(tr + 1024));
      } else if (PROJ) {
        const unsigned so = dst + (unsigned)(b * NH + cb) * 8192 + wl * 1024, vo = (unsigned)((row >> 4) * 4096 + hf * 256 + (row & 15) * 16);
        stx16(rs, vo, so, v[0]); stx16(rs, vo, so + 512, v[1]);
      } else if (SG == SG_W1) {
        const int grp = cb >> 1, sh = (grp & 1) ? -(1 + (grp >> 1)) : (1 + (grp >> 1));
        const int rho = 32 * b + row + sh + 2;
        const unsigned vo = (rho >= 2 && rho < S_ + 2) ? (unsigned)(rho >> 5) * 65536u + fo + (unsigned)((rho >> 4) & 1) * 1024u + (unsigned)(rho & 15) * 16u : 0xF0000000u;
        stx16(rs, vo, WS_H1S, v[0]); stx16(rs, vo, WS_H1S + 512, v[1]);
      } else if (SG == SG_W2) {
        if (!more) WAIT_V(0);
        res[0] = *(const LAS u32x4*)(lds + G_K + wl * 2048 + row * 64 + ((hf ^ (row >> 1)) & 3) * 16);
        res[1] = *(const LAS u32x4*)(lds + G_K + wl * 2048 + row * 64 + (((2 + hf) ^ (row >> 1)) & 3) * 16);
        float zs = 0.f, q2 = 0.f;
#pragma unroll
        for (int q = 0; q < 8; ++q) {
          const unsigned x = pack2(lo2f(res[q >> 2][q & 3]) + lo2f(v[q >> 2][q & 3]), hi2f(res[q >> 2][q & 3]) + hi2f(v[q >> 2][q & 3]));
          v[q >> 2][q & 3] = x; zs += lo2f(x) + hi2f(x); q2 = fmaf(lo2f(x), lo2f(x), fmaf(hi2f(x), hi2f(x), q2));
          asm volatile("" : "+v"(zs), "+v"(q2));
          piece(2 * i + 5, q, R >= i + 3 && i >= 2);
        }
        stx16(rs, fo + ro, WS_X2 + (unsigned)b * 65536u, v[0]); stx16(rs, fo + ro, WS_X2 + (unsigned)b * 65536u + 512, v[1]);
        const float mw = hsum(zs);
        q2 = hsum(q2);
        if (hf == 0) stx8(rs, (unsigned)((cb * 2 + (wl >> 1)) * 512 + row * 16 + (wl & 1) * 8), WS_ST5 + (unsigned)b * 8192, u32x2{f2u(mw), f2u(q2)});
      } else {
        stx16(rs, fo + ro, WS_U + (unsigned)b * 229376u, v[0]); stx16(rs, fo + ro, WS_U + (unsigned)b * 229376u + 512, v[1]);
      }
      if (PROJ && bstr == 3) {
#pragma unroll
        for (int q = 0; q < 2; ++q) {
          const int pc = (wk * 4 + wl) * 2 + q;
          if (pc < 660 && 3 * i + ZA + pc / 220 <= NSEG) stx16(rs, l16, X_REC + (unsigned)(3 * i + ZA) * RSTR + (unsigned)pc * 1024, u32x4{0u, 0u, 0u, 0u});
        }
      }
      SEG(3);
      pending = R >= i + 3 && i >= 2;
      if (!pending) { WAIT_V(0); announce(i); }
      if (R >= i + 3 && !(WEAVE && i >= 2)) fetch(2 * i + 5);
      if (!PROJ) WST(rs, wk, 16 + b);
      SEG(4);
    }
  }
  LBAR();
}

__device__ __forceinline__ void wait32(__amdgpu_buffer_rsrc_t rs, int first, int mine, int lane, unsigned need, unsigned& cap) {
  wait_ge(rs, 0, F_ST + (unsigned)(first + mine) * 128u, need, cap);
  wait_ge(rs, (unsigned)(lane & 31) * 128u, F_ST + (unsigned)first * 128u, need, cap);
}
__device__ __forceinline__ void sum_partials(__amdgpu_buffer_rsrc_t rs, unsigned l16, unsigned so, float (&a)[16]) {
  u32x4 pp[4][2];
#pragma unroll
  for (int k = 0; k < 4; ++k) { pp[k][0] = ldx16(rs, l16, so + k * 65536u); pp[k][1] = ldx16(rs, l16, so + k * 65536u + 1024); }
#pragma unroll
  for (int i = 0; i < 8; ++i) {
    f32x2 t = {0.f, 0.f};
#pragma unroll
    for (int k = 0; k < 4; ++k) t += f32x2{lo2f(pp[k][i >> 2][i & 3]), hi2f(pp[k][i >> 2][i & 3])};
    a[2 * i] = rbf(t[0]); a[2 * i + 1] = rbf(t[1]);
  }
}
__device__ __forceinline__ void put_row(__amdgpu_buffer_rsrc_t rs, unsigned l16, unsigned so, const float (&v)[16]) {
  stx16(rs, l16, so, u32x4{pack2(v[0], v[1]), pack2(v[2], v[3]), pack2(v[4], v[5]), pack2(v[6], v[7])});
  stx16(rs, l16, so + 1024, u32x4{pack2(v[8], v[9]), pack2(v[10], v[11]), pack2(v[12], v[13]), pack2(v[14], v[15])});
}
__device__ __forceinline__ void load_mod(__amdgpu_buffer_rsrc_t rs, int part, int lane, float (&v)[16], unsigned& cap) {
  for (;;) {
    asm volatile("" ::: "memory");
    u32x4 t[4];
#pragma unroll
    for (int i = 0; i < 4; ++i) t[i] = ldx16(rs, (unsigned)lane * 32, X_MOD + part * 4096 + (i >> 1) * 2048 + (i & 1) * 16);
    unsigned mn = 0xFFFFFFFFu;
#pragma unroll
    for (int i = 0; i < 16; ++i) { mn = min(mn, t[i >> 2][i & 3]); v[i] = hi2f(t[i >> 2][i & 3]); }
    if (__all(mn != 0u) || cap == 0u) break;
    cap = __builtin_amdgcn_readfirstlane(cap - 1u);
    __builtin_amdgcn_s_sleep(4);
  }
  cap = __builtin_amdgcn_readfirstlane(cap);
}
struct ModLoad { u32x4 w[8]; u16 c[8]; };
__device__ __forceinline__ void mod_load(const u16* wmod, const u16* cv, int grp, ModLoad& m) {
  const int kg = threadIdx.x >> 2, c8 = threadIdx.x & 3;
#pragma unroll
  for (int i = 0; i < 8; ++i) { m.w[i] = *(const u32x4*)(wmod + (size_t)(kg + 128 * i) * 6144 + grp * 32 + c8 * 8); m.c[i] = cv[kg + 128 * i]; }
}
__device__ __forceinline__ void mod_finish(__amdgpu_buffer_rsrc_t rs, LAS float* red, int grp, const ModLoad& m) {
  const int tid = threadIdx.x, kg = tid >> 2, c8 = tid & 3;
  float acc[8] = {0.f, 0.f, 0.f, 0.f, 0.f, 0.f, 0.f, 0.f};
#pragma unroll
  for (int i = 0; i < 8; ++i) {
    const float v = bf2f(m.c[i]), sc = rbf(v / (1.0f + __expf(-v)));
#pragma unroll
    for (int j = 0; j < 4; ++j) { acc[2 * j] += sc * lo2f(m.w[i][j]); acc[2 * j + 1] += sc * hi2f(m.w[i][j]); }
  }
#pragma unroll
  for (int j = 0; j < 8; ++j) red[kg * 33 + c8 * 8 + j] = acc[j];
  LBAR();
  float t = 0.f;
#pragma unroll
  for (int i = 0; i < 8; ++i) t += red[((tid >> 5) * 8 + i) * 33 + (tid & 31)];
  t = hsum(t);
  LBAR();
  if ((tid & 63) < 32) red[(tid >> 6) * 33 + (tid & 31)] = t;
  LBAR();
  if (tid < 32) {
    float u = 0.f;
#pragma unroll
    for (int i = 0; i < 8; ++i) u += red[i * 33 + tid];
    stx4(rs, (unsigned)tid * 4, X_MOD + (unsigned)grp * 128, f2u(rbf(u)) | 1u);
  }
}

__device__ __forceinline__ void stage_mid(__amdgpu_buffer_rsrc_t rs, const u16* x, int wk, int cu, unsigned& cap) {
  const int tid = threadIdx.x, lane = tid & 63, w = __builtin_amdgcn_readfirstlane(tid >> 6);
  const int id = w * 12 + cu, grp = id >> 5, rl = id & 31;
  const unsigned l16 = lane * 16;
  float ga[16], sc[16], sh[16];
  load_mod(rs, 2, lane, ga, cap); load_mod(rs, 4, lane, sc, cap); load_mod(rs, 3, lane, sh, cap);
#pragma unroll
  for (int i = 0; i < 16; ++i) sc[i] = rbf(1.0f + sc[i]);
  for (int seg = grp; seg < NSEG; seg += 3) {
    const int row = seg * 32 + rl;
    float v[16], a[16];
    load_row16(x + (size_t)row * D_, lane, v);
    wait32(rs, P_S2, rl, lane, (unsigned)seg + 1u, cap);
    sum_partials(rs, l16, WS_P2 + (unsigned)seg * 262144u + (unsigned)rl * 2048, a);
#pragma unroll
    for (int i = 0; i < 16; ++i) v[i] = rbf(v[i] + rbf(ga[i] * a[i]));
    put_row(rs, l16, WS_X1 + (unsigned)row * 2048, v);
    ln_row(v);
#pragma unroll
    for (int i = 0; i < 16; ++i) v[i] = rbf(rbf(v[i] * sc[i]) + sh[i]);
    put_row(rs, l16, WS_H2 + (unsigned)row * 2048, v);
    ln_row(v);
    put_row(rs, l16, WS_G + (unsigned)row * 2048, v);
    WAIT_V(0);
    if (lane == 0) stx4(rs, 0, F_ST + (unsigned)(P_S3 + grp * 32 + rl) * 128, (unsigned)seg + 1u);
    WST(rs, wk, 16 + seg);
  }
}

__device__ __forceinline__ void stage_out(__amdgpu_buffer_rsrc_t rs, u16* out, int wk, int cu, unsigned& cap) {
  const int tid = threadIdx.x, lane = tid & 63, w = __builtin_amdgcn_readfirstlane(tid >> 6);
  const int grp = w >> 2, rl = cu * 4 + (w & 3);
  const unsigned l16 = lane * 16;
  float gb[16];
  load_mod(rs, 5, lane, gb, cap);
  for (int b = grp; b < 65; b += 2) {
    const int t = 32 * b - 2 + rl;
    if (t < 0 || t >= S_) continue;
    wait_ge(rs, (unsigned)(lane & 7) * 128u, F_ST + P_S5 * 128u, (unsigned)b + 1u, cap);
    const unsigned xo = (unsigned)((lane >> 2) * 2048 + (lane & 3) * 256), xs = WS_X2 + (unsigned)b * 65536u + (unsigned)((rl >> 4) * 1024 + (rl & 15) * 16);
    const u32x4 x2a = ldx16(rs, xo, xs), x2b = ldx16(rs, xo, xs + 32768);
    const u32x4 x1a = ldx16(rs, l16, WS_X1 + (unsigned)t * 2048), x1b = ldx16(rs, l16, WS_X1 + (unsigned)t * 2048 + 1024);
    wait32(rs, P_S8, rl, lane, (unsigned)b + 1u, cap);
    float a[16];
    sum_partials(rs, l16, WS_P8 + (unsigned)b * 262144u + (unsigned)rl * 2048, a);
#pragma unroll
    for (int i = 0; i < 4; ++i) {
      a[2 * i] = lo2f(x1a[i]) + rbf(gb[2 * i] * rbf(lo2f(x2a[i]) + a[2 * i])); a[2 * i + 1] = hi2f(x1a[i]) + rbf(gb[2 * i + 1] * rbf(hi2f(x2a[i]) + a[2 * i + 1]));
      a[8 + 2 * i] = lo2f(x1b[i]) + rbf(gb[8 + 2 * i] * rbf(lo2f(x2b[i]) + a[8 + 2 * i])); a[9 + 2 * i] = hi2f(x1b[i]) + rbf(gb[9 + 2 * i] * rbf(hi2f(x2b[i]) + a[9 + 2 * i]));
    }
    store_row16(out + (size_t)t * D_, lane, a);
    WST(rs, wk, 16 + b);
  }
}

__device__ __forceinline__ void worker(const MainP& p, LAS unsigned char* lds, int xw, int j) {
  const int tid = threadIdx.x, lane = tid & 63, w = __builtin_amdgcn_readfirstlane(tid >> 6);
  const __amdgpu_buffer_rsrc_t rs = mk_rs(p.ws, WS_END);
  unsigned cap = SPIN_CAP;
  const int wk = xw * 32 + j;
  WST(rs, wk, 0);
  {
    const int c = j * 5 + xw, r0 = c * 8 + 2 * (w & 3);
    const bool two = r0 + 8 * NLN < S_;
    float v[4][16];
    ModLoad m;
    if (c >= NLN - 64) mod_load(p.wmod, p.c, c - (NLN - 64), m);
    if (w < 4) {
#pragma unroll
      for (int q = 0; q < 4; ++q) load_row16(p.x + (size_t)(r0 + (q & 1) + (two ? (q >> 1) * 8 * NLN : 0)) * D_, lane, v[q]);
    }
    if (c >= NLN - 64) mod_finish(rs, (LAS float*)(lds + G_XCH), c - (NLN - 64), m);
    WST(rs, wk, 5);
    if (w >= 4) {
      if (c < 128) {
        const __amdgpu_buffer_rsrc_t rsO = mk_rs(p.Wo, D_ * HD * 2);
#pragma unroll
        for (int q = 0; q < 16; ++q) dma16(rsO, (unsigned)lane * 16, (unsigned)((q * 128 + c) * 4 + (w & 3)) * 1024u, (unsigned)(size_t)lds + G_K + (w & 3) * 1024);
      }
      const __amdgpu_buffer_rsrc_t rsW = mk_rs(xw < 3 ? p.Wk : p.Wq, D_ * HD * 2);
      const int ub = lane >> 3;
      const unsigned vB = (unsigned)(((ub & 3) + 8 * (ub >> 2)) * HD + 16 * (((lane >> 1) & 3) ^ (ub >> 1)) + 8 * (lane & 1)) * 2u, lbase = (unsigned)(size_t)lds;
#pragma unroll
      for (int q = 0; q < 16; ++q) {
        const int wq = (w & 3) + 4 * (q >> 3);
        const unsigned so = (unsigned)((32 * (wq >> 2) + 16 * ((wq >> 1) & 1) + 4 * (wq & 1) + (((q >> 2) & 1) * 8 + (q & 3)) * 64) * HD + j * 128) * 2u;
        const unsigned d = lbase + ((q >> 2) & 1) * 65536 + (q & 3) * 16384 + wq * 1024;
        dma16(rsW, vB, so, d); dma16(rsW, vB, so + 128, d + 8192);
      }
    } else {
#pragma unroll
      for (int q = 0; q < 4; ++q) ln_row(v[q]);
      float sc[16], sh[16];
      load_mod(rs, 1, lane, sc, cap); load_mod(rs, 0, lane, sh, cap);
      WST(rs, wk, 6);
#pragma unroll
      for (int i = 0; i < 16; ++i) sc[i] = rbf(1.0f + sc[i]);
#pragma unroll
      for (int q = 0; q < 4; ++q) {
#pragma unroll
        for (int i = 0; i < 16; ++i) v[q][i] = rbf(rbf(v[q][i] * sc[i]) + sh[i]);
        if (q < 2 || two) put_row(rs, (unsigned)lane * 16, WS_H + (unsigned)(r0 + (q & 1) + (q >> 1) * 8 * NLN) * 2048, v[q]);
      }
      WAIT_V(0);
    }
    BAR();
    if (tid == 0) stx4(rs, 0, F_LN + c * 128, 1u);
    WST(rs, wk, 1);
  }
  {
    const u16* nw = xw == 0 ? p.Wo + (size_t)(j >> 3) * 1024 * D_ + (j & 7) * 128 : xw == 3 ? p.w4 + (size_t)(j >> 3) * 896 * D_ + (j & 7) * 128
                  : xw == 2 ? p.w3 + j * 128 : (j < 8 ? p.w1 : p.w2) + (j & 7) * 128;
    const int nrows = (xw == 4 || (xw == 1 && j >= 16) || (xw == 2 && j >= 28)) ? 0 : xw == 3 ? 896 : 1024;
    const int mg = xw < 3 ? 64 + wk : 160 + j;
    stage_gemm<SG_PJ>(lds, rs, xw < 3 ? p.Wk : p.Wq, wk, xw < 3 ? xw : xw == 3 ? QB0 : 0, j, P_PJ + min(xw, 3) * 32 + j + (xw == 4 ? 400 : 0), cap, xw < 3 ? WS_K : WS_Q, xw < 3 ? 3 : 1,
                      xw == 0 ? 22 : xw < 3 ? 21 : xw == 3 ? NSEG - QB0 : QB0, nw, nrows, xw == 2 ? FF : D_, xw == 3 ? nullptr : p.wmod + mg * 32);
    WST(rs, wk, 3);
  }
  typedef __attribute__((address_space(4))) const MainP* KArg;
  KArg kp = (KArg)__builtin_amdgcn_kernarg_segment_ptr();
  asm volatile("" : "+s"(kp));
  if (xw != 3) { const int mg = xw < 3 ? 64 + wk : 160 + j; ModLoad m; mod_load(kp->wmod, kp->c, mg, m); mod_finish(rs, (LAS float*)(lds + 65536), mg, m); }
  if (xw == 0) stage_gemm<SG_WO>(lds, rs, kp->Wo, wk, j >> 3, j & 7, P_S2 + j, cap);
  else if (xw == 3) stage_gemm<SG_W4>(lds, rs, kp->w4, wk, j >> 3, j & 7, P_S8 + j, cap);
  else if (xw == 4) {
    unsigned chs = 0u;
    for (int sg = w >> 1; sg < NSEG; sg += 4) { attend(rs, sg, j, w & 1, lane, chs, cap); WST(rs, wk, 16 + sg); }
  }
  else if (j >= 28) stage_out(rs, kp->out, wk, (xw - 1) * 4 + j - 28, cap);
  else if (xw == 2) stage_gemm<SG_W3>(lds, rs, kp->w3, wk, 0, j, P_S7 + j, cap);
  else if (j < 8) stage_gemm<SG_W1>(lds, rs, kp->w1, wk, 0, j, P_S4 + j, cap);
  else if (j < 16) stage_gemm<SG_W2>(lds, rs, kp->w2, wk, 0, j - 8, P_S5 + j - 8, cap);
  else stage_mid(rs, kp->x, wk, j - 16, cap);
  if (cap == 0u && lane == 0) stx4(rs, 0, X_CTL, 1u);
}

#define ST_DECL
#define ST0()
#define ST(i)
#define ST_FLUSH()
constexpr int R_W = 0, R_MEM = 131072, R_QK = R_MEM + 16384, R_KT = R_QK + 8192, R_RED = R_MEM + 16384, R_LDS = 163840;
__device__ __forceinline__ void bar_lo(int w) { asm volatile("s_cmp_ge_u32 %0, 4\n\ts_cbranch_scc1 1f\n\ts_barrier\n\t1:" :: "s"(w) : "scc", "memory"); }
__device__ __forceinline__ void bar_hi(int w) { asm volatile("s_cmp_lt_u32 %0, 4\n\ts_cbranch_scc1 1f\n\ts_setprio 3\n\ts_barrier\n\t1:" :: "s"(w) : "scc", "memory"); }
#define ISSUE(R) asm volatile("buffer_load_dwordx4 v[" R "], %[vo], %[rs], %[so] offen sc1" : "=&{v[" R "]}"(a) : [vo] "v"(vo), [rs] "s"(rs), [so] "s"(so) : "memory")
template <int I> __device__ __forceinline__ void issue(__amdgpu_buffer_rsrc_t rs, unsigned vo, unsigned so, u32x4& a) {
  if (I == 0) ISSUE("240:243"); else if (I == 1) ISSUE("244:247"); else if (I == 2) ISSUE("248:251"); else ISSUE("252:255");
}
#define ARRIVE(R0, R1, R2, R3, CNT) \
  asm volatile("s_waitcnt vmcnt(" #CNT ")\n\t" \
               "1:\n\t" \
               "v_min3_u32 %[t], v" #R0 ", v" #R1 ", v" #R2 "\n\t" \
               "v_min_u32 %[t], %[t], v" #R3 "\n\t" \
               "v_cmp_eq_u32 vcc, 0, %[t]\n\t" \
               "s_cbranch_vccz 2f\n\t" \
               "s_mov_b64 exec, vcc\n\t" \
               "buffer_load_dwordx4 v[" #R0 ":" #R3 "], %[vo], %[rs], %[so] offen sc1\n\t" \
               "s_mov_b64 exec, -1\n\t" \
               "s_waitcnt vmcnt(0)\n\t" \
               "s_sub_u32 %[cap], %[cap], 1\n\t" \
               "s_cbranch_scc0 1b\n\t" \
               "s_mov_b32 %[cap], 0\n\t" \
               "2:" \
               : "+{v[" #R0 ":" #R3 "]}"(a), [t] "=&v"(t), [cap] "+s"(cap) : [vo] "v"(vo), [rs] "s"(rs), [so] "s"(so) : "vcc", "scc", "memory")
template <int I> __device__ __forceinline__ void arrive(__amdgpu_buffer_rsrc_t rs, unsigned vo, unsigned so, u32x4& a, unsigned& cap) {
  unsigned t;
  if (I == 0) ARRIVE(240, 241, 242, 243, 3);
  else if (I == 1) ARRIVE(244, 245, 246, 247, 2);
  else if (I == 2) ARRIVE(248, 249, 250, 251, 1);
  else ARRIVE(252, 253, 254, 255, 0);
}
__device__ __forceinline__ unsigned half_of(unsigned v) { unsigned r; asm volatile("v_lshrrev_b32 %0, 1, %1" : "=v"(r) : "v"(v)); return r; }
__device__ __forceinline__ void issue2(__amdgpu_buffer_rsrc_t rs, unsigned l8, unsigned so, int w, u32x4& a) {
  asm volatile("s_cmp_ge_u32 %[w], 4\n\ts_cbranch_scc1 1f\n\tv_lshrrev_b32 v255, 1, %[l8]\n\t"
               "buffer_load_dwordx2 v[252:253], v255, %[rs], %[so] offen sc1\n\tbuffer_load_dwordx2 v[254:255], v255, %[rs], %[so] offen offset:512 sc1\n\t1:"
               : "=&{v[252:255]}"(a) : [l8] "v"(l8), [rs] "s"(rs), [so] "s"(so), [w] "s"(w) : "scc", "memory");
}
__device__ __forceinline__ void arrive2(__amdgpu_buffer_rsrc_t rs, unsigned l8, unsigned so, u32x4& a, unsigned& cap) {
  unsigned t;
  asm volatile("s_waitcnt vmcnt(0)\n\t"
               "1:\n\t"
               "v_min3_u32 %[t], v252, v253, v254\n\t"
               "v_min_u32 %[t], %[t], v255\n\t"
               "v_cmp_eq_u32 vcc, 0, %[t]\n\t"
               "s_cbranch_vccz 2f\n\t"
               "v_lshrrev_b32 %[t], 1, %[l8]\n\t"
               "buffer_load_dwordx2 v[252:253], %[t], %[rs], %[so] offen sc1\n\t"
               "buffer_load_dwordx2 v[254:255], %[t], %[rs], %[so] offen offset:512 sc1\n\t"
               "s_waitcnt vmcnt(0)\n\t"
               "s_sub_u32 %[cap], %[cap], 1\n\t"
               "s_cbranch_scc0 1b\n\t"
               "s_mov_b32 %[cap], 0\n\t"
               "2:"
               : "+{v[252:255]}"(a), [t] "=&v"(t), [cap] "+s"(cap) : [l8] "v"(l8), [rs] "s"(rs), [so] "s"(so) : "vcc", "scc", "memory");
}
__device__ __forceinline__ unsigned ror8(unsigned v) { return __builtin_amdgcn_update_dpp(0u, v, 0x128, 0xF, 0xF, true); }
template <class F>
__device__ __forceinline__ void gather_mem(__amdgpu_buffer_rsrc_t rs, unsigned lbase, int s, int w, unsigned l16, unsigned& cap, F&& meanwhile) {
  const unsigned so = X_MEM + s * RSTR + w * 2048;
  u32x4 a0, a1;
  issue<2>(rs, l16, so, a0); issue<3>(rs, l16, so + 1024, a1);
  meanwhile();
  LAS unsigned char* d = lad(l16, lbase + R_MEM + w * 2048);
  arrive<2>(rs, l16, so, a0, cap);
  *(LAS u32x4*)(d) = a0;
  arrive<3>(rs, l16, so + 1024, a1, cap);
  *(LAS u32x4*)(d + 1024) = a1;
}
__device__ __forceinline__ unsigned mem_off(int lane) { return (unsigned)((lane & 1) * 512 + (((lane >> 1) ^ ((lane & 1) * 16)) * 8)); }
__device__ __forceinline__ bf16x8 mem_frag(LAS unsigned char* mp, int chunk) { return cat8(trread(mp + chunk * 1024), trread(mp + chunk * 1024 + 256)); }
__device__ __forceinline__ bf16x16 w_frag(LAS unsigned char* p) {
  return cat16(cat8(trread(p), trread(p + 2048)), cat8(trread(p + 4096), trread(p + 6144)));
}
__device__ __forceinline__ bf16x16 l_frag(LAS unsigned char* p, int i) { return cat16(*(const LAS bf16x8*)(p + i * 2048), *(const LAS bf16x8*)(p + i * 2048 + 1024)); }
__device__ __forceinline__ void make_resident(LAS unsigned char* wb, unsigned lo, unsigned l16) {
#pragma unroll
  for (int c = 0; c < 2; ++c) {
    bf16x16 f[4];
#pragma unroll
    for (int t = 0; t < 4; ++t) f[t] = w_frag(wb + c * 8192 + (lo ^ (t << 5)));
    WAIT_L0(); SCHED();
#pragma unroll
    for (int t = 0; t < 4; ++t) {
      *(LAS bf16x8*)(wb + (c * 4 + t) * 2048 + l16) = __builtin_shufflevector(f[t], f[t], 0, 1, 2, 3, 4, 5, 6, 7);
      *(LAS bf16x8*)(wb + (c * 4 + t) * 2048 + 1024 + l16) = __builtin_shufflevector(f[t], f[t], 8, 9, 10, 11, 12, 13, 14, 15);
    }
    WAIT_L0(); SCHED();
  }
}
__device__ __forceinline__ f32x4 fold(f32x4 a, f32x4 b) {
  f32x4 r;
#pragma unroll
  for (int i = 0; i < 4; ++i) {
    const auto t = __builtin_amdgcn_permlane32_swap(f2u(a[i]), f2u(b[i]), false, false);
    r[i] = u2f(t[0]) + u2f(t[1]);
  }
  return r;
}

struct Sched { unsigned tau, prev; int ea, eb, d1; bool moved; };
template <int CTRL> __device__ __forceinline__ int dpp_max(int v) { return max(v, (int)__builtin_amdgcn_update_dpp((unsigned)v, (unsigned)v, CTRL, 0xF, 0xF, false)); }
__device__ __forceinline__ int wave_max(int v) {
  v = dpp_max<0xB1>(v); v = dpp_max<0x4E>(v); v = dpp_max<0x141>(v); v = dpp_max<0x140>(v);
  return max(max(__builtin_amdgcn_readlane(v, 0), __builtin_amdgcn_readlane(v, 16)), max(__builtin_amdgcn_readlane(v, 32), __builtin_amdgcn_readlane(v, 48)));
}
__device__ __forceinline__ void times_issue(__amdgpu_buffer_rsrc_t rs, unsigned l16, int s, unsigned& xb, unsigned& xa) {
  asm volatile("v_lshlrev_b32 %0, 3, %2\n\tv_and_b32 %1, 0xF80, %0\n\tbuffer_load_dword %0, %0, %3, %4 offen sc1\n\tbuffer_load_dword %1, %1, %3, %5 offen sc1"
               : "=&v"(xb), "=&v"(xa) : "v"(l16), "s"(rs), "s"(X_TS + (unsigned)s * RSTR), "s"(X_TS + (unsigned)s * RSTR + 8192) : "memory");
}
__device__ __forceinline__ void retime(__amdgpu_buffer_rsrc_t rs, unsigned l16, int s, unsigned xb, unsigned xa, Sched& t, const MainP& p, unsigned& cap) {
  if (s > 0) {
    while (!__all(min(xa, xb) != 0u) && cap != 0u) {
      times_issue(rs, l16, s, xb, xa);
      asm volatile("s_waitcnt vmcnt(0)" : "+v"(xb), "+v"(xa) :: "memory");
      cap = __builtin_amdgcn_readfirstlane(cap - 1u);
    }
    cap = __builtin_amdgcn_readfirstlane(cap);
    const int tb = wave_max((int)(xb - t.prev)), ta = wave_max((int)(xa - t.prev));
    const int late = tb + p.d3 - (int)(t.tau - t.prev);
    if (s > 2) {
      const int up = s < 5 ? 80 : 8, dn = s < 10 ? 40 : 8;
      if (!t.moved) t.ea = max(t.ea - dn, min(ta * 8, t.ea + up));
      if (ta + p.d3 <= t.d1) t.eb = max(t.eb - dn, min((tb - t.d1) * 8, t.eb + up));
    }
    t.moved = late > 0;
    if (late > 0) t.tau += late + min(p.d4, 8 * late);
  }
  t.d1 = (t.ea >> 3) + p.d1;
}
__device__ __forceinline__ void advance(Sched& t, const MainP& p) { t.prev = t.tau; t.tau += t.d1 + (t.eb >> 3) + p.d2; }

__device__ __forceinline__ void role_a(const MainP& p, LAS unsigned char* lds, int h) {
  const int tid = threadIdx.x, lane = tid & 63, w = __builtin_amdgcn_readfirstlane(tid >> 6);
  const int cg = w & 3, kh = w >> 2;
  const __amdgpu_buffer_rsrc_t rs = mk_rs(p.ws, WS_END);
  const unsigned lbase = (unsigned)(size_t)lds;
  unsigned l16 = lane * 16;
  asm volatile("" : "+v"(l16));
  const int sidx = (lane & 8) ? 0xEEEE : 0x4444;
  bf16x16 W[24];
  unsigned qoff;
  {
    const int fr = lane & 15, g = lane >> 4, tu = 4 * (g & 1) + (fr >> 2), ub = lane >> 3;
    const unsigned lo = (g >> 1) * 1024 + tu * 128 + ((tu >> 1) << 5) + (fr & 3) * 8;
    qoff = R_QK + ((fr >> 1) & 1) * 512 + (g >> 1) * 256 + (fr & 1) * 128 + (8 * (g & 1) + (fr >> 2)) * 8;
    const __amdgpu_buffer_rsrc_t rw = mk_rs(cg < 2 ? p.Wq : p.Wk, D_ * HD * 2);
    const unsigned vo = (unsigned)(((ub & 3) + 8 * (ub >> 2)) * HD + 16 * (((lane >> 1) & 3) ^ (ub >> 1)) + 8 * (lane & 1)) * 2u;
    const unsigned so = (unsigned)(kh * 512 * HD + h * DH + (cg & 1) * 64) * 2u;
    auto stage = [&](int c) {
#pragma unroll
      for (int rho = 0; rho < 8; ++rho) dma16(rw, vo, so + (unsigned)((c * 64 + 32 * (rho >> 2) + 16 * (rho & 1) + 4 * ((rho >> 1) & 1)) * HD) * 2u, lbase + R_W + w * 16384 + (c & 1) * 8192 + rho * 1024);
    };
    stage(0); stage(1);
#pragma unroll
    for (int c = 0; c < 6; ++c) {
      WAIT_V(8);
#pragma unroll
      for (int t = 0; t < 4; ++t) W[4 * c + t] = w_frag(lds + R_W + w * 16384 + (c & 1) * 8192 + (lo ^ (t << 5)));
      WAIT_L0(); SCHED();
      stage(c + 2);
    }
    WAIT_V(0);
    make_resident(lds + R_W + w * 16384, lo, l16);
  }
  WST(rs, 160 + (h >> 1), 4);
  unsigned cap = SPIN_CAP;
  wait_ge(rs, 0, F_ST + (unsigned)(P_PJ + h) * 128, 1u, cap);
  dma16x(rs, l16, WS_K + h * 8192 + w * 1024, lbase + R_KT + w * 1024);
  Sched tt; tt.tau = rt32() - 100000; tt.prev = tt.tau; tt.ea = 195 * 8; tt.eb = 118 * 8; tt.d1 = 0; tt.moved = true;
  const int cu_ = h; (void)cu_;
  ST_DECL
  for (int s = 0; s < NSEG; ++s) {
    LAS unsigned char* lp = lad(l16, lbase + R_W + w * 16384);
    bf16x16 L[2];
    L[0] = l_frag(lp, 0); L[1] = l_frag(lp, 1);
    unsigned lz = l16;
    asm volatile("" : "+v"(lz));
    LAS unsigned char* mp = lad(mem_off((int)(lz >> 4)), lbase + R_MEM + kh * 8192);
    SCHED();
    if (w == 7) wait_until(tt.tau);
    BAR();
    ST0();
    gather_mem(rs, lbase, s, (w + h) & 7, l16, cap, []() {});
    ST(1);
    LBAR();
    ST(2);
    f32x4 c[4];
#pragma unroll
    for (int t = 0; t < 4; ++t) c[t] = f32x4{0.f, 0.f, 0.f, 0.f};
    bf16x8 al, af[2];
    u32x4 vt0, vt1;
    u32x4 vmv;
    const unsigned vso = X_VM + (unsigned)s * RSTR + h * 4096 + cg * 1024;
    al = mem_frag(mp, 6); af[0] = mem_frag(mp, 0); af[1] = mem_frag(mp, 1);
#pragma unroll
    for (int i = 0; i < 8; ++i) {
      c[i & 3] = smf(al, L[i & 1], c[i & 3], sidx);
      SCHED();
      if (i == 3) al = mem_frag(mp, 7);
      if (i < 6) L[i & 1] = l_frag(lp, i + 2);
      if (i == 6) {
        const unsigned vo = WS_VT + (unsigned)(s * NH + h) * 8192 + cg * 2048;
        asm volatile("s_cmp_ge_u32 %5, 4\n\ts_cbranch_scc1 1f\n\t"
                     "buffer_load_dwordx4 %0, %2, %3, %4 offen sc1\n\tbuffer_load_dwordx4 %1, %2, %3, %4 offen offset:1024 sc1\n\t1:"
                     : "=&v"(vt0), "=&v"(vt1) : "v"(l16), "s"(rs), "s"(vo), "s"(w) : "scc", "memory");
      }
      SCHED();
#pragma unroll
      for (int r = 3 * i; r < 3 * i + 3; ++r) {
        c[(r + 1) & 3] = smf(af[(r >> 2) & 1], W[(r & ~3) + ((r + 1) & 3)], c[(r + 1) & 3], sidx);
        SCHED();
        if ((r & 3) == 3 && (r >> 2) + 2 < 6) af[(r >> 2) & 1] = mem_frag(mp, (r >> 2) + 2);
        SCHED();
      }
    }
    {
      LAS unsigned char* qw = lad(half_of(l16), lbase + R_QK + cg * 2048 + kh * 512);
      const f32x4 f0 = fold(c[0], c[1]);
      *(LAS u32x2*)(qw) = u32x2{pack2(f0[0], f0[1]), pack2(f0[2], f0[3])};
      const f32x4 f1 = fold(c[2], c[3]);
      *(LAS u32x2*)(qw + 1024) = u32x2{pack2(f1[0], f1[1]), pack2(f1[2], f1[3])};
    }
    ST(3);
    if (w < 4) {
      bf16x8 k1[4], k2[4];
      LAS unsigned char* kb = lad(l16, lbase + R_KT);
#pragma unroll
      for (int ks = 0; ks < 2; ++ks) { k1[ks] = *(const LAS bf16x8*)(kb + ks * 1024); k2[ks] = *(const LAS bf16x8*)(kb + 4096 + ks * 1024); }
      unsigned ov;
      {
        unsigned ot;
        asm volatile("v_and_b32 %0, 0x30, %2\n\tv_lshrrev_b32 %1, 2, %2\n\tv_and_b32 %1, 0xC0, %1\n\tv_or_b32 %0, %0, %1\n\t"
                     "v_and_b32 %1, 0x40, %2\n\tv_lshl_or_b32 %0, %1, 9, %0\n\tv_and_b32 %1, 0x80, %2\n\tv_lshl_or_b32 %0, %1, 24, %0"
                     : "=&v"(ov), "=&v"(ot) : "v"(l16));
      }
      LBAR();
      ST(4);
      issue2(rs, l16, vso, w, vmv);
      f32x4 t0 = {0.f, 0.f, 0.f, 0.f}, t1 = t0, t2 = t0;
      LAS unsigned char* qb = lad(qoff, lbase);
      bf16x8 qf[4], kf[4];
#pragma unroll
      for (int ks = 0; ks < 2; ++ks) {
        qf[ks] = cat8(trread(qb + ks * 1024), trread(qb + ks * 1024 + 32));
        kf[ks] = cat8(trread(qb + 4096 + ks * 1024), trread(qb + 4096 + ks * 1024 + 32));
      }
      SCHED();
#pragma unroll
      for (int ks = 0; ks < 4; ++ks) {
        t1 = mfma32(k1[ks], qf[ks], t1); t2 = mfma32(k2[ks], qf[ks], t2); t0 = mfma32(kf[ks], qf[ks], t0);
        SCHED();
        if (ks < 2) {
          qf[ks + 2] = cat8(trread(qb + (ks + 2) * 1024), trread(qb + (ks + 2) * 1024 + 32));
          k1[ks + 2] = *(const LAS bf16x8*)(kb + (ks + 2) * 1024); k2[ks + 2] = *(const LAS bf16x8*)(kb + 4096 + (ks + 2) * 1024);
          kf[ks + 2] = cat8(trread(qb + 4096 + (ks + 2) * 1024), trread(qb + 4096 + (ks + 2) * 1024 + 32));
        }
        SCHED();
      }
      ST(5);
      asm volatile("s_waitcnt vmcnt(2)" : "+v"(vt0), "+v"(vt1) :: "memory");
      {
        const float sl = 0.08838834764831845f * 1.4426950408889634f;
        float z[4] = {t1[0], t1[1], t1[2], t1[3]}, y = t0[0], mx;
        {
          float u1 = t0[1], u2 = t0[2], u3 = t0[3];
#define M8(Z, B) "v_add_f32_dpp " Z ", " Z ", " Z " row_ror:8 row_mask:0xf bank_mask:0x3\n\tv_add_f32_dpp " Z ", " B ", " B " row_ror:8 row_mask:0xf bank_mask:0xc\n\t"
          asm volatile("s_nop 7\n\t"
              "v_permlane32_swap_b32 %4, %6\n\tv_permlane32_swap_b32 %5, %7\n\t"
              M8("%0", "%9") "v_add_f32 %4, %4, %6\n\tv_add_f32 %5, %5, %7\n\t" M8("%1", "%10") M8("%2", "%11") M8("%4", "%5") M8("%3", "%12")
              "v_max3_f32 %8, %0, %1, %2\n\tv_max3_f32 %8, %8, %4, %3\n\ts_nop 1\n\t"
              "v_max_f32_dpp %8, %8, %8 row_ror:8 row_mask:0xf bank_mask:0xf\n\tv_mov_b32 %5, %8\n\ts_nop 1\n\t"
              "v_permlane16_swap_b32 %8, %5\n\tv_max_f32 %8, %8, %5\n\tv_mov_b32 %5, %8\n\ts_nop 1\n\t"
              "v_permlane32_swap_b32 %8, %5\n\tv_max_f32 %8, %8, %5"
              : "+v"(z[0]), "+v"(z[1]), "+v"(z[2]), "+v"(z[3]), "+v"(y), "+v"(u1), "+v"(u2), "+v"(u3), "=&v"(mx) : "v"(t2[0]), "v"(t2[1]), "v"(t2[2]), "v"(t2[3]));
#undef M8
        }
        const float nm = -mx * sl;
        y = __builtin_amdgcn_exp2f(fmaf(y, sl, nm));
#pragma unroll
        for (int r = 0; r < 4; ++r) z[r] = __builtin_amdgcn_exp2f(fmaf(z[r], sl, nm));
        const unsigned pm1 = pack2(y, u2f(ror8(f2u(y))));
        const auto pms = __builtin_amdgcn_permlane32_swap(pm1, pm1, false, false);
        const u32x2 pm = {pms[0], pms[1]};
        const unsigned p0 = pack2(z[0], z[1]), p1 = pack2(z[2], z[3]);
        const u32x4 pt = {p0, p1, ror8(p0), ror8(p1)};
        f32x4 o0 = mfma32(__builtin_bit_cast(bf16x8, vt0), __builtin_bit_cast(bf16x8, pt), f32x4{0.f, 0.f, 0.f, 0.f});
        f32x4 o1 = mfma32(__builtin_bit_cast(bf16x8, vt1), __builtin_bit_cast(bf16x8, pt), f32x4{0.f, 0.f, 0.f, 0.f});
        float inv = __builtin_amdgcn_rcpf(gsum(dpp_add<0x128>(((z[0] + z[1]) + (z[2] + z[3])) + y)));
        asm volatile("" : "+v"(inv));
        arrive2(rs, l16, vso, vmv, cap);
        const f32x4 n0 = mfma16n(u32x2{vmv[0], vmv[1]}, pm, f32x4{0.f, 0.f, 0.f, 0.f}), n1 = mfma16n(u32x2{vmv[2], vmv[3]}, pm, f32x4{0.f, 0.f, 0.f, 0.f});
        SCHED();
        o0 *= inv; o1 *= inv;
        {
          const u32x4 v = {nz(pack2(fmaf(n0[0], inv, o0[0]), fmaf(n0[1], inv, o0[1]))), nz(pack2(fmaf(n0[2], inv, o0[2]), fmaf(n0[3], inv, o0[3]))),
                           nz(pack2(fmaf(n1[0], inv, o1[0]), fmaf(n1[1], inv, o1[1]))), nz(pack2(fmaf(n1[2], inv, o1[2]), fmaf(n1[3], inv, o1[3])))};
          stx16(rs, ov, X_OX + s * RSTR + h * 1024 + w * 256, v);
        }
        if (w == 3) { const unsigned t = max(rt32(), 1u); if (l16 == 0) stx4(rs, 0, X_TS + (unsigned)(s + 1) * RSTR + (unsigned)(64 + h) * 128, t); }
        ST(8);
      }
    } else {
      LBAR();
      LAS unsigned char* qb = lad(qoff, lbase + 4096 + (w - 4) * 1024);
      u32x4 kf = __builtin_bit_cast(u32x4, cat8(trread(qb), trread(qb + 32)));
#pragma unroll
      for (int i = 0; i < 4; ++i) kf[i] = pack2(dpp_add<0x128>(lo2f(kf[i])), dpp_add<0x128>(hi2f(kf[i])));
      stx16(rs, l16, WS_KM + (unsigned)(s * NH + h) * 4096 + (w - 4) * 1024, kf);
      WAIT_V(0);
    }
    LBAR();
    if (w == 4 && l16 == 0) stx4(rs, 0, F_CH + h * 128, (unsigned)s + 1u);
    ST(9);
    if (w == 7) {
      unsigned xb, xa;
      times_issue(rs, l16, s, xb, xa);
      asm volatile("s_waitcnt vmcnt(0)" : "+v"(xb), "+v"(xa) :: "memory");
      retime(rs, l16, s, xb, xa, tt, p, cap); advance(tt, p);
    }
    if (s + 1 < NSEG) {
      unsigned fo;
      asm volatile("v_mov_b32 %0, 0" : "=v"(fo));
      wait_ge(rs, fo, F_ST + (unsigned)(P_PJ + ((s + 1) % 3) * 32 + h) * 128, (unsigned)s + 2u, cap);
      dma16x(rs, l16, WS_K + (unsigned)((s + 1) * NH + h) * 8192 + w * 1024, lbase + R_KT + w * 1024);
    }
  }
  ST_FLUSH();
  if (cap == 0u && l16 == 0) stx4(rs, 0, X_CTL, 1u);
}

__device__ __forceinline__ void role_vb(const MainP& p, LAS unsigned char* lds, int rg, int cb) {
  const int tid = threadIdx.x, lane = tid & 63, w = __builtin_amdgcn_readfirstlane(tid >> 6);
  const int kh = w >> 2, wr = (w + cb) & 7;
  const __amdgpu_buffer_rsrc_t rs = mk_rs(p.ws, WS_END);
  const unsigned lbase = (unsigned)(size_t)lds;
  unsigned l16 = lane * 16, moff = mem_off(lane), roff, soff = (unsigned)((lane >> 5) * 256 + (lane & 31) * 4);
  const int sidx = (lane & 8) ? 0xEEEE : 0x4444;
  bf16x16 WV[8], WB[16];
  {
    const int fr = lane & 15, g = lane >> 4, tu = 4 * (g & 1) + (fr >> 2), ub = lane >> 3;
    { const int sl = mem_slot(16 * (g >> 1) + fr); roff = R_RED + w * 512 + ((sl >> 5) * 32 + (sl & 15) * 2 + (g & 1)) * 8; }
    const __amdgpu_buffer_rsrc_t rv = mk_rs(p.Wv, D_ * HD * 2), ro = mk_rs(p.Wo, D_ * HD * 2);
    const unsigned vv = (unsigned)(((ub & 3) + 8 * (ub >> 2)) * HD + 16 * (((lane >> 1) & 3) ^ (ub >> 1)) + 8 * (lane & 1)) * 2u;
    const unsigned sv = (unsigned)((32 * (w >> 2) + 16 * (w & 1) + 4 * ((w >> 1) & 1)) * HD + cb * DH + rg * 64) * 2u;
#pragma unroll
    for (int c = 0; c < 16; ++c) dma16(rv, vv, sv + (unsigned)c * 64 * HD * 2, lbase + R_W + c * 8192 + w * 1024);
    WAIT_V(0); BAR();
    const unsigned lo = (g >> 1) * 1024 + tu * 128 + (((tu >> 1) ^ (w & 3)) << 5) + (fr & 3) * 8;
#pragma unroll
    for (int c = 0; c < 8; ++c) WV[c] = w_frag(lds + R_W + (kh * 8 + c) * 8192 + lo);
    LBAR();
    const int u = (lane >> 2) & 7;
    const unsigned vw = (unsigned)((16 * (u >> 2) + 8 * ((u >> 1) & 1) + 4 * (lane >> 5) + (u & 1)) * D_ + (lane & 3) * 8) * 2u;
    const unsigned sw = (unsigned)(wr * 512 * D_ + cb * 32) * 2u;
    const unsigned bo = (g >> 1) * 1024 + tu * 64 + (fr & 3) * 8;
    auto stageo = [&](int hd) {
#pragma unroll
      for (int q = 0; q < 8; ++q)
        dma16(ro, vw, sw + (unsigned)((hd * 128 + 64 * (q >> 2) + 32 * (q & 1) + 2 * ((q >> 1) & 1)) * D_) * 2u, lbase + R_W + w * 16384 + (hd & 1) * 8192 + q * 1024);
    };
    stageo(0); stageo(1);
#pragma unroll
    for (int hd = 0; hd < 4; ++hd) {
      if (hd < 3) wait_v<8>(); else wait_v<0>();
#pragma unroll
      for (int f = 0; f < 4; ++f) WB[hd * 4 + f] = w_frag(lds + R_W + w * 16384 + (hd & 1) * 8192 + (f >> 1) * 512 + (f & 1) * 32 + bo);
      WAIT_L0(); SCHED();
      if (hd < 2) stageo(hd + 2);
    }
  }
  WST(rs, 176 + rg * 8 + (cb >> 2), 4);
  unsigned hsrc = (unsigned)((lane >> 3) * 2048 + (((lane & 7) ^ (lane >> 3)) << 4)), hoff = (unsigned)((lane & 15) * 128 + (((lane >> 4) ^ (lane & 7)) << 4));
  asm volatile("" : "+v"(l16), "+v"(moff), "+v"(roff), "+v"(soff), "+v"(hsrc), "+v"(hoff));
  unsigned cap = SPIN_CAP;
  auto h_piece = [&](int b, int q) { dma16x(rs, hsrc, WS_H + (unsigned)b * 65536u + (w & 3) * 16384 + (2 * q + kh) * 128, lbase + R_W + (b & 1) * 65536 + (q * 8 + w) * 1024); };
  auto h_fetch = [&](int b) {
#pragma unroll
    for (int q = 0; q < 8; ++q) h_piece(b, q);
  };
  f32x4 x0, x1;
  auto v_part = [&](int b, auto part, auto&& each) {
    constexpr int K0 = decltype(part)::value * 8;
    LAS unsigned char* a0 = lad(hoff, lbase + R_W + (b & 1) * 65536 + kh * 32768);
    LAS unsigned char* a1 = lad<64>(hoff, lbase + R_W + (b & 1) * 65536 + kh * 32768);
    bf16x8 af[3][2];
    auto rd = [&](int k) { LAS unsigned char* q = ((k & 1) ? a1 : a0) + (k >> 1) * 4096; af[k % 3][0] = *(const LAS bf16x8*)(q); af[k % 3][1] = *(const LAS bf16x8*)(q + 2048); };
    rd(K0); rd(K0 + 1);
    SCHED();
#pragma unroll
    for (int k = K0; k < K0 + 8; ++k) {
      if (k + 2 < K0 + 8) rd(k + 2);
      const bf16x8 wf = (k & 1) ? __builtin_shufflevector(WV[k >> 1], WV[k >> 1], 8, 9, 10, 11, 12, 13, 14, 15) : __builtin_shufflevector(WV[k >> 1], WV[k >> 1], 0, 1, 2, 3, 4, 5, 6, 7);
      x0 = mfma32(af[k % 3][0], wf, k ? x0 : f32x4{0.f, 0.f, 0.f, 0.f}); x1 = mfma32(af[k % 3][1], wf, k ? x1 : f32x4{0.f, 0.f, 0.f, 0.f});
      asm volatile("" : "+v"(x0), "+v"(x1));
      SCHED();
      each(k);
      SCHED();
    }
    if (K0) {
      LAS unsigned char* xv = lad(l16, lbase + R_RED + 4096 + (w & 3) * 2048);
      asm volatile("s_nop 7\n\ts_cmp_lt_u32 %3, 4\n\ts_cbranch_scc1 1f\n\tds_write_b128 %0, %1\n\tds_write_b128 %0, %2 offset:1024\n\t1:" :: "v"(xv), "v"(x0), "v"(x1), "s"(w) : "scc", "memory");
    }
  };
  auto v_out = [&](int b) {
    if (!kh && b < NSEG) {
      LAS unsigned char* xv = lad(l16, lbase + R_RED + 4096 + (w & 3) * 2048);
      x0 += *(const LAS f32x4*)(xv); x1 += *(const LAS f32x4*)(xv + 1024);
      stx16(rs, l16, WS_VT + (unsigned)(b * NH + cb) * 8192 + (rg * 4 + w) * 1024, u32x4{pack2(x0[0], x0[1]), pack2(x0[2], x0[3]), pack2(x1[0], x1[1]), pack2(x1[2], x1[3])});
    }
  };
  typedef std::integral_constant<int, 0> P0; typedef std::integral_constant<int, 1> P1;
  BAR();
  wait_ge(rs, l16 * 8, F_LN, 1u, cap);
  h_fetch(0); h_fetch(1);
  WAIT_V(8); BAR();
  v_part(0, P0{}, [](int) {}); v_part(0, P1{}, [](int) {});
  WAIT_V(0); LBAR();
  v_out(0); h_fetch(2);
  LBAR();
  v_part(1, P0{}, [](int) {}); v_part(1, P1{}, [](int) {});
  WAIT_V(0); LBAR();
  v_out(1); h_fetch(3);
  v_part(2, P0{}, [](int) {});
  wait_ge(rs, l16 * 8, F_LN + 8192, 1u, cap); wait_ge(rs, (l16 & 0x1F0) * 8, F_LN + 16384, 1u, cap);
  WAIT_V(0); BAR();
  if (w == 0) {
    const int ln = l16 >> 4, sl = (ln >> 1) & 15, col = cb * 32 + (sl >> 3) * 16 + (ln >> 5) * 8 + (sl & 4) + (sl & 1) * 2 + ((sl >> 1) & 1), r = rg * 4 + (ln & 1) * 2;
    stx4(rs, soff, X_MEM + (cb >> 1) * 1024 + rg * 512 + ((cb ^ rg) & 1) * 128, nz((unsigned)p.mem0[r * D_ + col] | ((unsigned)p.mem0[(r + 1) * D_ + col] << 16)));
  }
  Sched tt; tt.tau = rt32() - 100000; tt.prev = tt.tau; tt.ea = 195 * 8; tt.eb = 118 * 8; tt.d1 = 0; tt.moved = true;
  const int cu_ = 32 + (rg * 32 + cb) / 2; (void)cu_;
  ST_DECL
  for (int s = 0; s < NSEG; ++s) {
    if (w == 7) wait_until(tt.tau);
    BAR();
    ST0();
    gather_mem(rs, lbase, s, wr, l16, cap, [&]() { v_part(s + 2, P1{}, [](int) {}); });
    ST(1);
    LBAR();
    ST(2);
    {
      f32x4 c0 = {0.f, 0.f, 0.f, 0.f};
      LAS unsigned char* mp = lad(moff, lbase + R_MEM + kh * 8192);
      bf16x8 af[6];
#pragma unroll
      for (int c = 0; c < 6; ++c) af[c] = mem_frag(mp, c);
      SCHED();
#pragma unroll
      for (int c = 0; c < 8; ++c) { c0 = smf(af[c % 6], WV[c], c0, sidx); SCHED(); if (c < 2) { af[c] = mem_frag(mp, 6 + c); SCHED(); } }
      const f32x4 f = fold(c0, c0);
      if (l16 < 512) stx8(rs, half_of(l16), X_VM + (unsigned)s * RSTR + cb * 4096 + (rg * 4 + (w & 3)) * 512 + kh * 256, u32x2{nz(pack2(f[0], f[1])), nz(pack2(f[2], f[3]))});
    }
    ST(3);
    {
      unsigned xb, xa;
      times_issue(rs, w == 7 ? l16 : 0x1E000000u, s, xb, xa);
      h_fetch(min(s + 4, NSEG - 2 + (s & 1)));
      v_out(s + 2);
      asm volatile("s_waitcnt vmcnt(8)" : "+v"(xb), "+v"(xa) :: "memory");
      if (w == 7) retime(rs, l16, s, xb, xa, tt, p, cap);
    }
    if (s + 1 < NSEG) {
      u32x4 a[4];
      const unsigned so = X_OX + s * RSTR + rg * 32768 + wr * 4096;
      if (w == 7) wait_until(tt.tau + tt.d1);
      BAR();
      ST(4);
      issue<0>(rs, l16, so, a[0]);
      v_part(s + 3, P0{}, [&](int k) {
        if (k == 0) issue<1>(rs, l16, so + 1024, a[1]);
        if (k == 2) issue<2>(rs, l16, so + 2048, a[2]);
        if (k == 4) issue<3>(rs, l16, so + 3072, a[3]);
      });
      f32x4 c[4];
#pragma unroll
      for (int t = 0; t < 4; ++t) c[t] = f32x4{0.f, 0.f, 0.f, 0.f};
#pragma unroll
      for (int i = 0; i < 4; ++i) {
        if (i == 0) arrive<0>(rs, l16, so, a[0], cap); else if (i == 1) arrive<1>(rs, l16, so + 1024, a[1], cap);
        else if (i == 2) arrive<2>(rs, l16, so + 2048, a[2], cap); else arrive<3>(rs, l16, so + 3072, a[3], cap);
#pragma unroll
        for (int t = 0; t < 4; ++t) c[t] = smf(__builtin_bit_cast(bf16x8, a[i]), WB[i * 4 + t], c[t], sidx);
        SCHED();
      }
      ST(5);
      {
        const bool odd = (l16 & 256) != 0;
        const f32x4 f = fold(odd ? c[2] : c[0], odd ? c[3] : c[1]);
        const auto e0 = __builtin_amdgcn_permlane16_swap(f2u(f[0]), f2u(f[2]), false, false), e1 = __builtin_amdgcn_permlane16_swap(f2u(f[1]), f2u(f[3]), false, false);
        *(LAS f32x2*)(lad(roff, lbase)) = f32x2{u2f(e0[0]) + u2f(e0[1]), u2f(e1[0]) + u2f(e1[1])};
      }
      ST(6);
      LBAR();
      ST(7);
      if (w == 0) {
        LAS unsigned char* rp = lad(half_of(l16), lbase + R_RED);
        f32x2 v[8];
#pragma unroll
        for (int i = 0; i < 8; ++i) v[i] = *(const LAS f32x2*)(rp + i * 512);
        const f32x2 sum = ((v[0] + v[1]) + (v[2] + v[3])) + ((v[4] + v[5]) + (v[6] + v[7]));
        stx4(rs, soff, X_MEM + (s + 1) * RSTR + (cb >> 1) * 1024 + rg * 512 + ((cb ^ rg) & 1) * 128, nz(pack2(sum[0], sum[1])));
        const unsigned t = max(rt32(), 1u);
        if (l16 == 0) stx4(rs, 0, X_TS + (unsigned)(s + 1) * RSTR + (unsigned)(rg * 32 + cb) * 128, t);
      }
      ST(8);
    }
    advance(tt, p);
  }
  ST_FLUSH();
  if (cap == 0u && l16 == 0) stx4(rs, 0, X_CTL, 1u);
}

#if defined(__HIP_DEVICE_COMPILE__)
__attribute__((target("no-packed-fp32-ops")))
#endif
__global__ void __launch_bounds__(512, 2) k_main(MainP p) {
  extern __shared__ __attribute__((aligned(16))) unsigned char smem[];
  const int b = blockIdx.x, x = b & 7, j = b >> 3;
  if (x >= 3) worker(p, (LAS unsigned char*)smem, x - 3, j);
  else if (x == 0) role_a(p, (LAS unsigned char*)smem, j);
  else role_vb(p, (LAS unsigned char*)smem, x - 1, j);
}

extern "C" void kernel_launch(void* const* d_in, const int* in_sizes, int n_in, void* d_out, int out_size, void* d_ws, size_t ws_size,
                              hipStream_t stream) {
  unsigned char* ws = (unsigned char*)d_ws;
  (void)hipMemsetAsync(ws, 0, X_ZERO, stream);
  MainP p;
  p.x = (const u16*)d_in[0]; p.mem0 = (const u16*)d_in[1]; p.c = (const u16*)d_in[12]; p.wmod = (const u16*)d_in[13]; p.Wq = (const u16*)d_in[2]; p.Wk = (const u16*)d_in[3]; p.Wv = (const u16*)d_in[4]; p.Wo = (const u16*)d_in[5];
  p.w1 = (const u16*)d_in[8]; p.w2 = (const u16*)d_in[9]; p.w3 = (const u16*)d_in[10]; p.w4 = (const u16*)d_in[11];
  p.out = (u16*)d_out; p.ws = ws; p.d1 = 8; p.d2 = 8; p.d3 = 2; p.d4 = 100;
  (void)hipFuncSetAttribute((const void*)k_main, hipFuncAttributeMaxDynamicSharedMemorySize, R_LDS);
  k_main<<<256, 512, R_LDS, stream>>>(p);
}
```

```cpp
#include <hip/hip_runtime.h>
#include <hip/hip_bf16.h>
#include <cstdlib>
#include <type_traits>
#pragma clang diagnostic ignored "-Winline-asm"

#define LAS __attribute__((address_space(3)))
#define GAS __attribute__((address_space(1)))
typedef unsigned short u16;
typedef __bf16 bf16x2 __attribute__((ext_vector_type(2)));
typedef __bf16 bf16x4 __attribute__((ext_vector_type(4)));
typedef __bf16 bf16x8 __attribute__((ext_vector_type(8)));
typedef __bf16 bf16x16 __attribute__((ext_vector_type(16)));
typedef short s16x4 __attribute__((ext_vector_type(4)));
typedef float f32x2 __attribute__((ext_vector_type(2)));
typedef float f32x4 __attribute__((ext_vector_type(4)));
typedef float f32x16 __attribute__((ext_vector_type(16)));
typedef unsigned u32x2 __attribute__((ext_vector_type(2)));
typedef unsigned u32x4 __attribute__((ext_vector_type(4)));

constexpr int S_ = 2048, D_ = 1024, NH = 32, DH = 128, HD = 4096, NSEG = 64, FF = 3584;

constexpr unsigned MB = 1u << 20;
constexpr unsigned F_LN = 0;
constexpr unsigned F_ST = 24576;
constexpr unsigned F_CH = F_ST + 98304;
constexpr unsigned X_CTL = F_CH + 4096;
constexpr unsigned X_MOD = X_CTL + 4096;
constexpr unsigned X_REC = X_MOD + 24576;
constexpr unsigned RSTR = 225280;
constexpr int ZA = 8;
constexpr unsigned X_MEM = X_REC;
constexpr unsigned X_OX = X_REC + 16384;
constexpr unsigned X_VM = X_REC + 81920;
constexpr unsigned X_TS = X_REC + 212992;
constexpr unsigned X_ZERO = X_REC + ZA * RSTR;
constexpr unsigned WS_H = 16 * MB;
constexpr unsigned WS_Q = 20 * MB;
constexpr unsigned WS_K = 36 * MB;
constexpr unsigned WS_VT = 52 * MB;
constexpr unsigned WS_O = 68 * MB;
constexpr unsigned WS_X1 = 84 * MB;
constexpr unsigned WS_H2 = 88 * MB;
constexpr unsigned WS_G = 92 * MB;
constexpr unsigned WS_H1S = 96 * MB;
constexpr unsigned WS_X2 = 101 * MB;
constexpr unsigned WS_ST5 = 106 * MB;
constexpr unsigned WS_U = 108 * MB;
constexpr unsigned WS_KM = 124 * MB;
constexpr unsigned WS_P2 = 132 * MB;
constexpr unsigned WS_P8 = 164 * MB;
constexpr unsigned WS_END = 198 * MB;

__device__ __forceinline__ float bf2f(unsigned short b) { return __builtin_bit_cast(float, (unsigned)b << 16); }
__device__ __forceinline__ unsigned f2u(float f) { return __builtin_bit_cast(unsigned, f); }
__device__ __forceinline__ float u2f(unsigned u) { return __builtin_bit_cast(float, u); }
__device__ __forceinline__ float lo2f(unsigned w) { return __builtin_bit_cast(float, w << 16); }
__device__ __forceinline__ float hi2f(unsigned w) { return __builtin_bit_cast(float, w & 0xFFFF0000u); }
__device__ __forceinline__ unsigned pack2(float a, float b) { bf16x2 v; v[0] = (__bf16)a; v[1] = (__bf16)b; return __builtin_bit_cast(unsigned, v); }
__device__ __forceinline__ float rbf(float a) { return (float)(__bf16)a; }
__device__ __forceinline__ unsigned nz(unsigned w) { return max(w, 1u); }
__device__ __forceinline__ float gelu_tanh(float x) {
  const float e = __builtin_amdgcn_exp2f(x * fmaf(x * x, -0.1029432397f, -2.302208198f));
  return x * __builtin_amdgcn_rcpf(1.0f + e);
}
template <int CTRL> __device__ __forceinline__ float dpp_add(float v) { return v + u2f(__builtin_amdgcn_update_dpp(0u, f2u(v), CTRL, 0xF, 0xF, true)); }
__device__ __forceinline__ float wave_sum(float v) {
  v = dpp_add<0xB1>(v); v = dpp_add<0x4E>(v); v = dpp_add<0x141>(v); v = dpp_add<0x140>(v);
  auto t = __builtin_amdgcn_permlane16_swap(f2u(v), f2u(v), false, false);
  v = u2f(t[0]) + u2f(t[1]);
  t = __builtin_amdgcn_permlane32_swap(f2u(v), f2u(v), false, false);
  return u2f(t[0]) + u2f(t[1]);
}
#define WAIT_V(n) asm volatile("s_waitcnt vmcnt(" #n ")" ::: "memory")
#define WAIT_L0() asm volatile("s_waitcnt lgkmcnt(0)" ::: "memory")
#define BAR() do { asm volatile("" ::: "memory"); __builtin_amdgcn_s_barrier(); asm volatile("" ::: "memory"); } while (0)
#define LBAR() do { WAIT_L0(); BAR(); } while (0)
template <int N> __device__ __forceinline__ void wait_v() { asm volatile("s_waitcnt vmcnt(%0)" :: "n"(N) : "memory"); }
__device__ __forceinline__ void dma16(__amdgpu_buffer_rsrc_t rs, unsigned voff, unsigned soff, unsigned ldsa) {
  asm volatile("s_mov_b32 m0, %0\n\ts_nop 3\n\tbuffer_load_dwordx4 %1, %2, %3 offen lds" :: "s"(ldsa), "v"(voff), "s"(rs), "s"(soff) : "m0", "memory");
}
__device__ __forceinline__ void dma16x(__amdgpu_buffer_rsrc_t rs, unsigned voff, unsigned soff, unsigned ldsa) {
  asm volatile("s_mov_b32 m0, %0\n\ts_nop 3\n\tbuffer_load_dwordx4 %1, %2, %3 offen sc1 lds" :: "s"(ldsa), "v"(voff), "s"(rs), "s"(soff) : "m0", "memory");
}
__device__ __forceinline__ u32x4 ldx16(__amdgpu_buffer_rsrc_t rs, unsigned voff, unsigned soff) { return __builtin_amdgcn_raw_buffer_load_b128(rs, voff, soff, 16); }
__device__ __forceinline__ u32x2 ldx8(__amdgpu_buffer_rsrc_t rs, unsigned voff, unsigned soff) { return __builtin_amdgcn_raw_buffer_load_b64(rs, voff, soff, 16); }
__device__ __forceinline__ unsigned ldx4(__amdgpu_buffer_rsrc_t rs, unsigned voff, unsigned soff) { return __builtin_amdgcn_raw_buffer_load_b32(rs, voff, soff, 16); }
__device__ __forceinline__ void stx16(__amdgpu_buffer_rsrc_t rs, unsigned voff, unsigned soff, u32x4 v) {
  __builtin_amdgcn_raw_buffer_store_b128(v, rs, voff, soff, 16);
  asm volatile("s_nop 1" :: "v"(v) : "memory");
}
__device__ __forceinline__ void stx8(__amdgpu_buffer_rsrc_t rs, unsigned voff, unsigned soff, u32x2 v) { __builtin_amdgcn_raw_buffer_store_b64(v, rs, voff, soff, 16); }
__device__ __forceinline__ void stx4(__amdgpu_buffer_rsrc_t rs, unsigned voff, unsigned soff, unsigned v) { __builtin_amdgcn_raw_buffer_store_b32(v, rs, voff, soff, 16); }
__device__ __forceinline__ void wait_ge(__amdgpu_buffer_rsrc_t rs, unsigned voff, unsigned soff, unsigned need, unsigned& cap) {
  unsigned t;
  asm volatile("s_nop 4\n\t"
               "1:\n\t"
               "buffer_load_dword %[t], %[vo], %[rs], %[so] offen sc1\n\t"
               "s_waitcnt vmcnt(0)\n\t"
               "v_cmp_gt_u32 vcc, %[need], %[t]\n\t"
               "s_cmp_eq_u64 vcc, 0\n\t"
               "s_cbranch_scc1 2f\n\t"
               "s_sleep 2\n\t"
               "s_sub_u32 %[cap], %[cap], 1\n\t"
               "s_cbranch_scc0 1b\n\t"
               "s_mov_b32 %[cap], 0\n\t"
               "2:"
               : [t] "=&v"(t), [cap] "+s"(cap) : [vo] "v"(voff), [rs] "s"(rs), [so] "s"(soff), [need] "s"(need) : "vcc", "scc", "memory");
}
__device__ __forceinline__ void wait_seen(__amdgpu_buffer_rsrc_t rs, unsigned voff, unsigned soff, unsigned need, unsigned& seen, unsigned& cap) {
  if (seen >= need) return;
  unsigned t;
  asm volatile("s_nop 4\n\t"
               "1:\n\t"
               "buffer_load_dword %[t], %[vo], %[rs], %[so] offen sc1\n\t"
               "s_waitcnt vmcnt(0)\n\t"
               "v_cmp_gt_u32 vcc, %[need], %[t]\n\t"
               "s_cbranch_vccz 2f\n\t"
               "s_sleep 1\n\t"
               "s_sub_u32 %[cap], %[cap], 1\n\t"
               "s_cbranch_scc0 1b\n\t"
               "s_mov_b32 %[cap], 0\n\t"
               "2:"
               : [t] "=&v"(t), [cap] "+s"(cap) : [vo] "v"(voff), [rs] "s"(rs), [so] "s"(soff), [need] "s"(need) : "vcc", "scc", "memory");
  seen = min((unsigned)__builtin_amdgcn_readlane(t, 0), (unsigned)__builtin_amdgcn_readlane(t, 1));
}
__device__ __forceinline__ __amdgpu_buffer_rsrc_t mk_rs(const void* p, unsigned bytes) { return __builtin_amdgcn_make_buffer_rsrc((void*)p, 0, (int)bytes, 0x00020000); }
#define SCHED() __builtin_amdgcn_sched_barrier(0)
__device__ __forceinline__ unsigned now32() { unsigned long long t; asm volatile("s_memtime %0\n\ts_waitcnt lgkmcnt(0)" : "=s"(t) :: "memory"); return (unsigned)t; }
__device__ __forceinline__ unsigned rt32() { unsigned long long t; asm volatile("s_memrealtime %0\n\ts_waitcnt lgkmcnt(0)" : "=s"(t) :: "memory"); return (unsigned)t; }
__device__ __forceinline__ void wait_until(unsigned t) { while ((int)(rt32() - t) < 0) {} }
template <int X = 0> __device__ __forceinline__ LAS unsigned char* lad(unsigned v, unsigned u) {
  unsigned a;
  if (X == 0) asm volatile("v_add_u32 %0, %1, %2" : "=v"(a) : "s"(u), "v"(v));
  else asm volatile("v_xor_b32 %0, %3, %2\n\tv_add_u32 %0, %1, %0" : "=&v"(a) : "s"(u), "v"(v), "n"(X));
  return (LAS unsigned char*)(size_t)a;
}
__device__ __forceinline__ bf16x4 trread(LAS unsigned char* p) { return __builtin_amdgcn_ds_read_tr16_b64_v4bf16((LAS bf16x4*)p); }
__device__ __forceinline__ bf16x8 cat8(bf16x4 a, bf16x4 b) { return __builtin_shufflevector(a, b, 0, 1, 2, 3, 4, 5, 6, 7); }
__device__ __forceinline__ bf16x16 cat16(bf16x8 a, bf16x8 b) { return __builtin_shufflevector(a, b, 0, 1, 2, 3, 4, 5, 6, 7, 8, 9, 10, 11, 12, 13, 14, 15); }
__device__ __forceinline__ f32x4 mfma32(bf16x8 a, bf16x8 b, f32x4 c) { return __builtin_amdgcn_mfma_f32_16x16x32_bf16(a, b, c, 0, 0, 0); }
__device__ __forceinline__ f32x4 mfma16(u32x2 a, u32x2 b, f32x4 c) {
  return mfma32(__builtin_bit_cast(bf16x8, u32x4{a[0], a[1], 0u, 0u}), __builtin_bit_cast(bf16x8, u32x4{b[0], b[1], 0u, 0u}), c);
}
__device__ __forceinline__ f32x4 mfma16n(u32x2 a, u32x2 b, f32x4 c) { return __builtin_amdgcn_mfma_f32_16x16x16bf16_1k(__builtin_bit_cast(s16x4, a), __builtin_bit_cast(s16x4, b), c, 0, 0, 0); }
__device__ __forceinline__ f32x4 smf(bf16x8 a, bf16x16 b, f32x4 c, int idx) { return __builtin_amdgcn_smfmac_f32_16x16x64_bf16(a, b, c, idx, 0, 0); }

__device__ __forceinline__ void ln_row(float (&x)[16]) {
  float s = 0.f;
#pragma unroll
  for (int i = 0; i < 16; ++i) s += x[i];
  const float mean = wave_sum(s) * (1.0f / 1024.0f);
  float q = 0.f;
#pragma unroll
  for (int i = 0; i < 16; ++i) { const float d = x[i] - mean; q += d * d; }
  const float var = wave_sum(q) * (1.0f / 1024.0f);
  const float m = rbf(mean), isd = __builtin_amdgcn_rcpf(rbf(__builtin_amdgcn_sqrtf(rbf(rbf(var) + 1e-5f))));
#pragma unroll
  for (int i = 0; i < 16; ++i) x[i] = rbf(rbf(x[i] - m) * isd);
}
__device__ __forceinline__ void load_row16(const u16* rowp, int lane, float (&x)[16]) {
  const u32x4 a = *(const u32x4*)(rowp + lane * 8), b = *(const u32x4*)(rowp + 512 + lane * 8);
#pragma unroll
  for (int i = 0; i < 4; ++i) { x[2 * i] = lo2f(a[i]); x[2 * i + 1] = hi2f(a[i]); x[8 + 2 * i] = lo2f(b[i]); x[9 + 2 * i] = hi2f(b[i]); }
}
__device__ __forceinline__ void store_row16(u16* rowp, int lane, const float (&x)[16]) {
  u32x4 a, b;
#pragma unroll
  for (int i = 0; i < 4; ++i) { a[i] = pack2(x[2 * i], x[2 * i + 1]); b[i] = pack2(x[8 + 2 * i], x[9 + 2 * i]); }
  *(u32x4*)(rowp + lane * 8) = a; *(u32x4*)(rowp + 512 + lane * 8) = b;
}
__host__ __device__ __forceinline__ int mem_slot(int dl) { return ((dl >> 3) & 1) * 32 + (dl >> 4) * 8 + (dl & 4) + (dl & 1) * 2 + ((dl >> 1) & 1); }

#define WST(rs, wk, i)
struct MainP { const u16 *x, *mem0, *Wq, *Wk, *Wv, *Wo, *w1, *w2, *w3, *w4, *c, *wmod; u16* out; unsigned char* ws; int d1, d2, d3, d4; };
constexpr unsigned SPIN_CAP = 1u << 18;

__device__ __forceinline__ float gmax(float v) {
  auto t = __builtin_amdgcn_permlane16_swap(f2u(v), f2u(v), false, false);
  v = fmaxf(u2f(t[0]), u2f(t[1]));
  t = __builtin_amdgcn_permlane32_swap(f2u(v), f2u(v), false, false);
  return fmaxf(u2f(t[0]), u2f(t[1]));
}
__device__ __forceinline__ float gsum(float v) {
  auto t = __builtin_amdgcn_permlane16_swap(f2u(v), f2u(v), false, false);
  v = u2f(t[0]) + u2f(t[1]);
  t = __builtin_amdgcn_permlane32_swap(f2u(v), f2u(v), false, false);
  return u2f(t[0]) + u2f(t[1]);
}
__device__ __forceinline__ void publish(__amdgpu_buffer_rsrc_t rs, unsigned word, unsigned val) {
  WAIT_V(0); BAR();
  if (threadIdx.x == 0) stx4(rs, 0, word, val);
}

constexpr int QB0 = 20;
constexpr int P_S2 = 0, P_S8 = 32, P_S7 = 64, P_S4 = 96, P_S5 = 104, P_S3 = 112, P_PJ = 208, P_S1 = 336;
constexpr int NLN = 160;

enum { SG_WO = 0, SG_W1 = 1, SG_W2 = 2, SG_W3 = 3, SG_W4 = 4, SG_PJ = 5 };
constexpr int G_XCH = 131072, G_K = G_XCH + 16384, G_CTL = G_K + 8192;
__device__ __forceinline__ f32x16 mfma3232(bf16x8 a, bf16x8 b, f32x16 c) { return __builtin_amdgcn_mfma_f32_32x32x16_bf16(a, b, c, 0, 0, 0); }
__device__ __forceinline__ void widen32(const f32x16& a, u32x4 (&v)[2]) {
#pragma unroll
  for (int u = 0; u < 2; ++u) {
    const auto r0 = __builtin_amdgcn_permlane32_swap(pack2(a[8 * u], a[8 * u + 1]), pack2(a[8 * u + 4], a[8 * u + 5]), false, false);
    const auto r1 = __builtin_amdgcn_permlane32_swap(pack2(a[8 * u + 2], a[8 * u + 3]), pack2(a[8 * u + 6], a[8 * u + 7]), false, false);
    v[u] = u32x4{r0[0], r1[0], r0[1], r1[1]};
  }
}
__device__ __forceinline__ unsigned umin4v(u32x4 v) { return min(min(v[0], v[1]), min(v[2], v[3])); }
__device__ __forceinline__ float hsum(float v) { const auto t = __builtin_amdgcn_permlane32_swap(f2u(v), f2u(v), false, false); return u2f(t[0]) + u2f(t[1]); }

__device__ __forceinline__ void attend(__amdgpu_buffer_rsrc_t rs, int seg, int h, int t, int lane, unsigned& chs, unsigned& cap) {
  const int fr = lane & 15, g = lane >> 4;
  const unsigned l16 = lane * 16, ub = (unsigned)(seg * NH + h);
  wait_seen(rs, 0, F_CH + h * 128, (unsigned)max(seg, 1), chs, cap);
  wait_ge(rs, (lane & 1) && seg >= QB0 ? 96u * 128u : (unsigned)(seg % 3) * 4096u, F_ST + (unsigned)(P_PJ + h) * 128, (unsigned)seg + 1u, cap);
  f32x4 s0 = {0.f, 0.f, 0.f, 0.f}, s1 = s0, s2 = s0;
  u32x4 q[4], kmf[4];
  u32x2 vmf[8];
  const unsigned em = (unsigned)(fr >> 3) * 512 + (unsigned)g * 128 + (unsigned)(8 * ((fr >> 2) & 1) + (fr & 3)) * 8;
  auto mem_load = [&](unsigned off) {
#pragma unroll
    for (int ks = 0; ks < 4; ++ks) kmf[ks] = ldx16(rs, l16 + off, WS_KM + ub * 4096 + ks * 1024);
#pragma unroll
    for (int i = 0; i < 8; ++i) vmf[i] = ldx8(rs, em + off, X_VM + (unsigned)seg * RSTR + h * 4096 + (i >> 1) * 1024 + (i & 1) * 32);
  };
  const bool have = chs > (unsigned)seg;
  f32x4 o[8];
  float mt = -3.0e38f, lt = 0.f;
  const float scale = 0.08838834764831845f, L2E = 1.4426950408889634f;
  {
    u32x4 k0[4], k1[4], vv[8];
#pragma unroll
    for (int ks = 0; ks < 4; ++ks) {
      q[ks] = ldx16(rs, l16, WS_Q + ub * 8192 + t * 4096 + ks * 1024);
      k0[ks] = ldx16(rs, l16, WS_K + ub * 8192 + ks * 1024); k1[ks] = ldx16(rs, t ? l16 : 0xF0000000u, WS_K + ub * 8192 + 4096 + ks * 1024);
    }
    const unsigned ev = (unsigned)(fr >> 3) * 1024 + (unsigned)(g * 16 + 8 * ((fr >> 2) & 1) + (fr & 3)) * 16;
#pragma unroll
    for (int i = 0; i < 8; ++i) vv[i] = ldx16(rs, ev, WS_VT + ub * 8192 + (i >> 1) * 2048 + (i & 1) * 64);
#pragma unroll
    for (int ks = 0; ks < 4; ++ks) {
      s1 = mfma32(__builtin_bit_cast(bf16x8, k0[ks]), __builtin_bit_cast(bf16x8, q[ks]), s1); s2 = mfma32(__builtin_bit_cast(bf16x8, k1[ks]), __builtin_bit_cast(bf16x8, q[ks]), s2);
    }
    SCHED();
    mem_load(have ? 0u : 0xF0000000u);
    SCHED();
    float b[4], c[4];
#pragma unroll
    for (int r = 0; r < 4; ++r) {
      const int j = 4 * g + r;
      b[r] = (t == 1 || j <= fr) ? rbf(rbf(s1[r]) * scale) : -3.0e38f;
      c[r] = (t == 1 && j <= fr) ? rbf(rbf(s2[r]) * scale) : -3.0e38f;
      mt = fmaxf(mt, fmaxf(b[r], c[r]));
    }
    mt = gmax(mt);
#pragma unroll
    for (int r = 0; r < 4; ++r) { b[r] = __builtin_amdgcn_exp2f((b[r] - mt) * L2E); c[r] = __builtin_amdgcn_exp2f((c[r] - mt) * L2E); lt += b[r] + c[r]; }
    const u32x4 pt = {pack2(b[0], b[1]), pack2(b[2], b[3]), pack2(c[0], c[1]), pack2(c[2], c[3])};
#pragma unroll
    for (int i = 0; i < 8; ++i) o[i] = mfma32(__builtin_bit_cast(bf16x8, vv[i]), __builtin_bit_cast(bf16x8, pt), f32x4{0.f, 0.f, 0.f, 0.f});
    lt = gsum(lt);
  }
  asm volatile("" : "+v"(o[0]), "+v"(o[1]), "+v"(o[2]), "+v"(o[3]), "+v"(o[4]), "+v"(o[5]), "+v"(o[6]), "+v"(o[7]), "+v"(lt));
  if (!have) { wait_seen(rs, 0, F_CH + h * 128, (unsigned)seg + 1u, chs, cap); mem_load(0u); }
#pragma unroll
  for (int ks = 0; ks < 4; ++ks) s0 = mfma32(__builtin_bit_cast(bf16x8, kmf[ks]), __builtin_bit_cast(bf16x8, q[ks]), s0);
  float a[4];
#pragma unroll
  for (int r = 0; r < 4; ++r) a[r] = rbf(rbf(s0[r]) * scale);
  const float mx = fmaxf(mt, gmax(fmaxf(fmaxf(a[0], a[1]), fmaxf(a[2], a[3]))));
#pragma unroll
  for (int r = 0; r < 4; ++r) a[r] = __builtin_amdgcn_exp2f((a[r] - mx) * L2E);
  const u32x2 pm = {pack2(a[0], a[1]), pack2(a[2], a[3])};
  const float al = __builtin_amdgcn_exp2f((mt - mx) * L2E);
  const float inv = __builtin_amdgcn_rcpf(fmaf(al, lt, 0.5f * gsum((a[0] + a[1]) + (a[2] + a[3])))), ai = al * inv;
#pragma unroll
  for (int u = 0; u < 4; ++u) {
    const f32x4 x = o[2 * u] * ai + mfma16n(vmf[2 * u], pm, f32x4{0.f, 0.f, 0.f, 0.f}) * inv, y = o[2 * u + 1] * ai + mfma16n(vmf[2 * u + 1], pm, f32x4{0.f, 0.f, 0.f, 0.f}) * inv;
    stx16(rs, l16, WS_O + (unsigned)seg * 262144 + (unsigned)((h * 4 + u) * 2 + t) * 1024, u32x4{pack2(x[0], x[1]), pack2(x[2], x[3]), pack2(y[0], y[1]), pack2(y[2], y[3])});
  }
  WAIT_V(0);
  if (lane == 0) stx4(rs, 0, F_ST + (unsigned)(P_S1 + (seg & 3) * 64 + h * 2 + t) * 128, (unsigned)seg + 1u);
}

template <int SG>
__device__ __forceinline__ void stage_gemm(LAS unsigned char* lds, __amdgpu_buffer_rsrc_t rs, const u16* Wg, int wk, int kq, int cb, int pid, unsigned& cap,
                                           unsigned dst = 0, int bstr = 1, int nb = 0, const u16* side = nullptr, int srows = 0, int sld = 0, const u16* side3 = nullptr) {
  constexpr bool PROJ = SG == SG_PJ, ROWM = (SG == SG_W1 || PROJ);
  constexpr int KS = (SG == SG_W4) ? 28 : 32;
  constexpr int LDW = PROJ ? HD : (SG == SG_W3) ? FF : D_;
  constexpr int WROWS = (SG == SG_WO) ? HD : (SG == SG_W4 ? FF : D_);
  const int NBLK = PROJ ? nb : (SG == SG_WO || SG == SG_W1) ? 64 : 65;
  constexpr unsigned ABASE = PROJ ? WS_H : SG == SG_WO ? WS_O : SG == SG_W1 ? WS_G : SG == SG_W2 ? WS_H1S : SG == SG_W3 ? WS_X2 : WS_U;
  constexpr unsigned ASTR = SG == SG_WO ? 262144u : SG == SG_W4 ? 229376u : 65536u;
  constexpr int NPC = KS / 4;
  const unsigned myword = F_ST + (unsigned)pid * 128u;
  int tid = threadIdx.x; asm volatile("" : "+v"(tid));
  const int lane = tid & 63, w = __builtin_amdgcn_readfirstlane(tid >> 6), row = lane & 31, hf = lane >> 5;
  const int kh = w >> 2, wl = w & 3;
  const int koff = PROJ ? 0 : kq;
  const unsigned lbase = (unsigned)(size_t)lds, l16 = lane * 16;
  const int col0 = cb * 128 + wl * 32;
  bf16x8 Wf[KS];
  {
    const __amdgpu_buffer_rsrc_t rsW = mk_rs(Wg, (unsigned)WROWS * LDW * 2u);
    const int ub = lane >> 3, tu = 4 * hf + ((lane & 15) >> 2);
    const unsigned vB = (unsigned)(((ub & 3) + 8 * (ub >> 2)) * LDW + 16 * (((lane >> 1) & 3) ^ (ub >> 1)) + 8 * (lane & 1)) * 2u;
    const unsigned sB = (unsigned)((koff * KS * 32 + 32 * (w >> 2) + 16 * ((w >> 1) & 1) + 4 * (w & 1)) * LDW + cb * 128) * 2u;
    const int boff = kh * 65536 + (wl >> 1) * 8192 + tu * 128 + (((tu >> 1) ^ (2 * (wl & 1) + ((lane >> 4) & 1))) << 5) + (lane & 3) * 8;
#pragma unroll
    for (int ps = 0; ps < 2; ++ps) {
      BAR();
#pragma unroll
      for (int hh = 0; hh < 2; ++hh)
#pragma unroll
        for (int i = 0; i < 4; ++i)
          if (4 * ps + i < NPC && !(PROJ && ps == 0)) {
            const unsigned so = sB + (unsigned)((hh * NPC + 4 * ps + i) * 64 * LDW) * 2u, d = lbase + hh * 65536 + i * 16384 + w * 1024;
            dma16(rsW, vB, so, d); dma16(rsW, vB, so + 128, d + 8192);
          }
      WAIT_V(0); BAR();
#pragma unroll
      for (int i = 0; i < 4; ++i)
        if (4 * ps + i < NPC) {
#pragma unroll
          for (int k = 0; k < 4; ++k) { LAS unsigned char* q = lds + (boff + i * 16384 + k * 2048); Wf[(4 * ps + i) * 4 + k] = cat8(trread(q), trread(q + 1024)); }
        }
      WAIT_L0();
    }
    if (SG == SG_W1) {
      const u32x4 z = {0u, 0u, 0u, 0u};
      stx16(rs, (unsigned)tid * 16, WS_H1S + cb * 8192, z); stx16(rs, (unsigned)tid * 16, WS_H1S + 64 * 65536 + cb * 8192, z);
      WAIT_V(0);
    }
    if (tid < 8) *(LAS unsigned*)(lds + G_CTL + tid * 4) = 0u;
    LBAR();
  }
  const f32x16 zero16 = {0.f, 0.f, 0.f, 0.f, 0.f, 0.f, 0.f, 0.f, 0.f, 0.f, 0.f, 0.f, 0.f, 0.f, 0.f, 0.f};
  auto put16 = [&](LAS unsigned char* p, const f32x16& a) {
#pragma unroll
    for (int q = 0; q < 4; ++q) *(LAS f32x4*)(p + q * 1024) = f32x4{a[4 * q], a[4 * q + 1], a[4 * q + 2], a[4 * q + 3]};
  };
  if (SG == SG_W3) {
    const u32x4 one4 = {0x3F803F80u, 0x3F803F80u, 0x3F803F80u, 0x3F803F80u};
    f32x16 cs = zero16;
#pragma unroll
    for (int k = 0; k < KS; ++k) cs = mfma3232(Wf[k], __builtin_bit_cast(bf16x8, one4), cs);
    if (kh == 0) put16(lds + wl * 4096 + l16, cs);
    LBAR();
    if (kh == 1 && row == 0) {
#pragma unroll
      for (int q = 0; q < 4; ++q)
        *(LAS f32x4*)(lds + G_K + wl * 128 + hf * 64 + q * 16) = *(const LAS f32x4*)(lds + wl * 4096 + q * 1024 + l16) + f32x4{cs[4 * q], cs[4 * q + 1], cs[4 * q + 2], cs[4 * q + 3]};
    }
    LBAR();
  }
  auto blk = [&](int i) -> int { return PROJ ? bstr * i + kq : i; };
  const unsigned aoff = ROWM ? (unsigned)(row * 128 + ((hf ^ ((row >> 1) & 7)) << 4)) : (unsigned)((row >> 4) * 1024 + hf * 256 + (row & 15) * 16);
  const unsigned vA = ROWM ? (unsigned)((lane >> 3) * 2048 + (((lane & 7) ^ ((4 * wl + (lane >> 4)) & 7)) << 4)) : l16;
  auto piece = [&](int hb, int q, bool on) {
    const int i = hb >> 1, hh = hb & 1;
    const unsigned so = ABASE + (unsigned)blk(i) * ASTR + (unsigned)koff * (KS * 2048) + (ROWM ? wl * 16384 + hh * 1024 : (hh * KS + wl) * 1024);
    if (on) dma16x(rs, vA, so + q * (ROWM ? 128 : 4096), lbase + (i & 1) * 65536 + hh * 32768 + wl * 1024 + q * 4096);
  };
  auto fetch = [&](int hb) {
#pragma unroll
    for (int q = 0; q < NPC; ++q) piece(hb, q, true);
  };
  constexpr bool WEAVE = (SG == SG_W1 || SG == SG_W2 || SG == SG_W3);
  auto products = [&](int i, f32x16& acc, auto&& first, auto&& early, auto&& handover, bool soon) {
    const unsigned ub = lbase + (i & 1) * 65536 + kh * 32768;
    LAS unsigned char* ab[4];
    ab[0] = lad(aoff, ub);
    if (ROWM) { ab[1] = lad<32>(aoff, ub); ab[2] = lad<64>(aoff, ub); ab[3] = lad<96>(aoff, ub); }
    constexpr int PD = 10;
    bf16x8 af[PD + 1];
    auto rd = [&](int k) { af[k % (PD + 1)] = ROWM ? *(const LAS bf16x8*)(ab[k & 3] + (k >> 2) * 4096) : *(const LAS bf16x8*)(ab[0] + (k >> 1) * 2048 + (k & 1) * 512); };
#pragma unroll
    for (int k = 0; k < PD; ++k) rd(k);
    SCHED();
#pragma unroll
    for (int k = 0; k < KS; ++k) {
      if (k + PD < KS) rd(k + PD);
      acc = mfma3232(Wf[k], af[k % (PD + 1)], k ? acc : zero16);
      SCHED();
      first(k);
      SCHED();
      if (k == KS - 10) { early(); SCHED(); }
      if (k == 9) { if (soon) handover(); SCHED(); }
      if (k == KS - 5) { if (!soon) handover(); SCHED(); }
    }
    asm volatile("" : "+v"(acc));
  };
  WST(rs, wk, PROJ ? 2 : 10);
#define SEG(x)
  auto announce = [&](int i) {
    if (lane == 0) {
      const unsigned c = __hip_atomic_fetch_add((LAS unsigned*)(lds + G_CTL), 1u, __ATOMIC_RELAXED, __HIP_MEMORY_SCOPE_WORKGROUP);
      if ((c & 3u) == 3u) stx4(rs, 0, myword, (unsigned)blk(i) + 1u);
    }
  };
  if (kh == 0) {
    auto pvo = [&](int i) -> unsigned {
      const int b = blk(i);
      if (PROJ) return F_LN + (unsigned)((b % (NLN / 4)) * 4 + (lane & 3)) * 128u;
      if (SG == SG_WO) return F_ST + (unsigned)(P_S1 + (b & 3) * 64 + kq * 16 + (lane & 15)) * 128u;
      if (SG == SG_W1) return F_ST + (unsigned)(P_S3 + (b % 3) * 32 + (lane & 31)) * 128u;
      if (SG == SG_W2) return F_ST + (unsigned)(P_S4 + (lane & 7)) * 128u;
      if (SG == SG_W3) return F_ST + (unsigned)(P_S5 + (lane & 7)) * 128u;
      return F_ST + (unsigned)(P_S7 + kq * 7 + min(lane & 7, 6)) * 128u;
    };
    auto need = [&](int i) -> unsigned { return PROJ ? 1u : (SG == SG_W2 && i == 64) ? 64u : (unsigned)i + 1u; };
    unsigned nl = 0;
    int R = 0, Rp = 0;
    for (int i = 0; i < NBLK; ++i) {
      if (R <= i) { wait_ge(rs, pvo(i), 0, need(i), cap); SEG(6); fetch(2 * i); fetch(2 * i + 1); SEG(7); }
      const unsigned pk = w == 0 ? (lane < 32 ? pvo(i + 1) : pvo(i + 2)) : 0xF0000000u;
      if (Rp <= i) { if (R <= i + 1) wait_v<NPC>(); else wait_v<2 * NPC>(); }
      if (R >= i + 2 || Rp > i) BAR();
      else {
        nl += 4;
        if (lane == 0) (void)__hip_atomic_fetch_add((LAS unsigned*)(lds + G_CTL + 8), 1u, __ATOMIC_RELAXED, __HIP_MEMORY_SCOPE_WORKGROUP);
        while (__builtin_amdgcn_readfirstlane(*(const volatile LAS unsigned*)(lds + G_CTL + 8)) < nl) {}
        asm volatile("" ::: "memory");
      }
      SEG(1);
      f32x16 acc;
      unsigned peek;
      asm volatile("buffer_load_dword %0, %1, %2, 0 offen sc1" : "=v"(peek) : "v"(pk), "s"(rs) : "memory");
      u32x4 st[8];
      float mean = 0.f, q2 = 0.f;
      if (SG == SG_W3) {
#pragma unroll
        for (int q = 0; q < 8; ++q) st[q] = ldx16(rs, l16, WS_ST5 + (unsigned)i * 8192 + q * 1024);
      }
      products(i, acc, [&](int k) {
        if (SG == SG_W3) {
          if (k >= 18 && k < 26) { mean += u2f(st[k - 18][0]) + u2f(st[k - 18][2]); q2 += u2f(st[k - 18][1]) + u2f(st[k - 18][3]); asm volatile("" : "+v"(mean), "+v"(q2)); }
          if (k == 26) { mean = hsum(mean) * (1.0f / 1024.0f); q2 = hsum(q2); asm volatile("" : "+v"(mean), "+v"(q2)); }
          if (k == 28) { q2 = rbf(rbf(fmaxf(fmaf(-mean, mean, q2 * (1.0f / 1024.0f)), 0.f)) + 1e-5f); asm volatile("" : "+v"(q2)); }
          if (k == 29) { q2 = __builtin_amdgcn_rcpf(rbf(__builtin_amdgcn_sqrtf(q2))); mean = rbf(mean); asm volatile("" : "+v"(q2), "+v"(mean)); }
        }
      }, []() {}, [&]() {
        SEG(4);
        WAIT_V(0);
        SEG(0);
        asm volatile("" : "+v"(peek));
        if (w == 0) {
          const unsigned long long ok = __ballot(peek >= (lane < 32 ? need(i + 1) : need(i + 2)));
          const bool r1 = (R >= i + 2 || (unsigned)ok == 0xFFFFFFFFu) && i + 1 < NBLK, r2 = r1 && (unsigned)(ok >> 32) == 0xFFFFFFFFu && i + 2 < NBLK;
          if (lane == 0) *(volatile LAS unsigned*)(lds + G_CTL + 4) = (unsigned)(i + 1 + (r1 ? 1 : 0) + (r2 ? 1 : 0));
        }
        LBAR();
        if (PROJ && i < 16) {
          if (side3) dma16(mk_rs(side3, 0x7FFFFFFFu), (unsigned)((lane >> 2) * 6144 + (lane & 3) * 8) * 2u, (unsigned)((64 * i + 16 * wl) * 6144) * 2u, lbase + G_K);
          if (64 * i < srows) {
            const __amdgpu_buffer_rsrc_t rsN = mk_rs(side, 0x7FFFFFFFu);
#pragma unroll
            for (int q = 0; q < 4; ++q) dma16(rsN, (unsigned)((lane >> 4) * sld + (lane & 15) * 8) * 2u, (unsigned)((64 * i + 16 * wl + 4 * q) * sld) * 2u, lbase + G_K + wl * 2048);
          }
        }
      }, Rp <= i);
      SEG(2);
      const unsigned rv = *(const volatile LAS unsigned*)(lds + G_CTL + 4);
      if (SG == SG_W3) {
        const float m = mean;
        *(LAS float*)(lds + G_K + 1024 + wl * 256 + lane * 4) = q2;
#pragma unroll
        for (int q = 0; q < 4; ++q) {
          const f32x4 cs = *(const LAS f32x4*)(lds + G_K + wl * 128 + hf * 64 + q * 16);
#pragma unroll
          for (int e = 0; e < 4; ++e) acc[4 * q + e] = fmaf(-m, cs[e], acc[4 * q + e]);
        }
      }
      put16(lad(l16, lbase + G_XCH + wl * 4096), acc);
      asm volatile("" ::: "memory");
      if (lane == 0) *(volatile LAS unsigned*)(lds + G_CTL + 16 + wl * 4) = (unsigned)i + 1u;
      const bool soon = Rp <= i;
      Rp = R; R = __builtin_amdgcn_readfirstlane(rv);
      SEG(3);
      if (R >= i + 2 && Rp < i + 2) { fetch(2 * i + 2); fetch(2 * i + 3); }
      if (R >= i + 3) {
        if (soon) { while (__builtin_amdgcn_readfirstlane(umin4v(*(const volatile LAS u32x4*)(lds + G_CTL + 16))) <= (unsigned)i) {} asm volatile("" ::: "memory"); }
        fetch(2 * i + 4);
      }
      SEG(5);
    }
  } else {
    bool pending = false;
    int Rp = 0;
    for (int i = 0; i < NBLK; ++i) {
      const int b = blk(i);
      BAR();
      SEG(0);
      const unsigned rv = *(const volatile LAS unsigned*)(lds + G_CTL + 4);
      u32x4 res[2];
      float isd;
      if (SG == SG_W2) {
#pragma unroll
        for (int u = 0; u < 2; ++u) {
          const int t = 32 * b - 2 + 16 * u + (lane >> 2);
          dma16x(rs, (t >= 0 && t < S_) ? (unsigned)(t * D_ + col0 + ((lane ^ (lane >> 3)) & 3) * 8) * 2u : 0xF0000000u, WS_H2, lbase + G_K + wl * 2048 + u * 1024);
        }
      }
      f32x16 acc;
      int R = 0;
      LAS unsigned char* xr = lad(l16, lbase + G_XCH + wl * 4096);
      f32x4 xa[4];
      unsigned xf;
      auto take = [&]() {
        asm volatile("" ::: "memory");
        xf = *(const volatile LAS unsigned*)(lds + G_CTL + 16 + wl * 4);
        asm volatile("" ::: "memory");
#pragma unroll
        for (int q = 0; q < 4; ++q) xa[q] = *(const LAS f32x4*)(xr + q * 1024);
        if (SG == SG_W3) isd = *(const LAS float*)(lds + G_K + 1024 + wl * 256 + lane * 4);
        asm volatile("" ::: "memory");
      };
      products(i, acc, [&](int k) {
      }, [&]() {
        take();
      }, [&]() {
        SEG(5);
        R = __builtin_amdgcn_readfirstlane(rv);
        if (R >= i + 3 || Rp >= i + 2) {
          WAIT_V(0);
          LBAR();
          if (pending && tid == 256) stx4(rs, 0, myword, (unsigned)blk(i - 1) + 1u);
        }
      }, false);
      const bool more = R >= i + 3 || Rp >= i + 2;
      Rp = R;
      SEG(1);
      while (__builtin_amdgcn_readfirstlane(xf) <= (unsigned)i) take();
#pragma unroll
      for (int q = 0; q < 16; ++q) { acc[q] += xa[q >> 2][q & 3]; if (SG == SG_W3) acc[q] *= isd; }
      SEG(2);
      const unsigned fo = (unsigned)((col0 >> 5) * 2048 + hf * 256), ro = (unsigned)((row >> 4) * 1024 + (row & 15) * 16);
      u32x4 v[2];
      if (SG == SG_W1 || SG == SG_W3) {
#pragma unroll
        for (int q = 0; q < 16; q += 2) {
          float g0 = gelu_tanh(acc[q]), g1 = gelu_tanh(acc[q + 1]);
          asm volatile("" : "+v"(g0), "+v"(g1));
          acc[q] = g0; acc[q + 1] = g1;
          piece(2 * i + 5, q >> 1, R >= i + 3 && i >= 2);
        }
      }
      widen32(acc, v);
      if (SG == SG_WO || SG == SG_W4) {
        LAS unsigned char* tw = lds + G_XCH + wl * 4096 + row * 64;
        *(LAS u32x4*)(tw + ((hf ^ (row >> 1)) & 3) * 16) = v[0]; *(LAS u32x4*)(tw + (((2 + hf) ^ (row >> 1)) & 3) * 16) = v[1];
        asm volatile("" ::: "memory");
        LAS unsigned char* tr = lds + G_XCH + wl * 4096 + (lane >> 2) * 64 + ((lane ^ (lane >> 3)) & 3) * 16;
        const unsigned so = (SG == SG_WO ? WS_P2 : WS_P8) + (unsigned)(b * 4 + kq) * 65536u, vo = (unsigned)((lane >> 2) * D_ + col0 + (lane & 3) * 8) * 2u;
        stx16(rs, vo, so, *(const LAS u32x4*)(tr)); stx16(rs, vo, so + 32768, *(const LAS u32x4*)(tr + 1024));
      } else if (PROJ) {
        const unsigned so = dst + (unsigned)(b * NH + cb) * 8192 + wl * 1024, vo = (unsigned)((row >> 4) * 4096 + hf * 256 + (row & 15) * 16);
        stx16(rs, vo, so, v[0]); stx16(rs, vo, so + 512, v[1]);
      } else if (SG == SG_W1) {
        const int grp = cb >> 1, sh = (grp & 1) ? -(1 + (grp >> 1)) : (1 + (grp >> 1));
        const int rho = 32 * b + row + sh + 2;
        const unsigned vo = (rho >= 2 && rho < S_ + 2) ? (unsigned)(rho >> 5) * 65536u + fo + (unsigned)((rho >> 4) & 1) * 1024u + (unsigned)(rho & 15) * 16u : 0xF0000000u;
        stx16(rs, vo, WS_H1S, v[0]); stx16(rs, vo, WS_H1S + 512, v[1]);
      } else if (SG == SG_W2) {
        if (!more) WAIT_V(0);
        res[0] = *(const LAS u32x4*)(lds + G_K + wl * 2048 + row * 64 + ((hf ^ (row >> 1)) & 3) * 16);
        res[1] = *(const LAS u32x4*)(lds + G_K + wl * 2048 + row * 64 + (((2 + hf) ^ (row >> 1)) & 3) * 16);
        float zs = 0.f, q2 = 0.f;
#pragma unroll
        for (int q = 0; q < 8; ++q) {
          const unsigned x = pack2(lo2f(res[q >> 2][q & 3]) + lo2f(v[q >> 2][q & 3]), hi2f(res[q >> 2][q & 3]) + hi2f(v[q >> 2][q & 3]));
          v[q >> 2][q & 3] = x; zs += lo2f(x) + hi2f(x); q2 = fmaf(lo2f(x), lo2f(x), fmaf(hi2f(x), hi2f(x), q2));
          asm volatile("" : "+v"(zs), "+v"(q2));
          piece(2 * i + 5, q, R >= i + 3 && i >= 2);
        }
        stx16(rs, fo + ro, WS_X2 + (unsigned)b * 65536u, v[0]); stx16(rs, fo + ro, WS_X2 + (unsigned)b * 65536u + 512, v[1]);
        const float mw = hsum(zs);
        q2 = hsum(q2);
        if (hf == 0) stx8(rs, (unsigned)((cb * 2 + (wl >> 1)) * 512 + row * 16 + (wl & 1) * 8), WS_ST5 + (unsigned)b * 8192, u32x2{f2u(mw), f2u(q2)});
      } else {
        stx16(rs, fo + ro, WS_U + (unsigned)b * 229376u, v[0]); stx16(rs, fo + ro, WS_U + (unsigned)b * 229376u + 512, v[1]);
      }
      if (PROJ && bstr == 3) {
#pragma unroll
        for (int q = 0; q < 2; ++q) {
          const int pc = (wk * 4 + wl) * 2 + q;
          if (pc < 660 && 3 * i + ZA + pc / 220 <= NSEG) stx16(rs, l16, X_REC + (unsigned)(3 * i + ZA) * RSTR + (unsigned)pc * 1024, u32x4{0u, 0u, 0u, 0u});
        }
      }
      SEG(3);
      pending = R >= i + 3 && i >= 2;
      if (!pending) { WAIT_V(0); announce(i); }
      if (R >= i + 3 && !(WEAVE && i >= 2)) fetch(2 * i + 5);
      if (!PROJ) WST(rs, wk, 16 + b);
      SEG(4);
    }
  }
  LBAR();
}

__device__ __forceinline__ void wait32(__amdgpu_buffer_rsrc_t rs, int first, int mine, int lane, unsigned need, unsigned& cap) {
  wait_ge(rs, 0, F_ST + (unsigned)(first + mine) * 128u, need, cap);
  wait_ge(rs, (unsigned)(lane & 31) * 128u, F_ST + (unsigned)first * 128u, need, cap);
}
__device__ __forceinline__ void sum_partials(__amdgpu_buffer_rsrc_t rs, unsigned l16, unsigned so, float (&a)[16]) {
  u32x4 pp[4][2];
#pragma unroll
  for (int k = 0; k < 4; ++k) { pp[k][0] = ldx16(rs, l16, so + k * 65536u); pp[k][1] = ldx16(rs, l16, so + k * 65536u + 1024); }
#pragma unroll
  for (int i = 0; i < 8; ++i) {
    f32x2 t = {0.f, 0.f};
#pragma unroll
    for (int k = 0; k < 4; ++k) t += f32x2{lo2f(pp[k][i >> 2][i & 3]), hi2f(pp[k][i >> 2][i & 3])};
    a[2 * i] = rbf(t[0]); a[2 * i + 1] = rbf(t[1]);
  }
}
__device__ __forceinline__ void put_row(__amdgpu_buffer_rsrc_t rs, unsigned l16, unsigned so, const float (&v)[16]) {
  stx16(rs, l16, so, u32x4{pack2(v[0], v[1]), pack2(v[2], v[3]), pack2(v[4], v[5]), pack2(v[6], v[7])});
  stx16(rs, l16, so + 1024, u32x4{pack2(v[8], v[9]), pack2(v[10], v[11]), pack2(v[12], v[13]), pack2(v[14], v[15])});
}
__device__ __forceinline__ void load_mod(__amdgpu_buffer_rsrc_t rs, int part, int lane, float (&v)[16], unsigned& cap) {
  for (;;) {
    asm volatile("" ::: "memory");
    u32x4 t[4];
#pragma unroll
    for (int i = 0; i < 4; ++i) t[i] = ldx16(rs, (unsigned)lane * 32, X_MOD + part * 4096 + (i >> 1) * 2048 + (i & 1) * 16);
    unsigned mn = 0xFFFFFFFFu;
#pragma unroll
    for (int i = 0; i < 16; ++i) { mn = min(mn, t[i >> 2][i & 3]); v[i] = hi2f(t[i >> 2][i & 3]); }
    if (__all(mn != 0u) || cap == 0u) break;
    cap = __builtin_amdgcn_readfirstlane(cap - 1u);
    __builtin_amdgcn_s_sleep(4);
  }
  cap = __builtin_amdgcn_readfirstlane(cap);
}
__device__ __forceinline__ void mod_part(__amdgpu_buffer_rsrc_t rs, const u16* wmod, const u16* cv, LAS float* red, int grp) {
  const int tid = threadIdx.x, kg = tid >> 2, c8 = tid & 3;
  u32x4 wv[8]; u16 cc[8];
#pragma unroll
  for (int i = 0; i < 8; ++i) cc[i] = cv[kg + 128 * i];
#pragma unroll
  for (int i = 0; i < 8; ++i) wv[i] = *(const u32x4*)(wmod + (size_t)(kg + 128 * i) * 6144 + grp * 32 + c8 * 8);
  SCHED();
  float acc[8] = {0.f, 0.f, 0.f, 0.f, 0.f, 0.f, 0.f, 0.f};
#pragma unroll
  for (int i = 0; i < 8; ++i) {
    const float v = bf2f(cc[i]), sc = rbf(v * __builtin_amdgcn_rcpf(1.0f + __builtin_amdgcn_exp2f(-1.4426950408889634f * v)));
#pragma unroll
    for (int j = 0; j < 4; ++j) { acc[2 * j] += sc * lo2f(wv[i][j]); acc[2 * j + 1] += sc * hi2f(wv[i][j]); }
  }
#pragma unroll
  for (int j = 0; j < 8; ++j) acc[j] = gsum(dpp_add<0x128>(dpp_add<0x124>(acc[j])));
  if ((tid & 63) < 4) {
    *(LAS f32x4*)(red + (tid >> 6) * 32 + c8 * 8) = f32x4{acc[0], acc[1], acc[2], acc[3]}; *(LAS f32x4*)(red + (tid >> 6) * 32 + c8 * 8 + 4) = f32x4{acc[4], acc[5], acc[6], acc[7]};
  }
  LBAR();
  if (tid < 32) {
    float u = 0.f;
#pragma unroll
    for (int i = 0; i < 8; ++i) u += red[i * 32 + tid];
    stx4(rs, (unsigned)tid * 4, X_MOD + (unsigned)grp * 128, f2u(rbf(u)) | 1u);
  }
}

__device__ __forceinline__ void stage_mid(__amdgpu_buffer_rsrc_t rs, const u16* x, int wk, int cu, unsigned& cap) {
  const int tid = threadIdx.x, lane = tid & 63, w = __builtin_amdgcn_readfirstlane(tid >> 6);
  const int id = w * 12 + cu, grp = id >> 5, rl = id & 31;
  const unsigned l16 = lane * 16;
  float ga[16], sc[16], sh[16];
  load_mod(rs, 2, lane, ga, cap); load_mod(rs, 4, lane, sc, cap); load_mod(rs, 3, lane, sh, cap);
#pragma unroll
  for (int i = 0; i < 16; ++i) sc[i] = rbf(1.0f + sc[i]);
  for (int seg = grp; seg < NSEG; seg += 3) {
    const int row = seg * 32 + rl;
    float v[16], a[16];
    load_row16(x + (size_t)row * D_, lane, v);
    wait32(rs, P_S2, rl, lane, (unsigned)seg + 1u, cap);
    sum_partials(rs, l16, WS_P2 + (unsigned)seg * 262144u + (unsigned)rl * 2048, a);
#pragma unroll
    for (int i = 0; i < 16; ++i) v[i] = rbf(v[i] + rbf(ga[i] * a[i]));
    put_row(rs, l16, WS_X1 + (unsigned)row * 2048, v);
    ln_row(v);
#pragma unroll
    for (int i = 0; i < 16; ++i) v[i] = rbf(rbf(v[i] * sc[i]) + sh[i]);
    put_row(rs, l16, WS_H2 + (unsigned)row * 2048, v);
    ln_row(v);
    put_row(rs, l16, WS_G + (unsigned)row * 2048, v);
    WAIT_V(0);
    if (lane == 0) stx4(rs, 0, F_ST + (unsigned)(P_S3 + grp * 32 + rl) * 128, (unsigned)seg + 1u);
    WST(rs, wk, 16 + seg);
  }
}

__device__ __forceinline__ void stage_out(__amdgpu_buffer_rsrc_t rs, u16* out, int wk, int cu, unsigned& cap) {
  const int tid = threadIdx.x, lane = tid & 63, w = __builtin_amdgcn_readfirstlane(tid >> 6);
  const int grp = w >> 2, rl = cu * 4 + (w & 3);
  const unsigned l16 = lane * 16;
  float gb[16];
  load_mod(rs, 5, lane, gb, cap);
  for (int b = grp; b < 65; b += 2) {
    const int t = 32 * b - 2 + rl;
    if (t < 0 || t >= S_) continue;
    wait_ge(rs, (unsigned)(lane & 7) * 128u, F_ST + P_S5 * 128u, (unsigned)b + 1u, cap);
    const unsigned xo = (unsigned)((lane >> 2) * 2048 + (lane & 3) * 256), xs = WS_X2 + (unsigned)b * 65536u + (unsigned)((rl >> 4) * 1024 + (rl & 15) * 16);
    const u32x4 x2a = ldx16(rs, xo, xs), x2b = ldx16(rs, xo, xs + 32768);
    const u32x4 x1a = ldx16(rs, l16, WS_X1 + (unsigned)t * 2048), x1b = ldx16(rs, l16, WS_X1 + (unsigned)t * 2048 + 1024);
    wait32(rs, P_S8, rl, lane, (unsigned)b + 1u, cap);
    float a[16];
    sum_partials(rs, l16, WS_P8 + (unsigned)b * 262144u + (unsigned)rl * 2048, a);
#pragma unroll
    for (int i = 0; i < 4; ++i) {
      a[2 * i] = lo2f(x1a[i]) + rbf(gb[2 * i] * rbf(lo2f(x2a[i]) + a[2 * i])); a[2 * i + 1] = hi2f(x1a[i]) + rbf(gb[2 * i + 1] * rbf(hi2f(x2a[i]) + a[2 * i + 1]));
      a[8 + 2 * i] = lo2f(x1b[i]) + rbf(gb[8 + 2 * i] * rbf(lo2f(x2b[i]) + a[8 + 2 * i])); a[9 + 2 * i] = hi2f(x1b[i]) + rbf(gb[9 + 2 * i] * rbf(hi2f(x2b[i]) + a[9 + 2 * i]));
    }
    store_row16(out + (size_t)t * D_, lane, a);
    WST(rs, wk, 16 + b);
  }
}

__device__ __forceinline__ void worker(const MainP& p, LAS unsigned char* lds, int xw, int j) {
  const int tid = threadIdx.x, lane = tid & 63, w = __builtin_amdgcn_readfirstlane(tid >> 6);
  const __amdgpu_buffer_rsrc_t rs = mk_rs(p.ws, WS_END);
  unsigned cap = SPIN_CAP;
  const int wk = xw * 32 + j;
  WST(rs, wk, 0);
  {
    const int c = j * 5 + xw, r0 = c * 8 + 2 * (w & 3);
    const bool two = r0 + 8 * NLN < S_;
    float v[4][16];
    if (c >= NLN - 64) mod_part(rs, p.wmod, p.c, (LAS float*)(lds + G_XCH), c - (NLN - 64));
    WST(rs, wk, 5);
    if (w < 4) {
#pragma unroll
      for (int q = 0; q < 4; ++q) load_row16(p.x + (size_t)(r0 + (q & 1) + (two ? (q >> 1) * 8 * NLN : 0)) * D_, lane, v[q]);
    }
    if (w >= 4) {
      if (c < 64) {
        const __amdgpu_buffer_rsrc_t rsO = mk_rs(p.Wo, D_ * HD * 2);
#pragma unroll
        for (int q = 0; q < 32; ++q) dma16(rsO, (unsigned)lane * 16, (unsigned)((q * 64 + c) * 4 + (w & 3)) * 1024u, (unsigned)(size_t)lds + G_K + (w & 3) * 1024);
      }
      const __amdgpu_buffer_rsrc_t rsW = mk_rs(xw < 3 ? p.Wk : p.Wq, D_ * HD * 2);
      const int ub = lane >> 3;
      const unsigned vB = (unsigned)(((ub & 3) + 8 * (ub >> 2)) * HD + 16 * (((lane >> 1) & 3) ^ (ub >> 1)) + 8 * (lane & 1)) * 2u, lbase = (unsigned)(size_t)lds;
#pragma unroll
      for (int q = 0; q < 16; ++q) {
        const int wq = (w & 3) + 4 * (q >> 3);
        const unsigned so = (unsigned)((32 * (wq >> 2) + 16 * ((wq >> 1) & 1) + 4 * (wq & 1) + (((q >> 2) & 1) * 8 + (q & 3)) * 64) * HD + j * 128) * 2u;
        const unsigned d = lbase + ((q >> 2) & 1) * 65536 + (q & 3) * 16384 + wq * 1024;
        dma16(rsW, vB, so, d); dma16(rsW, vB, so + 128, d + 8192);
      }
    } else {
#pragma unroll
      for (int q = 0; q < 4; ++q) ln_row(v[q]);
      float sc[16], sh[16];
      load_mod(rs, 1, lane, sc, cap); load_mod(rs, 0, lane, sh, cap);
      WST(rs, wk, 6);
#pragma unroll
      for (int i = 0; i < 16; ++i) sc[i] = rbf(1.0f + sc[i]);
#pragma unroll
      for (int q = 0; q < 4; ++q) {
#pragma unroll
        for (int i = 0; i < 16; ++i) v[q][i] = rbf(rbf(v[q][i] * sc[i]) + sh[i]);
        if (q < 2 || two) put_row(rs, (unsigned)lane * 16, WS_H + (unsigned)(r0 + (q & 1) + (q >> 1) * 8 * NLN) * 2048, v[q]);
      }
      WAIT_V(0);
    }
    BAR();
    if (tid == 0) stx4(rs, 0, F_LN + c * 128, 1u);
    WST(rs, wk, 1);
  }
  {
    const u16* nw = xw == 0 ? p.Wo + (size_t)(j >> 3) * 1024 * D_ + (j & 7) * 128 : xw == 3 ? p.w4 + (size_t)(j >> 3) * 896 * D_ + (j & 7) * 128
                  : xw == 2 ? p.w3 + j * 128 : (j < 8 ? p.w1 : p.w2) + (j & 7) * 128;
    const int nrows = (xw == 4 || (xw == 1 && j >= 16) || (xw == 2 && j >= 28)) ? 0 : xw == 3 ? 896 : 1024;
    const int mg = xw < 3 ? 64 + wk : 160 + j;
    stage_gemm<SG_PJ>(lds, rs, xw < 3 ? p.Wk : p.Wq, wk, xw < 3 ? xw : xw == 3 ? QB0 : 0, j, P_PJ + min(xw, 3) * 32 + j + (xw == 4 ? 400 : 0), cap, xw < 3 ? WS_K : WS_Q, xw < 3 ? 3 : 1,
                      xw == 0 ? 22 : xw < 3 ? 21 : xw == 3 ? NSEG - QB0 : QB0, nw, nrows, xw == 2 ? FF : D_, xw == 3 ? nullptr : p.wmod + mg * 32);
    WST(rs, wk, 3);
  }
  typedef __attribute__((address_space(4))) const MainP* KArg;
  KArg kp = (KArg)__builtin_amdgcn_kernarg_segment_ptr();
  asm volatile("" : "+s"(kp));
  if (xw != 3) { const int mg = xw < 3 ? 64 + wk : 160 + j; mod_part(rs, kp->wmod, kp->c, (LAS float*)(lds + G_XCH), mg); }
  if (xw == 0) stage_gemm<SG_WO>(lds, rs, kp->Wo, wk, j >> 3, j & 7, P_S2 + j, cap);
  else if (xw == 3) stage_gemm<SG_W4>(lds, rs, kp->w4, wk, j >> 3, j & 7, P_S8 + j, cap);
  else if (xw == 4) {
    unsigned chs = 0u;
    for (int sg = w >> 1; sg < NSEG; sg += 4) { attend(rs, sg, j, w & 1, lane, chs, cap); WST(rs, wk, 16 + sg); }
  }
  else if (j >= 28) stage_out(rs, kp->out, wk, (xw - 1) * 4 + j - 28, cap);
  else if (xw == 2) stage_gemm<SG_W3>(lds, rs, kp->w3, wk, 0, j, P_S7 + j, cap);
  else if (j < 8) stage_gemm<SG_W1>(lds, rs, kp->w1, wk, 0, j, P_S4 + j, cap);
  else if (j < 16) stage_gemm<SG_W2>(lds, rs, kp->w2, wk, 0, j - 8, P_S5 + j - 8, cap);
  else stage_mid(rs, kp->x, wk, j - 16, cap);
  if (cap == 0u && lane == 0) stx4(rs, 0, X_CTL, 1u);
}

#define ST_DECL
#define ST0()
#define ST(i)
#define ST_FLUSH()
constexpr int R_W = 0, R_MEM = 131072, R_QK = R_MEM + 16384, R_KT = R_QK + 8192, R_RED = R_MEM + 16384, R_LDS = 163840;
__device__ __forceinline__ void bar_lo(int w) { asm volatile("s_cmp_ge_u32 %0, 4\n\ts_cbranch_scc1 1f\n\ts_barrier\n\t1:" :: "s"(w) : "scc", "memory"); }
__device__ __forceinline__ void bar_hi(int w) { asm volatile("s_cmp_lt_u32 %0, 4\n\ts_cbranch_scc1 1f\n\ts_setprio 3\n\ts_barrier\n\t1:" :: "s"(w) : "scc", "memory"); }
#define ISSUE(R) asm volatile("buffer_load_dwordx4 v[" R "], %[vo], %[rs], %[so] offen sc1" : "=&{v[" R "]}"(a) : [vo] "v"(vo), [rs] "s"(rs), [so] "s"(so) : "memory")
template <int I> __device__ __forceinline__ void issue(__amdgpu_buffer_rsrc_t rs, unsigned vo, unsigned so, u32x4& a) {
  if (I == 0) ISSUE("240:243"); else if (I == 1) ISSUE("244:247"); else if (I == 2) ISSUE("248:251"); else ISSUE("252:255");
}
#define ARRIVE(R0, R1, R2, R3, CNT) \
  asm volatile("s_waitcnt vmcnt(" #CNT ")\n\t" \
               "1:\n\t" \
               "v_min3_u32 %[t], v" #R0 ", v" #R1 ", v" #R2 "\n\t" \
               "v_min_u32 %[t], %[t], v" #R3 "\n\t" \
               "v_cmp_eq_u32 vcc, 0, %[t]\n\t" \
               "s_cbranch_vccz 2f\n\t" \
               "s_mov_b64 exec, vcc\n\t" \
               "buffer_load_dwordx4 v[" #R0 ":" #R3 "], %[vo], %[rs], %[so] offen sc1\n\t" \
               "s_mov_b64 exec, -1\n\t" \
               "s_waitcnt vmcnt(0)\n\t" \
               "s_sub_u32 %[cap], %[cap], 1\n\t" \
               "s_cbranch_scc0 1b\n\t" \
               "s_mov_b32 %[cap], 0\n\t" \
               "2:" \
               : "+{v[" #R0 ":" #R3 "]}"(a), [t] "=&v"(t), [cap] "+s"(cap) : [vo] "v"(vo), [rs] "s"(rs), [so] "s"(so) : "vcc", "scc", "memory")
template <int I> __device__ __forceinline__ void arrive(__amdgpu_buffer_rsrc_t rs, unsigned vo, unsigned so, u32x4& a, unsigned& cap) {
  unsigned t;
  if (I == 0) ARRIVE(240, 241, 242, 243, 3);
  else if (I == 1) ARRIVE(244, 245, 246, 247, 2);
  else if (I == 2) ARRIVE(248, 249, 250, 251, 1);
  else ARRIVE(252, 253, 254, 255, 0);
}
__device__ __forceinline__ unsigned half_of(unsigned v) { unsigned r; asm volatile("v_lshrrev_b32 %0, 1, %1" : "=v"(r) : "v"(v)); return r; }
__device__ __forceinline__ void issue2(__amdgpu_buffer_rsrc_t rs, unsigned l8, unsigned so, int w, u32x4& a) {
  asm volatile("s_cmp_ge_u32 %[w], 4\n\ts_cbranch_scc1 1f\n\tv_lshrrev_b32 v255, 1, %[l8]\n\t"
               "buffer_load_dwordx2 v[252:253], v255, %[rs], %[so] offen sc1\n\tbuffer_load_dwordx2 v[254:255], v255, %[rs], %[so] offen offset:512 sc1\n\t1:"
               : "=&{v[252:255]}"(a) : [l8] "v"(l8), [rs] "s"(rs), [so] "s"(so), [w] "s"(w) : "scc", "memory");
}
__device__ __forceinline__ void arrive2(__amdgpu_buffer_rsrc_t rs, unsigned l8, unsigned so, u32x4& a, unsigned& cap) {
  unsigned t;
  asm volatile("s_waitcnt vmcnt(0)\n\t"
               "1:\n\t"
               "v_min3_u32 %[t], v252, v253, v254\n\t"
               "v_min_u32 %[t], %[t], v255\n\t"
               "v_cmp_eq_u32 vcc, 0, %[t]\n\t"
               "s_cbranch_vccz 2f\n\t"
               "v_lshrrev_b32 %[t], 1, %[l8]\n\t"
               "buffer_load_dwordx2 v[252:253], %[t], %[rs], %[so] offen sc1\n\t"
               "buffer_load_dwordx2 v[254:255], %[t], %[rs], %[so] offen offset:512 sc1\n\t"
               "s_waitcnt vmcnt(0)\n\t"
               "s_sub_u32 %[cap], %[cap], 1\n\t"
               "s_cbranch_scc0 1b\n\t"
               "s_mov_b32 %[cap], 0\n\t"
               "2:"
               : "+{v[252:255]}"(a), [t] "=&v"(t), [cap] "+s"(cap) : [l8] "v"(l8), [rs] "s"(rs), [so] "s"(so) : "vcc", "scc", "memory");
}
__device__ __forceinline__ unsigned ror8(unsigned v) { return __builtin_amdgcn_update_dpp(0u, v, 0x128, 0xF, 0xF, true); }
template <class F>
__device__ __forceinline__ void gather_mem(__amdgpu_buffer_rsrc_t rs, unsigned lbase, int s, int w, unsigned l16, unsigned& cap, F&& meanwhile) {
  const unsigned so = X_MEM + s * RSTR + w * 2048;
  u32x4 a0, a1;
  issue<2>(rs, l16, so, a0); issue<3>(rs, l16, so + 1024, a1);
  meanwhile();
  LAS unsigned char* d = lad(l16, lbase + R_MEM + w * 2048);
  arrive<2>(rs, l16, so, a0, cap);
  *(LAS u32x4*)(d) = a0;
  arrive<3>(rs, l16, so + 1024, a1, cap);
  *(LAS u32x4*)(d + 1024) = a1;
}
__device__ __forceinline__ unsigned mem_off(int lane) { return (unsigned)((lane & 1) * 512 + (((lane >> 1) ^ ((lane & 1) * 16)) * 8)); }
__device__ __forceinline__ bf16x8 mem_frag(LAS unsigned char* mp, int chunk) { return cat8(trread(mp + chunk * 1024), trread(mp + chunk * 1024 + 256)); }
__device__ __forceinline__ bf16x16 w_frag(LAS unsigned char* p) {
  return cat16(cat8(trread(p), trread(p + 2048)), cat8(trread(p + 4096), trread(p + 6144)));
}
__device__ __forceinline__ bf16x16 l_frag(LAS unsigned char* p, int i) { return cat16(*(const LAS bf16x8*)(p + i * 2048), *(const LAS bf16x8*)(p + i * 2048 + 1024)); }
__device__ __forceinline__ void make_resident(LAS unsigned char* wb, unsigned lo, unsigned l16) {
#pragma unroll
  for (int c = 0; c < 2; ++c) {
    bf16x16 f[4];
#pragma unroll
    for (int t = 0; t < 4; ++t) f[t] = w_frag(wb + c * 8192 + (lo ^ (t << 5)));
    WAIT_L0(); SCHED();
#pragma unroll
    for (int t = 0; t < 4; ++t) {
      *(LAS bf16x8*)(wb + (c * 4 + t) * 2048 + l16) = __builtin_shufflevector(f[t], f[t], 0, 1, 2, 3, 4, 5, 6, 7);
      *(LAS bf16x8*)(wb + (c * 4 + t) * 2048 + 1024 + l16) = __builtin_shufflevector(f[t], f[t], 8, 9, 10, 11, 12, 13, 14, 15);
    }
    WAIT_L0(); SCHED();
  }
}
__device__ __forceinline__ f32x4 fold(f32x4 a, f32x4 b) {
  f32x4 r;
#pragma unroll
  for (int i = 0; i < 4; ++i) {
    const auto t = __builtin_amdgcn_permlane32_swap(f2u(a[i]), f2u(b[i]), false, false);
    r[i] = u2f(t[0]) + u2f(t[1]);
  }
  return r;
}

struct Sched { unsigned tau, prev; int ea, eb, d1; bool moved; };
template <int CTRL> __device__ __forceinline__ int dpp_max(int v) { return max(v, (int)__builtin_amdgcn_update_dpp((unsigned)v, (unsigned)v, CTRL, 0xF, 0xF, false)); }
__device__ __forceinline__ int wave_max(int v) {
  v = dpp_max<0xB1>(v); v = dpp_max<0x4E>(v); v = dpp_max<0x141>(v); v = dpp_max<0x140>(v);
  return max(max(__builtin_amdgcn_readlane(v, 0), __builtin_amdgcn_readlane(v, 16)), max(__builtin_amdgcn_readlane(v, 32), __builtin_amdgcn_readlane(v, 48)));
}
__device__ __forceinline__ void times_issue(__amdgpu_buffer_rsrc_t rs, unsigned l16, int s, unsigned& xb, unsigned& xa) {
  asm volatile("v_lshlrev_b32 %0, 3, %2\n\tv_and_b32 %1, 0xF80, %0\n\tbuffer_load_dword %0, %0, %3, %4 offen sc1\n\tbuffer_load_dword %1, %1, %3, %5 offen sc1"
               : "=&v"(xb), "=&v"(xa) : "v"(l16), "s"(rs), "s"(X_TS + (unsigned)s * RSTR), "s"(X_TS + (unsigned)s * RSTR + 8192) : "memory");
}
__device__ __forceinline__ void retime(__amdgpu_buffer_rsrc_t rs, unsigned l16, int s, unsigned xb, unsigned xa, Sched& t, const MainP& p, unsigned& cap) {
  if (s > 0) {
    while (!__all(min(xa, xb) != 0u) && cap != 0u) {
      times_issue(rs, l16, s, xb, xa);
      asm volatile("s_waitcnt vmcnt(0)" : "+v"(xb), "+v"(xa) :: "memory");
      cap = __builtin_amdgcn_readfirstlane(cap - 1u);
    }
    cap = __builtin_amdgcn_readfirstlane(cap);
    const int tb = wave_max((int)(xb - t.prev)), ta = wave_max((int)(xa - t.prev));
    const int late = tb + p.d3 - (int)(t.tau - t.prev);
    if (s > 2) {
      const int up = s < 5 ? 80 : 8, dn = s < 10 ? 40 : 8;
      if (!t.moved) t.ea = max(t.ea - dn, min(ta * 8, t.ea + up));
      if (ta + p.d3 <= t.d1) t.eb = max(t.eb - dn, min((tb - t.d1) * 8, t.eb + up));
    }
    t.moved = late > 0;
    if (late > 0) t.tau += late + min(p.d4, 8 * late);
  }
  t.d1 = (t.ea >> 3) + p.d1;
}
__device__ __forceinline__ void advance(Sched& t, const MainP& p) { t.prev = t.tau; t.tau += t.d1 + (t.eb >> 3) + p.d2; }

__device__ __forceinline__ void role_a(const MainP& p, LAS unsigned char* lds, int h) {
  const int tid = threadIdx.x, lane = tid & 63, w = __builtin_amdgcn_readfirstlane(tid >> 6);
  const int cg = w & 3, kh = w >> 2;
  const __amdgpu_buffer_rsrc_t rs = mk_rs(p.ws, WS_END);
  const unsigned lbase = (unsigned)(size_t)lds;
  unsigned l16 = lane * 16;
  asm volatile("" : "+v"(l16));
  const int sidx = (lane & 8) ? 0xEEEE : 0x4444;
  bf16x16 W[24];
  unsigned qoff;
  {
    const int fr = lane & 15, g = lane >> 4, tu = 4 * (g & 1) + (fr >> 2), ub = lane >> 3;
    const unsigned lo = (g >> 1) * 1024 + tu * 128 + ((tu >> 1) << 5) + (fr & 3) * 8;
    qoff = R_QK + ((fr >> 1) & 1) * 512 + (g >> 1) * 256 + (fr & 1) * 128 + (8 * (g & 1) + (fr >> 2)) * 8;
    const __amdgpu_buffer_rsrc_t rw = mk_rs(cg < 2 ? p.Wq : p.Wk, D_ * HD * 2);
    const unsigned vo = (unsigned)(((ub & 3) + 8 * (ub >> 2)) * HD + 16 * (((lane >> 1) & 3) ^ (ub >> 1)) + 8 * (lane & 1)) * 2u;
    const unsigned so = (unsigned)(kh * 512 * HD + h * DH + (cg & 1) * 64) * 2u;
    auto stage = [&](int c) {
#pragma unroll
      for (int rho = 0; rho < 8; ++rho) dma16(rw, vo, so + (unsigned)((c * 64 + 32 * (rho >> 2) + 16 * (rho & 1) + 4 * ((rho >> 1) & 1)) * HD) * 2u, lbase + R_W + w * 16384 + (c & 1) * 8192 + rho * 1024);
    };
    stage(0); stage(1);
#pragma unroll
    for (int c = 0; c < 6; ++c) {
      WAIT_V(8);
#pragma unroll
      for (int t = 0; t < 4; ++t) W[4 * c + t] = w_frag(lds + R_W + w * 16384 + (c & 1) * 8192 + (lo ^ (t << 5)));
      WAIT_L0(); SCHED();
      stage(c + 2);
    }
    WAIT_V(0);
    make_resident(lds + R_W + w * 16384, lo, l16);
  }
  WST(rs, 160 + (h >> 1), 4);
  unsigned cap = SPIN_CAP;
  wait_ge(rs, 0, F_ST + (unsigned)(P_PJ + h) * 128, 1u, cap);
  dma16x(rs, l16, WS_K + h * 8192 + w * 1024, lbase + R_KT + w * 1024);
  Sched tt; tt.tau = rt32() - 100000; tt.prev = tt.tau; tt.ea = 195 * 8; tt.eb = 118 * 8; tt.d1 = 0; tt.moved = true;
  const int cu_ = h; (void)cu_;
  ST_DECL
  for (int s = 0; s < NSEG; ++s) {
    LAS unsigned char* lp = lad(l16, lbase + R_W + w * 16384);
    bf16x16 L[2];
    L[0] = l_frag(lp, 0); L[1] = l_frag(lp, 1);
    unsigned lz = l16;
    asm volatile("" : "+v"(lz));
    LAS unsigned char* mp = lad(mem_off((int)(lz >> 4)), lbase + R_MEM + kh * 8192);
    SCHED();
    if (w == 7) wait_until(tt.tau);
    BAR();
    ST0();
    gather_mem(rs, lbase, s, (w + h) & 7, l16, cap, []() {});
    ST(1);
    LBAR();
    ST(2);
    f32x4 c[4];
#pragma unroll
    for (int t = 0; t < 4; ++t) c[t] = f32x4{0.f, 0.f, 0.f, 0.f};
    bf16x8 al, af[2];
    u32x4 vt0, vt1;
    u32x4 vmv;
    const unsigned vso = X_VM + (unsigned)s * RSTR + h * 4096 + cg * 1024;
    al = mem_frag(mp, 6); af[0] = mem_frag(mp, 0); af[1] = mem_frag(mp, 1);
#pragma unroll
    for (int i = 0; i < 8; ++i) {
      c[i & 3] = smf(al, L[i & 1], c[i & 3], sidx);
      SCHED();
      if (i == 3) al = mem_frag(mp, 7);
      if (i < 6) L[i & 1] = l_frag(lp, i + 2);
      if (i == 6) {
        const unsigned vo = WS_VT + (unsigned)(s * NH + h) * 8192 + cg * 2048;
        asm volatile("s_cmp_ge_u32 %5, 4\n\ts_cbranch_scc1 1f\n\t"
                     "buffer_load_dwordx4 %0, %2, %3, %4 offen sc1\n\tbuffer_load_dwordx4 %1, %2, %3, %4 offen offset:1024 sc1\n\t1:"
                     : "=&v"(vt0), "=&v"(vt1) : "v"(l16), "s"(rs), "s"(vo), "s"(w) : "scc", "memory");
      }
      SCHED();
#pragma unroll
      for (int r = 3 * i; r < 3 * i + 3; ++r) {
        c[(r + 1) & 3] = smf(af[(r >> 2) & 1], W[(r & ~3) + ((r + 1) & 3)], c[(r + 1) & 3], sidx);
        SCHED();
        if ((r & 3) == 3 && (r >> 2) + 2 < 6) af[(r >> 2) & 1] = mem_frag(mp, (r >> 2) + 2);
        SCHED();
      }
    }
    {
      LAS unsigned char* qw = lad(half_of(l16), lbase + R_QK + cg * 2048 + kh * 512);
      const f32x4 f0 = fold(c[0], c[1]);
      *(LAS u32x2*)(qw) = u32x2{pack2(f0[0], f0[1]), pack2(f0[2], f0[3])};
      const f32x4 f1 = fold(c[2], c[3]);
      *(LAS u32x2*)(qw + 1024) = u32x2{pack2(f1[0], f1[1]), pack2(f1[2], f1[3])};
    }
    ST(3);
    if (w < 4) {
      bf16x8 k1[4], k2[4];
      LAS unsigned char* kb = lad(l16, lbase + R_KT);
#pragma unroll
      for (int ks = 0; ks < 2; ++ks) { k1[ks] = *(const LAS bf16x8*)(kb + ks * 1024); k2[ks] = *(const LAS bf16x8*)(kb + 4096 + ks * 1024); }
      unsigned ov;
      {
        unsigned ot;
        asm volatile("v_and_b32 %0, 0x30, %2\n\tv_lshrrev_b32 %1, 2, %2\n\tv_and_b32 %1, 0xC0, %1\n\tv_or_b32 %0, %0, %1\n\t"
                     "v_and_b32 %1, 0x40, %2\n\tv_lshl_or_b32 %0, %1, 9, %0\n\tv_and_b32 %1, 0x80, %2\n\tv_lshl_or_b32 %0, %1, 24, %0"
                     : "=&v"(ov), "=&v"(ot) : "v"(l16));
      }
      LBAR();
      ST(4);
      issue2(rs, l16, vso, w, vmv);
      f32x4 t0 = {0.f, 0.f, 0.f, 0.f}, t1 = t0, t2 = t0;
      LAS unsigned char* qb = lad(qoff, lbase);
      bf16x8 qf[4], kf[4];
#pragma unroll
      for (int ks = 0; ks < 2; ++ks) {
        qf[ks] = cat8(trread(qb + ks * 1024), trread(qb + ks * 1024 + 32));
        kf[ks] = cat8(trread(qb + 4096 + ks * 1024), trread(qb + 4096 + ks * 1024 + 32));
      }
      SCHED();
#pragma unroll
      for (int ks = 0; ks < 4; ++ks) {
        t1 = mfma32(k1[ks], qf[ks], t1); t2 = mfma32(k2[ks], qf[ks], t2); t0 = mfma32(kf[ks], qf[ks], t0);
        SCHED();
        if (ks < 2) {
          qf[ks + 2] = cat8(trread(qb + (ks + 2) * 1024), trread(qb + (ks + 2) * 1024 + 32));
          k1[ks + 2] = *(const LAS bf16x8*)(kb + (ks + 2) * 1024); k2[ks + 2] = *(const LAS bf16x8*)(kb + 4096 + (ks + 2) * 1024);
          kf[ks + 2] = cat8(trread(qb + 4096 + (ks + 2) * 1024), trread(qb + 4096 + (ks + 2) * 1024 + 32));
        }
        SCHED();
      }
      ST(5);
      asm volatile("s_waitcnt vmcnt(2)" : "+v"(vt0), "+v"(vt1) :: "memory");
      {
        const float sl = 0.08838834764831845f * 1.4426950408889634f;
        float z[4] = {t1[0], t1[1], t1[2], t1[3]}, y = t0[0], mx;
        {
          float u1 = t0[1], u2 = t0[2], u3 = t0[3];
#define M8(Z, B) "v_add_f32_dpp " Z ", " Z ", " Z " row_ror:8 row_mask:0xf bank_mask:0x3\n\tv_add_f32_dpp " Z ", " B ", " B " row_ror:8 row_mask:0xf bank_mask:0xc\n\t"
          asm volatile("s_nop 7\n\t"
              "v_permlane32_swap_b32 %4, %6\n\tv_permlane32_swap_b32 %5, %7\n\t"
              M8("%0", "%9") "v_add_f32 %4, %4, %6\n\tv_add_f32 %5, %5, %7\n\t" M8("%1", "%10") M8("%2", "%11") M8("%4", "%5") M8("%3", "%12")
              "v_max3_f32 %8, %0, %1, %2\n\tv_max3_f32 %8, %8, %4, %3\n\ts_nop 1\n\t"
              "v_max_f32_dpp %8, %8, %8 row_ror:8 row_mask:0xf bank_mask:0xf\n\tv_mov_b32 %5, %8\n\ts_nop 1\n\t"
              "v_permlane16_swap_b32 %8, %5\n\tv_max_f32 %8, %8, %5\n\tv_mov_b32 %5, %8\n\ts_nop 1\n\t"
              "v_permlane32_swap_b32 %8, %5\n\tv_max_f32 %8, %8, %5"
              : "+v"(z[0]), "+v"(z[1]), "+v"(z[2]), "+v"(z[3]), "+v"(y), "+v"(u1), "+v"(u2), "+v"(u3), "=&v"(mx) : "v"(t2[0]), "v"(t2[1]), "v"(t2[2]), "v"(t2[3]));
#undef M8
        }
        const float nm = -mx * sl;
        y = __builtin_amdgcn_exp2f(fmaf(y, sl, nm));
#pragma unroll
        for (int r = 0; r < 4; ++r) z[r] = __builtin_amdgcn_exp2f(fmaf(z[r], sl, nm));
        const unsigned pm1 = pack2(y, u2f(ror8(f2u(y))));
        const auto pms = __builtin_amdgcn_permlane32_swap(pm1, pm1, false, false);
        const u32x2 pm = {pms[0], pms[1]};
        const unsigned p0 = pack2(z[0], z[1]), p1 = pack2(z[2], z[3]);
        const u32x4 pt = {p0, p1, ror8(p0), ror8(p1)};
        f32x4 o0 = mfma32(__builtin_bit_cast(bf16x8, vt0), __builtin_bit_cast(bf16x8, pt), f32x4{0.f, 0.f, 0.f, 0.f});
        f32x4 o1 = mfma32(__builtin_bit_cast(bf16x8, vt1), __builtin_bit_cast(bf16x8, pt), f32x4{0.f, 0.f, 0.f, 0.f});
        float inv = __builtin_amdgcn_rcpf(gsum(dpp_add<0x128>(((z[0] + z[1]) + (z[2] + z[3])) + y)));
        asm volatile("" : "+v"(inv));
        arrive2(rs, l16, vso, vmv, cap);
        const f32x4 n0 = mfma16n(u32x2{vmv[0], vmv[1]}, pm, f32x4{0.f, 0.f, 0.f, 0.f}), n1 = mfma16n(u32x2{vmv[2], vmv[3]}, pm, f32x4{0.f, 0.f, 0.f, 0.f});
        SCHED();
        o0 *= inv; o1 *= inv;
        {
          const u32x4 v = {nz(pack2(fmaf(n0[0], inv, o0[0]), fmaf(n0[1], inv, o0[1]))), nz(pack2(fmaf(n0[2], inv, o0[2]), fmaf(n0[3], inv, o0[3]))),
                           nz(pack2(fmaf(n1[0], inv, o1[0]), fmaf(n1[1], inv, o1[1]))), nz(pack2(fmaf(n1[2], inv, o1[2]), fmaf(n1[3], inv, o1[3])))};
          stx16(rs, ov, X_OX + s * RSTR + h * 1024 + w * 256, v);
        }
        if (w == 3) { const unsigned t = max(rt32(), 1u); if (l16 == 0) stx4(rs, 0, X_TS + (unsigned)(s + 1) * RSTR + (unsigned)(64 + h) * 128, t); }
        ST(8);
      }
    } else {
      LBAR();
      LAS unsigned char* qb = lad(qoff, lbase + 4096 + (w - 4) * 1024);
      u32x4 kf = __builtin_bit_cast(u32x4, cat8(trread(qb), trread(qb + 32)));
#pragma unroll
      for (int i = 0; i < 4; ++i) kf[i] = pack2(dpp_add<0x128>(lo2f(kf[i])), dpp_add<0x128>(hi2f(kf[i])));
      stx16(rs, l16, WS_KM + (unsigned)(s * NH + h) * 4096 + (w - 4) * 1024, kf);
      WAIT_V(0);
    }
    LBAR();
    if (w == 4 && l16 == 0) stx4(rs, 0, F_CH + h * 128, (unsigned)s + 1u);
    ST(9);
    if (w == 7) {
      unsigned xb, xa;
      times_issue(rs, l16, s, xb, xa);
      asm volatile("s_waitcnt vmcnt(0)" : "+v"(xb), "+v"(xa) :: "memory");
      retime(rs, l16, s, xb, xa, tt, p, cap); advance(tt, p);
    }
    if (s + 1 < NSEG) {
      unsigned fo;
      asm volatile("v_mov_b32 %0, 0" : "=v"(fo));
      wait_ge(rs, fo, F_ST + (unsigned)(P_PJ + ((s + 1) % 3) * 32 + h) * 128, (unsigned)s + 2u, cap);
      dma16x(rs, l16, WS_K + (unsigned)((s + 1) * NH + h) * 8192 + w * 1024, lbase + R_KT + w * 1024);
    }
  }
  ST_FLUSH();
  if (cap == 0u && l16 == 0) stx4(rs, 0, X_CTL, 1u);
}

__device__ __forceinline__ void role_vb(const MainP& p, LAS unsigned char* lds, int rg, int cb) {
  const int tid = threadIdx.x, lane = tid & 63, w = __builtin_amdgcn_readfirstlane(tid >> 6);
  const int kh = w >> 2, wr = (w + cb) & 7;
  const __amdgpu_buffer_rsrc_t rs = mk_rs(p.ws, WS_END);
  const unsigned lbase = (unsigned)(size_t)lds;
  unsigned l16 = lane * 16, moff = mem_off(lane), roff, soff = (unsigned)((lane >> 5) * 256 + (lane & 31) * 4);
  const int sidx = (lane & 8) ? 0xEEEE : 0x4444;
  bf16x16 WV[8], WB[16];
  {
    const int fr = lane & 15, g = lane >> 4, tu = 4 * (g & 1) + (fr >> 2), ub = lane >> 3;
    { const int sl = mem_slot(16 * (g >> 1) + fr); roff = R_RED + w * 512 + ((sl >> 5) * 32 + (sl & 15) * 2 + (g & 1)) * 8; }
    const __amdgpu_buffer_rsrc_t rv = mk_rs(p.Wv, D_ * HD * 2), ro = mk_rs(p.Wo, D_ * HD * 2);
    const unsigned vv = (unsigned)(((ub & 3) + 8 * (ub >> 2)) * HD + 16 * (((lane >> 1) & 3) ^ (ub >> 1)) + 8 * (lane & 1)) * 2u;
    const unsigned sv = (unsigned)((32 * (w >> 2) + 16 * (w & 1) + 4 * ((w >> 1) & 1)) * HD + cb * DH + rg * 64) * 2u;
#pragma unroll
    for (int c = 0; c < 16; ++c) dma16(rv, vv, sv + (unsigned)c * 64 * HD * 2, lbase + R_W + c * 8192 + w * 1024);
    WAIT_V(0); BAR();
    const unsigned lo = (g >> 1) * 1024 + tu * 128 + (((tu >> 1) ^ (w & 3)) << 5) + (fr & 3) * 8;
#pragma unroll
    for (int c = 0; c < 8; ++c) WV[c] = w_frag(lds + R_W + (kh * 8 + c) * 8192 + lo);
    LBAR();
    const int u = (lane >> 2) & 7;
    const unsigned vw = (unsigned)((16 * (u >> 2) + 8 * ((u >> 1) & 1) + 4 * (lane >> 5) + (u & 1)) * D_ + (lane & 3) * 8) * 2u;
    const unsigned sw = (unsigned)(wr * 512 * D_ + cb * 32) * 2u;
    const unsigned bo = (g >> 1) * 1024 + tu * 64 + (fr & 3) * 8;
    auto stageo = [&](int hd) {
#pragma unroll
      for (int q = 0; q < 8; ++q)
        dma16(ro, vw, sw + (unsigned)((hd * 128 + 64 * (q >> 2) + 32 * (q & 1) + 2 * ((q >> 1) & 1)) * D_) * 2u, lbase + R_W + w * 16384 + (hd & 1) * 8192 + q * 1024);
    };
    stageo(0); stageo(1);
#pragma unroll
    for (int hd = 0; hd < 4; ++hd) {
      if (hd < 3) wait_v<8>(); else wait_v<0>();
#pragma unroll
      for (int f = 0; f < 4; ++f) WB[hd * 4 + f] = w_frag(lds + R_W + w * 16384 + (hd & 1) * 8192 + (f >> 1) * 512 + (f & 1) * 32 + bo);
      WAIT_L0(); SCHED();
      if (hd < 2) stageo(hd + 2);
    }
  }
  WST(rs, 176 + rg * 8 + (cb >> 2), 4);
  unsigned hsrc = (unsigned)((lane >> 3) * 2048 + (((lane & 7) ^ (lane >> 3)) << 4)), hoff = (unsigned)((lane & 15) * 128 + (((lane >> 4) ^ (lane & 7)) << 4));
  asm volatile("" : "+v"(l16), "+v"(moff), "+v"(roff), "+v"(soff), "+v"(hsrc), "+v"(hoff));
  unsigned cap = SPIN_CAP;
  auto h_piece = [&](int b, int q) { dma16x(rs, hsrc, WS_H + (unsigned)b * 65536u + (w & 3) * 16384 + (2 * q + kh) * 128, lbase + R_W + (b & 1) * 65536 + (q * 8 + w) * 1024); };
  auto h_fetch = [&](int b) {
#pragma unroll
    for (int q = 0; q < 8; ++q) h_piece(b, q);
  };
  f32x4 x0, x1;
  auto v_part = [&](int b, auto part, auto&& each) {
    constexpr int K0 = decltype(part)::value * 8;
    LAS unsigned char* a0 = lad(hoff, lbase + R_W + (b & 1) * 65536 + kh * 32768);
    LAS unsigned char* a1 = lad<64>(hoff, lbase + R_W + (b & 1) * 65536 + kh * 32768);
    bf16x8 af[3][2];
    auto rd = [&](int k) { LAS unsigned char* q = ((k & 1) ? a1 : a0) + (k >> 1) * 4096; af[k % 3][0] = *(const LAS bf16x8*)(q); af[k % 3][1] = *(const LAS bf16x8*)(q + 2048); };
    rd(K0); rd(K0 + 1);
    SCHED();
#pragma unroll
    for (int k = K0; k < K0 + 8; ++k) {
      if (k + 2 < K0 + 8) rd(k + 2);
      const bf16x8 wf = (k & 1) ? __builtin_shufflevector(WV[k >> 1], WV[k >> 1], 8, 9, 10, 11, 12, 13, 14, 15) : __builtin_shufflevector(WV[k >> 1], WV[k >> 1], 0, 1, 2, 3, 4, 5, 6, 7);
      x0 = mfma32(af[k % 3][0], wf, k ? x0 : f32x4{0.f, 0.f, 0.f, 0.f}); x1 = mfma32(af[k % 3][1], wf, k ? x1 : f32x4{0.f, 0.f, 0.f, 0.f});
      asm volatile("" : "+v"(x0), "+v"(x1));
      SCHED();
      each(k);
      SCHED();
    }
    if (K0) {
      LAS unsigned char* xv = lad(l16, lbase + R_RED + 4096 + (w & 3) * 2048);
      asm volatile("s_nop 7\n\ts_cmp_lt_u32 %3, 4\n\ts_cbranch_scc1 1f\n\tds_write_b128 %0, %1\n\tds_write_b128 %0, %2 offset:1024\n\t1:" :: "v"(xv), "v"(x0), "v"(x1), "s"(w) : "scc", "memory");
    }
  };
  auto v_out = [&](int b) {
    if (!kh && b < NSEG) {
      LAS unsigned char* xv = lad(l16, lbase + R_RED + 4096 + (w & 3) * 2048);
      x0 += *(const LAS f32x4*)(xv); x1 += *(const LAS f32x4*)(xv + 1024);
      stx16(rs, l16, WS_VT + (unsigned)(b * NH + cb) * 8192 + (rg * 4 + w) * 1024, u32x4{pack2(x0[0], x0[1]), pack2(x0[2], x0[3]), pack2(x1[0], x1[1]), pack2(x1[2], x1[3])});
    }
  };
  typedef std::integral_constant<int, 0> P0; typedef std::integral_constant<int, 1> P1;
  BAR();
  wait_ge(rs, l16 * 8, F_LN, 1u, cap);
  h_fetch(0); h_fetch(1);
  WAIT_V(8); BAR();
  v_part(0, P0{}, [](int) {}); v_part(0, P1{}, [](int) {});
  WAIT_V(0); LBAR();
  v_out(0); h_fetch(2);
  LBAR();
  v_part(1, P0{}, [](int) {}); v_part(1, P1{}, [](int) {});
  WAIT_V(0); LBAR();
  v_out(1); h_fetch(3);
  v_part(2, P0{}, [](int) {});
  wait_ge(rs, l16 * 8, F_LN + 8192, 1u, cap); wait_ge(rs, (l16 & 0x1F0) * 8, F_LN + 16384, 1u, cap);
  WAIT_V(0); BAR();
  if (w == 0) {
    const int ln = l16 >> 4, sl = (ln >> 1) & 15, col = cb * 32 + (sl >> 3) * 16 + (ln >> 5) * 8 + (sl & 4) + (sl & 1) * 2 + ((sl >> 1) & 1), r = rg * 4 + (ln & 1) * 2;
    stx4(rs, soff, X_MEM + (cb >> 1) * 1024 + rg * 512 + ((cb ^ rg) & 1) * 128, nz((unsigned)p.mem0[r * D_ + col] | ((unsigned)p.mem0[(r + 1) * D_ + col] << 16)));
  }
  Sched tt; tt.tau = rt32() - 100000; tt.prev = tt.tau; tt.ea = 195 * 8; tt.eb = 118 * 8; tt.d1 = 0; tt.moved = true;
  const int cu_ = 32 + (rg * 32 + cb) / 2; (void)cu_;
  ST_DECL
  for (int s = 0; s < NSEG; ++s) {
    if (w == 7) wait_until(tt.tau);
    BAR();
    ST0();
    gather_mem(rs, lbase, s, wr, l16, cap, [&]() { v_part(s + 2, P1{}, [](int) {}); });
    ST(1);
    LBAR();
    ST(2);
    {
      f32x4 c0 = {0.f, 0.f, 0.f, 0.f};
      LAS unsigned char* mp = lad(moff, lbase + R_MEM + kh * 8192);
      bf16x8 af[6];
#pragma unroll
      for (int c = 0; c < 6; ++c) af[c] = mem_frag(mp, c);
      SCHED();
#pragma unroll
      for (int c = 0; c < 8; ++c) { c0 = smf(af[c % 6], WV[c], c0, sidx); SCHED(); if (c < 2) { af[c] = mem_frag(mp, 6 + c); SCHED(); } }
      const f32x4 f = fold(c0, c0);
      if (l16 < 512) stx8(rs, half_of(l16), X_VM + (unsigned)s * RSTR + cb * 4096 + (rg * 4 + (w & 3)) * 512 + kh * 256, u32x2{nz(pack2(f[0], f[1])), nz(pack2(f[2], f[3]))});
    }
    ST(3);
    {
      unsigned xb, xa;
      times_issue(rs, w == 7 ? l16 : 0x1E000000u, s, xb, xa);
      h_fetch(min(s + 4, NSEG - 2 + (s & 1)));
      v_out(s + 2);
      asm volatile("s_waitcnt vmcnt(8)" : "+v"(xb), "+v"(xa) :: "memory");
      if (w == 7) retime(rs, l16, s, xb, xa, tt, p, cap);
    }
    if (s + 1 < NSEG) {
      u32x4 a[4];
      const unsigned so = X_OX + s * RSTR + rg * 32768 + wr * 4096;
      if (w == 7) wait_until(tt.tau + tt.d1);
      BAR();
      ST(4);
      issue<0>(rs, l16, so, a[0]);
      v_part(s + 3, P0{}, [&](int k) {
        if (k == 0) issue<1>(rs, l16, so + 1024, a[1]);
        if (k == 2) issue<2>(rs, l16, so + 2048, a[2]);
        if (k == 4) issue<3>(rs, l16, so + 3072, a[3]);
      });
      f32x4 c[4];
#pragma unroll
      for (int t = 0; t < 4; ++t) c[t] = f32x4{0.f, 0.f, 0.f, 0.f};
#pragma unroll
      for (int i = 0; i < 4; ++i) {
        if (i == 0) arrive<0>(rs, l16, so, a[0], cap); else if (i == 1) arrive<1>(rs, l16, so + 1024, a[1], cap);
        else if (i == 2) arrive<2>(rs, l16, so + 2048, a[2], cap); else arrive<3>(rs, l16, so + 3072, a[3], cap);
#pragma unroll
        for (int t = 0; t < 4; ++t) c[t] = smf(__builtin_bit_cast(bf16x8, a[i]), WB[i * 4 + t], c[t], sidx);
        SCHED();
      }
      ST(5);
      {
        const bool odd = (l16 & 256) != 0;
        const f32x4 f = fold(odd ? c[2] : c[0], odd ? c[3] : c[1]);
        const auto e0 = __builtin_amdgcn_permlane16_swap(f2u(f[0]), f2u(f[2]), false, false), e1 = __builtin_amdgcn_permlane16_swap(f2u(f[1]), f2u(f[3]), false, false);
        *(LAS f32x2*)(lad(roff, lbase)) = f32x2{u2f(e0[0]) + u2f(e0[1]), u2f(e1[0]) + u2f(e1[1])};
      }
      ST(6);
      LBAR();
      ST(7);
      if (w == 0) {
        LAS unsigned char* rp = lad(half_of(l16), lbase + R_RED);
        f32x2 v[8];
#pragma unroll
        for (int i = 0; i < 8; ++i) v[i] = *(const LAS f32x2*)(rp + i * 512);
        const f32x2 sum = ((v[0] + v[1]) + (v[2] + v[3])) + ((v[4] + v[5]) + (v[6] + v[7]));
        stx4(rs, soff, X_MEM + (s + 1) * RSTR + (cb >> 1) * 1024 + rg * 512 + ((cb ^ rg) & 1) * 128, nz(pack2(sum[0], sum[1])));
        const unsigned t = max(rt32(), 1u);
        if (l16 == 0) stx4(rs, 0, X_TS + (unsigned)(s + 1) * RSTR + (unsigned)(rg * 32 + cb) * 128, t);
      }
      ST(8);
    }
    advance(tt, p);
  }
  ST_FLUSH();
  if (cap == 0u && l16 == 0) stx4(rs, 0, X_CTL, 1u);
}

#if defined(__HIP_DEVICE_COMPILE__)
__attribute__((target("no-packed-fp32-ops")))
#endif
__global__ void __launch_bounds__(512, 2) k_main(MainP p) {
  extern __shared__ __attribute__((aligned(16))) unsigned char smem[];
  const int b = blockIdx.x, x = b & 7, j = b >> 3;
  if (x >= 3) worker(p, (LAS unsigned char*)smem, x - 3, j);
  else if (x == 0) role_a(p, (LAS unsigned char*)smem, j);
  else role_vb(p, (LAS unsigned char*)smem, x - 1, j);
}

extern "C" void kernel_launch(void* const* d_in, const int* in_sizes, int n_in, void* d_out, int out_size, void* d_ws, size_t ws_size,
                              hipStream_t stream) {
  unsigned char* ws = (unsigned char*)d_ws;
  (void)hipMemsetAsync(ws, 0, X_ZERO, stream);
  MainP p;
  p.x = (const u16*)d_in[0]; p.mem0 = (const u16*)d_in[1]; p.c = (const u16*)d_in[12]; p.wmod = (const u16*)d_in[13]; p.Wq = (const u16*)d_in[2]; p.Wk = (const u16*)d_in[3]; p.Wv = (const u16*)d_in[4]; p.Wo = (const u16*)d_in[5];
  p.w1 = (const u16*)d_in[8]; p.w2 = (const u16*)d_in[9]; p.w3 = (const u16*)d_in[10]; p.w4 = (const u16*)d_in[11];
  p.out = (u16*)d_out; p.ws = ws; p.d1 = 8; p.d2 = 8; p.d3 = 2; p.d4 = 100;
  (void)hipFuncSetAttribute((const void*)k_main, hipFuncAttributeMaxDynamicSharedMemorySize, R_LDS);
  k_main<<<256, 512, R_LDS, stream>>>(p);
}
```

```cpp
#include <hip/hip_runtime.h>
#include <hip/hip_bf16.h>
#include <cstdlib>
#include <type_traits>
#pragma clang diagnostic ignored "-Winline-asm"

#define LAS __attribute__((address_space(3)))
#define GAS __attribute__((address_space(1)))
typedef unsigned short u16;
typedef __bf16 bf16x2 __attribute__((ext_vector_type(2)));
typedef __bf16 bf16x4 __attribute__((ext_vector_type(4)));
typedef __bf16 bf16x8 __attribute__((ext_vector_type(8)));
typedef __bf16 bf16x16 __attribute__((ext_vector_type(16)));
typedef short s16x4 __attribute__((ext_vector_type(4)));
typedef float f32x2 __attribute__((ext_vector_type(2)));
typedef float f32x4 __attribute__((ext_vector_type(4)));
typedef float f32x16 __attribute__((ext_vector_type(16)));
typedef unsigned u32x2 __attribute__((ext_vector_type(2)));
typedef unsigned u32x4 __attribute__((ext_vector_type(4)));

constexpr int S_ = 2048, D_ = 1024, NH = 32, DH = 128, HD = 4096, NSEG = 64, FF = 3584;

constexpr unsigned MB = 1u << 20;
constexpr unsigned F_LN = 0;
constexpr unsigned F_ST = 24576;
constexpr unsigned F_CH = F_ST + 98304;
constexpr unsigned X_CTL = F_CH + 4096;
constexpr unsigned X_MOD = X_CTL + 4096;
constexpr unsigned X_REC = X_MOD + 24576;
constexpr unsigned RSTR = 225280;
constexpr int ZA = 8;
constexpr unsigned X_MEM = X_REC;
constexpr unsigned X_OX = X_REC + 16384;
constexpr unsigned X_VM = X_REC + 81920;
constexpr unsigned X_TS = X_REC + 212992;
constexpr unsigned X_ZERO = X_REC + ZA * RSTR;
constexpr unsigned WS_H = 16 * MB;
constexpr unsigned WS_Q = 20 * MB;
constexpr unsigned WS_K = 36 * MB;
constexpr unsigned WS_VT = 52 * MB;
constexpr unsigned WS_O = 68 * MB;
constexpr unsigned WS_X1 = 84 * MB;
constexpr unsigned WS_H2 = 88 * MB;
constexpr unsigned WS_G = 92 * MB;
constexpr unsigned WS_H1S = 96 * MB;
constexpr unsigned WS_X2 = 101 * MB;
constexpr unsigned WS_ST5 = 106 * MB;
constexpr unsigned WS_U = 108 * MB;
constexpr unsigned WS_KM = 124 * MB;
constexpr unsigned WS_P2 = 132 * MB;
constexpr unsigned WS_P8 = 164 * MB;
constexpr unsigned WS_END = 198 * MB;

__device__ __forceinline__ float bf2f(unsigned short b) { return __builtin_bit_cast(float, (unsigned)b << 16); }
__device__ __forceinline__ unsigned f2u(float f) { return __builtin_bit_cast(unsigned, f); }
__device__ __forceinline__ float u2f(unsigned u) { return __builtin_bit_cast(float, u); }
__device__ __forceinline__ float lo2f(unsigned w) { return __builtin_bit_cast(float, w << 16); }
__device__ __forceinline__ float hi2f(unsigned w) { return __builtin_bit_cast(float, w & 0xFFFF0000u); }
__device__ __forceinline__ unsigned pack2(float a, float b) { bf16x2 v; v[0] = (__bf16)a; v[1] = (__bf16)b; return __builtin_bit_cast(unsigned, v); }
__device__ __forceinline__ float rbf(float a) { return (float)(__bf16)a; }
__device__ __forceinline__ unsigned nz(unsigned w) { return max(w, 1u); }
__device__ __forceinline__ float gelu_tanh(float x) {
  const float e = __builtin_amdgcn_exp2f(x * fmaf(x * x, -0.1029432397f, -2.302208198f));
  return x * __builtin_amdgcn_rcpf(1.0f + e);
}
template <int CTRL> __device__ __forceinline__ float dpp_add(float v) { return v + u2f(__builtin_amdgcn_update_dpp(0u, f2u(v), CTRL, 0xF, 0xF, true)); }
__device__ __forceinline__ float wave_sum(float v) {
  v = dpp_add<0xB1>(v); v = dpp_add<0x4E>(v); v = dpp_add<0x141>(v); v = dpp_add<0x140>(v);
  auto t = __builtin_amdgcn_permlane16_swap(f2u(v), f2u(v), false, false);
  v = u2f(t[0]) + u2f(t[1]);
  t = __builtin_amdgcn_permlane32_swap(f2u(v), f2u(v), false, false);
  return u2f(t[0]) + u2f(t[1]);
}
#define WAIT_V(n) asm volatile("s_waitcnt vmcnt(" #n ")" ::: "memory")
#define WAIT_L0() asm volatile("s_waitcnt lgkmcnt(0)" ::: "memory")
#define BAR() do { asm volatile("" ::: "memory"); __builtin_amdgcn_s_barrier(); asm volatile("" ::: "memory"); } while (0)
#define LBAR() do { WAIT_L0(); BAR(); } while (0)
template <int N> __device__ __forceinline__ void wait_v() { asm volatile("s_waitcnt vmcnt(%0)" :: "n"(N) : "memory"); }
__device__ __forceinline__ void dma16(__amdgpu_buffer_rsrc_t rs, unsigned voff, unsigned soff, unsigned ldsa) {
  asm volatile("s_mov_b32 m0, %0\n\ts_nop 3\n\tbuffer_load_dwordx4 %1, %2, %3 offen lds" :: "s"(ldsa), "v"(voff), "s"(rs), "s"(soff) : "m0", "memory");
}
template <bool NT> __device__ __forceinline__ void dma16n(__amdgpu_buffer_rsrc_t rs, unsigned voff, unsigned soff, unsigned ldsa) {
  if (NT) asm volatile("s_mov_b32 m0, %0\n\ts_nop 3\n\tbuffer_load_dwordx4 %1, %2, %3 offen nt lds" :: "s"(ldsa), "v"(voff), "s"(rs), "s"(soff) : "m0", "memory");
  else dma16(rs, voff, soff, ldsa);
}
#define NTM 1
__device__ __forceinline__ void dma16x(__amdgpu_buffer_rsrc_t rs, unsigned voff, unsigned soff, unsigned ldsa) {
  asm volatile("s_mov_b32 m0, %0\n\ts_nop 3\n\tbuffer_load_dwordx4 %1, %2, %3 offen sc1 lds" :: "s"(ldsa), "v"(voff), "s"(rs), "s"(soff) : "m0", "memory");
}
__device__ __forceinline__ u32x4 ldx16(__amdgpu_buffer_rsrc_t rs, unsigned voff, unsigned soff) { return __builtin_amdgcn_raw_buffer_load_b128(rs, voff, soff, 16); }
__device__ __forceinline__ u32x2 ldx8(__amdgpu_buffer_rsrc_t rs, unsigned voff, unsigned soff) { return __builtin_amdgcn_raw_buffer_load_b64(rs, voff, soff, 16); }
__device__ __forceinline__ unsigned ldx4(__amdgpu_buffer_rsrc_t rs, unsigned voff, unsigned soff) { return __builtin_amdgcn_raw_buffer_load_b32(rs, voff, soff, 16); }
__device__ __forceinline__ void stx16(__amdgpu_buffer_rsrc_t rs, unsigned voff, unsigned soff, u32x4 v) {
  __builtin_amdgcn_raw_buffer_store_b128(v, rs, voff, soff, 16);
  asm volatile("s_nop 1" :: "v"(v) : "memory");
}
__device__ __forceinline__ void stx8(__amdgpu_buffer_rsrc_t rs, unsigned voff, unsigned soff, u32x2 v) { __builtin_amdgcn_raw_buffer_store_b64(v, rs, voff, soff, 16); }
__device__ __forceinline__ void stx4(__amdgpu_buffer_rsrc_t rs, unsigned voff, unsigned soff, unsigned v) { __builtin_amdgcn_raw_buffer_store_b32(v, rs, voff, soff, 16); }
__device__ __forceinline__ void wait_ge(__amdgpu_buffer_rsrc_t rs, unsigned voff, unsigned soff, unsigned need, unsigned& cap) {
  unsigned t;
  asm volatile("s_nop 4\n\t"
               "1:\n\t"
               "buffer_load_dword %[t], %[vo], %[rs], %[so] offen sc1\n\t"
               "s_waitcnt vmcnt(0)\n\t"
               "v_cmp_gt_u32 vcc, %[need], %[t]\n\t"
               "s_cmp_eq_u64 vcc, 0\n\t"
               "s_cbranch_scc1 2f\n\t"
               "s_sleep 2\n\t"
               "s_sub_u32 %[cap], %[cap], 1\n\t"
               "s_cbranch_scc0 1b\n\t"
               "s_mov_b32 %[cap], 0\n\t"
               "2:"
               : [t] "=&v"(t), [cap] "+s"(cap) : [vo] "v"(voff), [rs] "s"(rs), [so] "s"(soff), [need] "s"(need) : "vcc", "scc", "memory");
}
__device__ __forceinline__ void wait_seen(__amdgpu_buffer_rsrc_t rs, unsigned voff, unsigned soff, unsigned need, unsigned& seen, unsigned& cap) {
  if (seen >= need) return;
  unsigned t;
  asm volatile("s_nop 4\n\t"
               "1:\n\t"
               "buffer_load_dword %[t], %[vo], %[rs], %[so] offen sc1\n\t"
               "s_waitcnt vmcnt(0)\n\t"
               "v_cmp_gt_u32 vcc, %[need], %[t]\n\t"
               "s_cbranch_vccz 2f\n\t"
               "s_sleep 1\n\t"
               "s_sub_u32 %[cap], %[cap], 1\n\t"
               "s_cbranch_scc0 1b\n\t"
               "s_mov_b32 %[cap], 0\n\t"
               "2:"
               : [t] "=&v"(t), [cap] "+s"(cap) : [vo] "v"(voff), [rs] "s"(rs), [so] "s"(soff), [need] "s"(need) : "vcc", "scc", "memory");
  seen = min((unsigned)__builtin_amdgcn_readlane(t, 0), (unsigned)__builtin_amdgcn_readlane(t, 1));
}
__device__ __forceinline__ __amdgpu_buffer_rsrc_t mk_rs(const void* p, unsigned bytes) { return __builtin_amdgcn_make_buffer_rsrc((void*)p, 0, (int)bytes, 0x00020000); }
#define SCHED() __builtin_amdgcn_sched_barrier(0)
__device__ __forceinline__ unsigned now32() { unsigned long long t; asm volatile("s_memtime %0\n\ts_waitcnt lgkmcnt(0)" : "=s"(t) :: "memory"); return (unsigned)t; }
__device__ __forceinline__ unsigned rt32() { unsigned long long t; asm volatile("s_memrealtime %0\n\ts_waitcnt lgkmcnt(0)" : "=s"(t) :: "memory"); return (unsigned)t; }
__device__ __forceinline__ void wait_until(unsigned t) { while ((int)(rt32() - t) < 0) {} }
template <int X = 0> __device__ __forceinline__ LAS unsigned char* lad(unsigned v, unsigned u) {
  unsigned a;
  if (X == 0) asm volatile("v_add_u32 %0, %1, %2" : "=v"(a) : "s"(u), "v"(v));
  else asm volatile("v_xor_b32 %0, %3, %2\n\tv_add_u32 %0, %1, %0" : "=&v"(a) : "s"(u), "v"(v), "n"(X));
  return (LAS unsigned char*)(size_t)a;
}
__device__ __forceinline__ bf16x4 trread(LAS unsigned char* p) { return __builtin_amdgcn_ds_read_tr16_b64_v4bf16((LAS bf16x4*)p); }
__device__ __forceinline__ bf16x8 cat8(bf16x4 a, bf16x4 b) { return __builtin_shufflevector(a, b, 0, 1, 2, 3, 4, 5, 6, 7); }
__device__ __forceinline__ bf16x16 cat16(bf16x8 a, bf16x8 b) { return __builtin_shufflevector(a, b, 0, 1, 2, 3, 4, 5, 6, 7, 8, 9, 10, 11, 12, 13, 14, 15); }
__device__ __forceinline__ f32x4 mfma32(bf16x8 a, bf16x8 b, f32x4 c) { return __builtin_amdgcn_mfma_f32_16x16x32_bf16(a, b, c, 0, 0, 0); }
__device__ __forceinline__ f32x4 mfma16(u32x2 a, u32x2 b, f32x4 c) {
  return mfma32(__builtin_bit_cast(bf16x8, u32x4{a[0], a[1], 0u, 0u}), __builtin_bit_cast(bf16x8, u32x4{b[0], b[1], 0u, 0u}), c);
}
__device__ __forceinline__ f32x4 mfma16n(u32x2 a, u32x2 b, f32x4 c) { return __builtin_amdgcn_mfma_f32_16x16x16bf16_1k(__builtin_bit_cast(s16x4, a), __builtin_bit_cast(s16x4, b), c, 0, 0, 0); }
__device__ __forceinline__ f32x4 smf(bf16x8 a, bf16x16 b, f32x4 c, int idx) { return __builtin_amdgcn_smfmac_f32_16x16x64_bf16(a, b, c, idx, 0, 0); }

__device__ __forceinline__ void ln_row(float (&x)[16]) {
  float s = 0.f;
#pragma unroll
  for (int i = 0; i < 16; ++i) s += x[i];
  const float mean = wave_sum(s) * (1.0f / 1024.0f);
  float q = 0.f;
#pragma unroll
  for (int i = 0; i < 16; ++i) { const float d = x[i] - mean; q += d * d; }
  const float var = wave_sum(q) * (1.0f / 1024.0f);
  const float m = rbf(mean), isd = __builtin_amdgcn_rcpf(rbf(__builtin_amdgcn_sqrtf(rbf(rbf(var) + 1e-5f))));
#pragma unroll
  for (int i = 0; i < 16; ++i) x[i] = rbf(rbf(x[i] - m) * isd);
}
__device__ __forceinline__ void load_row16(const u16* rowp, int lane, float (&x)[16]) {
  const u32x4 a = *(const u32x4*)(rowp + lane * 8), b = *(const u32x4*)(rowp + 512 + lane * 8);
#pragma unroll
  for (int i = 0; i < 4; ++i) { x[2 * i] = lo2f(a[i]); x[2 * i + 1] = hi2f(a[i]); x[8 + 2 * i] = lo2f(b[i]); x[9 + 2 * i] = hi2f(b[i]); }
}
__device__ __forceinline__ void store_row16(u16* rowp, int lane, const float (&x)[16]) {
  u32x4 a, b;
#pragma unroll
  for (int i = 0; i < 4; ++i) { a[i] = pack2(x[2 * i], x[2 * i + 1]); b[i] = pack2(x[8 + 2 * i], x[9 + 2 * i]); }
  *(u32x4*)(rowp + lane * 8) = a; *(u32x4*)(rowp + 512 + lane * 8) = b;
}
__host__ __device__ __forceinline__ int mem_slot(int dl) { return ((dl >> 3) & 1) * 32 + (dl >> 4) * 8 + (dl & 4) + (dl & 1) * 2 + ((dl >> 1) & 1); }

#define WST(rs, wk, i)
struct MainP { const u16 *x, *mem0, *Wq, *Wk, *Wv, *Wo, *w1, *w2, *w3, *w4, *c, *wmod; u16* out; unsigned char* ws; int d1, d2, d3, d4; };
constexpr unsigned SPIN_CAP = 1u << 18;

__device__ __forceinline__ float gmax(float v) {
  auto t = __builtin_amdgcn_permlane16_swap(f2u(v), f2u(v), false, false);
  v = fmaxf(u2f(t[0]), u2f(t[1]));
  t = __builtin_amdgcn_permlane32_swap(f2u(v), f2u(v), false, false);
  return fmaxf(u2f(t[0]), u2f(t[1]));
}
__device__ __forceinline__ float gsum(float v) {
  auto t = __builtin_amdgcn_permlane16_swap(f2u(v), f2u(v), false, false);
  v = u2f(t[0]) + u2f(t[1]);
  t = __builtin_amdgcn_permlane32_swap(f2u(v), f2u(v), false, false);
  return u2f(t[0]) + u2f(t[1]);
}
__device__ __forceinline__ void publish(__amdgpu_buffer_rsrc_t rs, unsigned word, unsigned val) {
  WAIT_V(0); BAR();
  if (threadIdx.x == 0) stx4(rs, 0, word, val);
}

constexpr int QB0 = 20;
constexpr int P_S2 = 0, P_S8 = 32, P_S7 = 64, P_S4 = 96, P_S5 = 104, P_S3 = 112, P_PJ = 208, P_S1 = 336;
constexpr int NLN = 160;

enum { SG_WO = 0, SG_W1 = 1, SG_W2 = 2, SG_W3 = 3, SG_W4 = 4, SG_PJ = 5 };
constexpr int G_XCH = 131072, G_K = G_XCH + 16384, G_CTL = G_K + 8192;
__device__ __forceinline__ f32x16 mfma3232(bf16x8 a, bf16x8 b, f32x16 c) { return __builtin_amdgcn_mfma_f32_32x32x16_bf16(a, b, c, 0, 0, 0); }
__device__ __forceinline__ void widen32(const f32x16& a, u32x4 (&v)[2]) {
#pragma unroll
  for (int u = 0; u < 2; ++u) {
    const auto r0 = __builtin_amdgcn_permlane32_swap(pack2(a[8 * u], a[8 * u + 1]), pack2(a[8 * u + 4], a[8 * u + 5]), false, false);
    const auto r1 = __builtin_amdgcn_permlane32_swap(pack2(a[8 * u + 2], a[8 * u + 3]), pack2(a[8 * u + 6], a[8 * u + 7]), false, false);
    v[u] = u32x4{r0[0], r1[0], r0[1], r1[1]};
  }
}
__device__ __forceinline__ unsigned umin4v(u32x4 v) { return min(min(v[0], v[1]), min(v[2], v[3])); }
__device__ __forceinline__ float hsum(float v) { const auto t = __builtin_amdgcn_permlane32_swap(f2u(v), f2u(v), false, false); return u2f(t[0]) + u2f(t[1]); }

__device__ __forceinline__ void attend(__amdgpu_buffer_rsrc_t rs, int seg, int h, int t, int lane, unsigned& chs, unsigned& cap) {
  const int fr = lane & 15, g = lane >> 4;
  const unsigned l16 = lane * 16, ub = (unsigned)(seg * NH + h);
  wait_seen(rs, 0, F_CH + h * 128, (unsigned)max(seg, 1), chs, cap);
  wait_ge(rs, (lane & 1) && seg >= QB0 ? 96u * 128u : (unsigned)(seg % 3) * 4096u, F_ST + (unsigned)(P_PJ + h) * 128, (unsigned)seg + 1u, cap);
  f32x4 s0 = {0.f, 0.f, 0.f, 0.f}, s1 = s0, s2 = s0;
  u32x4 q[4], kmf[4];
  u32x2 vmf[8];
  const unsigned em = (unsigned)(fr >> 3) * 512 + (unsigned)g * 128 + (unsigned)(8 * ((fr >> 2) & 1) + (fr & 3)) * 8;
  auto mem_load = [&](unsigned off) {
#pragma unroll
    for (int ks = 0; ks < 4; ++ks) kmf[ks] = ldx16(rs, l16 + off, WS_KM + ub * 4096 + ks * 1024);
#pragma unroll
    for (int i = 0; i < 8; ++i) vmf[i] = ldx8(rs, em + off, X_VM + (unsigned)seg * RSTR + h * 4096 + (i >> 1) * 1024 + (i & 1) * 32);
  };
  const bool have = chs > (unsigned)seg;
  f32x4 o[8];
  float mt = -3.0e38f, lt = 0.f;
  const float scale = 0.08838834764831845f, L2E = 1.4426950408889634f;
  {
    u32x4 k0[4], k1[4], vv[8];
#pragma unroll
    for (int ks = 0; ks < 4; ++ks) {
      q[ks] = ldx16(rs, l16, WS_Q + ub * 8192 + t * 4096 + ks * 1024);
      k0[ks] = ldx16(rs, l16, WS_K + ub * 8192 + ks * 1024); k1[ks] = ldx16(rs, t ? l16 : 0xF0000000u, WS_K + ub * 8192 + 4096 + ks * 1024);
    }
    const unsigned ev = (unsigned)(fr >> 3) * 1024 + (unsigned)(g * 16 + 8 * ((fr >> 2) & 1) + (fr & 3)) * 16;
#pragma unroll
    for (int i = 0; i < 8; ++i) vv[i] = ldx16(rs, ev, WS_VT + ub * 8192 + (i >> 1) * 2048 + (i & 1) * 64);
#pragma unroll
    for (int ks = 0; ks < 4; ++ks) {
      s1 = mfma32(__builtin_bit_cast(bf16x8, k0[ks]), __builtin_bit_cast(bf16x8, q[ks]), s1); s2 = mfma32(__builtin_bit_cast(bf16x8, k1[ks]), __builtin_bit_cast(bf16x8, q[ks]), s2);
    }
    SCHED();
    mem_load(have ? 0u : 0xF0000000u);
    SCHED();
    float b[4], c[4];
#pragma unroll
    for (int r = 0; r < 4; ++r) {
      const int j = 4 * g + r;
      b[r] = (t == 1 || j <= fr) ? rbf(rbf(s1[r]) * scale) : -3.0e38f;
      c[r] = (t == 1 && j <= fr) ? rbf(rbf(s2[r]) * scale) : -3.0e38f;
      mt = fmaxf(mt, fmaxf(b[r], c[r]));
    }
    mt = gmax(mt);
#pragma unroll
    for (int r = 0; r < 4; ++r) { b[r] = __builtin_amdgcn_exp2f((b[r] - mt) * L2E); c[r] = __builtin_amdgcn_exp2f((c[r] - mt) * L2E); lt += b[r] + c[r]; }
    const u32x4 pt = {pack2(b[0], b[1]), pack2(b[2], b[3]), pack2(c[0], c[1]), pack2(c[2], c[3])};
#pragma unroll
    for (int i = 0; i < 8; ++i) o[i] = mfma32(__builtin_bit_cast(bf16x8, vv[i]), __builtin_bit_cast(bf16x8, pt), f32x4{0.f, 0.f, 0.f, 0.f});
    lt = gsum(lt);
  }
  asm volatile("" : "+v"(o[0]), "+v"(o[1]), "+v"(o[2]), "+v"(o[3]), "+v"(o[4]), "+v"(o[5]), "+v"(o[6]), "+v"(o[7]), "+v"(lt));
  if (!have) { wait_seen(rs, 0, F_CH + h * 128, (unsigned)seg + 1u, chs, cap); mem_load(0u); }
#pragma unroll
  for (int ks = 0; ks < 4; ++ks) s0 = mfma32(__builtin_bit_cast(bf16x8, kmf[ks]), __builtin_bit_cast(bf16x8, q[ks]), s0);
  float a[4];
#pragma unroll
  for (int r = 0; r < 4; ++r) a[r] = rbf(rbf(s0[r]) * scale);
  const float mx = fmaxf(mt, gmax(fmaxf(fmaxf(a[0], a[1]), fmaxf(a[2], a[3]))));
#pragma unroll
  for (int r = 0; r < 4; ++r) a[r] = __builtin_amdgcn_exp2f((a[r] - mx) * L2E);
  const u32x2 pm = {pack2(a[0], a[1]), pack2(a[2], a[3])};
  const float al = __builtin_amdgcn_exp2f((mt - mx) * L2E);
  const float inv = __builtin_amdgcn_rcpf(fmaf(al, lt, 0.5f * gsum((a[0] + a[1]) + (a[2] + a[3])))), ai = al * inv;
#pragma unroll
  for (int u = 0; u < 4; ++u) {
    const f32x4 x = o[2 * u] * ai + mfma16n(vmf[2 * u], pm, f32x4{0.f, 0.f, 0.f, 0.f}) * inv, y = o[2 * u + 1] * ai + mfma16n(vmf[2 * u + 1], pm, f32x4{0.f, 0.f, 0.f, 0.f}) * inv;
    stx16(rs, l16, WS_O + (unsigned)seg * 262144 + (unsigned)((h * 4 + u) * 2 + t) * 1024, u32x4{pack2(x[0], x[1]), pack2(x[2], x[3]), pack2(y[0], y[1]), pack2(y[2], y[3])});
  }
  WAIT_V(0);
  if (lane == 0) stx4(rs, 0, F_ST + (unsigned)(P_S1 + (seg & 3) * 64 + h * 2 + t) * 128, (unsigned)seg + 1u);
}

template <int SG>
__device__ __forceinline__ void stage_gemm(LAS unsigned char* lds, __amdgpu_buffer_rsrc_t rs, const u16* Wg, int wk, int kq, int cb, int pid, unsigned& cap,
                                           unsigned dst = 0, int bstr = 1, int nb = 0, const u16* side = nullptr, int srows = 0, int sld = 0, const u16* side3 = nullptr) {
  constexpr bool PROJ = SG == SG_PJ, ROWM = (SG == SG_W1 || PROJ);
  constexpr int KS = (SG == SG_W4) ? 28 : 32;
  constexpr int LDW = PROJ ? HD : (SG == SG_W3) ? FF : D_;
  constexpr int WROWS = (SG == SG_WO) ? HD : (SG == SG_W4 ? FF : D_);
  const int NBLK = PROJ ? nb : (SG == SG_WO || SG == SG_W1) ? 64 : 65;
  constexpr unsigned ABASE = PROJ ? WS_H : SG == SG_WO ? WS_O : SG == SG_W1 ? WS_G : SG == SG_W2 ? WS_H1S : SG == SG_W3 ? WS_X2 : WS_U;
  constexpr unsigned ASTR = SG == SG_WO ? 262144u : SG == SG_W4 ? 229376u : 65536u;
  constexpr int NPC = KS / 4;
  const unsigned myword = F_ST + (unsigned)pid * 128u;
  int tid = threadIdx.x; asm volatile("" : "+v"(tid));
  const int lane = tid & 63, w = __builtin_amdgcn_readfirstlane(tid >> 6), row = lane & 31, hf = lane >> 5;
  const int kh = w >> 2, wl = w & 3;
  const int koff = PROJ ? 0 : kq;
  const unsigned lbase = (unsigned)(size_t)lds, l16 = lane * 16;
  const int col0 = cb * 128 + wl * 32;
  bf16x8 Wf[KS];
  {
    const __amdgpu_buffer_rsrc_t rsW = mk_rs(Wg, (unsigned)WROWS * LDW * 2u);
    const int ub = lane >> 3, tu = 4 * hf + ((lane & 15) >> 2);
    const unsigned vB = (unsigned)(((ub & 3) + 8 * (ub >> 2)) * LDW + 16 * (((lane >> 1) & 3) ^ (ub >> 1)) + 8 * (lane & 1)) * 2u;
    const unsigned sB = (unsigned)((koff * KS * 32 + 32 * (w >> 2) + 16 * ((w >> 1) & 1) + 4 * (w & 1)) * LDW + cb * 128) * 2u;
    const int boff = kh * 65536 + (wl >> 1) * 8192 + tu * 128 + (((tu >> 1) ^ (2 * (wl & 1) + ((lane >> 4) & 1))) << 5) + (lane & 3) * 8;
#pragma unroll
    for (int ps = 0; ps < 2; ++ps) {
      BAR();
#pragma unroll
      for (int hh = 0; hh < 2; ++hh)
#pragma unroll
        for (int i = 0; i < 4; ++i)
          if (4 * ps + i < NPC && !(PROJ && ps == 0)) {
            const unsigned so = sB + (unsigned)((hh * NPC + 4 * ps + i) * 64 * LDW) * 2u, d = lbase + hh * 65536 + i * 16384 + w * 1024;
            dma16n<(NTM & 16) != 0>(rsW, vB, so, d); dma16n<(NTM & 16) != 0>(rsW, vB, so + 128, d + 8192);
          }
      WAIT_V(0); BAR();
#pragma unroll
      for (int i = 0; i < 4; ++i)
        if (4 * ps + i < NPC) {
#pragma unroll
          for (int k = 0; k < 4; ++k) { LAS unsigned char* q = lds + (boff + i * 16384 + k * 2048); Wf[(4 * ps + i) * 4 + k] = cat8(trread(q), trread(q + 1024)); }
        }
      WAIT_L0();
    }
    if (SG == SG_W1) {
      const u32x4 z = {0u, 0u, 0u, 0u};
      stx16(rs, (unsigned)tid * 16, WS_H1S + cb * 8192, z); stx16(rs, (unsigned)tid * 16, WS_H1S + 64 * 65536 + cb * 8192, z);
      WAIT_V(0);
    }
    if (tid < 8) *(LAS unsigned*)(lds + G_CTL + tid * 4) = 0u;
    LBAR();
  }
  const f32x16 zero16 = {0.f, 0.f, 0.f, 0.f, 0.f, 0.f, 0.f, 0.f, 0.f, 0.f, 0.f, 0.f, 0.f, 0.f, 0.f, 0.f};
  auto put16 = [&](LAS unsigned char* p, const f32x16& a) {
#pragma unroll
    for (int q = 0; q < 4; ++q) *(LAS f32x4*)(p + q * 1024) = f32x4{a[4 * q], a[4 * q + 1], a[4 * q + 2], a[4 * q + 3]};
  };
  if (SG == SG_W3) {
    const u32x4 one4 = {0x3F803F80u, 0x3F803F80u, 0x3F803F80u, 0x3F803F80u};
    f32x16 cs = zero16;
#pragma unroll
    for (int k = 0; k < KS; ++k) cs = mfma3232(Wf[k], __builtin_bit_cast(bf16x8, one4), cs);
    if (kh == 0) put16(lds + wl * 4096 + l16, cs);
    LBAR();
    if (kh == 1 && row == 0) {
#pragma unroll
      for (int q = 0; q < 4; ++q)
        *(LAS f32x4*)(lds + G_K + wl * 128 + hf * 64 + q * 16) = *(const LAS f32x4*)(lds + wl * 4096 + q * 1024 + l16) + f32x4{cs[4 * q], cs[4 * q + 1], cs[4 * q + 2], cs[4 * q + 3]};
    }
    LBAR();
  }
  auto blk = [&](int i) -> int { return PROJ ? bstr * i + kq : i; };
  const unsigned aoff = ROWM ? (unsigned)(row * 128 + ((hf ^ ((row >> 1) & 7)) << 4)) : (unsigned)((row >> 4) * 1024 + hf * 256 + (row & 15) * 16);
  const unsigned vA = ROWM ? (unsigned)((lane >> 3) * 2048 + (((lane & 7) ^ ((4 * wl + (lane >> 4)) & 7)) << 4)) : l16;
  auto piece = [&](int hb, int q, bool on) {
    const int i = hb >> 1, hh = hb & 1;
    const unsigned so = ABASE + (unsigned)blk(i) * ASTR + (unsigned)koff * (KS * 2048) + (ROWM ? wl * 16384 + hh * 1024 : (hh * KS + wl) * 1024);
    if (on) dma16x(rs, vA, so + q * (ROWM ? 128 : 4096), lbase + (i & 1) * 65536 + hh * 32768 + wl * 1024 + q * 4096);
  };
  auto fetch = [&](int hb) {
#pragma unroll
    for (int q = 0; q < NPC; ++q) piece(hb, q, true);
  };
  constexpr bool WEAVE = (SG == SG_W1 || SG == SG_W2 || SG == SG_W3);
  auto products = [&](int i, f32x16& acc, auto&& first, auto&& early, auto&& handover, bool soon) {
    const unsigned ub = lbase + (i & 1) * 65536 + kh * 32768;
    LAS unsigned char* ab[4];
    ab[0] = lad(aoff, ub);
    if (ROWM) { ab[1] = lad<32>(aoff, ub); ab[2] = lad<64>(aoff, ub); ab[3] = lad<96>(aoff, ub); }
    constexpr int PD = 10;
    bf16x8 af[PD + 1];
    auto rd = [&](int k) { af[k % (PD + 1)] = ROWM ? *(const LAS bf16x8*)(ab[k & 3] + (k >> 2) * 4096) : *(const LAS bf16x8*)(ab[0] + (k >> 1) * 2048 + (k & 1) * 512); };
#pragma unroll
    for (int k = 0; k < PD; ++k) rd(k);
    SCHED();
#pragma unroll
    for (int k = 0; k < KS; ++k) {
      if (k + PD < KS) rd(k + PD);
      acc = mfma3232(Wf[k], af[k % (PD + 1)], k ? acc : zero16);
      SCHED();
      first(k);
      SCHED();
      if (k == KS - 10) { early(); SCHED(); }
      if (k == 9) { if (soon) handover(); SCHED(); }
      if (k == KS - 5) { if (!soon) handover(); SCHED(); }
    }
    asm volatile("" : "+v"(acc));
  };
  WST(rs, wk, PROJ ? 2 : 10);
#define SEG(x)
  auto announce = [&](int i) {
    if (lane == 0) {
      const unsigned c = __hip_atomic_fetch_add((LAS unsigned*)(lds + G_CTL), 1u, __ATOMIC_RELAXED, __HIP_MEMORY_SCOPE_WORKGROUP);
      if ((c & 3u) == 3u) stx4(rs, 0, myword, (unsigned)blk(i) + 1u);
    }
  };
  if (kh == 0) {
    auto pvo = [&](int i) -> unsigned {
      const int b = blk(i);
      if (PROJ) return F_LN + (unsigned)((b % (NLN / 4)) * 4 + (lane & 3)) * 128u + (lane & 12);
      if (SG == SG_WO) return F_ST + (unsigned)(P_S1 + (b & 3) * 64 + kq * 16 + (lane & 15)) * 128u;
      if (SG == SG_W1) return F_ST + (unsigned)(P_S3 + (b % 3) * 32 + (lane & 31)) * 128u;
      if (SG == SG_W2) return F_ST + (unsigned)(P_S4 + (lane & 7)) * 128u;
      if (SG == SG_W3) return F_ST + (unsigned)(P_S5 + (lane & 7)) * 128u;
      return F_ST + (unsigned)(P_S7 + kq * 7 + min(lane & 7, 6)) * 128u;
    };
    auto need = [&](int i) -> unsigned { return PROJ ? 1u : (SG == SG_W2 && i == 64) ? 64u : (unsigned)i + 1u; };
    unsigned nl = 0;
    int R = 0, Rp = 0;
    for (int i = 0; i < NBLK; ++i) {
      if (R <= i) { wait_ge(rs, pvo(i), 0, need(i), cap); SEG(6); fetch(2 * i); fetch(2 * i + 1); SEG(7); }
      const unsigned pk = w == 0 ? (lane < 32 ? pvo(i + 1) : pvo(i + 2)) : 0xF0000000u;
      if (Rp <= i) { if (R <= i + 1) wait_v<NPC>(); else wait_v<2 * NPC>(); }
      if (R >= i + 2 || Rp > i) BAR();
      else {
        nl += 4;
        if (lane == 0) (void)__hip_atomic_fetch_add((LAS unsigned*)(lds + G_CTL + 8), 1u, __ATOMIC_RELAXED, __HIP_MEMORY_SCOPE_WORKGROUP);
        while (__builtin_amdgcn_readfirstlane(*(const volatile LAS unsigned*)(lds + G_CTL + 8)) < nl) {}
        asm volatile("" ::: "memory");
      }
      SEG(1);
      f32x16 acc;
      unsigned peek;
      asm volatile("buffer_load_dword %0, %1, %2, 0 offen sc1" : "=v"(peek) : "v"(pk), "s"(rs) : "memory");
      u32x4 st[8];
      float mean = 0.f, q2 = 0.f;
      if (SG == SG_W3) {
#pragma unroll
        for (int q = 0; q < 8; ++q) st[q] = ldx16(rs, l16, WS_ST5 + (unsigned)i * 8192 + q * 1024);
      }
      products(i, acc, [&](int k) {
        if (SG == SG_W3) {
          if (k >= 18 && k < 26) { mean += u2f(st[k - 18][0]) + u2f(st[k - 18][2]); q2 += u2f(st[k - 18][1]) + u2f(st[k - 18][3]); asm volatile("" : "+v"(mean), "+v"(q2)); }
          if (k == 26) { mean = hsum(mean) * (1.0f / 1024.0f); q2 = hsum(q2); asm volatile("" : "+v"(mean), "+v"(q2)); }
          if (k == 28) { q2 = rbf(rbf(fmaxf(fmaf(-mean, mean, q2 * (1.0f / 1024.0f)), 0.f)) + 1e-5f); asm volatile("" : "+v"(q2)); }
          if (k == 29) { q2 = __builtin_amdgcn_rcpf(rbf(__builtin_amdgcn_sqrtf(q2))); mean = rbf(mean); asm volatile("" : "+v"(q2), "+v"(mean)); }
        }
      }, []() {}, [&]() {
        SEG(4);
        WAIT_V(0);
        SEG(0);
        asm volatile("" : "+v"(peek));
        if (w == 0) {
          const unsigned long long ok = __ballot(peek >= (lane < 32 ? need(i + 1) : need(i + 2)));
          const bool r1 = (R >= i + 2 || (unsigned)ok == 0xFFFFFFFFu) && i + 1 < NBLK, r2 = r1 && (unsigned)(ok >> 32) == 0xFFFFFFFFu && i + 2 < NBLK;
          if (lane == 0) *(volatile LAS unsigned*)(lds + G_CTL + 4) = (unsigned)(i + 1 + (r1 ? 1 : 0) + (r2 ? 1 : 0));
        }
        LBAR();
        if (PROJ && i < 16) {
          if (side3) dma16(mk_rs(side3, 0x7FFFFFFFu), (unsigned)((lane >> 2) * 6144 + (lane & 3) * 8) * 2u, (unsigned)((64 * i + 16 * wl) * 6144) * 2u, lbase + G_K);
          if (64 * i < srows) {
            const __amdgpu_buffer_rsrc_t rsN = mk_rs(side, 0x7FFFFFFFu);
#pragma unroll
            for (int q = 0; q < 4; ++q) dma16(rsN, (unsigned)((lane >> 4) * sld + (lane & 15) * 8) * 2u, (unsigned)((64 * i + 16 * wl + 4 * q) * sld) * 2u, lbase + G_K + wl * 2048);
          }
        }
      }, Rp <= i);
      SEG(2);
      const unsigned rv = *(const volatile LAS unsigned*)(lds + G_CTL + 4);
      if (SG == SG_W3) {
        const float m = mean;
        *(LAS float*)(lds + G_K + 1024 + wl * 256 + lane * 4) = q2;
#pragma unroll
        for (int q = 0; q < 4; ++q) {
          const f32x4 cs = *(const LAS f32x4*)(lds + G_K + wl * 128 + hf * 64 + q * 16);
#pragma unroll
          for (int e = 0; e < 4; ++e) acc[4 * q + e] = fmaf(-m, cs[e], acc[4 * q + e]);
        }
      }
      put16(lad(l16, lbase + G_XCH + wl * 4096), acc);
      asm volatile("" ::: "memory");
      if (lane == 0) *(volatile LAS unsigned*)(lds + G_CTL + 16 + wl * 4) = (unsigned)i + 1u;
      const bool soon = Rp <= i;
      Rp = R; R = __builtin_amdgcn_readfirstlane(rv);
      SEG(3);
      if (R >= i + 2 && Rp < i + 2) { fetch(2 * i + 2); fetch(2 * i + 3); }
      if (R >= i + 3) {
        if (soon) { while (__builtin_amdgcn_readfirstlane(umin4v(*(const volatile LAS u32x4*)(lds + G_CTL + 16))) <= (unsigned)i) {} asm volatile("" ::: "memory"); }
        fetch(2 * i + 4);
      }
      SEG(5);
    }
  } else {
    bool pending = false;
    int Rp = 0;
    for (int i = 0; i < NBLK; ++i) {
      const int b = blk(i);
      BAR();
      SEG(0);
      const unsigned rv = *(const volatile LAS unsigned*)(lds + G_CTL + 4);
      u32x4 res[2];
      float isd;
      if (SG == SG_W2) {
#pragma unroll
        for (int u = 0; u < 2; ++u) {
          const int t = 32 * b - 2 + 16 * u + (lane >> 2);
          dma16x(rs, (t >= 0 && t < S_) ? (unsigned)(t * D_ + col0 + ((lane ^ (lane >> 3)) & 3) * 8) * 2u : 0xF0000000u, WS_H2, lbase + G_K + wl * 2048 + u * 1024);
        }
      }
      f32x16 acc;
      int R = 0;
      LAS unsigned char* xr = lad(l16, lbase + G_XCH + wl * 4096);
      f32x4 xa[4];
      unsigned xf;
      auto take = [&]() {
        asm volatile("" ::: "memory");
        xf = *(const volatile LAS unsigned*)(lds + G_CTL + 16 + wl * 4);
        asm volatile("" ::: "memory");
#pragma unroll
        for (int q = 0; q < 4; ++q) xa[q] = *(const LAS f32x4*)(xr + q * 1024);
        if (SG == SG_W3) isd = *(const LAS float*)(lds + G_K + 1024 + wl * 256 + lane * 4);
        asm volatile("" ::: "memory");
      };
      products(i, acc, [&](int k) {
      }, [&]() {
        take();
      }, [&]() {
        SEG(5);
        R = __builtin_amdgcn_readfirstlane(rv);
        if (R >= i + 3 || Rp >= i + 2) {
          WAIT_V(0);
          LBAR();
          if (pending && tid == 256) stx4(rs, 0, myword, (unsigned)blk(i - 1) + 1u);
        }
      }, false);
      const bool more = R >= i + 3 || Rp >= i + 2;
      Rp = R;
      SEG(1);
      while (__builtin_amdgcn_readfirstlane(xf) <= (unsigned)i) take();
#pragma unroll
      for (int q = 0; q < 16; ++q) { acc[q] += xa[q >> 2][q & 3]; if (SG == SG_W3) acc[q] *= isd; }
      SEG(2);
      const unsigned fo = (unsigned)((col0 >> 5) * 2048 + hf * 256), ro = (unsigned)((row >> 4) * 1024 + (row & 15) * 16);
      u32x4 v[2];
      if (SG == SG_W1 || SG == SG_W3) {
#pragma unroll
        for (int q = 0; q < 16; q += 2) {
          float g0 = gelu_tanh(acc[q]), g1 = gelu_tanh(acc[q + 1]);
          asm volatile("" : "+v"(g0), "+v"(g1));
          acc[q] = g0; acc[q + 1] = g1;
          piece(2 * i + 5, q >> 1, R >= i + 3 && i >= 2);
        }
      }
      widen32(acc, v);
      if (SG == SG_WO || SG == SG_W4) {
        LAS unsigned char* tw = lds + G_XCH + wl * 4096 + row * 64;
        *(LAS u32x4*)(tw + ((hf ^ (row >> 1)) & 3) * 16) = v[0]; *(LAS u32x4*)(tw + (((2 + hf) ^ (row >> 1)) & 3) * 16) = v[1];
        asm volatile("" ::: "memory");
        LAS unsigned char* tr = lds + G_XCH + wl * 4096 + (lane >> 2) * 64 + ((lane ^ (lane >> 3)) & 3) * 16;
        const unsigned so = (SG == SG_WO ? WS_P2 : WS_P8) + (unsigned)(b * 4 + kq) * 65536u, vo = (unsigned)((lane >> 2) * D_ + col0 + (lane & 3) * 8) * 2u;
        stx16(rs, vo, so, *(const LAS u32x4*)(tr)); stx16(rs, vo, so + 32768, *(const LAS u32x4*)(tr + 1024));
      } else if (PROJ) {
        const unsigned so = dst + (unsigned)(b * NH + cb) * 8192 + wl * 1024, vo = (unsigned)((row >> 4) * 4096 + hf * 256 + (row & 15) * 16);
        stx16(rs, vo, so, v[0]); stx16(rs, vo, so + 512, v[1]);
      } else if (SG == SG_W1) {
        const int grp = cb >> 1, sh = (grp & 1) ? -(1 + (grp >> 1)) : (1 + (grp >> 1));
        const int rho = 32 * b + row + sh + 2;
        const unsigned vo = (rho >= 2 && rho < S_ + 2) ? (unsigned)(rho >> 5) * 65536u + fo + (unsigned)((rho >> 4) & 1) * 1024u + (unsigned)(rho & 15) * 16u : 0xF0000000u;
        stx16(rs, vo, WS_H1S, v[0]); stx16(rs, vo, WS_H1S + 512, v[1]);
      } else if (SG == SG_W2) {
        if (!more) WAIT_V(0);
        res[0] = *(const LAS u32x4*)(lds + G_K + wl * 2048 + row * 64 + ((hf ^ (row >> 1)) & 3) * 16);
        res[1] = *(const LAS u32x4*)(lds + G_K + wl * 2048 + row * 64 + (((2 + hf) ^ (row >> 1)) & 3) * 16);
        float zs = 0.f, q2 = 0.f;
#pragma unroll
        for (int q = 0; q < 8; ++q) {
          const unsigned x = pack2(lo2f(res[q >> 2][q & 3]) + lo2f(v[q >> 2][q & 3]), hi2f(res[q >> 2][q & 3]) + hi2f(v[q >> 2][q & 3]));
          v[q >> 2][q & 3] = x; zs += lo2f(x) + hi2f(x); q2 = fmaf(lo2f(x), lo2f(x), fmaf(hi2f(x), hi2f(x), q2));
          asm volatile("" : "+v"(zs), "+v"(q2));
          piece(2 * i + 5, q, R >= i + 3 && i >= 2);
        }
        stx16(rs, fo + ro, WS_X2 + (unsigned)b * 65536u, v[0]); stx16(rs, fo + ro, WS_X2 + (unsigned)b * 65536u + 512, v[1]);
        const float mw = hsum(zs);
        q2 = hsum(q2);
        if (hf == 0) stx8(rs, (unsigned)((cb * 2 + (wl >> 1)) * 512 + row * 16 + (wl & 1) * 8), WS_ST5 + (unsigned)b * 8192, u32x2{f2u(mw), f2u(q2)});
      } else {
        stx16(rs, fo + ro, WS_U + (unsigned)b * 229376u, v[0]); stx16(rs, fo + ro, WS_U + (unsigned)b * 229376u + 512, v[1]);
      }
      if (PROJ && bstr == 3) {
#pragma unroll
        for (int q = 0; q < 2; ++q) {
          const int pc = (wk * 4 + wl) * 2 + q;
          if (pc < 660 && 3 * i + ZA + pc / 220 <= NSEG) stx16(rs, l16, X_REC + (unsigned)(3 * i + ZA) * RSTR + (unsigned)pc * 1024, u32x4{0u, 0u, 0u, 0u});
        }
      }
      SEG(3);
      pending = R >= i + 3 && i >= 2;
      if (!pending) { WAIT_V(0); announce(i); }
      if (R >= i + 3 && !(WEAVE && i >= 2)) fetch(2 * i + 5);
      if (!PROJ) WST(rs, wk, 16 + b);
      SEG(4);
    }
  }
  LBAR();
}

__device__ __forceinline__ void wait32(__amdgpu_buffer_rsrc_t rs, int first, int mine, int lane, unsigned need, unsigned& cap) {
  wait_ge(rs, 0, F_ST + (unsigned)(first + mine) * 128u, need, cap);
  wait_ge(rs, (unsigned)(lane & 31) * 128u, F_ST + (unsigned)first * 128u, need, cap);
}
__device__ __forceinline__ void sum_partials(__amdgpu_buffer_rsrc_t rs, unsigned l16, unsigned so, float (&a)[16]) {
  u32x4 pp[4][2];
#pragma unroll
  for (int k = 0; k < 4; ++k) { pp[k][0] = ldx16(rs, l16, so + k * 65536u); pp[k][1] = ldx16(rs, l16, so + k * 65536u + 1024); }
#pragma unroll
  for (int i = 0; i < 8; ++i) {
    f32x2 t = {0.f, 0.f};
#pragma unroll
    for (int k = 0; k < 4; ++k) t += f32x2{lo2f(pp[k][i >> 2][i & 3]), hi2f(pp[k][i >> 2][i & 3])};
    a[2 * i] = rbf(t[0]); a[2 * i + 1] = rbf(t[1]);
  }
}
__device__ __forceinline__ void put_row(__amdgpu_buffer_rsrc_t rs, unsigned l16, unsigned so, const float (&v)[16]) {
  stx16(rs, l16, so, u32x4{pack2(v[0], v[1]), pack2(v[2], v[3]), pack2(v[4], v[5]), pack2(v[6], v[7])});
  stx16(rs, l16, so + 1024, u32x4{pack2(v[8], v[9]), pack2(v[10], v[11]), pack2(v[12], v[13]), pack2(v[14], v[15])});
}
__device__ __forceinline__ void load_mod(__amdgpu_buffer_rsrc_t rs, int part, int lane, float (&v)[16], unsigned& cap) {
  for (;;) {
    asm volatile("" ::: "memory");
    u32x4 t[4];
#pragma unroll
    for (int i = 0; i < 4; ++i) t[i] = ldx16(rs, (unsigned)lane * 32, X_MOD + part * 4096 + (i >> 1) * 2048 + (i & 1) * 16);
    unsigned mn = 0xFFFFFFFFu;
#pragma unroll
    for (int i = 0; i < 16; ++i) { mn = min(mn, t[i >> 2][i & 3]); v[i] = hi2f(t[i >> 2][i & 3]); }
    if (__all(mn != 0u) || cap == 0u) break;
    cap = __builtin_amdgcn_readfirstlane(cap - 1u);
    __builtin_amdgcn_s_sleep(4);
  }
  cap = __builtin_amdgcn_readfirstlane(cap);
}
__device__ __forceinline__ void mod_part(__amdgpu_buffer_rsrc_t rs, const u16* wmod, const u16* cv, LAS float* red, int grp) {
  const int tid = threadIdx.x, kg = tid >> 2, c8 = tid & 3;
  u32x4 wv[8]; u16 cc[8];
#pragma unroll
  for (int i = 0; i < 8; ++i) cc[i] = cv[kg + 128 * i];
#pragma unroll
  for (int i = 0; i < 8; ++i) { const u32x4* q_ = (const u32x4*)(wmod + (size_t)(kg + 128 * i) * 6144 + grp * 32 + c8 * 8); wv[i] = (NTM & 8) ? __builtin_nontemporal_load(q_) : *q_; }
  SCHED();
  float acc[8] = {0.f, 0.f, 0.f, 0.f, 0.f, 0.f, 0.f, 0.f};
#pragma unroll
  for (int i = 0; i < 8; ++i) {
    const float v = bf2f(cc[i]), sc = rbf(v * __builtin_amdgcn_rcpf(1.0f + __builtin_amdgcn_exp2f(-1.4426950408889634f * v)));
#pragma unroll
    for (int j = 0; j < 4; ++j) { acc[2 * j] += sc * lo2f(wv[i][j]); acc[2 * j + 1] += sc * hi2f(wv[i][j]); }
  }
#pragma unroll
  for (int j = 0; j < 8; ++j) acc[j] = gsum(dpp_add<0x128>(dpp_add<0x124>(acc[j])));
  if ((tid & 63) < 4) {
    *(LAS f32x4*)(red + (tid >> 6) * 32 + c8 * 8) = f32x4{acc[0], acc[1], acc[2], acc[3]}; *(LAS f32x4*)(red + (tid >> 6) * 32 + c8 * 8 + 4) = f32x4{acc[4], acc[5], acc[6], acc[7]};
  }
  LBAR();
  if (tid < 32) {
    float u = 0.f;
#pragma unroll
    for (int i = 0; i < 8; ++i) u += red[i * 32 + tid];
    stx4(rs, (unsigned)tid * 4, X_MOD + (unsigned)grp * 128, f2u(rbf(u)) | 1u);
  }
}

__device__ __forceinline__ void stage_mid(__amdgpu_buffer_rsrc_t rs, const u16* x, int wk, int cu, unsigned& cap) {
  const int tid = threadIdx.x, lane = tid & 63, w = __builtin_amdgcn_readfirstlane(tid >> 6);
  const int id = w * 12 + cu, grp = id >> 5, rl = id & 31;
  const unsigned l16 = lane * 16;
  float ga[16], sc[16], sh[16];
  load_mod(rs, 2, lane, ga, cap); load_mod(rs, 4, lane, sc, cap); load_mod(rs, 3, lane, sh, cap);
#pragma unroll
  for (int i = 0; i < 16; ++i) sc[i] = rbf(1.0f + sc[i]);
  for (int seg = grp; seg < NSEG; seg += 3) {
    const int row = seg * 32 + rl;
    float v[16], a[16];
    load_row16(x + (size_t)row * D_, lane, v);
    wait32(rs, P_S2, rl, lane, (unsigned)seg + 1u, cap);
    sum_partials(rs, l16, WS_P2 + (unsigned)seg * 262144u + (unsigned)rl * 2048, a);
#pragma unroll
    for (int i = 0; i < 16; ++i) v[i] = rbf(v[i] + rbf(ga[i] * a[i]));
    put_row(rs, l16, WS_X1 + (unsigned)row * 2048, v);
    ln_row(v);
#pragma unroll
    for (int i = 0; i < 16; ++i) v[i] = rbf(rbf(v[i] * sc[i]) + sh[i]);
    put_row(rs, l16, WS_H2 + (unsigned)row * 2048, v);
    ln_row(v);
    put_row(rs, l16, WS_G + (unsigned)row * 2048, v);
    WAIT_V(0);
    if (lane == 0) stx4(rs, 0, F_ST + (unsigned)(P_S3 + grp * 32 + rl) * 128, (unsigned)seg + 1u);
    WST(rs, wk, 16 + seg);
  }
}

__device__ __forceinline__ void stage_out(__amdgpu_buffer_rsrc_t rs, u16* out, int wk, int cu, unsigned& cap) {
  const int tid = threadIdx.x, lane = tid & 63, w = __builtin_amdgcn_readfirstlane(tid >> 6);
  const int grp = w >> 2, rl = cu * 4 + (w & 3);
  const unsigned l16 = lane * 16;
  float gb[16];
  load_mod(rs, 5, lane, gb, cap);
  for (int b = grp; b < 65; b += 2) {
    const int t = 32 * b - 2 + rl;
    if (t < 0 || t >= S_) continue;
    wait_ge(rs, (unsigned)(lane & 7) * 128u, F_ST + P_S5 * 128u, (unsigned)b + 1u, cap);
    const unsigned xo = (unsigned)((lane >> 2) * 2048 + (lane & 3) * 256), xs = WS_X2 + (unsigned)b * 65536u + (unsigned)((rl >> 4) * 1024 + (rl & 15) * 16);
    const u32x4 x2a = ldx16(rs, xo, xs), x2b = ldx16(rs, xo, xs + 32768);
    const u32x4 x1a = ldx16(rs, l16, WS_X1 + (unsigned)t * 2048), x1b = ldx16(rs, l16, WS_X1 + (unsigned)t * 2048 + 1024);
    wait32(rs, P_S8, rl, lane, (unsigned)b + 1u, cap);
    float a[16];
    sum_partials(rs, l16, WS_P8 + (unsigned)b * 262144u + (unsigned)rl * 2048, a);
#pragma unroll
    for (int i = 0; i < 4; ++i) {
      a[2 * i] = lo2f(x1a[i]) + rbf(gb[2 * i] * rbf(lo2f(x2a[i]) + a[2 * i])); a[2 * i + 1] = hi2f(x1a[i]) + rbf(gb[2 * i + 1] * rbf(hi2f(x2a[i]) + a[2 * i + 1]));
      a[8 + 2 * i] = lo2f(x1b[i]) + rbf(gb[8 + 2 * i] * rbf(lo2f(x2b[i]) + a[8 + 2 * i])); a[9 + 2 * i] = hi2f(x1b[i]) + rbf(gb[9 + 2 * i] * rbf(hi2f(x2b[i]) + a[9 + 2 * i]));
    }
    store_row16(out + (size_t)t * D_, lane, a);
    WST(rs, wk, 16 + b);
  }
}

__device__ __forceinline__ void worker(const MainP& p, LAS unsigned char* lds, int xw, int j) {
  const int tid = threadIdx.x, lane = tid & 63, w = __builtin_amdgcn_readfirstlane(tid >> 6);
  const __amdgpu_buffer_rsrc_t rs = mk_rs(p.ws, WS_END);
  unsigned cap = SPIN_CAP;
  const int wk = xw * 32 + j;
  WST(rs, wk, 0);
  {
    const int c = xw < 3 ? j * 3 + xw : 90 + j * 2 + xw + 3, r0 = c * 8 + 2 * (w & 3);
    const bool two = r0 + 8 * NLN < S_;
    float v[4][16];
    if (c >= NLN - 64) mod_part(rs, p.wmod, p.c, (LAS float*)(lds + G_XCH), c - (NLN - 64));
    WST(rs, wk, 5);
    if (w < 4) {
#pragma unroll
      for (int q = 0; q < 4; ++q) load_row16(p.x + (size_t)(r0 + (q & 1) + (two ? (q >> 1) * 8 * NLN : 0)) * D_, lane, v[q]);
    }
    if (w >= 4) {
      if (xw == 1 || xw == 2) {
        const __amdgpu_buffer_rsrc_t rsO = mk_rs(p.Wo, D_ * HD * 2);
#pragma unroll
        for (int q = 0; q < 32; ++q) dma16(rsO, (unsigned)lane * 16, (unsigned)((q * 64 + wk - 32) * 4 + (w & 3)) * 1024u, (unsigned)(size_t)lds + G_K + (w & 3) * 1024);
        wait_v<24>();
      }
      const __amdgpu_buffer_rsrc_t rsW = mk_rs(xw < 3 ? p.Wk : p.Wq, D_ * HD * 2);
      const int ub = lane >> 3;
      const unsigned vB = (unsigned)(((ub & 3) + 8 * (ub >> 2)) * HD + 16 * (((lane >> 1) & 3) ^ (ub >> 1)) + 8 * (lane & 1)) * 2u, lbase = (unsigned)(size_t)lds;
#pragma unroll
      for (int q = 0; q < 16; ++q) {
        const int wq = (w & 3) + 4 * (q >> 3);
        const unsigned so = (unsigned)((32 * (wq >> 2) + 16 * ((wq >> 1) & 1) + 4 * (wq & 1) + (((q >> 2) & 1) * 8 + (q & 3)) * 64) * HD + j * 128) * 2u;
        const unsigned d = lbase + ((q >> 2) & 1) * 65536 + (q & 3) * 16384 + wq * 1024;
        dma16n<(NTM & 16) != 0>(rsW, vB, so, d); dma16n<(NTM & 16) != 0>(rsW, vB, so + 128, d + 8192);
      }
    } else {
#pragma unroll
      for (int q = 0; q < 4; ++q) ln_row(v[q]);
      float sc[16], sh[16];
      load_mod(rs, 1, lane, sc, cap); load_mod(rs, 0, lane, sh, cap);
      WST(rs, wk, 6);
#pragma unroll
      for (int i = 0; i < 16; ++i) sc[i] = rbf(1.0f + sc[i]);
#pragma unroll
      for (int q = 0; q < 4; ++q) {
#pragma unroll
        for (int i = 0; i < 16; ++i) v[q][i] = rbf(rbf(v[q][i] * sc[i]) + sh[i]);
        if (q < 2 || two) put_row(rs, (unsigned)lane * 16, WS_H + (unsigned)(r0 + (q & 1) + (q >> 1) * 8 * NLN) * 2048, v[q]);
      }
      WAIT_V(0);
      if (lane == 0) stx4(rs, 0, F_LN + c * 128 + w * 4, 1u);
    }
    WST(rs, wk, 1);
  }
  {
    const u16* nw = xw == 0 ? p.Wo + (size_t)(j >> 3) * 1024 * D_ + (j & 7) * 128 : xw == 3 ? p.w4 + (size_t)(j >> 3) * 896 * D_ + (j & 7) * 128
                  : xw == 2 ? p.w3 + j * 128 : (j < 8 ? p.w1 : p.w2) + (j & 7) * 128;
    const int nrows = (xw == 4 || (xw == 1 && j >= 16) || (xw == 2 && j >= 28)) ? 0 : xw == 3 ? 896 : 1024;
    const int mg = xw < 3 ? 64 + wk : 160 + j;
    stage_gemm<SG_PJ>(lds, rs, xw < 3 ? p.Wk : p.Wq, wk, xw < 3 ? xw : xw == 3 ? QB0 : 0, j, P_PJ + min(xw, 3) * 32 + j + (xw == 4 ? 400 : 0), cap, xw < 3 ? WS_K : WS_Q, xw < 3 ? 3 : 1,
                      xw == 0 ? 22 : xw < 3 ? 21 : xw == 3 ? NSEG - QB0 : QB0, nw, nrows, xw == 2 ? FF : D_, xw == 3 ? nullptr : p.wmod + mg * 32);
    WST(rs, wk, 3);
  }
  typedef __attribute__((address_space(4))) const MainP* KArg;
  KArg kp = (KArg)__builtin_amdgcn_kernarg_segment_ptr();
  asm volatile("" : "+s"(kp));
  if (xw != 3) { const int mg = xw < 3 ? 64 + wk : 160 + j; mod_part(rs, kp->wmod, kp->c, (LAS float*)(lds + G_XCH), mg); }
  if (xw == 0) stage_gemm<SG_WO>(lds, rs, kp->Wo, wk, j >> 3, j & 7, P_S2 + j, cap);
  else if (xw == 3) stage_gemm<SG_W4>(lds, rs, kp->w4, wk, j >> 3, j & 7, P_S8 + j, cap);
  else if (xw == 4) {
    unsigned chs = 0u;
    for (int sg = w >> 1; sg < NSEG; sg += 4) { attend(rs, sg, j, w & 1, lane, chs, cap); WST(rs, wk, 16 + sg); }
  }
  else if (j >= 28) stage_out(rs, kp->out, wk, (xw - 1) * 4 + j - 28, cap);
  else if (xw == 2) stage_gemm<SG_W3>(lds, rs, kp->w3, wk, 0, j, P_S7 + j, cap);
  else if (j < 8) stage_gemm<SG_W1>(lds, rs, kp->w1, wk, 0, j, P_S4 + j, cap);
  else if (j < 16) stage_gemm<SG_W2>(lds, rs, kp->w2, wk, 0, j - 8, P_S5 + j - 8, cap);
  else stage_mid(rs, kp->x, wk, j - 16, cap);
  if (cap == 0u && lane == 0) stx4(rs, 0, X_CTL, 1u);
}

#define ST_DECL
#define ST0()
#define ST(i)
#define ST_FLUSH()
constexpr int R_W = 0, R_MEM = 131072, R_QK = R_MEM + 16384, R_KT = R_QK + 8192, R_RED = R_MEM + 16384, R_LDS = 163840;
__device__ __forceinline__ void bar_lo(int w) { asm volatile("s_cmp_ge_u32 %0, 4\n\ts_cbranch_scc1 1f\n\ts_barrier\n\t1:" :: "s"(w) : "scc", "memory"); }
__device__ __forceinline__ void bar_hi(int w) { asm volatile("s_cmp_lt_u32 %0, 4\n\ts_cbranch_scc1 1f\n\ts_setprio 3\n\ts_barrier\n\t1:" :: "s"(w) : "scc", "memory"); }
#define ISSUE(R) asm volatile("buffer_load_dwordx4 v[" R "], %[vo], %[rs], %[so] offen sc1" : "=&{v[" R "]}"(a) : [vo] "v"(vo), [rs] "s"(rs), [so] "s"(so) : "memory")
template <int I> __device__ __forceinline__ void issue(__amdgpu_buffer_rsrc_t rs, unsigned vo, unsigned so, u32x4& a) {
  if (I == 0) ISSUE("240:243"); else if (I == 1) ISSUE("244:247"); else if (I == 2) ISSUE("248:251"); else ISSUE("252:255");
}
#define ARRIVE(R0, R1, R2, R3, CNT) \
  asm volatile("s_waitcnt vmcnt(" #CNT ")\n\t" \
               "1:\n\t" \
               "v_min3_u32 %[t], v" #R0 ", v" #R1 ", v" #R2 "\n\t" \
               "v_min_u32 %[t], %[t], v" #R3 "\n\t" \
               "v_cmp_eq_u32 vcc, 0, %[t]\n\t" \
               "s_cbranch_vccz 2f\n\t" \
               "s_mov_b64 exec, vcc\n\t" \
               "buffer_load_dwordx4 v[" #R0 ":" #R3 "], %[vo], %[rs], %[so] offen sc1\n\t" \
               "s_mov_b64 exec, -1\n\t" \
               "s_waitcnt vmcnt(0)\n\t" \
               "s_sub_u32 %[cap], %[cap], 1\n\t" \
               "s_cbranch_scc0 1b\n\t" \
               "s_mov_b32 %[cap], 0\n\t" \
               "2:" \
               : "+{v[" #R0 ":" #R3 "]}"(a), [t] "=&v"(t), [cap] "+s"(cap) : [vo] "v"(vo), [rs] "s"(rs), [so] "s"(so) : "vcc", "scc", "memory")
template <int I> __device__ __forceinline__ void arrive(__amdgpu_buffer_rsrc_t rs, unsigned vo, unsigned so, u32x4& a, unsigned& cap) {
  unsigned t;
  if (I == 0) ARRIVE(240, 241, 242, 243, 3);
  else if (I == 1) ARRIVE(244, 245, 246, 247, 2);
  else if (I == 2) ARRIVE(248, 249, 250, 251, 1);
  else ARRIVE(252, 253, 254, 255, 0);
}
__device__ __forceinline__ unsigned half_of(unsigned v) { unsigned r; asm volatile("v_lshrrev_b32 %0, 1, %1" : "=v"(r) : "v"(v)); return r; }
__device__ __forceinline__ void issue2(__amdgpu_buffer_rsrc_t rs, unsigned l8, unsigned so, int w, u32x4& a) {
  asm volatile("s_cmp_ge_u32 %[w], 4\n\ts_cbranch_scc1 1f\n\tv_lshrrev_b32 v255, 1, %[l8]\n\t"
               "buffer_load_dwordx2 v[252:253], v255, %[rs], %[so] offen sc1\n\tbuffer_load_dwordx2 v[254:255], v255, %[rs], %[so] offen offset:512 sc1\n\t1:"
               : "=&{v[252:255]}"(a) : [l8] "v"(l8), [rs] "s"(rs), [so] "s"(so), [w] "s"(w) : "scc", "memory");
}
__device__ __forceinline__ void arrive2(__amdgpu_buffer_rsrc_t rs, unsigned l8, unsigned so, u32x4& a, unsigned& cap) {
  unsigned t;
  asm volatile("s_waitcnt vmcnt(0)\n\t"
               "1:\n\t"
               "v_min3_u32 %[t], v252, v253, v254\n\t"
               "v_min_u32 %[t], %[t], v255\n\t"
               "v_cmp_eq_u32 vcc, 0, %[t]\n\t"
               "s_cbranch_vccz 2f\n\t"
               "v_lshrrev_b32 %[t], 1, %[l8]\n\t"
               "buffer_load_dwordx2 v[252:253], %[t], %[rs], %[so] offen sc1\n\t"
               "buffer_load_dwordx2 v[254:255], %[t], %[rs], %[so] offen offset:512 sc1\n\t"
               "s_waitcnt vmcnt(0)\n\t"
               "s_sub_u32 %[cap], %[cap], 1\n\t"
               "s_cbranch_scc0 1b\n\t"
               "s_mov_b32 %[cap], 0\n\t"
               "2:"
               : "+{v[252:255]}"(a), [t] "=&v"(t), [cap] "+s"(cap) : [l8] "v"(l8), [rs] "s"(rs), [so] "s"(so) : "vcc", "scc", "memory");
}
__device__ __forceinline__ unsigned ror8(unsigned v) { return __builtin_amdgcn_update_dpp(0u, v, 0x128, 0xF, 0xF, true); }
template <class F>
__device__ __forceinline__ void gather_mem(__amdgpu_buffer_rsrc_t rs, unsigned lbase, int s, int w, unsigned l16, unsigned& cap, F&& meanwhile) {
  const unsigned so = X_MEM + s * RSTR + w * 2048;
  u32x4 a0, a1;
  issue<2>(rs, l16, so, a0); issue<3>(rs, l16, so + 1024, a1);
  meanwhile();
  LAS unsigned char* d = lad(l16, lbase + R_MEM + w * 2048);
  arrive<2>(rs, l16, so, a0, cap);
  *(LAS u32x4*)(d) = a0;
  arrive<3>(rs, l16, so + 1024, a1, cap);
  *(LAS u32x4*)(d + 1024) = a1;
}
__device__ __forceinline__ unsigned mem_off(int lane) { return (unsigned)((lane & 1) * 512 + (((lane >> 1) ^ ((lane & 1) * 16)) * 8)); }
__device__ __forceinline__ bf16x8 mem_frag(LAS unsigned char* mp, int chunk) { return cat8(trread(mp + chunk * 1024), trread(mp + chunk * 1024 + 256)); }
__device__ __forceinline__ bf16x16 w_frag(LAS unsigned char* p) {
  return cat16(cat8(trread(p), trread(p + 2048)), cat8(trread(p + 4096), trread(p + 6144)));
}
__device__ __forceinline__ bf16x16 l_frag(LAS unsigned char* p, int i) { return cat16(*(const LAS bf16x8*)(p + i * 2048), *(const LAS bf16x8*)(p + i * 2048 + 1024)); }
__device__ __forceinline__ void make_resident(LAS unsigned char* wb, unsigned lo, unsigned l16) {
#pragma unroll
  for (int c = 0; c < 2; ++c) {
    bf16x16 f[4];
#pragma unroll
    for (int t = 0; t < 4; ++t) f[t] = w_frag(wb + c * 8192 + (lo ^ (t << 5)));
    WAIT_L0(); SCHED();
#pragma unroll
    for (int t = 0; t < 4; ++t) {
      *(LAS bf16x8*)(wb + (c * 4 + t) * 2048 + l16) = __builtin_shufflevector(f[t], f[t], 0, 1, 2, 3, 4, 5, 6, 7);
      *(LAS bf16x8*)(wb + (c * 4 + t) * 2048 + 1024 + l16) = __builtin_shufflevector(f[t], f[t], 8, 9, 10, 11, 12, 13, 14, 15);
    }
    WAIT_L0(); SCHED();
  }
}
__device__ __forceinline__ f32x4 fold(f32x4 a, f32x4 b) {
  f32x4 r;
#pragma unroll
  for (int i = 0; i < 4; ++i) {
    const auto t = __builtin_amdgcn_permlane32_swap(f2u(a[i]), f2u(b[i]), false, false);
    r[i] = u2f(t[0]) + u2f(t[1]);
  }
  return r;
}

struct Sched { unsigned tau, prev; int ea, eb, d1; bool moved; };
template <int CTRL> __device__ __forceinline__ int dpp_max(int v) { return max(v, (int)__builtin_amdgcn_update_dpp((unsigned)v, (unsigned)v, CTRL, 0xF, 0xF, false)); }
__device__ __forceinline__ int wave_max(int v) {
  v = dpp_max<0xB1>(v); v = dpp_max<0x4E>(v); v = dpp_max<0x141>(v); v = dpp_max<0x140>(v);
  return max(max(__builtin_amdgcn_readlane(v, 0), __builtin_amdgcn_readlane(v, 16)), max(__builtin_amdgcn_readlane(v, 32), __builtin_amdgcn_readlane(v, 48)));
}
__device__ __forceinline__ void times_issue(__amdgpu_buffer_rsrc_t rs, unsigned l16, int s, unsigned& xb, unsigned& xa) {
  asm volatile("v_lshlrev_b32 %0, 3, %2\n\tv_and_b32 %1, 0xF80, %0\n\tbuffer_load_dword %0, %0, %3, %4 offen sc1\n\tbuffer_load_dword %1, %1, %3, %5 offen sc1"
               : "=&v"(xb), "=&v"(xa) : "v"(l16), "s"(rs), "s"(X_TS + (unsigned)s * RSTR), "s"(X_TS + (unsigned)s * RSTR + 8192) : "memory");
}
__device__ __forceinline__ void retime(__amdgpu_buffer_rsrc_t rs, unsigned l16, int s, unsigned xb, unsigned xa, Sched& t, const MainP& p, unsigned& cap) {
  if (s > 0) {
    while (!__all(min(xa, xb) != 0u) && cap != 0u) {
      times_issue(rs, l16, s, xb, xa);
      asm volatile("s_waitcnt vmcnt(0)" : "+v"(xb), "+v"(xa) :: "memory");
      cap = __builtin_amdgcn_readfirstlane(cap - 1u);
    }
    cap = __builtin_amdgcn_readfirstlane(cap);
    const int tb = wave_max((int)(xb - t.prev)), ta = wave_max((int)(xa - t.prev));
    const int late = tb + p.d3 - (int)(t.tau - t.prev);
    if (s > 2) {
      const int up = s < 5 ? 80 : 8, dn = s < 10 ? 40 : 8;
      if (!t.moved) t.ea = max(t.ea - dn, min(ta * 8, t.ea + up));
      if (ta + p.d3 <= t.d1) t.eb = max(t.eb - dn, min((tb - t.d1) * 8, t.eb + up));
    }
    t.moved = late > 0;
    if (late > 0) t.tau += late + min(p.d4, 8 * late);
  }
  t.d1 = (t.ea >> 3) + p.d1;
}
__device__ __forceinline__ void advance(Sched& t, const MainP& p) { t.prev = t.tau; t.tau += t.d1 + (t.eb >> 3) + p.d2; }

__device__ __forceinline__ void role_a(const MainP& p, LAS unsigned char* lds, int h) {
  const int tid = threadIdx.x, lane = tid & 63, w = __builtin_amdgcn_readfirstlane(tid >> 6);
  const int cg = w & 3, kh = w >> 2;
  const __amdgpu_buffer_rsrc_t rs = mk_rs(p.ws, WS_END);
  const unsigned lbase = (unsigned)(size_t)lds;
  unsigned l16 = lane * 16;
  asm volatile("" : "+v"(l16));
  const int sidx = (lane & 8) ? 0xEEEE : 0x4444;
  bf16x16 W[24];
  unsigned qoff;
  {
    const int fr = lane & 15, g = lane >> 4, tu = 4 * (g & 1) + (fr >> 2), ub = lane >> 3;
    const unsigned lo = (g >> 1) * 1024 + tu * 128 + ((tu >> 1) << 5) + (fr & 3) * 8;
    qoff = R_QK + ((fr >> 1) & 1) * 512 + (g >> 1) * 256 + (fr & 1) * 128 + (8 * (g & 1) + (fr >> 2)) * 8;
    const __amdgpu_buffer_rsrc_t rw = mk_rs(cg < 2 ? p.Wq : p.Wk, D_ * HD * 2);
    const unsigned vo = (unsigned)(((ub & 3) + 8 * (ub >> 2)) * HD + 16 * (((lane >> 1) & 3) ^ (ub >> 1)) + 8 * (lane & 1)) * 2u;
    const unsigned so = (unsigned)(kh * 512 * HD + h * DH + (cg & 1) * 64) * 2u;
    auto stage = [&](int c) {
#pragma unroll
      for (int rho = 0; rho < 8; ++rho) dma16n<(NTM & 2) != 0>(rw, vo, so + (unsigned)((c * 64 + 32 * (rho >> 2) + 16 * (rho & 1) + 4 * ((rho >> 1) & 1)) * HD) * 2u, lbase + R_W + w * 16384 + (c & 1) * 8192 + rho * 1024);
    };
    stage(0); stage(1);
#pragma unroll
    for (int c = 0; c < 6; ++c) {
      WAIT_V(8);
#pragma unroll
      for (int t = 0; t < 4; ++t) W[4 * c + t] = w_frag(lds + R_W + w * 16384 + (c & 1) * 8192 + (lo ^ (t << 5)));
      WAIT_L0(); SCHED();
      stage(c + 2);
    }
    WAIT_V(0);
    make_resident(lds + R_W + w * 16384, lo, l16);
  }
  WST(rs, 160 + (h >> 1), 4);
  unsigned cap = SPIN_CAP;
  wait_ge(rs, 0, F_ST + (unsigned)(P_PJ + h) * 128, 1u, cap);
  dma16x(rs, l16, WS_K + h * 8192 + w * 1024, lbase + R_KT + w * 1024);
  Sched tt; tt.tau = rt32() - 100000; tt.prev = tt.tau; tt.ea = 195 * 8; tt.eb = 118 * 8; tt.d1 = 0; tt.moved = true;
  const int cu_ = h; (void)cu_;
  ST_DECL
  for (int s = 0; s < NSEG; ++s) {
    LAS unsigned char* lp = lad(l16, lbase + R_W + w * 16384);
    bf16x16 L[2];
    L[0] = l_frag(lp, 0); L[1] = l_frag(lp, 1);
    unsigned lz = l16;
    asm volatile("" : "+v"(lz));
    LAS unsigned char* mp = lad(mem_off((int)(lz >> 4)), lbase + R_MEM + kh * 8192);
    SCHED();
    if (w == 7) wait_until(tt.tau);
    BAR();
    ST0();
    gather_mem(rs, lbase, s, (w + h) & 7, l16, cap, []() {});
    ST(1);
    LBAR();
    ST(2);
    f32x4 c[4];
#pragma unroll
    for (int t = 0; t < 4; ++t) c[t] = f32x4{0.f, 0.f, 0.f, 0.f};
    bf16x8 al, af[2];
    u32x4 vt0, vt1;
    u32x4 vmv;
    const unsigned vso = X_VM + (unsigned)s * RSTR + h * 4096 + cg * 1024;
    al = mem_frag(mp, 6); af[0] = mem_frag(mp, 0); af[1] = mem_frag(mp, 1);
#pragma unroll
    for (int i = 0; i < 8; ++i) {
      c[i & 3] = smf(al, L[i & 1], c[i & 3], sidx);
      SCHED();
      if (i == 3) al = mem_frag(mp, 7);
      if (i < 6) L[i & 1] = l_frag(lp, i + 2);
      if (i == 6) {
        const unsigned vo = WS_VT + (unsigned)(s * NH + h) * 8192 + cg * 2048;
        asm volatile("s_cmp_ge_u32 %5, 4\n\ts_cbranch_scc1 1f\n\t"
                     "buffer_load_dwordx4 %0, %2, %3, %4 offen sc1\n\tbuffer_load_dwordx4 %1, %2, %3, %4 offen offset:1024 sc1\n\t1:"
                     : "=&v"(vt0), "=&v"(vt1) : "v"(l16), "s"(rs), "s"(vo), "s"(w) : "scc", "memory");
      }
      SCHED();
#pragma unroll
      for (int r = 3 * i; r < 3 * i + 3; ++r) {
        c[(r + 1) & 3] = smf(af[(r >> 2) & 1], W[(r & ~3) + ((r + 1) & 3)], c[(r + 1) & 3], sidx);
        SCHED();
        if ((r & 3) == 3 && (r >> 2) + 2 < 6) af[(r >> 2) & 1] = mem_frag(mp, (r >> 2) + 2);
        SCHED();
      }
    }
    {
      LAS unsigned char* qw = lad(half_of(l16), lbase + R_QK + cg * 2048 + kh * 512);
      const f32x4 f0 = fold(c[0], c[1]);
      *(LAS u32x2*)(qw) = u32x2{pack2(f0[0], f0[1]), pack2(f0[2], f0[3])};
      const f32x4 f1 = fold(c[2], c[3]);
      *(LAS u32x2*)(qw + 1024) = u32x2{pack2(f1[0], f1[1]), pack2(f1[2], f1[3])};
    }
    ST(3);
    if (w < 4) {
      bf16x8 k1[4], k2[4];
      LAS unsigned char* kb = lad(l16, lbase + R_KT);
#pragma unroll
      for (int ks = 0; ks < 2; ++ks) { k1[ks] = *(const LAS bf16x8*)(kb + ks * 1024); k2[ks] = *(const LAS bf16x8*)(kb + 4096 + ks * 1024); }
      unsigned ov;
      {
        unsigned ot;
        asm volatile("v_and_b32 %0, 0x30, %2\n\tv_lshrrev_b32 %1, 2, %2\n\tv_and_b32 %1, 0xC0, %1\n\tv_or_b32 %0, %0, %1\n\t"
                     "v_and_b32 %1, 0x40, %2\n\tv_lshl_or_b32 %0, %1, 9, %0\n\tv_and_b32 %1, 0x80, %2\n\tv_lshl_or_b32 %0, %1, 24, %0"
                     : "=&v"(ov), "=&v"(ot) : "v"(l16));
      }
      LBAR();
      ST(4);
      issue2(rs, l16, vso, w, vmv);
      f32x4 t0 = {0.f, 0.f, 0.f, 0.f}, t1 = t0, t2 = t0;
      LAS unsigned char* qb = lad(qoff, lbase);
      bf16x8 qf[4], kf[4];
#pragma unroll
      for (int ks = 0; ks < 2; ++ks) {
        qf[ks] = cat8(trread(qb + ks * 1024), trread(qb + ks * 1024 + 32));
        kf[ks] = cat8(trread(qb + 4096 + ks * 1024), trread(qb + 4096 + ks * 1024 + 32));
      }
      SCHED();
#pragma unroll
      for (int ks = 0; ks < 4; ++ks) {
        t1 = mfma32(k1[ks], qf[ks], t1); t2 = mfma32(k2[ks], qf[ks], t2); t0 = mfma32(kf[ks], qf[ks], t0);
        SCHED();
        if (ks < 2) {
          qf[ks + 2] = cat8(trread(qb + (ks + 2) * 1024), trread(qb + (ks + 2) * 1024 + 32));
          k1[ks + 2] = *(const LAS bf16x8*)(kb + (ks + 2) * 1024); k2[ks + 2] = *(const LAS bf16x8*)(kb + 4096 + (ks + 2) * 1024);
          kf[ks + 2] = cat8(trread(qb + 4096 + (ks + 2) * 1024), trread(qb + 4096 + (ks + 2) * 1024 + 32));
        }
        SCHED();
      }
      ST(5);
      asm volatile("s_waitcnt vmcnt(2)" : "+v"(vt0), "+v"(vt1) :: "memory");
      {
        const float sl = 0.08838834764831845f * 1.4426950408889634f;
        float z[4] = {t1[0], t1[1], t1[2], t1[3]}, y = t0[0], mx;
        {
          float u1 = t0[1], u2 = t0[2], u3 = t0[3];
#define M8(Z, B) "v_add_f32_dpp " Z ", " Z ", " Z " row_ror:8 row_mask:0xf bank_mask:0x3\n\tv_add_f32_dpp " Z ", " B ", " B " row_ror:8 row_mask:0xf bank_mask:0xc\n\t"
          asm volatile("s_nop 7\n\t"
              "v_permlane32_swap_b32 %4, %6\n\tv_permlane32_swap_b32 %5, %7\n\t"
              M8("%0", "%9") "v_add_f32 %4, %4, %6\n\tv_add_f32 %5, %5, %7\n\t" M8("%1", "%10") M8("%2", "%11") M8("%4", "%5") M8("%3", "%12")
              "v_max3_f32 %8, %0, %1, %2\n\tv_max3_f32 %8, %8, %4, %3\n\ts_nop 1\n\t"
              "v_max_f32_dpp %8, %8, %8 row_ror:8 row_mask:0xf bank_mask:0xf\n\tv_mov_b32 %5, %8\n\ts_nop 1\n\t"
              "v_permlane16_swap_b32 %8, %5\n\tv_max_f32 %8, %8, %5\n\tv_mov_b32 %5, %8\n\ts_nop 1\n\t"
              "v_permlane32_swap_b32 %8, %5\n\tv_max_f32 %8, %8, %5"
              : "+v"(z[0]), "+v"(z[1]), "+v"(z[2]), "+v"(z[3]), "+v"(y), "+v"(u1), "+v"(u2), "+v"(u3), "=&v"(mx) : "v"(t2[0]), "v"(t2[1]), "v"(t2[2]), "v"(t2[3]));
#undef M8
        }
        const float nm = -mx * sl;
        y = __builtin_amdgcn_exp2f(fmaf(y, sl, nm));
#pragma unroll
        for (int r = 0; r < 4; ++r) z[r] = __builtin_amdgcn_exp2f(fmaf(z[r], sl, nm));
        const unsigned pm1 = pack2(y, u2f(ror8(f2u(y))));
        const auto pms = __builtin_amdgcn_permlane32_swap(pm1, pm1, false, false);
        const u32x2 pm = {pms[0], pms[1]};
        const unsigned p0 = pack2(z[0], z[1]), p1 = pack2(z[2], z[3]);
        const u32x4 pt = {p0, p1, ror8(p0), ror8(p1)};
        f32x4 o0 = mfma32(__builtin_bit_cast(bf16x8, vt0), __builtin_bit_cast(bf16x8, pt), f32x4{0.f, 0.f, 0.f, 0.f});
        f32x4 o1 = mfma32(__builtin_bit_cast(bf16x8, vt1), __builtin_bit_cast(bf16x8, pt), f32x4{0.f, 0.f, 0.f, 0.f});
        float inv = __builtin_amdgcn_rcpf(gsum(dpp_add<0x128>(((z[0] + z[1]) + (z[2] + z[3])) + y)));
        asm volatile("" : "+v"(inv));
        arrive2(rs, l16, vso, vmv, cap);
        const f32x4 n0 = mfma16n(u32x2{vmv[0], vmv[1]}, pm, f32x4{0.f, 0.f, 0.f, 0.f}), n1 = mfma16n(u32x2{vmv[2], vmv[3]}, pm, f32x4{0.f, 0.f, 0.f, 0.f});
        SCHED();
        o0 *= inv; o1 *= inv;
        {
          const u32x4 v = {nz(pack2(fmaf(n0[0], inv, o0[0]), fmaf(n0[1], inv, o0[1]))), nz(pack2(fmaf(n0[2], inv, o0[2]), fmaf(n0[3], inv, o0[3]))),
                           nz(pack2(fmaf(n1[0], inv, o1[0]), fmaf(n1[1], inv, o1[1]))), nz(pack2(fmaf(n1[2], inv, o1[2]), fmaf(n1[3], inv, o1[3])))};
          stx16(rs, ov, X_OX + s * RSTR + h * 1024 + w * 256, v);
        }
        if (w == 3) { const unsigned t = max(rt32(), 1u); if (l16 == 0) stx4(rs, 0, X_TS + (unsigned)(s + 1) * RSTR + (unsigned)(64 + h) * 128, t); }
        ST(8);
      }
    } else {
      LBAR();
      LAS unsigned char* qb = lad(qoff, lbase + 4096 + (w - 4) * 1024);
      u32x4 kf = __builtin_bit_cast(u32x4, cat8(trread(qb), trread(qb + 32)));
#pragma unroll
      for (int i = 0; i < 4; ++i) kf[i] = pack2(dpp_add<0x128>(lo2f(kf[i])), dpp_add<0x128>(hi2f(kf[i])));
      stx16(rs, l16, WS_KM + (unsigned)(s * NH + h) * 4096 + (w - 4) * 1024, kf);
      WAIT_V(0);
    }
    LBAR();
    if (w == 4 && l16 == 0) stx4(rs, 0, F_CH + h * 128, (unsigned)s + 1u);
    ST(9);
    if (w == 7) {
      unsigned xb, xa;
      times_issue(rs, l16, s, xb, xa);
      asm volatile("s_waitcnt vmcnt(0)" : "+v"(xb), "+v"(xa) :: "memory");
      retime(rs, l16, s, xb, xa, tt, p, cap); advance(tt, p);
    }
    if (s + 1 < NSEG) {
      unsigned fo;
      asm volatile("v_mov_b32 %0, 0" : "=v"(fo));
      wait_ge(rs, fo, F_ST + (unsigned)(P_PJ + ((s + 1) % 3) * 32 + h) * 128, (unsigned)s + 2u, cap);
      dma16x(rs, l16, WS_K + (unsigned)((s + 1) * NH + h) * 8192 + w * 1024, lbase + R_KT + w * 1024);
    }
  }
  ST_FLUSH();
  if (cap == 0u && l16 == 0) stx4(rs, 0, X_CTL, 1u);
}

__device__ __forceinline__ void role_vb(const MainP& p, LAS unsigned char* lds, int rg, int cb) {
  const int tid = threadIdx.x, lane = tid & 63, w = __builtin_amdgcn_readfirstlane(tid >> 6);
  const int kh = w >> 2, wr = (w + cb) & 7;
  const __amdgpu_buffer_rsrc_t rs = mk_rs(p.ws, WS_END);
  const unsigned lbase = (unsigned)(size_t)lds;
  unsigned l16 = lane * 16, moff = mem_off(lane), roff, soff = (unsigned)((lane >> 5) * 256 + (lane & 31) * 4);
  const int sidx = (lane & 8) ? 0xEEEE : 0x4444;
  bf16x16 WV[8], WB[16];
  {
    const int fr = lane & 15, g = lane >> 4, tu = 4 * (g & 1) + (fr >> 2), ub = lane >> 3;
    { const int sl = mem_slot(16 * (g >> 1) + fr); roff = R_RED + w * 512 + ((sl >> 5) * 32 + (sl & 15) * 2 + (g & 1)) * 8; }
    const __amdgpu_buffer_rsrc_t rv = mk_rs(p.Wv, D_ * HD * 2), ro = mk_rs(p.Wo, D_ * HD * 2);
    const unsigned vv = (unsigned)(((ub & 3) + 8 * (ub >> 2)) * HD + 16 * (((lane >> 1) & 3) ^ (ub >> 1)) + 8 * (lane & 1)) * 2u;
    const unsigned sv = (unsigned)((32 * (w >> 2) + 16 * (w & 1) + 4 * ((w >> 1) & 1)) * HD + cb * DH + rg * 64) * 2u;
#pragma unroll
    for (int c = 0; c < 16; ++c) dma16n<(NTM & 1) != 0>(rv, vv, sv + (unsigned)c * 64 * HD * 2, lbase + R_W + c * 8192 + w * 1024);
    WAIT_V(0); BAR();
    const unsigned lo = (g >> 1) * 1024 + tu * 128 + (((tu >> 1) ^ (w & 3)) << 5) + (fr & 3) * 8;
#pragma unroll
    for (int c = 0; c < 8; ++c) WV[c] = w_frag(lds + R_W + (kh * 8 + c) * 8192 + lo);
    LBAR();
    const int u = (lane >> 2) & 7;
    const unsigned vw = (unsigned)((16 * (u >> 2) + 8 * ((u >> 1) & 1) + 4 * (lane >> 5) + (u & 1)) * D_ + (lane & 3) * 8) * 2u;
    const unsigned sw = (unsigned)(wr * 512 * D_ + cb * 32) * 2u;
    const unsigned bo = (g >> 1) * 1024 + tu * 64 + (fr & 3) * 8;
    auto stageo = [&](int hd) {
#pragma unroll
      for (int q = 0; q < 8; ++q)
        dma16n<(NTM & 4) != 0>(ro, vw, sw + (unsigned)((hd * 128 + 64 * (q >> 2) + 32 * (q & 1) + 2 * ((q >> 1) & 1)) * D_) * 2u, lbase + R_W + w * 16384 + (hd & 1) * 8192 + q * 1024);
    };
    stageo(0); stageo(1);
#pragma unroll
    for (int hd = 0; hd < 4; ++hd) {
      if (hd < 3) wait_v<8>(); else wait_v<0>();
#pragma unroll
      for (int f = 0; f < 4; ++f) WB[hd * 4 + f] = w_frag(lds + R_W + w * 16384 + (hd & 1) * 8192 + (f >> 1) * 512 + (f & 1) * 32 + bo);
      WAIT_L0(); SCHED();
      if (hd < 2) stageo(hd + 2);
    }
  }
  WST(rs, 176 + rg * 8 + (cb >> 2), 4);
  unsigned hsrc = (unsigned)((lane >> 3) * 2048 + (((lane & 7) ^ (lane >> 3)) << 4)), hoff = (unsigned)((lane & 15) * 128 + (((lane >> 4) ^ (lane & 7)) << 4));
  asm volatile("" : "+v"(l16), "+v"(moff), "+v"(roff), "+v"(soff), "+v"(hsrc), "+v"(hoff));
  unsigned cap = SPIN_CAP;
  auto h_piece = [&](int b, int q) { dma16x(rs, hsrc, WS_H + (unsigned)b * 65536u + (w & 3) * 16384 + (2 * q + kh) * 128, lbase + R_W + (b & 1) * 65536 + (q * 8 + w) * 1024); };
  auto h_fetch = [&](int b) {
#pragma unroll
    for (int q = 0; q < 8; ++q) h_piece(b, q);
  };
  f32x4 x0, x1;
  auto v_part = [&](int b, auto part, auto&& each) {
    constexpr int K0 = decltype(part)::value * 8;
    LAS unsigned char* a0 = lad(hoff, lbase + R_W + (b & 1) * 65536 + kh * 32768);
    LAS unsigned char* a1 = lad<64>(hoff, lbase + R_W + (b & 1) * 65536 + kh * 32768);
    bf16x8 af[3][2];
    auto rd = [&](int k) { LAS unsigned char* q = ((k & 1) ? a1 : a0) + (k >> 1) * 4096; af[k % 3][0] = *(const LAS bf16x8*)(q); af[k % 3][1] = *(const LAS bf16x8*)(q + 2048); };
    rd(K0); rd(K0 + 1);
    SCHED();
#pragma unroll
    for (int k = K0; k < K0 + 8; ++k) {
      if (k + 2 < K0 + 8) rd(k + 2);
      const bf16x8 wf = (k & 1) ? __builtin_shufflevector(WV[k >> 1], WV[k >> 1], 8, 9, 10, 11, 12, 13, 14, 15) : __builtin_shufflevector(WV[k >> 1], WV[k >> 1], 0, 1, 2, 3, 4, 5, 6, 7);
      x0 = mfma32(af[k % 3][0], wf, k ? x0 : f32x4{0.f, 0.f, 0.f, 0.f}); x1 = mfma32(af[k % 3][1], wf, k ? x1 : f32x4{0.f, 0.f, 0.f, 0.f});
      asm volatile("" : "+v"(x0), "+v"(x1));
      SCHED();
      each(k);
      SCHED();
    }
    if (K0) {
      LAS unsigned char* xv = lad(l16, lbase + R_RED + 4096 + (w & 3) * 2048);
      asm volatile("s_nop 7\n\ts_cmp_lt_u32 %3, 4\n\ts_cbranch_scc1 1f\n\tds_write_b128 %0, %1\n\tds_write_b128 %0, %2 offset:1024\n\t1:" :: "v"(xv), "v"(x0), "v"(x1), "s"(w) : "scc", "memory");
    }
  };
  auto v_out = [&](int b) {
    if (!kh && b < NSEG) {
      LAS unsigned char* xv = lad(l16, lbase + R_RED + 4096 + (w & 3) * 2048);
      x0 += *(const LAS f32x4*)(xv); x1 += *(const LAS f32x4*)(xv + 1024);
      stx16(rs, l16, WS_VT + (unsigned)(b * NH + cb) * 8192 + (rg * 4 + w) * 1024, u32x4{pack2(x0[0], x0[1]), pack2(x0[2], x0[3]), pack2(x1[0], x1[1]), pack2(x1[2], x1[3])});
    }
  };
  typedef std::integral_constant<int, 0> P0; typedef std::integral_constant<int, 1> P1;
  BAR();
  wait_ge(rs, (l16 & 0xF0) * 8 + (l16 >> 8) * 4, F_LN, 1u, cap);
  h_fetch(0); h_fetch(1);
  WAIT_V(8); BAR();
  v_part(0, P0{}, [](int) {}); v_part(0, P1{}, [](int) {});
  WAIT_V(0); LBAR();
  v_out(0); h_fetch(2);
  LBAR();
  v_part(1, P0{}, [](int) {}); v_part(1, P1{}, [](int) {});
  WAIT_V(0); LBAR();
  v_out(1); h_fetch(3);
  v_part(2, P0{}, [](int) {});
  WAIT_V(0); BAR();
  if (w == 0) {
    const int ln = l16 >> 4, sl = (ln >> 1) & 15, col = cb * 32 + (sl >> 3) * 16 + (ln >> 5) * 8 + (sl & 4) + (sl & 1) * 2 + ((sl >> 1) & 1), r = rg * 4 + (ln & 1) * 2;
    stx4(rs, soff, X_MEM + (cb >> 1) * 1024 + rg * 512 + ((cb ^ rg) & 1) * 128, nz((unsigned)p.mem0[r * D_ + col] | ((unsigned)p.mem0[(r + 1) * D_ + col] << 16)));
  }
  Sched tt; tt.tau = rt32() - 100000; tt.prev = tt.tau; tt.ea = 195 * 8; tt.eb = 118 * 8; tt.d1 = 0; tt.moved = true;
  const int cu_ = 32 + (rg * 32 + cb) / 2; (void)cu_;
  ST_DECL
  for (int s = 0; s < NSEG; ++s) {
    if (w == 7) wait_until(tt.tau);
    BAR();
    ST0();
    gather_mem(rs, lbase, s, wr, l16, cap, [&]() { v_part(s + 2, P1{}, [](int) {}); });
    ST(1);
    LBAR();
    ST(2);
    {
      f32x4 c0 = {0.f, 0.f, 0.f, 0.f};
      LAS unsigned char* mp = lad(moff, lbase + R_MEM + kh * 8192);
      bf16x8 af[6];
#pragma unroll
      for (int c = 0; c < 6; ++c) af[c] = mem_frag(mp, c);
      SCHED();
#pragma unroll
      for (int c = 0; c < 8; ++c) { c0 = smf(af[c % 6], WV[c], c0, sidx); SCHED(); if (c < 2) { af[c] = mem_frag(mp, 6 + c); SCHED(); } }
      const f32x4 f = fold(c0, c0);
      if (l16 < 512) stx8(rs, half_of(l16), X_VM + (unsigned)s * RSTR + cb * 4096 + (rg * 4 + (w & 3)) * 512 + kh * 256, u32x2{nz(pack2(f[0], f[1])), nz(pack2(f[2], f[3]))});
    }
    ST(3);
    {
      unsigned xb, xa;
      times_issue(rs, w == 7 ? l16 : 0x1E000000u, s, xb, xa);
      if (s < 36 && !(s & 3)) wait_ge(rs, (l16 & 0xF0) * 8 + (l16 >> 8) * 4, F_LN + (unsigned)(s + 4) * 512, 1u, cap);
      h_fetch(min(s + 4, NSEG - 2 + (s & 1)));
      v_out(s + 2);
      asm volatile("s_waitcnt vmcnt(8)" : "+v"(xb), "+v"(xa) :: "memory");
      if (w == 7) retime(rs, l16, s, xb, xa, tt, p, cap);
    }
    if (s + 1 < NSEG) {
      u32x4 a[4];
      const unsigned so = X_OX + s * RSTR + rg * 32768 + wr * 4096;
      if (w == 7) wait_until(tt.tau + tt.d1);
      BAR();
      ST(4);
      issue<0>(rs, l16, so, a[0]);
      v_part(s + 3, P0{}, [&](int k) {
        if (k == 0) issue<1>(rs, l16, so + 1024, a[1]);
        if (k == 2) issue<2>(rs, l16, so + 2048, a[2]);
        if (k == 4) issue<3>(rs, l16, so + 3072, a[3]);
      });
      f32x4 c[4];
#pragma unroll
      for (int t = 0; t < 4; ++t) c[t] = f32x4{0.f, 0.f, 0.f, 0.f};
#pragma unroll
      for (int i = 0; i < 4; ++i) {
        if (i == 0) arrive<0>(rs, l16, so, a[0], cap); else if (i == 1) arrive<1>(rs, l16, so + 1024, a[1], cap);
        else if (i == 2) arrive<2>(rs, l16, so + 2048, a[2], cap); else arrive<3>(rs, l16, so + 3072, a[3], cap);
#pragma unroll
        for (int t = 0; t < 4; ++t) c[t] = smf(__builtin_bit_cast(bf16x8, a[i]), WB[i * 4 + t], c[t], sidx);
        SCHED();
      }
      ST(5);
      {
        const bool odd = (l16 & 256) != 0;
        const f32x4 f = fold(odd ? c[2] : c[0], odd ? c[3] : c[1]);
        const auto e0 = __builtin_amdgcn_permlane16_swap(f2u(f[0]), f2u(f[2]), false, false), e1 = __builtin_amdgcn_permlane16_swap(f2u(f[1]), f2u(f[3]), false, false);
        *(LAS f32x2*)(lad(roff, lbase)) = f32x2{u2f(e0[0]) + u2f(e0[1]), u2f(e1[0]) + u2f(e1[1])};
      }
      ST(6);
      LBAR();
      ST(7);
      if (w == 0) {
        LAS unsigned char* rp = lad(half_of(l16), lbase + R_RED);
        f32x2 v[8];
#pragma unroll
        for (int i = 0; i < 8; ++i) v[i] = *(const LAS f32x2*)(rp + i * 512);
        const f32x2 sum = ((v[0] + v[1]) + (v[2] + v[3])) + ((v[4] + v[5]) + (v[6] + v[7]));
        stx4(rs, soff, X_MEM + (s + 1) * RSTR + (cb >> 1) * 1024 + rg * 512 + ((cb ^ rg) & 1) * 128, nz(pack2(sum[0], sum[1])));
        const unsigned t = max(rt32(), 1u);
        if (l16 == 0) stx4(rs, 0, X_TS + (unsigned)(s + 1) * RSTR + (unsigned)(rg * 32 + cb) * 128, t);
      }
      ST(8);
    }
    advance(tt, p);
  }
  ST_FLUSH();
  if (cap == 0u && l16 == 0) stx4(rs, 0, X_CTL, 1u);
}

#if defined(__HIP_DEVICE_COMPILE__)
__attribute__((target("no-packed-fp32-ops")))
#endif
__global__ void __launch_bounds__(512, 2) k_main(MainP p) {
  extern __shared__ __attribute__((aligned(16))) unsigned char smem[];
  const int b = blockIdx.x, x = b & 7, j = b >> 3;
  if (x >= 3) worker(p, (LAS unsigned char*)smem, x - 3, j);
  else if (x == 0) role_a(p, (LAS unsigned char*)smem, j);
  else role_vb(p, (LAS unsigned char*)smem, x - 1, j);
}

extern "C" void kernel_launch(void* const* d_in, const int* in_sizes, int n_in, void* d_out, int out_size, void* d_ws, size_t ws_size,
                              hipStream_t stream) {
  unsigned char* ws = (unsigned char*)d_ws;
  (void)hipMemsetAsync(ws, 0, X_ZERO, stream);
  MainP p;
  p.x = (const u16*)d_in[0]; p.mem0 = (const u16*)d_in[1]; p.c = (const u16*)d_in[12]; p.wmod = (const u16*)d_in[13]; p.Wq = (const u16*)d_in[2]; p.Wk = (const u16*)d_in[3]; p.Wv = (const u16*)d_in[4]; p.Wo = (const u16*)d_in[5];
  p.w1 = (const u16*)d_in[8]; p.w2 = (const u16*)d_in[9]; p.w3 = (const u16*)d_in[10]; p.w4 = (const u16*)d_in[11];
  p.out = (u16*)d_out; p.ws = ws; p.d1 = 8; p.d2 = 8; p.d3 = 2; p.d4 = 100;
  (void)hipFuncSetAttribute((const void*)k_main, hipFuncAttributeMaxDynamicSharedMemorySize, R_LDS);
  k_main<<<256, 512, R_LDS, stream>>>(p);
}
```

```cpp
#include <hip/hip_runtime.h>
#include <hip/hip_bf16.h>
#include <cstdlib>
#include <type_traits>
#pragma clang diagnostic ignored "-Winline-asm"

#define LAS __attribute__((address_space(3)))
#define GAS __attribute__((address_space(1)))
typedef unsigned short u16;
typedef __bf16 bf16x2 __attribute__((ext_vector_type(2)));
typedef __bf16 bf16x4 __attribute__((ext_vector_type(4)));
typedef __bf16 bf16x8 __attribute__((ext_vector_type(8)));
typedef __bf16 bf16x16 __attribute__((ext_vector_type(16)));
typedef short s16x4 __attribute__((ext_vector_type(4)));
typedef float f32x2 __attribute__((ext_vector_type(2)));
typedef float f32x4 __attribute__((ext_vector_type(4)));
typedef float f32x16 __attribute__((ext_vector_type(16)));
typedef unsigned u32x2 __attribute__((ext_vector_type(2)));
typedef unsigned u32x4 __attribute__((ext_vector_type(4)));

constexpr int S_ = 2048, D_ = 1024, NH = 32, DH = 128, HD = 4096, NSEG = 64, FF = 3584;

constexpr unsigned MB = 1u << 20;
constexpr unsigned F_LN = 0;
constexpr unsigned F_ST = 24576;
constexpr unsigned F_CH = F_ST + 98304;
constexpr unsigned X_CTL = F_CH + 4096;
constexpr unsigned X_MOD = X_CTL + 4096;
constexpr unsigned X_REC = X_MOD + 24576;
constexpr unsigned RSTR = 229376;
constexpr int ZA = 8;
constexpr unsigned X_MEM = X_REC;
constexpr unsigned X_OX = X_REC + 16384;
constexpr unsigned X_VM = X_REC + 81920;
constexpr unsigned X_TS = X_REC + 212992;
constexpr unsigned X_ZERO = X_REC + 16384;
constexpr unsigned WS_H = 16 * MB;
constexpr unsigned WS_Q = 20 * MB;
constexpr unsigned WS_K = 36 * MB;
constexpr unsigned WS_VT = 52 * MB;
constexpr unsigned WS_O = 68 * MB;
constexpr unsigned WS_X1 = 84 * MB;
constexpr unsigned WS_H2 = 88 * MB;
constexpr unsigned WS_G = 92 * MB;
constexpr unsigned WS_H1S = 96 * MB;
constexpr unsigned WS_X2 = 101 * MB;
constexpr unsigned WS_ST5 = 106 * MB;
constexpr unsigned WS_U = 108 * MB;
constexpr unsigned WS_KM = 124 * MB;
constexpr unsigned WS_P2 = 132 * MB;
constexpr unsigned WS_P8 = 164 * MB;
constexpr unsigned WS_END = 198 * MB;

__device__ __forceinline__ float bf2f(unsigned short b) { return __builtin_bit_cast(float, (unsigned)b << 16); }
__device__ __forceinline__ unsigned f2u(float f) { return __builtin_bit_cast(unsigned, f); }
__device__ __forceinline__ float u2f(unsigned u) { return __builtin_bit_cast(float, u); }
__device__ __forceinline__ float lo2f(unsigned w) { return __builtin_bit_cast(float, w << 16); }
__device__ __forceinline__ float hi2f(unsigned w) { return __builtin_bit_cast(float, w & 0xFFFF0000u); }
__device__ __forceinline__ unsigned pack2(float a, float b) { bf16x2 v; v[0] = (__bf16)a; v[1] = (__bf16)b; return __builtin_bit_cast(unsigned, v); }
__device__ __forceinline__ float rbf(float a) { return (float)(__bf16)a; }
__device__ __forceinline__ unsigned nz(unsigned w) { return max(w, 1u); }
__device__ __forceinline__ float gelu_tanh(float x) {
  const float e = __builtin_amdgcn_exp2f(x * fmaf(x * x, -0.1029432397f, -2.302208198f));
  return x * __builtin_amdgcn_rcpf(1.0f + e);
}
template <int CTRL> __device__ __forceinline__ float dpp_add(float v) { return v + u2f(__builtin_amdgcn_update_dpp(0u, f2u(v), CTRL, 0xF, 0xF, true)); }
__device__ __forceinline__ float wave_sum(float v) {
  v = dpp_add<0xB1>(v); v = dpp_add<0x4E>(v); v = dpp_add<0x141>(v); v = dpp_add<0x140>(v);
  auto t = __builtin_amdgcn_permlane16_swap(f2u(v), f2u(v), false, false);
  v = u2f(t[0]) + u2f(t[1]);
  t = __builtin_amdgcn_permlane32_swap(f2u(v), f2u(v), false, false);
  return u2f(t[0]) + u2f(t[1]);
}
#define WAIT_V(n) asm volatile("s_waitcnt vmcnt(" #n ")" ::: "memory")
#define WAIT_L0() asm volatile("s_waitcnt lgkmcnt(0)" ::: "memory")
#define BAR() do { asm volatile("" ::: "memory"); __builtin_amdgcn_s_barrier(); asm volatile("" ::: "memory"); } while (0)
#define LBAR() do { WAIT_L0(); BAR(); } while (0)
template <int N> __device__ __forceinline__ void wait_v() { asm volatile("s_waitcnt vmcnt(%0)" :: "n"(N) : "memory"); }
__device__ __forceinline__ void dma16(__amdgpu_buffer_rsrc_t rs, unsigned voff, unsigned soff, unsigned ldsa) {
  asm volatile("s_mov_b32 m0, %0\n\ts_nop 3\n\tbuffer_load_dwordx4 %1, %2, %3 offen lds" :: "s"(ldsa), "v"(voff), "s"(rs), "s"(soff) : "m0", "memory");
}
template <bool NT> __device__ __forceinline__ void dma16n(__amdgpu_buffer_rsrc_t rs, unsigned voff, unsigned soff, unsigned ldsa) {
  if (NT) asm volatile("s_mov_b32 m0, %0\n\ts_nop 3\n\tbuffer_load_dwordx4 %1, %2, %3 offen nt lds" :: "s"(ldsa), "v"(voff), "s"(rs), "s"(soff) : "m0", "memory");
  else dma16(rs, voff, soff, ldsa);
}
#define NTM 1
__device__ __forceinline__ void dma16x(__amdgpu_buffer_rsrc_t rs, unsigned voff, unsigned soff, unsigned ldsa) {
  asm volatile("s_mov_b32 m0, %0\n\ts_nop 3\n\tbuffer_load_dwordx4 %1, %2, %3 offen sc1 lds" :: "s"(ldsa), "v"(voff), "s"(rs), "s"(soff) : "m0", "memory");
}
__device__ __forceinline__ u32x4 ldx16(__amdgpu_buffer_rsrc_t rs, unsigned voff, unsigned soff) { return __builtin_amdgcn_raw_buffer_load_b128(rs, voff, soff, 16); }
__device__ __forceinline__ u32x2 ldx8(__amdgpu_buffer_rsrc_t rs, unsigned voff, unsigned soff) { return __builtin_amdgcn_raw_buffer_load_b64(rs, voff, soff, 16); }
__device__ __forceinline__ unsigned ldx4(__amdgpu_buffer_rsrc_t rs, unsigned voff, unsigned soff) { return __builtin_amdgcn_raw_buffer_load_b32(rs, voff, soff, 16); }
__device__ __forceinline__ void stx16(__amdgpu_buffer_rsrc_t rs, unsigned voff, unsigned soff, u32x4 v) {
  __builtin_amdgcn_raw_buffer_store_b128(v, rs, voff, soff, 16);
  asm volatile("s_nop 1" :: "v"(v) : "memory");
}
__device__ __forceinline__ void stx8(__amdgpu_buffer_rsrc_t rs, unsigned voff, unsigned soff, u32x2 v) { __builtin_amdgcn_raw_buffer_store_b64(v, rs, voff, soff, 16); }
__device__ __forceinline__ void stx4(__amdgpu_buffer_rsrc_t rs, unsigned voff, unsigned soff, unsigned v) { __builtin_amdgcn_raw_buffer_store_b32(v, rs, voff, soff, 16); }
__device__ __forceinline__ void wait_ge(__amdgpu_buffer_rsrc_t rs, unsigned voff, unsigned soff, unsigned need, unsigned& cap) {
  unsigned t;
  asm volatile("s_nop 4\n\t"
               "1:\n\t"
               "buffer_load_dword %[t], %[vo], %[rs], %[so] offen sc1\n\t"
               "s_waitcnt vmcnt(0)\n\t"
               "v_cmp_gt_u32 vcc, %[need], %[t]\n\t"
               "s_cmp_eq_u64 vcc, 0\n\t"
               "s_cbranch_scc1 2f\n\t"
               "s_sleep 2\n\t"
               "s_sub_u32 %[cap], %[cap], 1\n\t"
               "s_cbranch_scc0 1b\n\t"
               "s_mov_b32 %[cap], 0\n\t"
               "2:"
               : [t] "=&v"(t), [cap] "+s"(cap) : [vo] "v"(voff), [rs] "s"(rs), [so] "s"(soff), [need] "s"(need) : "vcc", "scc", "memory");
}
__device__ __forceinline__ void wait_seen(__amdgpu_buffer_rsrc_t rs, unsigned voff, unsigned soff, unsigned need, unsigned& seen, unsigned& cap) {
  if (seen >= need) return;
  unsigned t;
  asm volatile("s_nop 4\n\t"
               "1:\n\t"
               "buffer_load_dword %[t], %[vo], %[rs], %[so] offen sc1\n\t"
               "s_waitcnt vmcnt(0)\n\t"
               "v_cmp_gt_u32 vcc, %[need], %[t]\n\t"
               "s_cbranch_vccz 2f\n\t"
               "s_sleep 1\n\t"
               "s_sub_u32 %[cap], %[cap], 1\n\t"
               "s_cbranch_scc0 1b\n\t"
               "s_mov_b32 %[cap], 0\n\t"
               "2:"
               : [t] "=&v"(t), [cap] "+s"(cap) : [vo] "v"(voff), [rs] "s"(rs), [so] "s"(soff), [need] "s"(need) : "vcc", "scc", "memory");
  seen = min((unsigned)__builtin_amdgcn_readlane(t, 0), (unsigned)__builtin_amdgcn_readlane(t, 1));
}
__device__ __forceinline__ __amdgpu_buffer_rsrc_t mk_rs(const void* p, unsigned bytes) { return __builtin_amdgcn_make_buffer_rsrc((void*)p, 0, (int)bytes, 0x00020000); }
#define SCHED() __builtin_amdgcn_sched_barrier(0)
__device__ __forceinline__ unsigned now32() { unsigned long long t; asm volatile("s_memtime %0\n\ts_waitcnt lgkmcnt(0)" : "=s"(t) :: "memory"); return (unsigned)t; }
__device__ __forceinline__ unsigned rt32() { unsigned long long t; asm volatile("s_memrealtime %0\n\ts_waitcnt lgkmcnt(0)" : "=s"(t) :: "memory"); return (unsigned)t; }
__device__ __forceinline__ void wait_until(unsigned t) { while ((int)(rt32() - t) < 0) {} }
template <int X = 0> __device__ __forceinline__ LAS unsigned char* lad(unsigned v, unsigned u) {
  unsigned a;
  if (X == 0) asm volatile("v_add_u32 %0, %1, %2" : "=v"(a) : "s"(u), "v"(v));
  else asm volatile("v_xor_b32 %0, %3, %2\n\tv_add_u32 %0, %1, %0" : "=&v"(a) : "s"(u), "v"(v), "n"(X));
  return (LAS unsigned char*)(size_t)a;
}
__device__ __forceinline__ bf16x4 trread(LAS unsigned char* p) { return __builtin_amdgcn_ds_read_tr16_b64_v4bf16((LAS bf16x4*)p); }
__device__ __forceinline__ bf16x8 cat8(bf16x4 a, bf16x4 b) { return __builtin_shufflevector(a, b, 0, 1, 2, 3, 4, 5, 6, 7); }
__device__ __forceinline__ bf16x16 cat16(bf16x8 a, bf16x8 b) { return __builtin_shufflevector(a, b, 0, 1, 2, 3, 4, 5, 6, 7, 8, 9, 10, 11, 12, 13, 14, 15); }
__device__ __forceinline__ f32x4 mfma32(bf16x8 a, bf16x8 b, f32x4 c) { return __builtin_amdgcn_mfma_f32_16x16x32_bf16(a, b, c, 0, 0, 0); }
__device__ __forceinline__ f32x4 mfma16(u32x2 a, u32x2 b, f32x4 c) {
  return mfma32(__builtin_bit_cast(bf16x8, u32x4{a[0], a[1], 0u, 0u}), __builtin_bit_cast(bf16x8, u32x4{b[0], b[1], 0u, 0u}), c);
}
__device__ __forceinline__ f32x4 mfma16n(u32x2 a, u32x2 b, f32x4 c) { return __builtin_amdgcn_mfma_f32_16x16x16bf16_1k(__builtin_bit_cast(s16x4, a), __builtin_bit_cast(s16x4, b), c, 0, 0, 0); }
__device__ __forceinline__ f32x4 smf(bf16x8 a, bf16x16 b, f32x4 c, int idx) { return __builtin_amdgcn_smfmac_f32_16x16x64_bf16(a, b, c, idx, 0, 0); }

__device__ __forceinline__ void ln_row(float (&x)[16]) {
  float s = 0.f;
#pragma unroll
  for (int i = 0; i < 16; ++i) s += x[i];
  const float mean = wave_sum(s) * (1.0f / 1024.0f);
  float q = 0.f;
#pragma unroll
  for (int i = 0; i < 16; ++i) { const float d = x[i] - mean; q += d * d; }
  const float var = wave_sum(q) * (1.0f / 1024.0f);
  const float m = rbf(mean), isd = __builtin_amdgcn_rcpf(rbf(__builtin_amdgcn_sqrtf(rbf(rbf(var) + 1e-5f))));
#pragma unroll
  for (int i = 0; i < 16; ++i) x[i] = rbf(rbf(x[i] - m) * isd);
}
__device__ __forceinline__ void load_row16(const u16* rowp, int lane, float (&x)[16]) {
  const u32x4 a = *(const u32x4*)(rowp + lane * 8), b = *(const u32x4*)(rowp + 512 + lane * 8);
#pragma unroll
  for (int i = 0; i < 4; ++i) { x[2 * i] = lo2f(a[i]); x[2 * i + 1] = hi2f(a[i]); x[8 + 2 * i] = lo2f(b[i]); x[9 + 2 * i] = hi2f(b[i]); }
}
__device__ __forceinline__ void store_row16(u16* rowp, int lane, const float (&x)[16]) {
  u32x4 a, b;
#pragma unroll
  for (int i = 0; i < 4; ++i) { a[i] = pack2(x[2 * i], x[2 * i + 1]); b[i] = pack2(x[8 + 2 * i], x[9 + 2 * i]); }
  *(u32x4*)(rowp + lane * 8) = a; *(u32x4*)(rowp + 512 + lane * 8) = b;
}
__host__ __device__ __forceinline__ int mem_slot(int dl) { return ((dl >> 3) & 1) * 32 + (dl >> 4) * 8 + (dl & 4) + (dl & 1) * 2 + ((dl >> 1) & 1); }

#define WST(rs, wk, i)
struct MainP { const u16 *x, *mem0, *Wq, *Wk, *Wv, *Wo, *w1, *w2, *w3, *w4, *c, *wmod; u16* out; unsigned char* ws; int d1, d2, d3, d4; };
constexpr unsigned SPIN_CAP = 1u << 18;

__device__ __forceinline__ float gmax(float v) {
  auto t = __builtin_amdgcn_permlane16_swap(f2u(v), f2u(v), false, false);
  v = fmaxf(u2f(t[0]), u2f(t[1]));
  t = __builtin_amdgcn_permlane32_swap(f2u(v), f2u(v), false, false);
  return fmaxf(u2f(t[0]), u2f(t[1]));
}
__device__ __forceinline__ float gsum(float v) {
  auto t = __builtin_amdgcn_permlane16_swap(f2u(v), f2u(v), false, false);
  v = u2f(t[0]) + u2f(t[1]);
  t = __builtin_amdgcn_permlane32_swap(f2u(v), f2u(v), false, false);
  return u2f(t[0]) + u2f(t[1]);
}
__device__ __forceinline__ void publish(__amdgpu_buffer_rsrc_t rs, unsigned word, unsigned val) {
  WAIT_V(0); BAR();
  if (threadIdx.x == 0) stx4(rs, 0, word, val);
}

constexpr int QB0 = 20;
constexpr int P_S2 = 0, P_S8 = 32, P_S7 = 64, P_S4 = 96, P_S5 = 104, P_S3 = 112, P_PJ = 208, P_S1 = 336;
constexpr int NLN = 160;

enum { SG_WO = 0, SG_W1 = 1, SG_W2 = 2, SG_W3 = 3, SG_W4 = 4, SG_PJ = 5 };
constexpr int G_XCH = 131072, G_K = G_XCH + 16384, G_CTL = G_K + 8192;
__device__ __forceinline__ f32x16 mfma3232(bf16x8 a, bf16x8 b, f32x16 c) { return __builtin_amdgcn_mfma_f32_32x32x16_bf16(a, b, c, 0, 0, 0); }
__device__ __forceinline__ void widen32(const f32x16& a, u32x4 (&v)[2]) {
#pragma unroll
  for (int u = 0; u < 2; ++u) {
    const auto r0 = __builtin_amdgcn_permlane32_swap(pack2(a[8 * u], a[8 * u + 1]), pack2(a[8 * u + 4], a[8 * u + 5]), false, false);
    const auto r1 = __builtin_amdgcn_permlane32_swap(pack2(a[8 * u + 2], a[8 * u + 3]), pack2(a[8 * u + 6], a[8 * u + 7]), false, false);
    v[u] = u32x4{r0[0], r1[0], r0[1], r1[1]};
  }
}
__device__ __forceinline__ unsigned umin4v(u32x4 v) { return min(min(v[0], v[1]), min(v[2], v[3])); }
__device__ __forceinline__ float hsum(float v) { const auto t = __builtin_amdgcn_permlane32_swap(f2u(v), f2u(v), false, false); return u2f(t[0]) + u2f(t[1]); }

__device__ __forceinline__ void attend(__amdgpu_buffer_rsrc_t rs, int seg, int h, int t, int lane, unsigned& chs, unsigned& cap) {
  const int fr = lane & 15, g = lane >> 4;
  const unsigned l16 = lane * 16, ub = (unsigned)(seg * NH + h);
  wait_seen(rs, 0, F_CH + h * 128, (unsigned)max(seg, 1), chs, cap);
  wait_ge(rs, (lane & 1) && seg >= QB0 ? 96u * 128u : (unsigned)(seg % 3) * 4096u, F_ST + (unsigned)(P_PJ + h) * 128, (unsigned)seg + 1u, cap);
  f32x4 s0 = {0.f, 0.f, 0.f, 0.f}, s1 = s0, s2 = s0;
  u32x4 q[4], kmf[4];
  u32x2 vmf[8];
  const unsigned em = (unsigned)(fr >> 3) * 512 + (unsigned)g * 128 + (unsigned)(8 * ((fr >> 2) & 1) + (fr & 3)) * 8;
  auto mem_load = [&](unsigned off) {
#pragma unroll
    for (int ks = 0; ks < 4; ++ks) kmf[ks] = ldx16(rs, l16 + off, WS_KM + ub * 4096 + ks * 1024);
#pragma unroll
    for (int i = 0; i < 8; ++i) vmf[i] = ldx8(rs, em + off, X_VM + (unsigned)seg * RSTR + h * 4096 + (i >> 1) * 1024 + (i & 1) * 32);
  };
  const bool have = chs > (unsigned)seg;
  f32x4 o[8];
  float mt = -3.0e38f, lt = 0.f;
  const float scale = 0.08838834764831845f, L2E = 1.4426950408889634f;
  {
    u32x4 k0[4], k1[4], vv[8];
#pragma unroll
    for (int ks = 0; ks < 4; ++ks) {
      q[ks] = ldx16(rs, l16, WS_Q + ub * 8192 + t * 4096 + ks * 1024);
      k0[ks] = ldx16(rs, l16, WS_K + ub * 8192 + ks * 1024); k1[ks] = ldx16(rs, t ? l16 : 0xF0000000u, WS_K + ub * 8192 + 4096 + ks * 1024);
    }
    const unsigned ev = (unsigned)(fr >> 3) * 1024 + (unsigned)(g * 16 + 8 * ((fr >> 2) & 1) + (fr & 3)) * 16;
#pragma unroll
    for (int i = 0; i < 8; ++i) vv[i] = ldx16(rs, ev, WS_VT + ub * 8192 + (i >> 1) * 2048 + (i & 1) * 64);
#pragma unroll
    for (int ks = 0; ks < 4; ++ks) {
      s1 = mfma32(__builtin_bit_cast(bf16x8, k0[ks]), __builtin_bit_cast(bf16x8, q[ks]), s1); s2 = mfma32(__builtin_bit_cast(bf16x8, k1[ks]), __builtin_bit_cast(bf16x8, q[ks]), s2);
    }
    SCHED();
    mem_load(have ? 0u : 0xF0000000u);
    SCHED();
    float b[4], c[4];
#pragma unroll
    for (int r = 0; r < 4; ++r) {
      const int j = 4 * g + r;
      b[r] = (t == 1 || j <= fr) ? rbf(rbf(s1[r]) * scale) : -3.0e38f;
      c[r] = (t == 1 && j <= fr) ? rbf(rbf(s2[r]) * scale) : -3.0e38f;
      mt = fmaxf(mt, fmaxf(b[r], c[r]));
    }
    mt = gmax(mt);
#pragma unroll
    for (int r = 0; r < 4; ++r) { b[r] = __builtin_amdgcn_exp2f((b[r] - mt) * L2E); c[r] = __builtin_amdgcn_exp2f((c[r] - mt) * L2E); lt += b[r] + c[r]; }
    const u32x4 pt = {pack2(b[0], b[1]), pack2(b[2], b[3]), pack2(c[0], c[1]), pack2(c[2], c[3])};
#pragma unroll
    for (int i = 0; i < 8; ++i) o[i] = mfma32(__builtin_bit_cast(bf16x8, vv[i]), __builtin_bit_cast(bf16x8, pt), f32x4{0.f, 0.f, 0.f, 0.f});
    lt = gsum(lt);
  }
  asm volatile("" : "+v"(o[0]), "+v"(o[1]), "+v"(o[2]), "+v"(o[3]), "+v"(o[4]), "+v"(o[5]), "+v"(o[6]), "+v"(o[7]), "+v"(lt));
  if (!have) { wait_seen(rs, 0, F_CH + h * 128, (unsigned)seg + 1u, chs, cap); mem_load(0u); }
#pragma unroll
  for (int ks = 0; ks < 4; ++ks) s0 = mfma32(__builtin_bit_cast(bf16x8, kmf[ks]), __builtin_bit_cast(bf16x8, q[ks]), s0);
  float a[4];
#pragma unroll
  for (int r = 0; r < 4; ++r) a[r] = rbf(rbf(s0[r]) * scale);
  const float mx = fmaxf(mt, gmax(fmaxf(fmaxf(a[0], a[1]), fmaxf(a[2], a[3]))));
#pragma unroll
  for (int r = 0; r < 4; ++r) a[r] = __builtin_amdgcn_exp2f((a[r] - mx) * L2E);
  const u32x2 pm = {pack2(a[0], a[1]), pack2(a[2], a[3])};
  const float al = __builtin_amdgcn_exp2f((mt - mx) * L2E);
  const float inv = __builtin_amdgcn_rcpf(fmaf(al, lt, 0.5f * gsum((a[0] + a[1]) + (a[2] + a[3])))), ai = al * inv;
#pragma unroll
  for (int u = 0; u < 4; ++u) {
    const f32x4 x = o[2 * u] * ai + mfma16n(vmf[2 * u], pm, f32x4{0.f, 0.f, 0.f, 0.f}) * inv, y = o[2 * u + 1] * ai + mfma16n(vmf[2 * u + 1], pm, f32x4{0.f, 0.f, 0.f, 0.f}) * inv;
    stx16(rs, l16, WS_O + (unsigned)seg * 262144 + (unsigned)((h * 4 + u) * 2 + t) * 1024, u32x4{pack2(x[0], x[1]), pack2(x[2], x[3]), pack2(y[0], y[1]), pack2(y[2], y[3])});
  }
  WAIT_V(0);
  if (lane == 0) stx4(rs, 0, F_ST + (unsigned)(P_S1 + (seg & 3) * 64 + h * 2 + t) * 128, (unsigned)seg + 1u);
}

template <int SG>
__device__ __forceinline__ void stage_gemm(LAS unsigned char* lds, __amdgpu_buffer_rsrc_t rs, const u16* Wg, int wk, int kq, int cb, int pid, unsigned& cap,
                                           unsigned dst = 0, int bstr = 1, int nb = 0, const u16* side = nullptr, int srows = 0, int sld = 0, const u16* side3 = nullptr) {
  constexpr bool PROJ = SG == SG_PJ, ROWM = (SG == SG_W1 || PROJ);
  constexpr int KS = (SG == SG_W4) ? 28 : 32;
  constexpr int LDW = PROJ ? HD : (SG == SG_W3) ? FF : D_;
  constexpr int WROWS = (SG == SG_WO) ? HD : (SG == SG_W4 ? FF : D_);
  const int NBLK = PROJ ? nb : (SG == SG_WO || SG == SG_W1) ? 64 : 65;
  constexpr unsigned ABASE = PROJ ? WS_H : SG == SG_WO ? WS_O : SG == SG_W1 ? WS_G : SG == SG_W2 ? WS_H1S : SG == SG_W3 ? WS_X2 : WS_U;
  constexpr unsigned ASTR = SG == SG_WO ? 262144u : SG == SG_W4 ? 229376u : 65536u;
  constexpr int NPC = KS / 4;
  const unsigned myword = F_ST + (unsigned)pid * 128u;
  int tid = threadIdx.x; asm volatile("" : "+v"(tid));
  const int lane = tid & 63, w = __builtin_amdgcn_readfirstlane(tid >> 6), row = lane & 31, hf = lane >> 5;
  const int kh = w >> 2, wl = w & 3;
  const int koff = PROJ ? 0 : kq;
  const unsigned lbase = (unsigned)(size_t)lds, l16 = lane * 16;
  const int col0 = cb * 128 + wl * 32;
  bf16x8 Wf[KS];
  {
    const __amdgpu_buffer_rsrc_t rsW = mk_rs(Wg, (unsigned)WROWS * LDW * 2u);
    const int ub = lane >> 3, tu = 4 * hf + ((lane & 15) >> 2);
    const unsigned vB = (unsigned)(((ub & 3) + 8 * (ub >> 2)) * LDW + 16 * (((lane >> 1) & 3) ^ (ub >> 1)) + 8 * (lane & 1)) * 2u;
    const unsigned sB = (unsigned)((koff * KS * 32 + 32 * (w >> 2) + 16 * ((w >> 1) & 1) + 4 * (w & 1)) * LDW + cb * 128) * 2u;
    const int boff = kh * 65536 + (wl >> 1) * 8192 + tu * 128 + (((tu >> 1) ^ (2 * (wl & 1) + ((lane >> 4) & 1))) << 5) + (lane & 3) * 8;
#pragma unroll
    for (int ps = 0; ps < 2; ++ps) {
      BAR();
#pragma unroll
      for (int hh = 0; hh < 2; ++hh)
#pragma unroll
        for (int i = 0; i < 4; ++i)
          if (4 * ps + i < NPC && !(PROJ && ps == 0)) {
            const unsigned so = sB + (unsigned)((hh * NPC + 4 * ps + i) * 64 * LDW) * 2u, d = lbase + hh * 65536 + i * 16384 + w * 1024;
            dma16n<(NTM & 16) != 0>(rsW, vB, so, d); dma16n<(NTM & 16) != 0>(rsW, vB, so + 128, d + 8192);
          }
      WAIT_V(0); BAR();
#pragma unroll
      for (int i = 0; i < 4; ++i)
        if (4 * ps + i < NPC) {
#pragma unroll
          for (int k = 0; k < 4; ++k) { LAS unsigned char* q = lds + (boff + i * 16384 + k * 2048); Wf[(4 * ps + i) * 4 + k] = cat8(trread(q), trread(q + 1024)); }
        }
      WAIT_L0();
    }
    if (SG == SG_W1) {
      const u32x4 z = {0u, 0u, 0u, 0u};
      stx16(rs, (unsigned)tid * 16, WS_H1S + cb * 8192, z); stx16(rs, (unsigned)tid * 16, WS_H1S + 64 * 65536 + cb * 8192, z);
      WAIT_V(0);
    }
    if (tid < 8) *(LAS unsigned*)(lds + G_CTL + tid * 4) = 0u;
    LBAR();
  }
  const f32x16 zero16 = {0.f, 0.f, 0.f, 0.f, 0.f, 0.f, 0.f, 0.f, 0.f, 0.f, 0.f, 0.f, 0.f, 0.f, 0.f, 0.f};
  auto put16 = [&](LAS unsigned char* p, const f32x16& a) {
#pragma unroll
    for (int q = 0; q < 4; ++q) *(LAS f32x4*)(p + q * 1024) = f32x4{a[4 * q], a[4 * q + 1], a[4 * q + 2], a[4 * q + 3]};
  };
  if (SG == SG_W3) {
    const u32x4 one4 = {0x3F803F80u, 0x3F803F80u, 0x3F803F80u, 0x3F803F80u};
    f32x16 cs = zero16;
#pragma unroll
    for (int k = 0; k < KS; ++k) cs = mfma3232(Wf[k], __builtin_bit_cast(bf16x8, one4), cs);
    if (kh == 0) put16(lds + wl * 4096 + l16, cs);
    LBAR();
    if (kh == 1 && row == 0) {
#pragma unroll
      for (int q = 0; q < 4; ++q)
        *(LAS f32x4*)(lds + G_K + wl * 128 + hf * 64 + q * 16) = *(const LAS f32x4*)(lds + wl * 4096 + q * 1024 + l16) + f32x4{cs[4 * q], cs[4 * q + 1], cs[4 * q + 2], cs[4 * q + 3]};
    }
    LBAR();
  }
  auto blk = [&](int i) -> int { return PROJ ? bstr * i + kq : i; };
  const unsigned aoff = ROWM ? (unsigned)(row * 128 + ((hf ^ ((row >> 1) & 7)) << 4)) : (unsigned)((row >> 4) * 1024 + hf * 256 + (row & 15) * 16);
  const unsigned vA = ROWM ? (unsigned)((lane >> 3) * 2048 + (((lane & 7) ^ ((4 * wl + (lane >> 4)) & 7)) << 4)) : l16;
  auto piece = [&](int hb, int q, bool on) {
    const int i = hb >> 1, hh = hb & 1;
    const unsigned so = ABASE + (unsigned)blk(i) * ASTR + (unsigned)koff * (KS * 2048) + (ROWM ? wl * 16384 + hh * 1024 : (hh * KS + wl) * 1024);
    if (on) dma16x(rs, vA, so + q * (ROWM ? 128 : 4096), lbase + (i & 1) * 65536 + hh * 32768 + wl * 1024 + q * 4096);
  };
  auto fetch = [&](int hb) {
#pragma unroll
    for (int q = 0; q < NPC; ++q) piece(hb, q, true);
  };
  constexpr bool WEAVE = (SG == SG_W1 || SG == SG_W2 || SG == SG_W3);
  auto products = [&](int i, f32x16& acc, auto&& first, auto&& early, auto&& handover, bool soon) {
    const unsigned ub = lbase + (i & 1) * 65536 + kh * 32768;
    LAS unsigned char* ab[4];
    ab[0] = lad(aoff, ub);
    if (ROWM) { ab[1] = lad<32>(aoff, ub); ab[2] = lad<64>(aoff, ub); ab[3] = lad<96>(aoff, ub); }
    constexpr int PD = 10;
    bf16x8 af[PD + 1];
    auto rd = [&](int k) { af[k % (PD + 1)] = ROWM ? *(const LAS bf16x8*)(ab[k & 3] + (k >> 2) * 4096) : *(const LAS bf16x8*)(ab[0] + (k >> 1) * 2048 + (k & 1) * 512); };
#pragma unroll
    for (int k = 0; k < PD; ++k) rd(k);
    SCHED();
#pragma unroll
    for (int k = 0; k < KS; ++k) {
      if (k + PD < KS) rd(k + PD);
      acc = mfma3232(Wf[k], af[k % (PD + 1)], k ? acc : zero16);
      SCHED();
      first(k);
      SCHED();
      if (k == KS - 10) { early(); SCHED(); }
      if (k == 9) { if (soon) handover(); SCHED(); }
      if (k == KS - 5) { if (!soon) handover(); SCHED(); }
    }
    asm volatile("" : "+v"(acc));
  };
  WST(rs, wk, PROJ ? 2 : 10);
#define SEG(x)
  auto announce = [&](int i) {
    if (lane == 0) {
      const unsigned c = __hip_atomic_fetch_add((LAS unsigned*)(lds + G_CTL), 1u, __ATOMIC_RELAXED, __HIP_MEMORY_SCOPE_WORKGROUP);
      if ((c & 3u) == 3u) stx4(rs, 0, myword, (unsigned)blk(i) + 1u);
    }
  };
  if (kh == 0) {
    auto pvo = [&](int i) -> unsigned {
      const int b = blk(i);
      if (PROJ) return F_LN + (unsigned)((b % (NLN / 4)) * 4 + (lane & 3)) * 128u + (lane & 12);
      if (SG == SG_WO) return F_ST + (unsigned)(P_S1 + (b & 3) * 64 + kq * 16 + (lane & 15)) * 128u;
      if (SG == SG_W1) return F_ST + (unsigned)(P_S3 + (b % 3) * 32 + (lane & 31)) * 128u;
      if (SG == SG_W2) return F_ST + (unsigned)(P_S4 + (lane & 7)) * 128u;
      if (SG == SG_W3) return F_ST + (unsigned)(P_S5 + (lane & 7)) * 128u;
      return F_ST + (unsigned)(P_S7 + kq * 7 + min(lane & 7, 6)) * 128u;
    };
    auto need = [&](int i) -> unsigned { return PROJ ? 1u : (SG == SG_W2 && i == 64) ? 64u : (unsigned)i + 1u; };
    unsigned nl = 0;
    int R = 0, Rp = 0;
    for (int i = 0; i < NBLK; ++i) {
      if (R <= i) { wait_ge(rs, pvo(i), 0, need(i), cap); SEG(6); fetch(2 * i); fetch(2 * i + 1); SEG(7); }
      const unsigned pk = w == 0 ? (lane < 32 ? pvo(i + 1) : pvo(i + 2)) : 0xF0000000u;
      if (Rp <= i) { if (R <= i + 1) wait_v<NPC>(); else wait_v<2 * NPC>(); }
      if (R >= i + 2 || Rp > i) BAR();
      else {
        nl += 4;
        if (lane == 0) (void)__hip_atomic_fetch_add((LAS unsigned*)(lds + G_CTL + 8), 1u, __ATOMIC_RELAXED, __HIP_MEMORY_SCOPE_WORKGROUP);
        while (__builtin_amdgcn_readfirstlane(*(const volatile LAS unsigned*)(lds + G_CTL + 8)) < nl) {}
        asm volatile("" ::: "memory");
      }
      SEG(1);
      f32x16 acc;
      unsigned peek;
      asm volatile("buffer_load_dword %0, %1, %2, 0 offen sc1" : "=v"(peek) : "v"(pk), "s"(rs) : "memory");
      u32x4 st[8];
      float mean = 0.f, q2 = 0.f;
      if (SG == SG_W3) {
#pragma unroll
        for (int q = 0; q < 8; ++q) st[q] = ldx16(rs, l16, WS_ST5 + (unsigned)i * 8192 + q * 1024);
      }
      products(i, acc, [&](int k) {
        if (SG == SG_W3) {
          if (k >= 18 && k < 26) { mean += u2f(st[k - 18][0]) + u2f(st[k - 18][2]); q2 += u2f(st[k - 18][1]) + u2f(st[k - 18][3]); asm volatile("" : "+v"(mean), "+v"(q2)); }
          if (k == 26) { mean = hsum(mean) * (1.0f / 1024.0f); q2 = hsum(q2); asm volatile("" : "+v"(mean), "+v"(q2)); }
          if (k == 28) { q2 = rbf(rbf(fmaxf(fmaf(-mean, mean, q2 * (1.0f / 1024.0f)), 0.f)) + 1e-5f); asm volatile("" : "+v"(q2)); }
          if (k == 29) { q2 = __builtin_amdgcn_rcpf(rbf(__builtin_amdgcn_sqrtf(q2))); mean = rbf(mean); asm volatile("" : "+v"(q2), "+v"(mean)); }
        }
      }, []() {}, [&]() {
        SEG(4);
        WAIT_V(0);
        SEG(0);
        asm volatile("" : "+v"(peek));
        if (w == 0) {
          const unsigned long long ok = __ballot(peek >= (lane < 32 ? need(i + 1) : need(i + 2)));
          const bool r1 = (R >= i + 2 || (unsigned)ok == 0xFFFFFFFFu) && i + 1 < NBLK, r2 = r1 && (unsigned)(ok >> 32) == 0xFFFFFFFFu && i + 2 < NBLK;
          if (lane == 0) *(volatile LAS unsigned*)(lds + G_CTL + 4) = (unsigned)(i + 1 + (r1 ? 1 : 0) + (r2 ? 1 : 0));
        }
        LBAR();
        if (PROJ && i < 16) {
          if (side3) dma16(mk_rs(side3, 0x7FFFFFFFu), (unsigned)((lane >> 2) * 6144 + (lane & 3) * 8) * 2u, (unsigned)((64 * i + 16 * wl) * 6144) * 2u, lbase + G_K);
          if (64 * i < srows) {
            const __amdgpu_buffer_rsrc_t rsN = mk_rs(side, 0x7FFFFFFFu);
#pragma unroll
            for (int q = 0; q < 4; ++q) dma16(rsN, (unsigned)((lane >> 4) * sld + (lane & 15) * 8) * 2u, (unsigned)((64 * i + 16 * wl + 4 * q) * sld) * 2u, lbase + G_K + wl * 2048);
          }
        }
      }, Rp <= i);
      SEG(2);
      const unsigned rv = *(const volatile LAS unsigned*)(lds + G_CTL + 4);
      if (SG == SG_W3) {
        const float m = mean;
        *(LAS float*)(lds + G_K + 1024 + wl * 256 + lane * 4) = q2;
#pragma unroll
        for (int q = 0; q < 4; ++q) {
          const f32x4 cs = *(const LAS f32x4*)(lds + G_K + wl * 128 + hf * 64 + q * 16);
#pragma unroll
          for (int e = 0; e < 4; ++e) acc[4 * q + e] = fmaf(-m, cs[e], acc[4 * q + e]);
        }
      }
      put16(lad(l16, lbase + G_XCH + wl * 4096), acc);
      asm volatile("" ::: "memory");
      if (lane == 0) *(volatile LAS unsigned*)(lds + G_CTL + 16 + wl * 4) = (unsigned)i + 1u;
      const bool soon = Rp <= i;
      Rp = R; R = __builtin_amdgcn_readfirstlane(rv);
      SEG(3);
      if (R >= i + 2 && Rp < i + 2) { fetch(2 * i + 2); fetch(2 * i + 3); }
      if (R >= i + 3) {
        if (soon) { while (__builtin_amdgcn_readfirstlane(umin4v(*(const volatile LAS u32x4*)(lds + G_CTL + 16))) <= (unsigned)i) {} asm volatile("" ::: "memory"); }
        fetch(2 * i + 4);
      }
      SEG(5);
    }
  } else {
    bool pending = false;
    int Rp = 0;
    for (int i = 0; i < NBLK; ++i) {
      const int b = blk(i);
      BAR();
      SEG(0);
      const unsigned rv = *(const volatile LAS unsigned*)(lds + G_CTL + 4);
      u32x4 res[2];
      float isd;
      if (SG == SG_W2) {
#pragma unroll
        for (int u = 0; u < 2; ++u) {
          const int t = 32 * b - 2 + 16 * u + (lane >> 2);
          dma16x(rs, (t >= 0 && t < S_) ? (unsigned)(t * D_ + col0 + ((lane ^ (lane >> 3)) & 3) * 8) * 2u : 0xF0000000u, WS_H2, lbase + G_K + wl * 2048 + u * 1024);
        }
      }
      f32x16 acc;
      int R = 0;
      LAS unsigned char* xr = lad(l16, lbase + G_XCH + wl * 4096);
      f32x4 xa[4];
      unsigned xf;
      auto take = [&]() {
        asm volatile("" ::: "memory");
        xf = *(const volatile LAS unsigned*)(lds + G_CTL + 16 + wl * 4);
        asm volatile("" ::: "memory");
#pragma unroll
        for (int q = 0; q < 4; ++q) xa[q] = *(const LAS f32x4*)(xr + q * 1024);
        if (SG == SG_W3) isd = *(const LAS float*)(lds + G_K + 1024 + wl * 256 + lane * 4);
        asm volatile("" ::: "memory");
      };
      products(i, acc, [&](int k) {
      }, [&]() {
        take();
      }, [&]() {
        SEG(5);
        R = __builtin_amdgcn_readfirstlane(rv);
        if (R >= i + 3 || Rp >= i + 2) {
          WAIT_V(0);
          LBAR();
          if (pending && tid == 256) stx4(rs, 0, myword, (unsigned)blk(i - 1) + 1u);
        }
      }, false);
      const bool more = R >= i + 3 || Rp >= i + 2;
      Rp = R;
      SEG(1);
      while (__builtin_amdgcn_readfirstlane(xf) <= (unsigned)i) take();
#pragma unroll
      for (int q = 0; q < 16; ++q) { acc[q] += xa[q >> 2][q & 3]; if (SG == SG_W3) acc[q] *= isd; }
      SEG(2);
      const unsigned fo = (unsigned)((col0 >> 5) * 2048 + hf * 256), ro = (unsigned)((row >> 4) * 1024 + (row & 15) * 16);
      u32x4 v[2];
      if (SG == SG_W1 || SG == SG_W3) {
#pragma unroll
        for (int q = 0; q < 16; q += 2) {
          float g0 = gelu_tanh(acc[q]), g1 = gelu_tanh(acc[q + 1]);
          asm volatile("" : "+v"(g0), "+v"(g1));
          acc[q] = g0; acc[q + 1] = g1;
          piece(2 * i + 5, q >> 1, R >= i + 3 && i >= 2);
        }
      }
      widen32(acc, v);
      if (SG == SG_WO || SG == SG_W4) {
        LAS unsigned char* tw = lds + G_XCH + wl * 4096 + row * 64;
        *(LAS u32x4*)(tw + ((hf ^ (row >> 1)) & 3) * 16) = v[0]; *(LAS u32x4*)(tw + (((2 + hf) ^ (row >> 1)) & 3) * 16) = v[1];
        asm volatile("" ::: "memory");
        LAS unsigned char* tr = lds + G_XCH + wl * 4096 + (lane >> 2) * 64 + ((lane ^ (lane >> 3)) & 3) * 16;
        const unsigned so = (SG == SG_WO ? WS_P2 : WS_P8) + (unsigned)(b * 4 + kq) * 65536u, vo = (unsigned)((lane >> 2) * D_ + col0 + (lane & 3) * 8) * 2u;
        stx16(rs, vo, so, *(const LAS u32x4*)(tr)); stx16(rs, vo, so + 32768, *(const LAS u32x4*)(tr + 1024));
      } else if (PROJ) {
        const unsigned so = dst + (unsigned)(b * NH + cb) * 8192 + wl * 1024, vo = (unsigned)((row >> 4) * 4096 + hf * 256 + (row & 15) * 16);
        stx16(rs, vo, so, v[0]); stx16(rs, vo, so + 512, v[1]);
      } else if (SG == SG_W1) {
        const int grp = cb >> 1, sh = (grp & 1) ? -(1 + (grp >> 1)) : (1 + (grp >> 1));
        const int rho = 32 * b + row + sh + 2;
        const unsigned vo = (rho >= 2 && rho < S_ + 2) ? (unsigned)(rho >> 5) * 65536u + fo + (unsigned)((rho >> 4) & 1) * 1024u + (unsigned)(rho & 15) * 16u : 0xF0000000u;
        stx16(rs, vo, WS_H1S, v[0]); stx16(rs, vo, WS_H1S + 512, v[1]);
      } else if (SG == SG_W2) {
        if (!more) WAIT_V(0);
        res[0] = *(const LAS u32x4*)(lds + G_K + wl * 2048 + row * 64 + ((hf ^ (row >> 1)) & 3) * 16);
        res[1] = *(const LAS u32x4*)(lds + G_K + wl * 2048 + row * 64 + (((2 + hf) ^ (row >> 1)) & 3) * 16);
        float zs = 0.f, q2 = 0.f;
#pragma unroll
        for (int q = 0; q < 8; ++q) {
          const unsigned x = pack2(lo2f(res[q >> 2][q & 3]) + lo2f(v[q >> 2][q & 3]), hi2f(res[q >> 2][q & 3]) + hi2f(v[q >> 2][q & 3]));
          v[q >> 2][q & 3] = x; zs += lo2f(x) + hi2f(x); q2 = fmaf(lo2f(x), lo2f(x), fmaf(hi2f(x), hi2f(x), q2));
          asm volatile("" : "+v"(zs), "+v"(q2));
          piece(2 * i + 5, q, R >= i + 3 && i >= 2);
        }
        stx16(rs, fo + ro, WS_X2 + (unsigned)b * 65536u, v[0]); stx16(rs, fo + ro, WS_X2 + (unsigned)b * 65536u + 512, v[1]);
        const float mw = hsum(zs);
        q2 = hsum(q2);
        if (hf == 0) stx8(rs, (unsigned)((cb * 2 + (wl >> 1)) * 512 + row * 16 + (wl & 1) * 8), WS_ST5 + (unsigned)b * 8192, u32x2{f2u(mw), f2u(q2)});
      } else {
        stx16(rs, fo + ro, WS_U + (unsigned)b * 229376u, v[0]); stx16(rs, fo + ro, WS_U + (unsigned)b * 229376u + 512, v[1]);
      }
      if (PROJ && bstr == 3) {
#pragma unroll
        for (int q = 0; q < 2; ++q) {
          const int pc = (wk * 4 + wl) * 2 + q;
          if (pc < 672 && 3 * i + ZA + pc / 224 <= NSEG) stx16(rs, l16, X_REC + (unsigned)(3 * i + ZA) * RSTR + (unsigned)pc * 1024, u32x4{0u, 0u, 0u, 0u});
        }
      }
      SEG(3);
      pending = R >= i + 3 && i >= 2;
      if (!pending) { WAIT_V(0); announce(i); }
      if (R >= i + 3 && !(WEAVE && i >= 2)) fetch(2 * i + 5);
      if (!PROJ) WST(rs, wk, 16 + b);
      SEG(4);
    }
  }
  LBAR();
}

__device__ __forceinline__ void wait32(__amdgpu_buffer_rsrc_t rs, int first, int mine, int lane, unsigned need, unsigned& cap) {
  wait_ge(rs, 0, F_ST + (unsigned)(first + mine) * 128u, need, cap);
  wait_ge(rs, (unsigned)(lane & 31) * 128u, F_ST + (unsigned)first * 128u, need, cap);
}
__device__ __forceinline__ void sum_partials(__amdgpu_buffer_rsrc_t rs, unsigned l16, unsigned so, float (&a)[16]) {
  u32x4 pp[4][2];
#pragma unroll
  for (int k = 0; k < 4; ++k) { pp[k][0] = ldx16(rs, l16, so + k * 65536u); pp[k][1] = ldx16(rs, l16, so + k * 65536u + 1024); }
#pragma unroll
  for (int i = 0; i < 8; ++i) {
    f32x2 t = {0.f, 0.f};
#pragma unroll
    for (int k = 0; k < 4; ++k) t += f32x2{lo2f(pp[k][i >> 2][i & 3]), hi2f(pp[k][i >> 2][i & 3])};
    a[2 * i] = rbf(t[0]); a[2 * i + 1] = rbf(t[1]);
  }
}
__device__ __forceinline__ void put_row(__amdgpu_buffer_rsrc_t rs, unsigned l16, unsigned so, const float (&v)[16]) {
  stx16(rs, l16, so, u32x4{pack2(v[0], v[1]), pack2(v[2], v[3]), pack2(v[4], v[5]), pack2(v[6], v[7])});
  stx16(rs, l16, so + 1024, u32x4{pack2(v[8], v[9]), pack2(v[10], v[11]), pack2(v[12], v[13]), pack2(v[14], v[15])});
}
__device__ __forceinline__ void load_mod(__amdgpu_buffer_rsrc_t rs, int part, int lane, float (&v)[16], unsigned& cap) {
  for (;;) {
    asm volatile("" ::: "memory");
    u32x4 t[4];
#pragma unroll
    for (int i = 0; i < 4; ++i) t[i] = ldx16(rs, (unsigned)lane * 32, X_MOD + part * 4096 + (i >> 1) * 2048 + (i & 1) * 16);
    unsigned mn = 0xFFFFFFFFu;
#pragma unroll
    for (int i = 0; i < 16; ++i) { mn = min(mn, t[i >> 2][i & 3]); v[i] = hi2f(t[i >> 2][i & 3]); }
    if (__all(mn != 0u) || cap == 0u) break;
    cap = __builtin_amdgcn_readfirstlane(cap - 1u);
    __builtin_amdgcn_s_sleep(4);
  }
  cap = __builtin_amdgcn_readfirstlane(cap);
}
__device__ __forceinline__ void mod_part(__amdgpu_buffer_rsrc_t rs, const u16* wmod, const u16* cv, LAS float* red, int grp) {
  const int tid = threadIdx.x, kg = tid >> 2, c8 = tid & 3;
  u32x4 wv[8]; u16 cc[8];
#pragma unroll
  for (int i = 0; i < 8; ++i) cc[i] = cv[kg + 128 * i];
#pragma unroll
  for (int i = 0; i < 8; ++i) { const u32x4* q_ = (const u32x4*)(wmod + (size_t)(kg + 128 * i) * 6144 + grp * 32 + c8 * 8); wv[i] = (NTM & 8) ? __builtin_nontemporal_load(q_) : *q_; }
  SCHED();
  float acc[8] = {0.f, 0.f, 0.f, 0.f, 0.f, 0.f, 0.f, 0.f};
#pragma unroll
  for (int i = 0; i < 8; ++i) {
    const float v = bf2f(cc[i]), sc = rbf(v * __builtin_amdgcn_rcpf(1.0f + __builtin_amdgcn_exp2f(-1.4426950408889634f * v)));
#pragma unroll
    for (int j = 0; j < 4; ++j) { acc[2 * j] += sc * lo2f(wv[i][j]); acc[2 * j + 1] += sc * hi2f(wv[i][j]); }
  }
#pragma unroll
  for (int j = 0; j < 8; ++j) acc[j] = gsum(dpp_add<0x128>(dpp_add<0x124>(acc[j])));
  if ((tid & 63) < 4) {
    *(LAS f32x4*)(red + (tid >> 6) * 32 + c8 * 8) = f32x4{acc[0], acc[1], acc[2], acc[3]}; *(LAS f32x4*)(red + (tid >> 6) * 32 + c8 * 8 + 4) = f32x4{acc[4], acc[5], acc[6], acc[7]};
  }
  LBAR();
  if (tid < 32) {
    float u = 0.f;
#pragma unroll
    for (int i = 0; i < 8; ++i) u += red[i * 32 + tid];
    stx4(rs, (unsigned)tid * 4, X_MOD + (unsigned)grp * 128, f2u(rbf(u)) | 1u);
  }
}

__device__ __forceinline__ void stage_mid(__amdgpu_buffer_rsrc_t rs, const u16* x, int wk, int cu, unsigned& cap) {
  const int tid = threadIdx.x, lane = tid & 63, w = __builtin_amdgcn_readfirstlane(tid >> 6);
  const int id = w * 12 + cu, grp = id >> 5, rl = id & 31;
  const unsigned l16 = lane * 16;
  float ga[16], sc[16], sh[16];
  load_mod(rs, 2, lane, ga, cap); load_mod(rs, 4, lane, sc, cap); load_mod(rs, 3, lane, sh, cap);
#pragma unroll
  for (int i = 0; i < 16; ++i) sc[i] = rbf(1.0f + sc[i]);
  for (int seg = grp; seg < NSEG; seg += 3) {
    const int row = seg * 32 + rl;
    float v[16], a[16];
    load_row16(x + (size_t)row * D_, lane, v);
    wait32(rs, P_S2, rl, lane, (unsigned)seg + 1u, cap);
    sum_partials(rs, l16, WS_P2 + (unsigned)seg * 262144u + (unsigned)rl * 2048, a);
#pragma unroll
    for (int i = 0; i < 16; ++i) v[i] = rbf(v[i] + rbf(ga[i] * a[i]));
    put_row(rs, l16, WS_X1 + (unsigned)row * 2048, v);
    ln_row(v);
#pragma unroll
    for (int i = 0; i < 16; ++i) v[i] = rbf(rbf(v[i] * sc[i]) + sh[i]);
    put_row(rs, l16, WS_H2 + (unsigned)row * 2048, v);
    ln_row(v);
    put_row(rs, l16, WS_G + (unsigned)row * 2048, v);
    WAIT_V(0);
    if (lane == 0) stx4(rs, 0, F_ST + (unsigned)(P_S3 + grp * 32 + rl) * 128, (unsigned)seg + 1u);
    WST(rs, wk, 16 + seg);
  }
}

__device__ __forceinline__ void stage_out(__amdgpu_buffer_rsrc_t rs, u16* out, int wk, int cu, unsigned& cap) {
  const int tid = threadIdx.x, lane = tid & 63, w = __builtin_amdgcn_readfirstlane(tid >> 6);
  const int grp = w >> 2, rl = cu * 4 + (w & 3);
  const unsigned l16 = lane * 16;
  float gb[16];
  load_mod(rs, 5, lane, gb, cap);
  for (int b = grp; b < 65; b += 2) {
    const int t = 32 * b - 2 + rl;
    if (t < 0 || t >= S_) continue;
    wait_ge(rs, (unsigned)(lane & 7) * 128u, F_ST + P_S5 * 128u, (unsigned)b + 1u, cap);
    const unsigned xo = (unsigned)((lane >> 2) * 2048 + (lane & 3) * 256), xs = WS_X2 + (unsigned)b * 65536u + (unsigned)((rl >> 4) * 1024 + (rl & 15) * 16);
    const u32x4 x2a = ldx16(rs, xo, xs), x2b = ldx16(rs, xo, xs + 32768);
    const u32x4 x1a = ldx16(rs, l16, WS_X1 + (unsigned)t * 2048), x1b = ldx16(rs, l16, WS_X1 + (unsigned)t * 2048 + 1024);
    wait32(rs, P_S8, rl, lane, (unsigned)b + 1u, cap);
    float a[16];
    sum_partials(rs, l16, WS_P8 + (unsigned)b * 262144u + (unsigned)rl * 2048, a);
#pragma unroll
    for (int i = 0; i < 4; ++i) {
      a[2 * i] = lo2f(x1a[i]) + rbf(gb[2 * i] * rbf(lo2f(x2a[i]) + a[2 * i])); a[2 * i + 1] = hi2f(x1a[i]) + rbf(gb[2 * i + 1] * rbf(hi2f(x2a[i]) + a[2 * i + 1]));
      a[8 + 2 * i] = lo2f(x1b[i]) + rbf(gb[8 + 2 * i] * rbf(lo2f(x2b[i]) + a[8 + 2 * i])); a[9 + 2 * i] = hi2f(x1b[i]) + rbf(gb[9 + 2 * i] * rbf(hi2f(x2b[i]) + a[9 + 2 * i]));
    }
    store_row16(out + (size_t)t * D_, lane, a);
    WST(rs, wk, 16 + b);
  }
}

__device__ __forceinline__ void worker(const MainP& p, LAS unsigned char* lds, int xw, int j) {
  const int tid = threadIdx.x, lane = tid & 63, w = __builtin_amdgcn_readfirstlane(tid >> 6);
  const __amdgpu_buffer_rsrc_t rs = mk_rs(p.ws, WS_END);
  unsigned cap = SPIN_CAP;
  const int wk = xw * 32 + j;
  WST(rs, wk, 0);
  {
    const int c = xw < 3 ? j * 3 + xw : 90 + j * 2 + xw + 3, r0 = c * 8 + 2 * (w & 3);
    const bool two = r0 + 8 * NLN < S_;
    float v[4][16];
    if (c >= NLN - 64) mod_part(rs, p.wmod, p.c, (LAS float*)(lds + G_XCH), c - (NLN - 64));
    WST(rs, wk, 5);
    if (w < 4) {
#pragma unroll
      for (int q = 0; q < 4; ++q) load_row16(p.x + (size_t)(r0 + (q & 1) + (two ? (q >> 1) * 8 * NLN : 0)) * D_, lane, v[q]);
    }
    if (w >= 4) {
      if (xw == 1 || xw == 2) {
        const __amdgpu_buffer_rsrc_t rsO = mk_rs(p.Wo, D_ * HD * 2);
#pragma unroll
        for (int q = 0; q < 32; ++q) dma16(rsO, (unsigned)lane * 16, (unsigned)((q * 64 + wk - 32) * 4 + (w & 3)) * 1024u, (unsigned)(size_t)lds + G_K + (w & 3) * 1024);
        wait_v<24>();
      }
      const __amdgpu_buffer_rsrc_t rsW = mk_rs(xw < 3 ? p.Wk : p.Wq, D_ * HD * 2);
      const int ub = lane >> 3;
      const unsigned vB = (unsigned)(((ub & 3) + 8 * (ub >> 2)) * HD + 16 * (((lane >> 1) & 3) ^ (ub >> 1)) + 8 * (lane & 1)) * 2u, lbase = (unsigned)(size_t)lds;
#pragma unroll
      for (int q = 0; q < 16; ++q) {
        const int wq = (w & 3) + 4 * (q >> 3);
        const unsigned so = (unsigned)((32 * (wq >> 2) + 16 * ((wq >> 1) & 1) + 4 * (wq & 1) + (((q >> 2) & 1) * 8 + (q & 3)) * 64) * HD + j * 128) * 2u;
        const unsigned d = lbase + ((q >> 2) & 1) * 65536 + (q & 3) * 16384 + wq * 1024;
        dma16n<(NTM & 16) != 0>(rsW, vB, so, d); dma16n<(NTM & 16) != 0>(rsW, vB, so + 128, d + 8192);
      }
    } else {
#pragma unroll
      for (int q = 0; q < 4; ++q) ln_row(v[q]);
      float sc[16], sh[16];
      load_mod(rs, 1, lane, sc, cap); load_mod(rs, 0, lane, sh, cap);
      WST(rs, wk, 6);
#pragma unroll
      for (int i = 0; i < 16; ++i) sc[i] = rbf(1.0f + sc[i]);
#pragma unroll
      for (int q = 0; q < 4; ++q) {
#pragma unroll
        for (int i = 0; i < 16; ++i) v[q][i] = rbf(rbf(v[q][i] * sc[i]) + sh[i]);
        if (q < 2 || two) put_row(rs, (unsigned)lane * 16, WS_H + (unsigned)(r0 + (q & 1) + (q >> 1) * 8 * NLN) * 2048, v[q]);
      }
      WAIT_V(0);
      if (lane == 0) stx4(rs, 0, F_LN + c * 128 + w * 4, 1u);
    }
    WST(rs, wk, 1);
  }
  {
    const u16* nw = xw == 0 ? p.Wo + (size_t)(j >> 3) * 1024 * D_ + (j & 7) * 128 : xw == 3 ? p.w4 + (size_t)(j >> 3) * 896 * D_ + (j & 7) * 128
                  : xw == 2 ? p.w3 + j * 128 : (j < 8 ? p.w1 : p.w2) + (j & 7) * 128;
    const int nrows = (xw == 4 || (xw == 1 && j >= 16) || (xw == 2 && j >= 28)) ? 0 : xw == 3 ? 896 : 1024;
    const int mg = xw < 3 ? 64 + wk : 160 + j;
    stage_gemm<SG_PJ>(lds, rs, xw < 3 ? p.Wk : p.Wq, wk, xw < 3 ? xw : xw == 3 ? QB0 : 0, j, P_PJ + min(xw, 3) * 32 + j + (xw == 4 ? 400 : 0), cap, xw < 3 ? WS_K : WS_Q, xw < 3 ? 3 : 1,
                      xw == 0 ? 22 : xw < 3 ? 21 : xw == 3 ? NSEG - QB0 : QB0, nw, nrows, xw == 2 ? FF : D_, xw == 3 ? nullptr : p.wmod + mg * 32);
    WST(rs, wk, 3);
  }
  typedef __attribute__((address_space(4))) const MainP* KArg;
  KArg kp = (KArg)__builtin_amdgcn_kernarg_segment_ptr();
  asm volatile("" : "+s"(kp));
  if (xw != 3) { const int mg = xw < 3 ? 64 + wk : 160 + j; mod_part(rs, kp->wmod, kp->c, (LAS float*)(lds + G_XCH), mg); }
  if (xw == 0) stage_gemm<SG_WO>(lds, rs, kp->Wo, wk, j >> 3, j & 7, P_S2 + j, cap);
  else if (xw == 3) stage_gemm<SG_W4>(lds, rs, kp->w4, wk, j >> 3, j & 7, P_S8 + j, cap);
  else if (xw == 4) {
    unsigned chs = 0u;
    for (int sg = w >> 1; sg < NSEG; sg += 4) { attend(rs, sg, j, w & 1, lane, chs, cap); WST(rs, wk, 16 + sg); }
  }
  else if (j >= 28) stage_out(rs, kp->out, wk, (xw - 1) * 4 + j - 28, cap);
  else if (xw == 2) stage_gemm<SG_W3>(lds, rs, kp->w3, wk, 0, j, P_S7 + j, cap);
  else if (j < 8) stage_gemm<SG_W1>(lds, rs, kp->w1, wk, 0, j, P_S4 + j, cap);
  else if (j < 16) stage_gemm<SG_W2>(lds, rs, kp->w2, wk, 0, j - 8, P_S5 + j - 8, cap);
  else stage_mid(rs, kp->x, wk, j - 16, cap);
  if (cap == 0u && lane == 0) stx4(rs, 0, X_CTL, 1u);
}

#define ST_DECL
#define ST0()
#define ST(i)
#define ST_FLUSH()
#define VSL 1
#define NL 2
constexpr int R_W = 0, R_MEM = 131072, R_QK = R_MEM + 16384, R_KT = R_QK + 8192, R_RED = R_MEM + 16384, R_LDS = 163840;
__device__ __forceinline__ void bar_lo(int w) { asm volatile("s_cmp_ge_u32 %0, 4\n\ts_cbranch_scc1 1f\n\ts_barrier\n\t1:" :: "s"(w) : "scc", "memory"); }
__device__ __forceinline__ void bar_hi(int w) { asm volatile("s_cmp_lt_u32 %0, 4\n\ts_cbranch_scc1 1f\n\ts_setprio 3\n\ts_barrier\n\t1:" :: "s"(w) : "scc", "memory"); }
#define ISSUE(R) asm volatile("buffer_load_dwordx4 v[" R "], %[vo], %[rs], %[so] offen sc1" : "=&{v[" R "]}"(a) : [vo] "v"(vo), [rs] "s"(rs), [so] "s"(so) : "memory")
template <int I> __device__ __forceinline__ void issue(__amdgpu_buffer_rsrc_t rs, unsigned vo, unsigned so, u32x4& a) {
  if (I == 0) ISSUE("240:243"); else if (I == 1) ISSUE("244:247"); else if (I == 2) ISSUE("248:251"); else ISSUE("252:255");
}
#define ARRIVE(R0, R1, R2, R3, CNT) \
  asm volatile("s_waitcnt vmcnt(" #CNT ")\n\t" \
               "1:\n\t" \
               "v_min3_u32 %[t], v" #R0 ", v" #R1 ", v" #R2 "\n\t" \
               "v_min_u32 %[t], %[t], v" #R3 "\n\t" \
               "v_cmp_eq_u32 vcc, 0, %[t]\n\t" \
               "s_cbranch_vccz 2f\n\t" \
               "s_mov_b64 exec, vcc\n\t" \
               "buffer_load_dwordx4 v[" #R0 ":" #R3 "], %[vo], %[rs], %[so] offen sc1\n\t" \
               "s_mov_b64 exec, -1\n\t" \
               "s_waitcnt vmcnt(0)\n\t" \
               "s_sub_u32 %[cap], %[cap], 1\n\t" \
               "s_cbranch_scc0 1b\n\t" \
               "s_mov_b32 %[cap], 0\n\t" \
               "2:" \
               : "+{v[" #R0 ":" #R3 "]}"(a), [t] "=&v"(t), [cap] "+s"(cap) : [vo] "v"(vo), [rs] "s"(rs), [so] "s"(so) : "vcc", "scc", "memory")
template <int I> __device__ __forceinline__ void arrive(__amdgpu_buffer_rsrc_t rs, unsigned vo, unsigned so, u32x4& a, unsigned& cap) {
  unsigned t;
  if (I == 0) ARRIVE(240, 241, 242, 243, 3);
  else if (I == 1) ARRIVE(244, 245, 246, 247, 2);
  else if (I == 2) ARRIVE(248, 249, 250, 251, 1);
  else ARRIVE(252, 253, 254, 255, 0);
}
__device__ __forceinline__ unsigned half_of(unsigned v) { unsigned r; asm volatile("v_lshrrev_b32 %0, 1, %1" : "=v"(r) : "v"(v)); return r; }
__device__ __forceinline__ void issue2(__amdgpu_buffer_rsrc_t rs, unsigned l8, unsigned so, int w, u32x4& a) {
  asm volatile("s_cmp_ge_u32 %[w], 4\n\ts_cbranch_scc1 1f\n\tv_lshrrev_b32 v255, 1, %[l8]\n\t"
               "buffer_load_dwordx2 v[252:253], v255, %[rs], %[so] offen sc1\n\tbuffer_load_dwordx2 v[254:255], v255, %[rs], %[so] offen offset:512 sc1\n\t1:"
               : "=&{v[252:255]}"(a) : [l8] "v"(l8), [rs] "s"(rs), [so] "s"(so), [w] "s"(w) : "scc", "memory");
}
__device__ __forceinline__ void arrive2(__amdgpu_buffer_rsrc_t rs, unsigned l8, unsigned so, u32x4& a, unsigned& cap) {
  unsigned t;
  asm volatile("s_waitcnt vmcnt(0)\n\t"
               "1:\n\t"
               "v_min3_u32 %[t], v252, v253, v254\n\t"
               "v_min_u32 %[t], %[t], v255\n\t"
               "v_cmp_eq_u32 vcc, 0, %[t]\n\t"
               "s_cbranch_vccz 2f\n\t"
               "v_lshrrev_b32 %[t], 1, %[l8]\n\t"
               "buffer_load_dwordx2 v[252:253], %[t], %[rs], %[so] offen sc1\n\t"
               "buffer_load_dwordx2 v[254:255], %[t], %[rs], %[so] offen offset:512 sc1\n\t"
               "s_waitcnt vmcnt(0)\n\t"
               "s_sub_u32 %[cap], %[cap], 1\n\t"
               "s_cbranch_scc0 1b\n\t"
               "s_mov_b32 %[cap], 0\n\t"
               "2:"
               : "+{v[252:255]}"(a), [t] "=&v"(t), [cap] "+s"(cap) : [l8] "v"(l8), [rs] "s"(rs), [so] "s"(so) : "vcc", "scc", "memory");
}
__device__ __forceinline__ unsigned ror8(unsigned v) { return __builtin_amdgcn_update_dpp(0u, v, 0x128, 0xF, 0xF, true); }
template <class F>
__device__ __forceinline__ void gather_mem(__amdgpu_buffer_rsrc_t rs, unsigned lbase, int s, int w, unsigned l16, unsigned& cap, F&& meanwhile) {
  const unsigned so = X_MEM + s * RSTR + w * 2048;
  u32x4 a0, a1;
  issue<2>(rs, l16, so, a0); issue<3>(rs, l16, so + 1024, a1);
  meanwhile();
  LAS unsigned char* d = lad(l16, lbase + R_MEM + w * 2048);
  arrive<2>(rs, l16, so, a0, cap);
  *(LAS u32x4*)(d) = a0;
  arrive<3>(rs, l16, so + 1024, a1, cap);
  *(LAS u32x4*)(d + 1024) = a1;
}
__device__ __forceinline__ unsigned mem_off(int lane) { return (unsigned)((lane & 1) * 512 + (((lane >> 1) ^ ((lane & 1) * 16)) * 8)); }
__device__ __forceinline__ bf16x8 mem_frag(LAS unsigned char* mp, int chunk) { return cat8(trread(mp + chunk * 1024), trread(mp + chunk * 1024 + 256)); }
__device__ __forceinline__ bf16x16 w_frag(LAS unsigned char* p) {
  return cat16(cat8(trread(p), trread(p + 2048)), cat8(trread(p + 4096), trread(p + 6144)));
}
__device__ __forceinline__ bf16x16 l_frag(LAS unsigned char* p, int i) { return cat16(*(const LAS bf16x8*)(p + i * 2048), *(const LAS bf16x8*)(p + i * 2048 + 1024)); }
__device__ __forceinline__ void make_resident(LAS unsigned char* wb, unsigned lo, unsigned l16) {
#pragma unroll
  for (int c = 0; c < 2; ++c) {
    bf16x16 f[4];
#pragma unroll
    for (int t = 0; t < 4; ++t) f[t] = w_frag(wb + c * 8192 + (lo ^ (t << 5)));
    WAIT_L0(); SCHED();
#pragma unroll
    for (int t = 0; t < 4; ++t) {
      *(LAS bf16x8*)(wb + (c * 4 + t) * 2048 + l16) = __builtin_shufflevector(f[t], f[t], 0, 1, 2, 3, 4, 5, 6, 7);
      *(LAS bf16x8*)(wb + (c * 4 + t) * 2048 + 1024 + l16) = __builtin_shufflevector(f[t], f[t], 8, 9, 10, 11, 12, 13, 14, 15);
    }
    WAIT_L0(); SCHED();
  }
}
__device__ __forceinline__ f32x4 fold(f32x4 a, f32x4 b) {
  f32x4 r;
#pragma unroll
  for (int i = 0; i < 4; ++i) {
    const auto t = __builtin_amdgcn_permlane32_swap(f2u(a[i]), f2u(b[i]), false, false);
    r[i] = u2f(t[0]) + u2f(t[1]);
  }
  return r;
}

struct Sched { unsigned tau, og, sa; int ea, eb; };
template <int CTRL> __device__ __forceinline__ int dpp_max(int v) { return max(v, (int)__builtin_amdgcn_update_dpp((unsigned)v, (unsigned)v, CTRL, 0xF, 0xF, false)); }
__device__ __forceinline__ int row_max(int v) { v = dpp_max<0xB1>(v); v = dpp_max<0x4E>(v); v = dpp_max<0x141>(v); return dpp_max<0x140>(v); }
__device__ __forceinline__ void times_issue(__amdgpu_buffer_rsrc_t rs, unsigned l16, int s, unsigned& xb, unsigned& xa) {
  asm volatile("v_lshlrev_b32 %1, 3, %2\n\tbuffer_load_dword %0, %1, %3, %4 offen sc1\n\tbuffer_load_dword %1, %1, %3, %5 offen sc1"
               : "=&v"(xb), "=&v"(xa) : "v"(l16), "s"(rs), "s"(X_TS + (unsigned)s * RSTR), "s"(X_TS + (unsigned)s * RSTR + 8192) : "memory");
}
__device__ __forceinline__ void retime(__amdgpu_buffer_rsrc_t rs, unsigned l16, int s, unsigned xb, unsigned xa, Sched& t, const MainP& p, unsigned& cap) {
  while (!__all(s > 0 ? min(xa, xb) != 0u : (xa != 0u || l16 < 512)) && cap != 0u) {
    times_issue(rs, l16, s, xb, xa);
    asm volatile("s_waitcnt vmcnt(0)" : "+v"(xb), "+v"(xa) :: "memory");
    cap = __builtin_amdgcn_readfirstlane(cap - 1u);
  }
  cap = __builtin_amdgcn_readfirstlane(cap);
  const unsigned base = __builtin_amdgcn_readlane(xa, 32);
  const int vb = row_max((int)(xb - base)), va = row_max((int)(xa - base));
  const unsigned sa = base + max(__builtin_amdgcn_readlane(va, 32), __builtin_amdgcn_readlane(va, 48));
  if (s > 1) {
    const int ta = (int)(base - t.sa) + max(__builtin_amdgcn_readlane(va, 0), __builtin_amdgcn_readlane(va, 16));
    const int tb = (int)(base - t.og) + max(max(__builtin_amdgcn_readlane(vb, 0), __builtin_amdgcn_readlane(vb, 16)), max(__builtin_amdgcn_readlane(vb, 32), __builtin_amdgcn_readlane(vb, 48)));
    if (ta + p.d3 <= (int)(t.og - t.sa)) t.eb = max(t.eb - p.d4, min(tb * 8, t.eb + p.d4));
    t.ea = max(t.ea - p.d4, min(ta * 8, t.ea + p.d4));
  }
  t.sa = sa;
  t.og = sa + (t.ea >> 3) + p.d1;
  t.tau = t.og + (t.eb >> 3) + p.d2;
}

__device__ __forceinline__ void role_a(const MainP& p, LAS unsigned char* lds, int h) {
  const int tid = threadIdx.x, lane = tid & 63, w = __builtin_amdgcn_readfirstlane(tid >> 6);
  const int cg = w & 3, kh = w >> 2;
  const __amdgpu_buffer_rsrc_t rs = mk_rs(p.ws, WS_END);
  const unsigned lbase = (unsigned)(size_t)lds;
  unsigned l16 = lane * 16;
  asm volatile("" : "+v"(l16));
  const int sidx = (lane & 8) ? 0xEEEE : 0x4444;
  bf16x16 W[24];
  unsigned qoff;
  {
    const int fr = lane & 15, g = lane >> 4, tu = 4 * (g & 1) + (fr >> 2), ub = lane >> 3;
    const unsigned lo = (g >> 1) * 1024 + tu * 128 + ((tu >> 1) << 5) + (fr & 3) * 8;
    qoff = R_QK + ((fr >> 1) & 1) * 512 + (g >> 1) * 256 + (fr & 1) * 128 + (8 * (g & 1) + (fr >> 2)) * 8;
    const __amdgpu_buffer_rsrc_t rw = mk_rs(cg < 2 ? p.Wq : p.Wk, D_ * HD * 2);
    const unsigned vo = (unsigned)(((ub & 3) + 8 * (ub >> 2)) * HD + 16 * (((lane >> 1) & 3) ^ (ub >> 1)) + 8 * (lane & 1)) * 2u;
    const unsigned so = (unsigned)(kh * 512 * HD + h * DH + (cg & 1) * 64) * 2u;
    auto stage = [&](int c) {
#pragma unroll
      for (int rho = 0; rho < 8; ++rho) dma16n<(NTM & 2) != 0>(rw, vo, so + (unsigned)((c * 64 + 32 * (rho >> 2) + 16 * (rho & 1) + 4 * ((rho >> 1) & 1)) * HD) * 2u, lbase + R_W + w * 16384 + (c & 1) * 8192 + rho * 1024);
    };
    stage(0); stage(1);
#pragma unroll
    for (int c = 0; c < 6; ++c) {
      WAIT_V(8);
#pragma unroll
      for (int t = 0; t < 4; ++t) W[4 * c + t] = w_frag(lds + R_W + w * 16384 + (c & 1) * 8192 + (lo ^ (t << 5)));
      WAIT_L0(); SCHED();
      stage(c + 2);
    }
    WAIT_V(0);
    make_resident(lds + R_W + w * 16384, lo, l16);
  }
  WST(rs, 160 + (h >> 1), 4);
  unsigned cap = SPIN_CAP;
  wait_ge(rs, 0, F_ST + (unsigned)(P_PJ + h) * 128, 1u, cap);
  dma16x(rs, l16, WS_K + h * 8192 + w * 1024, lbase + R_KT + w * 1024);
  Sched tt; tt.tau = rt32(); tt.og = tt.sa = tt.tau; tt.ea = 127 * 8; tt.eb = 108 * 8;
  const int cu_ = h; (void)cu_;
  ST_DECL
  for (int s = 0; s < NSEG; ++s) {
    LAS unsigned char* lp = lad(l16, lbase + R_W + w * 16384);
    bf16x16 L[NL];
#pragma unroll
    for (int i = 0; i < NL; ++i) L[i] = l_frag(lp, i);
    unsigned lz = l16;
    asm volatile("" : "+v"(lz));
    LAS unsigned char* mp = lad(mem_off((int)(lz >> 4)), lbase + R_MEM + kh * 8192);
    SCHED();
    if (w == 7) wait_until(tt.tau);
    BAR();
    ST0();
    gather_mem(rs, lbase, s, (w + h) & 7, l16, cap, []() {});
    ST(1);
    LBAR();
    asm volatile("s_cmp_lg_u32 %0, 3\n\ts_cbranch_scc1 1f\n\ts_memrealtime vcc\n\ts_waitcnt lgkmcnt(0)\n\ts_max_u32 vcc_lo, vcc_lo, 1\n\tv_mov_b32 v255, vcc_lo\n\t"
                 "s_mov_b64 exec, 1\n\tbuffer_store_dword v255, off, %1, %2 sc1\n\ts_mov_b64 exec, -1\n\t1:"
                 :: "s"(w), "s"(rs), "s"(X_TS + (unsigned)s * RSTR + (unsigned)(96 + h) * 128) : "vcc", "scc", "v255", "memory");
    ST(2);
    f32x4 c[4];
#pragma unroll
    for (int t = 0; t < 4; ++t) c[t] = f32x4{0.f, 0.f, 0.f, 0.f};
    bf16x8 al, af[2];
    u32x4 vt0, vt1;
    u32x4 vmv;
    const unsigned vso = X_VM + (unsigned)s * RSTR + h * 4096 + cg * 1024;
    al = mem_frag(mp, 6); af[0] = mem_frag(mp, 0); af[1] = mem_frag(mp, 1);
#pragma unroll
    for (int i = 0; i < 8; ++i) {
      c[i & 3] = smf(al, L[i % NL], c[i & 3], sidx);
      SCHED();
      if (i == 3) al = mem_frag(mp, 7);
      if (i + NL < 8) L[i % NL] = l_frag(lp, i + NL);
      if (i == 8 - NL) {
        const unsigned vo = WS_VT + (unsigned)(s * NH + h) * 8192 + cg * 2048;
        asm volatile("s_cmp_ge_u32 %5, 4\n\ts_cbranch_scc1 1f\n\t"
                     "buffer_load_dwordx4 %0, %2, %3, %4 offen sc1\n\tbuffer_load_dwordx4 %1, %2, %3, %4 offen offset:1024 sc1\n\t1:"
                     : "=&v"(vt0), "=&v"(vt1) : "v"(l16), "s"(rs), "s"(vo), "s"(w) : "scc", "memory");
      }
      SCHED();
#pragma unroll
      for (int r = 3 * i; r < 3 * i + 3; ++r) {
        c[(r + 1) & 3] = smf(af[(r >> 2) & 1], W[(r & ~3) + ((r + 1) & 3)], c[(r + 1) & 3], sidx);
        SCHED();
        if ((r & 3) == 3 && (r >> 2) + 2 < 6) af[(r >> 2) & 1] = mem_frag(mp, (r >> 2) + 2);
        SCHED();
      }
    }
    {
      LAS unsigned char* qw = lad(half_of(l16), lbase + R_QK + cg * 2048 + kh * 512);
      const f32x4 f0 = fold(c[0], c[1]);
      *(LAS u32x2*)(qw) = u32x2{pack2(f0[0], f0[1]), pack2(f0[2], f0[3])};
      const f32x4 f1 = fold(c[2], c[3]);
      *(LAS u32x2*)(qw + 1024) = u32x2{pack2(f1[0], f1[1]), pack2(f1[2], f1[3])};
    }
    ST(3);
    if (w < 4) {
      bf16x8 k1[4], k2[4];
      LAS unsigned char* kb = lad(l16, lbase + R_KT);
#pragma unroll
      for (int ks = 0; ks < 2; ++ks) { k1[ks] = *(const LAS bf16x8*)(kb + ks * 1024); k2[ks] = *(const LAS bf16x8*)(kb + 4096 + ks * 1024); }
      unsigned ov;
      {
        unsigned ot;
        asm volatile("v_and_b32 %0, 0x30, %2\n\tv_lshrrev_b32 %1, 2, %2\n\tv_and_b32 %1, 0xC0, %1\n\tv_or_b32 %0, %0, %1\n\t"
                     "v_and_b32 %1, 0x40, %2\n\tv_lshl_or_b32 %0, %1, 9, %0\n\tv_and_b32 %1, 0x80, %2\n\tv_lshl_or_b32 %0, %1, 24, %0"
                     : "=&v"(ov), "=&v"(ot) : "v"(l16));
      }
      if (VSL >= 0) { if (VSL > 0) __builtin_amdgcn_s_sleep(VSL > 0 ? VSL : 1); issue2(rs, l16, vso, w, vmv); }
      LBAR();
      ST(4);
      if (VSL < 0) issue2(rs, l16, vso, w, vmv);
      f32x4 t0 = {0.f, 0.f, 0.f, 0.f}, t1 = t0, t2 = t0;
      LAS unsigned char* qb = lad(qoff, lbase);
      bf16x8 qf[4], kf[4];
#pragma unroll
      for (int ks = 0; ks < 2; ++ks) {
        qf[ks] = cat8(trread(qb + ks * 1024), trread(qb + ks * 1024 + 32));
        kf[ks] = cat8(trread(qb + 4096 + ks * 1024), trread(qb + 4096 + ks * 1024 + 32));
      }
      SCHED();
#pragma unroll
      for (int ks = 0; ks < 4; ++ks) {
        t1 = mfma32(k1[ks], qf[ks], t1); t2 = mfma32(k2[ks], qf[ks], t2); t0 = mfma32(kf[ks], qf[ks], t0);
        SCHED();
        if (ks < 2) {
          qf[ks + 2] = cat8(trread(qb + (ks + 2) * 1024), trread(qb + (ks + 2) * 1024 + 32));
          k1[ks + 2] = *(const LAS bf16x8*)(kb + (ks + 2) * 1024); k2[ks + 2] = *(const LAS bf16x8*)(kb + 4096 + (ks + 2) * 1024);
          kf[ks + 2] = cat8(trread(qb + 4096 + (ks + 2) * 1024), trread(qb + 4096 + (ks + 2) * 1024 + 32));
        }
        SCHED();
      }
      ST(5);
      asm volatile("s_waitcnt vmcnt(2)" : "+v"(vt0), "+v"(vt1) :: "memory");
      {
        const float sl = 0.08838834764831845f * 1.4426950408889634f;
        float z[4] = {t1[0], t1[1], t1[2], t1[3]}, y = t0[0], mx;
        {
          float u1 = t0[1], u2 = t0[2], u3 = t0[3];
#define M8(Z, B) "v_add_f32_dpp " Z ", " Z ", " Z " row_ror:8 row_mask:0xf bank_mask:0x3\n\tv_add_f32_dpp " Z ", " B ", " B " row_ror:8 row_mask:0xf bank_mask:0xc\n\t"
          asm volatile("s_nop 7\n\t"
              "v_permlane32_swap_b32 %4, %6\n\tv_permlane32_swap_b32 %5, %7\n\t"
              M8("%0", "%9") "v_add_f32 %4, %4, %6\n\tv_add_f32 %5, %5, %7\n\t" M8("%1", "%10") M8("%2", "%11") M8("%4", "%5") M8("%3", "%12")
              "v_max3_f32 %8, %0, %1, %2\n\tv_max3_f32 %8, %8, %4, %3\n\ts_nop 1\n\t"
              "v_max_f32_dpp %8, %8, %8 row_ror:8 row_mask:0xf bank_mask:0xf\n\tv_mov_b32 %5, %8\n\ts_nop 1\n\t"
              "v_permlane16_swap_b32 %8, %5\n\tv_max_f32 %8, %8, %5\n\tv_mov_b32 %5, %8\n\ts_nop 1\n\t"
              "v_permlane32_swap_b32 %8, %5\n\tv_max_f32 %8, %8, %5"
              : "+v"(z[0]), "+v"(z[1]), "+v"(z[2]), "+v"(z[3]), "+v"(y), "+v"(u1), "+v"(u2), "+v"(u3), "=&v"(mx) : "v"(t2[0]), "v"(t2[1]), "v"(t2[2]), "v"(t2[3]));
#undef M8
        }
        const float nm = -mx * sl;
        y = __builtin_amdgcn_exp2f(fmaf(y, sl, nm));
#pragma unroll
        for (int r = 0; r < 4; ++r) z[r] = __builtin_amdgcn_exp2f(fmaf(z[r], sl, nm));
        const unsigned pm1 = pack2(y, u2f(ror8(f2u(y))));
        const auto pms = __builtin_amdgcn_permlane32_swap(pm1, pm1, false, false);
        const u32x2 pm = {pms[0], pms[1]};
        const unsigned p0 = pack2(z[0], z[1]), p1 = pack2(z[2], z[3]);
        const u32x4 pt = {p0, p1, ror8(p0), ror8(p1)};
        f32x4 o0 = mfma32(__builtin_bit_cast(bf16x8, vt0), __builtin_bit_cast(bf16x8, pt), f32x4{0.f, 0.f, 0.f, 0.f});
        f32x4 o1 = mfma32(__builtin_bit_cast(bf16x8, vt1), __builtin_bit_cast(bf16x8, pt), f32x4{0.f, 0.f, 0.f, 0.f});
        float inv = __builtin_amdgcn_rcpf(gsum(dpp_add<0x128>(((z[0] + z[1]) + (z[2] + z[3])) + y)));
        asm volatile("" : "+v"(inv));
        arrive2(rs, l16, vso, vmv, cap);
        const f32x4 n0 = mfma16n(u32x2{vmv[0], vmv[1]}, pm, f32x4{0.f, 0.f, 0.f, 0.f}), n1 = mfma16n(u32x2{vmv[2], vmv[3]}, pm, f32x4{0.f, 0.f, 0.f, 0.f});
        SCHED();
        o0 *= inv; o1 *= inv;
        {
          const u32x4 v = {nz(pack2(fmaf(n0[0], inv, o0[0]), fmaf(n0[1], inv, o0[1]))), nz(pack2(fmaf(n0[2], inv, o0[2]), fmaf(n0[3], inv, o0[3]))),
                           nz(pack2(fmaf(n1[0], inv, o1[0]), fmaf(n1[1], inv, o1[1]))), nz(pack2(fmaf(n1[2], inv, o1[2]), fmaf(n1[3], inv, o1[3])))};
          stx16(rs, ov, X_OX + s * RSTR + h * 1024 + w * 256, v);
        }
        if (w == 3) { const unsigned t = max(rt32(), 1u); if (l16 == 0) stx4(rs, 0, X_TS + (unsigned)(s + 1) * RSTR + (unsigned)(64 + h) * 128, t); }
        ST(8);
      }
    } else {
      LBAR();
      LAS unsigned char* qb = lad(qoff, lbase + 4096 + (w - 4) * 1024);
      u32x4 kf = __builtin_bit_cast(u32x4, cat8(trread(qb), trread(qb + 32)));
#pragma unroll
      for (int i = 0; i < 4; ++i) kf[i] = pack2(dpp_add<0x128>(lo2f(kf[i])), dpp_add<0x128>(hi2f(kf[i])));
      stx16(rs, l16, WS_KM + (unsigned)(s * NH + h) * 4096 + (w - 4) * 1024, kf);
      WAIT_V(0);
    }
    LBAR();
    if (w == 4 && l16 == 0) stx4(rs, 0, F_CH + h * 128, (unsigned)s + 1u);
    ST(9);
    if (w == 7) {
      unsigned xb, xa;
      times_issue(rs, l16, s, xb, xa);
      asm volatile("s_waitcnt vmcnt(0)" : "+v"(xb), "+v"(xa) :: "memory");
      retime(rs, l16, s, xb, xa, tt, p, cap);
    }
    if (s + 1 < NSEG) {
      unsigned fo;
      asm volatile("v_mov_b32 %0, 0" : "=v"(fo));
      wait_ge(rs, fo, F_ST + (unsigned)(P_PJ + ((s + 1) % 3) * 32 + h) * 128, (unsigned)s + 2u, cap);
      dma16x(rs, l16, WS_K + (unsigned)((s + 1) * NH + h) * 8192 + w * 1024, lbase + R_KT + w * 1024);
    }
  }
  ST_FLUSH();
  if (cap == 0u && l16 == 0) stx4(rs, 0, X_CTL, 1u);
}

__device__ __forceinline__ void role_vb(const MainP& p, LAS unsigned char* lds, int rg, int cb) {
  const int tid = threadIdx.x, lane = tid & 63, w = __builtin_amdgcn_readfirstlane(tid >> 6);
  const int kh = w >> 2, wr = (w + cb) & 7;
  const __amdgpu_buffer_rsrc_t rs = mk_rs(p.ws, WS_END);
  const unsigned lbase = (unsigned)(size_t)lds;
  unsigned l16 = lane * 16, moff = mem_off(lane), roff, soff = (unsigned)((lane >> 5) * 256 + (lane & 31) * 4);
  const int sidx = (lane & 8) ? 0xEEEE : 0x4444;
  bf16x16 WV[8], WB[16];
  {
    const int fr = lane & 15, g = lane >> 4, tu = 4 * (g & 1) + (fr >> 2), ub = lane >> 3;
    { const int sl = mem_slot(16 * (g >> 1) + fr); roff = R_RED + w * 512 + ((sl >> 5) * 32 + (sl & 15) * 2 + (g & 1)) * 8; }
#pragma unroll
    for (int q = 0; q < 4; ++q) {
      const unsigned pc = 16u + (unsigned)(q * 512 + (rg * 32 + cb) * 8 + w);
      if (pc < ZA * 224u) stx16(rs, l16, X_REC + pc * 1024u, u32x4{0u, 0u, 0u, 0u});
    }
    const __amdgpu_buffer_rsrc_t rv = mk_rs(p.Wv, D_ * HD * 2), ro = mk_rs(p.Wo, D_ * HD * 2);
    const unsigned vv = (unsigned)(((ub & 3) + 8 * (ub >> 2)) * HD + 16 * (((lane >> 1) & 3) ^ (ub >> 1)) + 8 * (lane & 1)) * 2u;
    const unsigned sv = (unsigned)((32 * (w >> 2) + 16 * (w & 1) + 4 * ((w >> 1) & 1)) * HD + cb * DH + rg * 64) * 2u;
#pragma unroll
    for (int c = 0; c < 16; ++c) dma16n<(NTM & 1) != 0>(rv, vv, sv + (unsigned)c * 64 * HD * 2, lbase + R_W + c * 8192 + w * 1024);
    WAIT_V(0); BAR();
    const unsigned lo = (g >> 1) * 1024 + tu * 128 + (((tu >> 1) ^ (w & 3)) << 5) + (fr & 3) * 8;
#pragma unroll
    for (int c = 0; c < 8; ++c) WV[c] = w_frag(lds + R_W + (kh * 8 + c) * 8192 + lo);
    LBAR();
    const int u = (lane >> 2) & 7;
    const unsigned vw = (unsigned)((16 * (u >> 2) + 8 * ((u >> 1) & 1) + 4 * (lane >> 5) + (u & 1)) * D_ + (lane & 3) * 8) * 2u;
    const unsigned sw = (unsigned)(wr * 512 * D_ + cb * 32) * 2u;
    const unsigned bo = (g >> 1) * 1024 + tu * 64 + (fr & 3) * 8;
    auto stageo = [&](int hd) {
#pragma unroll
      for (int q = 0; q < 8; ++q)
        dma16n<(NTM & 4) != 0>(ro, vw, sw + (unsigned)((hd * 128 + 64 * (q >> 2) + 32 * (q & 1) + 2 * ((q >> 1) & 1)) * D_) * 2u, lbase + R_W + w * 16384 + (hd & 1) * 8192 + q * 1024);
    };
    stageo(0); stageo(1);
#pragma unroll
    for (int hd = 0; hd < 4; ++hd) {
      if (hd < 3) wait_v<8>(); else wait_v<0>();
#pragma unroll
      for (int f = 0; f < 4; ++f) WB[hd * 4 + f] = w_frag(lds + R_W + w * 16384 + (hd & 1) * 8192 + (f >> 1) * 512 + (f & 1) * 32 + bo);
      WAIT_L0(); SCHED();
      if (hd < 2) stageo(hd + 2);
    }
  }
  WST(rs, 176 + rg * 8 + (cb >> 2), 4);
  unsigned hsrc = (unsigned)((lane >> 3) * 2048 + (((lane & 7) ^ (lane >> 3)) << 4)), hoff = (unsigned)((lane & 15) * 128 + (((lane >> 4) ^ (lane & 7)) << 4));
  asm volatile("" : "+v"(l16), "+v"(moff), "+v"(roff), "+v"(soff), "+v"(hsrc), "+v"(hoff));
  unsigned cap = SPIN_CAP;
  auto h_piece = [&](int b, int q) { dma16x(rs, hsrc, WS_H + (unsigned)b * 65536u + (w & 3) * 16384 + (2 * q + kh) * 128, lbase + R_W + (b & 1) * 65536 + (q * 8 + w) * 1024); };
  auto h_fetch = [&](int b) {
#pragma unroll
    for (int q = 0; q < 8; ++q) h_piece(b, q);
  };
  f32x4 x0, x1;
  auto v_part = [&](int b, auto part, auto&& each) {
    constexpr int K0 = decltype(part)::value * 8;
    LAS unsigned char* a0 = lad(hoff, lbase + R_W + (b & 1) * 65536 + kh * 32768);
    LAS unsigned char* a1 = lad<64>(hoff, lbase + R_W + (b & 1) * 65536 + kh * 32768);
    bf16x8 af[3][2];
    auto rd = [&](int k) { LAS unsigned char* q = ((k & 1) ? a1 : a0) + (k >> 1) * 4096; af[k % 3][0] = *(const LAS bf16x8*)(q); af[k % 3][1] = *(const LAS bf16x8*)(q + 2048); };
    rd(K0); rd(K0 + 1);
    SCHED();
#pragma unroll
    for (int k = K0; k < K0 + 8; ++k) {
      if (k + 2 < K0 + 8) rd(k + 2);
      const bf16x8 wf = (k & 1) ? __builtin_shufflevector(WV[k >> 1], WV[k >> 1], 8, 9, 10, 11, 12, 13, 14, 15) : __builtin_shufflevector(WV[k >> 1], WV[k >> 1], 0, 1, 2, 3, 4, 5, 6, 7);
      x0 = mfma32(af[k % 3][0], wf, k ? x0 : f32x4{0.f, 0.f, 0.f, 0.f}); x1 = mfma32(af[k % 3][1], wf, k ? x1 : f32x4{0.f, 0.f, 0.f, 0.f});
      asm volatile("" : "+v"(x0), "+v"(x1));
      SCHED();
      each(k);
      SCHED();
    }
    if (K0) {
      LAS unsigned char* xv = lad(l16, lbase + R_RED + 4096 + (w & 3) * 2048);
      asm volatile("s_nop 7\n\ts_cmp_lt_u32 %3, 4\n\ts_cbranch_scc1 1f\n\tds_write_b128 %0, %1\n\tds_write_b128 %0, %2 offset:1024\n\t1:" :: "v"(xv), "v"(x0), "v"(x1), "s"(w) : "scc", "memory");
    }
  };
  auto v_out = [&](int b) {
    if (!kh && b < NSEG) {
      LAS unsigned char* xv = lad(l16, lbase + R_RED + 4096 + (w & 3) * 2048);
      x0 += *(const LAS f32x4*)(xv); x1 += *(const LAS f32x4*)(xv + 1024);
      stx16(rs, l16, WS_VT + (unsigned)(b * NH + cb) * 8192 + (rg * 4 + w) * 1024, u32x4{pack2(x0[0], x0[1]), pack2(x0[2], x0[3]), pack2(x1[0], x1[1]), pack2(x1[2], x1[3])});
    }
  };
  typedef std::integral_constant<int, 0> P0; typedef std::integral_constant<int, 1> P1;
  BAR();
  wait_ge(rs, (l16 & 0xF0) * 8 + (l16 >> 8) * 4, F_LN, 1u, cap);
  h_fetch(0); h_fetch(1);
  WAIT_V(8); BAR();
  v_part(0, P0{}, [](int) {}); v_part(0, P1{}, [](int) {});
  WAIT_V(0); LBAR();
  v_out(0); h_fetch(2);
  LBAR();
  v_part(1, P0{}, [](int) {}); v_part(1, P1{}, [](int) {});
  WAIT_V(0); LBAR();
  v_out(1); h_fetch(3);
  v_part(2, P0{}, [](int) {});
  wait_ge(rs, (l16 & 0xF0) * 8 + (l16 >> 8) * 4, F_LN + (unsigned)(1 + w) * 2048, 1u, cap);
  if (w == 0) wait_ge(rs, (l16 & 0xF0) * 8 + (l16 >> 8) * 4, F_LN + 9 * 2048, 1u, cap);
  WAIT_V(0); BAR();
  if (w == 0) {
    const int ln = l16 >> 4, sl = (ln >> 1) & 15, col = cb * 32 + (sl >> 3) * 16 + (ln >> 5) * 8 + (sl & 4) + (sl & 1) * 2 + ((sl >> 1) & 1), r = rg * 4 + (ln & 1) * 2;
    stx4(rs, soff, X_MEM + (cb >> 1) * 1024 + rg * 512 + ((cb ^ rg) & 1) * 128, nz((unsigned)p.mem0[r * D_ + col] | ((unsigned)p.mem0[(r + 1) * D_ + col] << 16)));
  }
  Sched tt; tt.tau = rt32(); tt.og = tt.sa = tt.tau; tt.ea = 127 * 8; tt.eb = 108 * 8;
  const int cu_ = 32 + (rg * 32 + cb) / 2; (void)cu_;
  ST_DECL
  for (int s = 0; s < NSEG; ++s) {
    if (w == 7) wait_until(tt.tau);
    BAR();
    ST0();
    gather_mem(rs, lbase, s, wr, l16, cap, [&]() { v_part(s + 2, P1{}, [](int) {}); });
    ST(1);
    LBAR();
    ST(2);
    {
      f32x4 c0 = {0.f, 0.f, 0.f, 0.f};
      LAS unsigned char* mp = lad(moff, lbase + R_MEM + kh * 8192);
      bf16x8 af[6];
#pragma unroll
      for (int c = 0; c < 6; ++c) af[c] = mem_frag(mp, c);
      SCHED();
#pragma unroll
      for (int c = 0; c < 8; ++c) { c0 = smf(af[c % 6], WV[c], c0, sidx); SCHED(); if (c < 2) { af[c] = mem_frag(mp, 6 + c); SCHED(); } }
      const f32x4 f = fold(c0, c0);
      if (l16 < 512) stx8(rs, half_of(l16), X_VM + (unsigned)s * RSTR + cb * 4096 + (rg * 4 + (w & 3)) * 512 + kh * 256, u32x2{nz(pack2(f[0], f[1])), nz(pack2(f[2], f[3]))});
    }
    ST(3);
    {
      unsigned xb, xa;
      times_issue(rs, w == 7 ? l16 : 0x1E000000u, s, xb, xa);
      h_fetch(min(s + 4, NSEG - 2 + (s & 1)));
      v_out(s + 2);
      asm volatile("s_waitcnt vmcnt(8)" : "+v"(xb), "+v"(xa) :: "memory");
      if (w == 7) retime(rs, l16, s, xb, xa, tt, p, cap);
    }
    if (s + 1 < NSEG) {
      u32x4 a[4];
      const unsigned so = X_OX + s * RSTR + rg * 32768 + wr * 4096;
      if (w == 7) wait_until(tt.og);
      BAR();
      ST(4);
      issue<0>(rs, l16, so, a[0]);
      v_part(s + 3, P0{}, [&](int k) {
        if (k == 0) issue<1>(rs, l16, so + 1024, a[1]);
        if (k == 2) issue<2>(rs, l16, so + 2048, a[2]);
        if (k == 4) issue<3>(rs, l16, so + 3072, a[3]);
      });
      f32x4 c[4];
#pragma unroll
      for (int t = 0; t < 4; ++t) c[t] = f32x4{0.f, 0.f, 0.f, 0.f};
#pragma unroll
      for (int i = 0; i < 4; ++i) {
        if (i == 0) arrive<0>(rs, l16, so, a[0], cap); else if (i == 1) arrive<1>(rs, l16, so + 1024, a[1], cap);
        else if (i == 2) arrive<2>(rs, l16, so + 2048, a[2], cap); else arrive<3>(rs, l16, so + 3072, a[3], cap);
#pragma unroll
        for (int t = 0; t < 4; ++t) c[t] = smf(__builtin_bit_cast(bf16x8, a[i]), WB[i * 4 + t], c[t], sidx);
        SCHED();
      }
      ST(5);
      {
        const bool odd = (l16 & 256) != 0;
        const f32x4 f = fold(odd ? c[2] : c[0], odd ? c[3] : c[1]);
        const auto e0 = __builtin_amdgcn_permlane16_swap(f2u(f[0]), f2u(f[2]), false, false), e1 = __builtin_amdgcn_permlane16_swap(f2u(f[1]), f2u(f[3]), false, false);
        *(LAS f32x2*)(lad(roff, lbase)) = f32x2{u2f(e0[0]) + u2f(e0[1]), u2f(e1[0]) + u2f(e1[1])};
      }
      ST(6);
      LBAR();
      ST(7);
      if (w == 0) {
        LAS unsigned char* rp = lad(half_of(l16), lbase + R_RED);
        f32x2 v[8];
#pragma unroll
        for (int i = 0; i < 8; ++i) v[i] = *(const LAS f32x2*)(rp + i * 512);
        const f32x2 sum = ((v[0] + v[1]) + (v[2] + v[3])) + ((v[4] + v[5]) + (v[6] + v[7]));
        stx4(rs, soff, X_MEM + (s + 1) * RSTR + (cb >> 1) * 1024 + rg * 512 + ((cb ^ rg) & 1) * 128, nz(pack2(sum[0], sum[1])));
        const unsigned t = max(rt32(), 1u);
        if (l16 == 0) stx4(rs, 0, X_TS + (unsigned)(s + 1) * RSTR + (unsigned)(rg * 32 + cb) * 128, t);
      }
      ST(8);
    }
  }
  ST_FLUSH();
  if (cap == 0u && l16 == 0) stx4(rs, 0, X_CTL, 1u);
}

#if defined(__HIP_DEVICE_COMPILE__)
__attribute__((target("no-packed-fp32-ops")))
#endif
__global__ void __launch_bounds__(512, 2) k_main(MainP p) {
  extern __shared__ __attribute__((aligned(16))) unsigned char smem[];
  const int b = blockIdx.x, x = b & 7, j = b >> 3;
  if (x >= 3) worker(p, (LAS unsigned char*)smem, x - 3, j);
  else if (x == 0) role_a(p, (LAS unsigned char*)smem, j);
  else role_vb(p, (LAS unsigned char*)smem, x - 1, j);
}

extern "C" void kernel_launch(void* const* d_in, const int* in_sizes, int n_in, void* d_out, int out_size, void* d_ws, size_t ws_size,
                              hipStream_t stream) {
  unsigned char* ws = (unsigned char*)d_ws;
  (void)hipMemsetAsync(ws, 0, X_ZERO, stream);
  MainP p;
  p.x = (const u16*)d_in[0]; p.mem0 = (const u16*)d_in[1]; p.c = (const u16*)d_in[12]; p.wmod = (const u16*)d_in[13]; p.Wq = (const u16*)d_in[2]; p.Wk = (const u16*)d_in[3]; p.Wv = (const u16*)d_in[4]; p.Wo = (const u16*)d_in[5];
  p.w1 = (const u16*)d_in[8]; p.w2 = (const u16*)d_in[9]; p.w3 = (const u16*)d_in[10]; p.w4 = (const u16*)d_in[11];
  p.out = (u16*)d_out; p.ws = ws; p.d1 = 6; p.d2 = 8; p.d3 = 2; p.d4 = 12;
  (void)hipFuncSetAttribute((const void*)k_main, hipFuncAttributeMaxDynamicSharedMemorySize, R_LDS);
  k_main<<<256, 512, R_LDS, stream>>>(p);
}
```

```cpp
#include <hip/hip_runtime.h>
#include <hip/hip_bf16.h>
#include <cstdlib>
#include <type_traits>
#pragma clang diagnostic ignored "-Winline-asm"

#define LAS __attribute__((address_space(3)))
#define GAS __attribute__((address_space(1)))
typedef unsigned short u16;
typedef __bf16 bf16x2 __attribute__((ext_vector_type(2)));
typedef __bf16 bf16x4 __attribute__((ext_vector_type(4)));
typedef __bf16 bf16x8 __attribute__((ext_vector_type(8)));
typedef __bf16 bf16x16 __attribute__((ext_vector_type(16)));
typedef short s16x4 __attribute__((ext_vector_type(4)));
typedef float f32x2 __attribute__((ext_vector_type(2)));
typedef float f32x4 __attribute__((ext_vector_type(4)));
typedef float f32x16 __attribute__((ext_vector_type(16)));
typedef unsigned u32x2 __attribute__((ext_vector_type(2)));
typedef unsigned u32x4 __attribute__((ext_vector_type(4)));

constexpr int S_ = 2048, D_ = 1024, NH = 32, DH = 128, HD = 4096, NSEG = 64, FF = 3584;

constexpr unsigned MB = 1u << 20;
constexpr unsigned F_LN = 0;
constexpr unsigned F_ST = 24576;
constexpr unsigned F_CH = F_ST + 98304;
constexpr unsigned X_CTL = F_CH + 4096;
constexpr unsigned X_MOD = X_CTL + 4096;
constexpr unsigned X_REC = X_MOD + 24576;
constexpr unsigned RSTR = 229376;
constexpr int ZA = 8;
constexpr unsigned X_MEM = X_REC;
constexpr unsigned X_OX = X_REC + 16384;
constexpr unsigned X_VM = X_REC + 81920;
constexpr unsigned X_TS = X_REC + 212992;
constexpr unsigned X_ZERO = X_REC + 16384;
constexpr unsigned WS_H = 16 * MB;
constexpr unsigned WS_Q = 20 * MB;
constexpr unsigned WS_K = 36 * MB;
constexpr unsigned WS_VT = 52 * MB;
constexpr unsigned WS_O = 68 * MB;
constexpr unsigned WS_X1 = 84 * MB;
constexpr unsigned WS_H2 = 88 * MB;
constexpr unsigned WS_G = 92 * MB;
constexpr unsigned WS_H1S = 96 * MB;
constexpr unsigned WS_X2 = 101 * MB;
constexpr unsigned WS_ST5 = 106 * MB;
constexpr unsigned WS_U = 108 * MB;
constexpr unsigned WS_KM = 124 * MB;
constexpr unsigned WS_P2 = 132 * MB;
constexpr unsigned WS_P8 = 164 * MB;
constexpr unsigned WS_END = 198 * MB;

__device__ __forceinline__ float bf2f(unsigned short b) { return __builtin_bit_cast(float, (unsigned)b << 16); }
__device__ __forceinline__ unsigned f2u(float f) { return __builtin_bit_cast(unsigned, f); }
__device__ __forceinline__ float u2f(unsigned u) { return __builtin_bit_cast(float, u); }
__device__ __forceinline__ float lo2f(unsigned w) { return __builtin_bit_cast(float, w << 16); }
__device__ __forceinline__ float hi2f(unsigned w) { return __builtin_bit_cast(float, w & 0xFFFF0000u); }
__device__ __forceinline__ unsigned pack2(float a, float b) { bf16x2 v; v[0] = (__bf16)a; v[1] = (__bf16)b; return __builtin_bit_cast(unsigned, v); }
__device__ __forceinline__ float rbf(float a) { return (float)(__bf16)a; }
__device__ __forceinline__ unsigned nz(unsigned w) { return max(w, 1u); }
__device__ __forceinline__ float gelu_tanh(float x) {
  const float e = __builtin_amdgcn_exp2f(x * fmaf(x * x, -0.1029432397f, -2.302208198f));
  return x * __builtin_amdgcn_rcpf(1.0f + e);
}
template <int CTRL> __device__ __forceinline__ float dpp_add(float v) { return v + u2f(__builtin_amdgcn_update_dpp(0u, f2u(v), CTRL, 0xF, 0xF, true)); }
__device__ __forceinline__ float wave_sum(float v) {
  v = dpp_add<0xB1>(v); v = dpp_add<0x4E>(v); v = dpp_add<0x141>(v); v = dpp_add<0x140>(v);
  auto t = __builtin_amdgcn_permlane16_swap(f2u(v), f2u(v), false, false);
  v = u2f(t[0]) + u2f(t[1]);
  t = __builtin_amdgcn_permlane32_swap(f2u(v), f2u(v), false, false);
  return u2f(t[0]) + u2f(t[1]);
}
#define WAIT_V(n) asm volatile("s_waitcnt vmcnt(" #n ")" ::: "memory")
#define WAIT_L0() asm volatile("s_waitcnt lgkmcnt(0)" ::: "memory")
#define BAR() do { asm volatile("" ::: "memory"); __builtin_amdgcn_s_barrier(); asm volatile("" ::: "memory"); } while (0)
#define LBAR() do { WAIT_L0(); BAR(); } while (0)
template <int N> __device__ __forceinline__ void wait_v() { asm volatile("s_waitcnt vmcnt(%0)" :: "n"(N) : "memory"); }
__device__ __forceinline__ void dma16(__amdgpu_buffer_rsrc_t rs, unsigned voff, unsigned soff, unsigned ldsa) {
  asm volatile("s_mov_b32 m0, %0\n\ts_nop 3\n\tbuffer_load_dwordx4 %1, %2, %3 offen lds" :: "s"(ldsa), "v"(voff), "s"(rs), "s"(soff) : "m0", "memory");
}
template <bool NT> __device__ __forceinline__ void dma16n(__amdgpu_buffer_rsrc_t rs, unsigned voff, unsigned soff, unsigned ldsa) {
  if (NT) asm volatile("s_mov_b32 m0, %0\n\ts_nop 3\n\tbuffer_load_dwordx4 %1, %2, %3 offen nt lds" :: "s"(ldsa), "v"(voff), "s"(rs), "s"(soff) : "m0", "memory");
  else dma16(rs, voff, soff, ldsa);
}
#define NTM 1
__device__ __forceinline__ void dma16x(__amdgpu_buffer_rsrc_t rs, unsigned voff, unsigned soff, unsigned ldsa) {
  asm volatile("s_mov_b32 m0, %0\n\ts_nop 3\n\tbuffer_load_dwordx4 %1, %2, %3 offen sc1 lds" :: "s"(ldsa), "v"(voff), "s"(rs), "s"(soff) : "m0", "memory");
}
__device__ __forceinline__ u32x4 ldx16(__amdgpu_buffer_rsrc_t rs, unsigned voff, unsigned soff) { return __builtin_amdgcn_raw_buffer_load_b128(rs, voff, soff, 16); }
__device__ __forceinline__ u32x2 ldx8(__amdgpu_buffer_rsrc_t rs, unsigned voff, unsigned soff) { return __builtin_amdgcn_raw_buffer_load_b64(rs, voff, soff, 16); }
__device__ __forceinline__ unsigned ldx4(__amdgpu_buffer_rsrc_t rs, unsigned voff, unsigned soff) { return __builtin_amdgcn_raw_buffer_load_b32(rs, voff, soff, 16); }
__device__ __forceinline__ void stx16(__amdgpu_buffer_rsrc_t rs, unsigned voff, unsigned soff, u32x4 v) {
  __builtin_amdgcn_raw_buffer_store_b128(v, rs, voff, soff, 16);
  asm volatile("s_nop 1" :: "v"(v) : "memory");
}
__device__ __forceinline__ void stx8(__amdgpu_buffer_rsrc_t rs, unsigned voff, unsigned soff, u32x2 v) { __builtin_amdgcn_raw_buffer_store_b64(v, rs, voff, soff, 16); }
__device__ __forceinline__ void stx4(__amdgpu_buffer_rsrc_t rs, unsigned voff, unsigned soff, unsigned v) { __builtin_amdgcn_raw_buffer_store_b32(v, rs, voff, soff, 16); }
__device__ __forceinline__ void wait_ge(__amdgpu_buffer_rsrc_t rs, unsigned voff, unsigned soff, unsigned need, unsigned& cap) {
  unsigned t;
  asm volatile("s_nop 4\n\t"
               "1:\n\t"
               "buffer_load_dword %[t], %[vo], %[rs], %[so] offen sc1\n\t"
               "s_waitcnt vmcnt(0)\n\t"
               "v_cmp_gt_u32 vcc, %[need], %[t]\n\t"
               "s_cmp_eq_u64 vcc, 0\n\t"
               "s_cbranch_scc1 2f\n\t"
               "s_sleep 2\n\t"
               "s_sub_u32 %[cap], %[cap], 1\n\t"
               "s_cbranch_scc0 1b\n\t"
               "s_mov_b32 %[cap], 0\n\t"
               "2:"
               : [t] "=&v"(t), [cap] "+s"(cap) : [vo] "v"(voff), [rs] "s"(rs), [so] "s"(soff), [need] "s"(need) : "vcc", "scc", "memory");
}
__device__ __forceinline__ void wait_seen(__amdgpu_buffer_rsrc_t rs, unsigned voff, unsigned soff, unsigned need, unsigned& seen, unsigned& cap) {
  if (seen >= need) return;
  unsigned t;
  asm volatile("s_nop 4\n\t"
               "1:\n\t"
               "buffer_load_dword %[t], %[vo], %[rs], %[so] offen sc1\n\t"
               "s_waitcnt vmcnt(0)\n\t"
               "v_cmp_gt_u32 vcc, %[need], %[t]\n\t"
               "s_cbranch_vccz 2f\n\t"
               "s_sleep 1\n\t"
               "s_sub_u32 %[cap], %[cap], 1\n\t"
               "s_cbranch_scc0 1b\n\t"
               "s_mov_b32 %[cap], 0\n\t"
               "2:"
               : [t] "=&v"(t), [cap] "+s"(cap) : [vo] "v"(voff), [rs] "s"(rs), [so] "s"(soff), [need] "s"(need) : "vcc", "scc", "memory");
  seen = min((unsigned)__builtin_amdgcn_readlane(t, 0), (unsigned)__builtin_amdgcn_readlane(t, 1));
}
__device__ __forceinline__ __amdgpu_buffer_rsrc_t mk_rs(const void* p, unsigned bytes) { return __builtin_amdgcn_make_buffer_rsrc((void*)p, 0, (int)bytes, 0x00020000); }
#define SCHED() __builtin_amdgcn_sched_barrier(0)
__device__ __forceinline__ unsigned now32() { unsigned long long t; asm volatile("s_memtime %0\n\ts_waitcnt lgkmcnt(0)" : "=s"(t) :: "memory"); return (unsigned)t; }
__device__ __forceinline__ unsigned rt32() { unsigned long long t; asm volatile("s_memrealtime %0\n\ts_waitcnt lgkmcnt(0)" : "=s"(t) :: "memory"); return (unsigned)t; }
__device__ __forceinline__ void wait_until(unsigned t) { while ((int)(rt32() - t) < 0) {} }
template <int X = 0> __device__ __forceinline__ LAS unsigned char* lad(unsigned v, unsigned u) {
  unsigned a;
  if (X == 0) asm volatile("v_add_u32 %0, %1, %2" : "=v"(a) : "s"(u), "v"(v));
  else asm volatile("v_xor_b32 %0, %3, %2\n\tv_add_u32 %0, %1, %0" : "=&v"(a) : "s"(u), "v"(v), "n"(X));
  return (LAS unsigned char*)(size_t)a;
}
__device__ __forceinline__ bf16x4 trread(LAS unsigned char* p) { return __builtin_amdgcn_ds_read_tr16_b64_v4bf16((LAS bf16x4*)p); }
__device__ __forceinline__ bf16x8 cat8(bf16x4 a, bf16x4 b) { return __builtin_shufflevector(a, b, 0, 1, 2, 3, 4, 5, 6, 7); }
__device__ __forceinline__ bf16x16 cat16(bf16x8 a, bf16x8 b) { return __builtin_shufflevector(a, b, 0, 1, 2, 3, 4, 5, 6, 7, 8, 9, 10, 11, 12, 13, 14, 15); }
__device__ __forceinline__ f32x4 mfma32(bf16x8 a, bf16x8 b, f32x4 c) { return __builtin_amdgcn_mfma_f32_16x16x32_bf16(a, b, c, 0, 0, 0); }
__device__ __forceinline__ f32x4 mfma16(u32x2 a, u32x2 b, f32x4 c) {
  return mfma32(__builtin_bit_cast(bf16x8, u32x4{a[0], a[1], 0u, 0u}), __builtin_bit_cast(bf16x8, u32x4{b[0], b[1], 0u, 0u}), c);
}
__device__ __forceinline__ f32x4 mfma16n(u32x2 a, u32x2 b, f32x4 c) { return __builtin_amdgcn_mfma_f32_16x16x16bf16_1k(__builtin_bit_cast(s16x4, a), __builtin_bit_cast(s16x4, b), c, 0, 0, 0); }
__device__ __forceinline__ f32x4 smf(bf16x8 a, bf16x16 b, f32x4 c, int idx) { return __builtin_amdgcn_smfmac_f32_16x16x64_bf16(a, b, c, idx, 0, 0); }

__device__ __forceinline__ void ln_row(float (&x)[16]) {
  float s = 0.f;
#pragma unroll
  for (int i = 0; i < 16; ++i) s += x[i];
  const float mean = wave_sum(s) * (1.0f / 1024.0f);
  float q = 0.f;
#pragma unroll
  for (int i = 0; i < 16; ++i) { const float d = x[i] - mean; q += d * d; }
  const float var = wave_sum(q) * (1.0f / 1024.0f);
  const float m = rbf(mean), isd = __builtin_amdgcn_rcpf(rbf(__builtin_amdgcn_sqrtf(rbf(rbf(var) + 1e-5f))));
#pragma unroll
  for (int i = 0; i < 16; ++i) x[i] = rbf(rbf(x[i] - m) * isd);
}
__device__ __forceinline__ void ln_stats(const float (&x)[16], float& m, float& isd) {
  float s = 0.f;
#pragma unroll
  for (int i = 0; i < 16; i += 4) s += (x[i] + x[i + 1]) + (x[i + 2] + x[i + 3]);
  const float mean = wave_sum(s) * (1.0f / 1024.0f);
  float q0 = 0.f, q1 = 0.f;
#pragma unroll
  for (int i = 0; i < 16; i += 2) { const float d0 = x[i] - mean, d1 = x[i + 1] - mean; q0 = fmaf(d0, d0, q0); q1 = fmaf(d1, d1, q1); }
  const float var = wave_sum(q0 + q1) * (1.0f / 1024.0f);
  m = rbf(mean); isd = __builtin_amdgcn_rcpf(rbf(__builtin_amdgcn_sqrtf(rbf(rbf(var) + 1e-5f))));
}
__device__ __forceinline__ void load_row16(const u16* rowp, int lane, float (&x)[16]) {
  const u32x4 a = *(const u32x4*)(rowp + lane * 8), b = *(const u32x4*)(rowp + 512 + lane * 8);
#pragma unroll
  for (int i = 0; i < 4; ++i) { x[2 * i] = lo2f(a[i]); x[2 * i + 1] = hi2f(a[i]); x[8 + 2 * i] = lo2f(b[i]); x[9 + 2 * i] = hi2f(b[i]); }
}
__device__ __forceinline__ void store_row16(u16* rowp, int lane, const float (&x)[16]) {
  u32x4 a, b;
#pragma unroll
  for (int i = 0; i < 4; ++i) { a[i] = pack2(x[2 * i], x[2 * i + 1]); b[i] = pack2(x[8 + 2 * i], x[9 + 2 * i]); }
  *(u32x4*)(rowp + lane * 8) = a; *(u32x4*)(rowp + 512 + lane * 8) = b;
}
__host__ __device__ __forceinline__ int mem_slot(int dl) { return ((dl >> 3) & 1) * 32 + (dl >> 4) * 8 + (dl & 4) + (dl & 1) * 2 + ((dl >> 1) & 1); }

#define WST(rs, wk, i)
struct MainP { const u16 *x, *mem0, *Wq, *Wk, *Wv, *Wo, *w1, *w2, *w3, *w4, *c, *wmod; u16* out; unsigned char* ws; int d1, d2, d3, d4; };
constexpr unsigned SPIN_CAP = 1u << 18;

__device__ __forceinline__ float gmax(float v) {
  auto t = __builtin_amdgcn_permlane16_swap(f2u(v), f2u(v), false, false);
  v = fmaxf(u2f(t[0]), u2f(t[1]));
  t = __builtin_amdgcn_permlane32_swap(f2u(v), f2u(v), false, false);
  return fmaxf(u2f(t[0]), u2f(t[1]));
}
__device__ __forceinline__ float gsum(float v) {
  auto t = __builtin_amdgcn_permlane16_swap(f2u(v), f2u(v), false, false);
  v = u2f(t[0]) + u2f(t[1]);
  t = __builtin_amdgcn_permlane32_swap(f2u(v), f2u(v), false, false);
  return u2f(t[0]) + u2f(t[1]);
}
__device__ __forceinline__ void publish(__amdgpu_buffer_rsrc_t rs, unsigned word, unsigned val) {
  WAIT_V(0); BAR();
  if (threadIdx.x == 0) stx4(rs, 0, word, val);
}

__device__ __forceinline__ int k_owner(int b) { return b < 60 ? b % 3 : 2; }
constexpr int QB0 = 10;
constexpr int P_S2 = 0, P_S8 = 32, P_S7 = 64, P_S4 = 96, P_S5 = 104, P_S3 = 112, P_PJ = 208, P_S1 = 336;
constexpr int NLN = 160;

enum { SG_WO = 0, SG_W1 = 1, SG_W2 = 2, SG_W3 = 3, SG_W4 = 4, SG_PJ = 5 };
constexpr int G_XCH = 131072, G_K = G_XCH + 16384, G_CTL = G_K + 8192;
__device__ __forceinline__ f32x16 mfma3232(bf16x8 a, bf16x8 b, f32x16 c) { return __builtin_amdgcn_mfma_f32_32x32x16_bf16(a, b, c, 0, 0, 0); }
__device__ __forceinline__ void widen32(const f32x16& a, u32x4 (&v)[2]) {
#pragma unroll
  for (int u = 0; u < 2; ++u) {
    const auto r0 = __builtin_amdgcn_permlane32_swap(pack2(a[8 * u], a[8 * u + 1]), pack2(a[8 * u + 4], a[8 * u + 5]), false, false);
    const auto r1 = __builtin_amdgcn_permlane32_swap(pack2(a[8 * u + 2], a[8 * u + 3]), pack2(a[8 * u + 6], a[8 * u + 7]), false, false);
    v[u] = u32x4{r0[0], r1[0], r0[1], r1[1]};
  }
}
__device__ __forceinline__ unsigned umin4v(u32x4 v) { return min(min(v[0], v[1]), min(v[2], v[3])); }
__device__ __forceinline__ float hsum(float v) { const auto t = __builtin_amdgcn_permlane32_swap(f2u(v), f2u(v), false, false); return u2f(t[0]) + u2f(t[1]); }

__device__ __forceinline__ void attend(__amdgpu_buffer_rsrc_t rs, int seg, int h, int t, int lane, unsigned& chs, unsigned& cap) {
  const int fr = lane & 15, g = lane >> 4;
  const unsigned l16 = lane * 16, ub = (unsigned)(seg * NH + h);
  wait_seen(rs, 0, F_CH + h * 128, (unsigned)max(seg, 1), chs, cap);
  wait_ge(rs, (lane & 1) && seg >= QB0 ? 96u * 128u : (unsigned)k_owner(seg) * 4096u, F_ST + (unsigned)(P_PJ + h) * 128, (unsigned)seg + 1u, cap);
  f32x4 s0 = {0.f, 0.f, 0.f, 0.f}, s1 = s0, s2 = s0;
  u32x4 q[4], kmf[4];
  u32x2 vmf[8];
  const unsigned em = (unsigned)(fr >> 3) * 512 + (unsigned)g * 128 + (unsigned)(8 * ((fr >> 2) & 1) + (fr & 3)) * 8;
  auto mem_load = [&](unsigned off) {
#pragma unroll
    for (int ks = 0; ks < 4; ++ks) kmf[ks] = ldx16(rs, l16 + off, WS_KM + ub * 4096 + ks * 1024);
#pragma unroll
    for (int i = 0; i < 8; ++i) vmf[i] = ldx8(rs, em + off, X_VM + (unsigned)seg * RSTR + h * 4096 + (i >> 1) * 1024 + (i & 1) * 32);
  };
  const bool have = chs > (unsigned)seg;
  f32x4 o[8];
  float mt = -3.0e38f, lt = 0.f;
  const float scale = 0.08838834764831845f, L2E = 1.4426950408889634f;
  {
    u32x4 k0[4], k1[4], vv[8];
#pragma unroll
    for (int ks = 0; ks < 4; ++ks) {
      q[ks] = ldx16(rs, l16, WS_Q + ub * 8192 + t * 4096 + ks * 1024);
      k0[ks] = ldx16(rs, l16, WS_K + ub * 8192 + ks * 1024); k1[ks] = ldx16(rs, t ? l16 : 0xF0000000u, WS_K + ub * 8192 + 4096 + ks * 1024);
    }
    const unsigned ev = (unsigned)(fr >> 3) * 1024 + (unsigned)(g * 16 + 8 * ((fr >> 2) & 1) + (fr & 3)) * 16;
#pragma unroll
    for (int i = 0; i < 8; ++i) vv[i] = ldx16(rs, ev, WS_VT + ub * 8192 + (i >> 1) * 2048 + (i & 1) * 64);
#pragma unroll
    for (int ks = 0; ks < 4; ++ks) {
      s1 = mfma32(__builtin_bit_cast(bf16x8, k0[ks]), __builtin_bit_cast(bf16x8, q[ks]), s1); s2 = mfma32(__builtin_bit_cast(bf16x8, k1[ks]), __builtin_bit_cast(bf16x8, q[ks]), s2);
    }
    SCHED();
    mem_load(have ? 0u : 0xF0000000u);
    SCHED();
    float b[4], c[4];
#pragma unroll
    for (int r = 0; r < 4; ++r) {
      const int j = 4 * g + r;
      b[r] = (t == 1 || j <= fr) ? rbf(rbf(s1[r]) * scale) : -3.0e38f;
      c[r] = (t == 1 && j <= fr) ? rbf(rbf(s2[r]) * scale) : -3.0e38f;
      mt = fmaxf(mt, fmaxf(b[r], c[r]));
    }
    mt = gmax(mt);
#pragma unroll
    for (int r = 0; r < 4; ++r) { b[r] = __builtin_amdgcn_exp2f((b[r] - mt) * L2E); c[r] = __builtin_amdgcn_exp2f((c[r] - mt) * L2E); lt += b[r] + c[r]; }
    const u32x4 pt = {pack2(b[0], b[1]), pack2(b[2], b[3]), pack2(c[0], c[1]), pack2(c[2], c[3])};
#pragma unroll
    for (int i = 0; i < 8; ++i) o[i] = mfma32(__builtin_bit_cast(bf16x8, vv[i]), __builtin_bit_cast(bf16x8, pt), f32x4{0.f, 0.f, 0.f, 0.f});
    lt = gsum(lt);
  }
  asm volatile("" : "+v"(o[0]), "+v"(o[1]), "+v"(o[2]), "+v"(o[3]), "+v"(o[4]), "+v"(o[5]), "+v"(o[6]), "+v"(o[7]), "+v"(lt));
  if (!have) { wait_seen(rs, 0, F_CH + h * 128, (unsigned)seg + 1u, chs, cap); mem_load(0u); }
#pragma unroll
  for (int ks = 0; ks < 4; ++ks) s0 = mfma32(__builtin_bit_cast(bf16x8, kmf[ks]), __builtin_bit_cast(bf16x8, q[ks]), s0);
  float a[4];
#pragma unroll
  for (int r = 0; r < 4; ++r) a[r] = rbf(rbf(s0[r]) * scale);
  const float mx = fmaxf(mt, gmax(fmaxf(fmaxf(a[0], a[1]), fmaxf(a[2], a[3]))));
#pragma unroll
  for (int r = 0; r < 4; ++r) a[r] = __builtin_amdgcn_exp2f((a[r] - mx) * L2E);
  const u32x2 pm = {pack2(a[0], a[1]), pack2(a[2], a[3])};
  const float al = __builtin_amdgcn_exp2f((mt - mx) * L2E);
  const float inv = __builtin_amdgcn_rcpf(fmaf(al, lt, 0.5f * gsum((a[0] + a[1]) + (a[2] + a[3])))), ai = al * inv;
#pragma unroll
  for (int u = 0; u < 4; ++u) {
    const f32x4 x = o[2 * u] * ai + mfma16n(vmf[2 * u], pm, f32x4{0.f, 0.f, 0.f, 0.f}) * inv, y = o[2 * u + 1] * ai + mfma16n(vmf[2 * u + 1], pm, f32x4{0.f, 0.f, 0.f, 0.f}) * inv;
    stx16(rs, l16, WS_O + (unsigned)seg * 262144 + (unsigned)((h * 4 + u) * 2 + t) * 1024, u32x4{pack2(x[0], x[1]), pack2(x[2], x[3]), pack2(y[0], y[1]), pack2(y[2], y[3])});
  }
  WAIT_V(0);
  if (lane == 0) stx4(rs, 0, F_ST + (unsigned)(P_S1 + (seg & 3) * 64 + h * 2 + t) * 128, (unsigned)seg + 1u);
}

template <int SG>
__device__ __forceinline__ void stage_gemm(LAS unsigned char* lds, __amdgpu_buffer_rsrc_t rs, const u16* Wg, int wk, int kq, int cb, int pid, unsigned& cap,
                                           unsigned dst = 0, int bstr = 1, int nb = 0) {
  constexpr bool PROJ = SG == SG_PJ, ROWM = (SG == SG_W1 || PROJ);
  constexpr int KS = (SG == SG_W4) ? 28 : 32;
  constexpr int LDW = PROJ ? HD : (SG == SG_W3) ? FF : D_;
  constexpr int WROWS = (SG == SG_WO) ? HD : (SG == SG_W4 ? FF : D_);
  const int NBLK = PROJ ? nb : (SG == SG_WO || SG == SG_W1) ? 64 : 65;
  constexpr unsigned ABASE = PROJ ? WS_H : SG == SG_WO ? WS_O : SG == SG_W1 ? WS_G : SG == SG_W2 ? WS_H1S : SG == SG_W3 ? WS_X2 : WS_U;
  constexpr unsigned ASTR = SG == SG_WO ? 262144u : SG == SG_W4 ? 229376u : 65536u;
  constexpr int NPC = KS / 4;
  const unsigned myword = F_ST + (unsigned)pid * 128u;
  int tid = threadIdx.x; asm volatile("" : "+v"(tid));
  const int lane = tid & 63, w = __builtin_amdgcn_readfirstlane(tid >> 6), row = lane & 31, hf = lane >> 5;
  const int kh = w >> 2, wl = w & 3;
  const int koff = PROJ ? 0 : kq;
  const unsigned lbase = (unsigned)(size_t)lds, l16 = lane * 16;
  const int col0 = cb * 128 + wl * 32;
  bf16x8 Wf[KS];
  {
    const __amdgpu_buffer_rsrc_t rsW = mk_rs(Wg, (unsigned)WROWS * LDW * 2u);
    const int ub = lane >> 3, tu = 4 * hf + ((lane & 15) >> 2);
    const unsigned vB = (unsigned)(((ub & 3) + 8 * (ub >> 2)) * LDW + 16 * (((lane >> 1) & 3) ^ (ub >> 1)) + 8 * (lane & 1)) * 2u;
    const unsigned sB = (unsigned)((koff * KS * 32 + 32 * (w >> 2) + 16 * ((w >> 1) & 1) + 4 * (w & 1)) * LDW + cb * 128) * 2u;
    const int boff = kh * 65536 + (wl >> 1) * 8192 + tu * 128 + (((tu >> 1) ^ (2 * (wl & 1) + ((lane >> 4) & 1))) << 5) + (lane & 3) * 8;
#pragma unroll
    for (int ps = 0; ps < 2; ++ps) {
      BAR();
#pragma unroll
      for (int hh = 0; hh < 2; ++hh)
#pragma unroll
        for (int i = 0; i < 4; ++i)
          if (4 * ps + i < NPC && !(PROJ && ps == 0)) {
            const unsigned so = sB + (unsigned)((hh * NPC + 4 * ps + i) * 64 * LDW) * 2u, d = lbase + hh * 65536 + i * 16384 + w * 1024;
            dma16n<(NTM & 16) != 0>(rsW, vB, so, d); dma16n<(NTM & 16) != 0>(rsW, vB, so + 128, d + 8192);
          }
      WAIT_V(0); BAR();
#pragma unroll
      for (int i = 0; i < 4; ++i)
        if (4 * ps + i < NPC) {
#pragma unroll
          for (int k = 0; k < 4; ++k) { LAS unsigned char* q = lds + (boff + i * 16384 + k * 2048); Wf[(4 * ps + i) * 4 + k] = cat8(trread(q), trread(q + 1024)); }
        }
      WAIT_L0();
    }
    if (SG == SG_W1) {
      const u32x4 z = {0u, 0u, 0u, 0u};
      stx16(rs, (unsigned)tid * 16, WS_H1S + cb * 8192, z); stx16(rs, (unsigned)tid * 16, WS_H1S + 64 * 65536 + cb * 8192, z);
      WAIT_V(0);
    }
    if (tid < 8) *(LAS unsigned*)(lds + G_CTL + tid * 4) = 0u;
    LBAR();
  }
  const f32x16 zero16 = {0.f, 0.f, 0.f, 0.f, 0.f, 0.f, 0.f, 0.f, 0.f, 0.f, 0.f, 0.f, 0.f, 0.f, 0.f, 0.f};
  auto put16 = [&](LAS unsigned char* p, const f32x16& a) {
#pragma unroll
    for (int q = 0; q < 4; ++q) *(LAS f32x4*)(p + q * 1024) = f32x4{a[4 * q], a[4 * q + 1], a[4 * q + 2], a[4 * q + 3]};
  };
  if (SG == SG_W3) {
    const u32x4 one4 = {0x3F803F80u, 0x3F803F80u, 0x3F803F80u, 0x3F803F80u};
    f32x16 cs = zero16;
#pragma unroll
    for (int k = 0; k < KS; ++k) cs = mfma3232(Wf[k], __builtin_bit_cast(bf16x8, one4), cs);
    if (kh == 0) put16(lds + wl * 4096 + l16, cs);
    LBAR();
    if (kh == 1 && row == 0) {
#pragma unroll
      for (int q = 0; q < 4; ++q)
        *(LAS f32x4*)(lds + G_K + wl * 128 + hf * 64 + q * 16) = *(const LAS f32x4*)(lds + wl * 4096 + q * 1024 + l16) + f32x4{cs[4 * q], cs[4 * q + 1], cs[4 * q + 2], cs[4 * q + 3]};
    }
    LBAR();
  }
  auto blk = [&](int i) -> int { return PROJ ? ((bstr == 3 && i >= 20) ? 40 + i : bstr * i + kq) : i; };
  const unsigned aoff = ROWM ? (unsigned)(row * 128 + ((hf ^ ((row >> 1) & 7)) << 4)) : (unsigned)((row >> 4) * 1024 + hf * 256 + (row & 15) * 16);
  const unsigned vA = ROWM ? (unsigned)((lane >> 3) * 2048 + (((lane & 7) ^ ((4 * wl + (lane >> 4)) & 7)) << 4)) : l16;
  auto piece = [&](int hb, int q, bool on) {
    const int i = hb >> 1, hh = hb & 1;
    const unsigned so = ABASE + (unsigned)blk(i) * ASTR + (unsigned)koff * (KS * 2048) + (ROWM ? wl * 16384 + hh * 1024 : (hh * KS + wl) * 1024);
    if (on) dma16x(rs, vA, so + q * (ROWM ? 128 : 4096), lbase + (i & 1) * 65536 + hh * 32768 + wl * 1024 + q * 4096);
  };
  auto fetch = [&](int hb) {
#pragma unroll
    for (int q = 0; q < NPC; ++q) piece(hb, q, true);
  };
  constexpr bool WEAVE = (SG == SG_W1 || SG == SG_W2 || SG == SG_W3);
  auto products = [&](int i, f32x16& acc, auto&& first, auto&& early, auto&& handover, bool soon) {
    const unsigned ub = lbase + (i & 1) * 65536 + kh * 32768;
    LAS unsigned char* ab[4];
    ab[0] = lad(aoff, ub);
    if (ROWM) { ab[1] = lad<32>(aoff, ub); ab[2] = lad<64>(aoff, ub); ab[3] = lad<96>(aoff, ub); }
    constexpr int PD = 10;
    bf16x8 af[PD + 1];
    auto rd = [&](int k) { af[k % (PD + 1)] = ROWM ? *(const LAS bf16x8*)(ab[k & 3] + (k >> 2) * 4096) : *(const LAS bf16x8*)(ab[0] + (k >> 1) * 2048 + (k & 1) * 512); };
#pragma unroll
    for (int k = 0; k < PD; ++k) rd(k);
    SCHED();
#pragma unroll
    for (int k = 0; k < KS; ++k) {
      if (k + PD < KS) rd(k + PD);
      acc = mfma3232(Wf[k], af[k % (PD + 1)], k ? acc : zero16);
      SCHED();
      first(k);
      SCHED();
      if (k == KS - 10) { early(); SCHED(); }
      if (k == 9) { if (soon) handover(); SCHED(); }
      if (k == KS - 5) { if (!soon) handover(); SCHED(); }
    }
    asm volatile("" : "+v"(acc));
  };
  WST(rs, wk, PROJ ? 2 : 10);
#define SEG(x)
  auto announce = [&](int i) {
    if (lane == 0) {
      const unsigned c = __hip_atomic_fetch_add((LAS unsigned*)(lds + G_CTL), 1u, __ATOMIC_RELAXED, __HIP_MEMORY_SCOPE_WORKGROUP);
      if ((c & 3u) == 3u) stx4(rs, 0, myword, (unsigned)blk(i) + 1u);
    }
  };
  if (kh == 0) {
    auto pvo = [&](int i) -> unsigned {
      const int b = blk(i);
      if (PROJ) return F_LN + (unsigned)((b % (NLN / 4)) * 4 + (lane & 3)) * 128u + (lane & 12);
      if (SG == SG_WO) return F_ST + (unsigned)(P_S1 + (b & 3) * 64 + kq * 16 + (lane & 15)) * 128u;
      if (SG == SG_W1) return F_ST + (unsigned)(P_S3 + (b % 3) * 32 + (lane & 31)) * 128u;
      if (SG == SG_W2) return F_ST + (unsigned)(P_S4 + (lane & 7)) * 128u;
      if (SG == SG_W3) return F_ST + (unsigned)(P_S5 + (lane & 7)) * 128u;
      return F_ST + (unsigned)(P_S7 + kq * 7 + min(lane & 7, 6)) * 128u;
    };
    auto need = [&](int i) -> unsigned { return PROJ ? 1u : (SG == SG_W2 && i == 64) ? 64u : (unsigned)i + 1u; };
    unsigned nl = 0;
    int R = 0, Rp = 0;
    for (int i = 0; i < NBLK; ++i) {
      if (R <= i) { wait_ge(rs, pvo(i), 0, need(i), cap); SEG(6); fetch(2 * i); fetch(2 * i + 1); SEG(7); }
      const unsigned pk = w == 0 ? (lane < 32 ? pvo(i + 1) : pvo(i + 2)) : 0xF0000000u;
      if (Rp <= i) { if (R <= i + 1) wait_v<NPC>(); else wait_v<2 * NPC>(); }
      if (R >= i + 2 || Rp > i) BAR();
      else {
        nl += 4;
        if (lane == 0) (void)__hip_atomic_fetch_add((LAS unsigned*)(lds + G_CTL + 8), 1u, __ATOMIC_RELAXED, __HIP_MEMORY_SCOPE_WORKGROUP);
        while (__builtin_amdgcn_readfirstlane(*(const volatile LAS unsigned*)(lds + G_CTL + 8)) < nl) {}
        asm volatile("" ::: "memory");
      }
      SEG(1);
      f32x16 acc;
      unsigned peek;
      asm volatile("buffer_load_dword %0, %1, %2, 0 offen sc1" : "=v"(peek) : "v"(pk), "s"(rs) : "memory");
      u32x4 st[8];
      float mean = 0.f, q2 = 0.f;
      if (SG == SG_W3) {
#pragma unroll
        for (int q = 0; q < 8; ++q) st[q] = ldx16(rs, l16, WS_ST5 + (unsigned)i * 8192 + q * 1024);
      }
      products(i, acc, [&](int k) {
        if (SG == SG_W3) {
          if (k >= 18 && k < 26) { mean += u2f(st[k - 18][0]) + u2f(st[k - 18][2]); q2 += u2f(st[k - 18][1]) + u2f(st[k - 18][3]); asm volatile("" : "+v"(mean), "+v"(q2)); }
          if (k == 26) { mean = hsum(mean) * (1.0f / 1024.0f); q2 = hsum(q2); asm volatile("" : "+v"(mean), "+v"(q2)); }
          if (k == 28) { q2 = rbf(rbf(fmaxf(fmaf(-mean, mean, q2 * (1.0f / 1024.0f)), 0.f)) + 1e-5f); asm volatile("" : "+v"(q2)); }
          if (k == 29) { q2 = __builtin_amdgcn_rcpf(rbf(__builtin_amdgcn_sqrtf(q2))); mean = rbf(mean); asm volatile("" : "+v"(q2), "+v"(mean)); }
        }
      }, []() {}, [&]() {
        SEG(4);
        WAIT_V(0);
        SEG(0);
        asm volatile("" : "+v"(peek));
        if (w == 0) {
          const unsigned long long ok = __ballot(peek >= (lane < 32 ? need(i + 1) : need(i + 2)));
          const bool r1 = (R >= i + 2 || (unsigned)ok == 0xFFFFFFFFu) && i + 1 < NBLK, r2 = r1 && (unsigned)(ok >> 32) == 0xFFFFFFFFu && i + 2 < NBLK;
          if (lane == 0) *(volatile LAS unsigned*)(lds + G_CTL + 4) = (unsigned)(i + 1 + (r1 ? 1 : 0) + (r2 ? 1 : 0));
        }
        LBAR();
      }, Rp <= i);
      SEG(2);
      const unsigned rv = *(const volatile LAS unsigned*)(lds + G_CTL + 4);
      if (SG == SG_W3) {
        const float m = mean;
        *(LAS float*)(lds + G_K + 1024 + wl * 256 + lane * 4) = q2;
#pragma unroll
        for (int q = 0; q < 4; ++q) {
          const f32x4 cs = *(const LAS f32x4*)(lds + G_K + wl * 128 + hf * 64 + q * 16);
#pragma unroll
          for (int e = 0; e < 4; ++e) acc[4 * q + e] = fmaf(-m, cs[e], acc[4 * q + e]);
        }
      }
      put16(lad(l16, lbase + G_XCH + wl * 4096), acc);
      asm volatile("" ::: "memory");
      if (lane == 0) *(volatile LAS unsigned*)(lds + G_CTL + 16 + wl * 4) = (unsigned)i + 1u;
      const bool soon = Rp <= i;
      Rp = R; R = __builtin_amdgcn_readfirstlane(rv);
      SEG(3);
      if (R >= i + 2 && Rp < i + 2) { fetch(2 * i + 2); fetch(2 * i + 3); }
      if (R >= i + 3) {
        if (soon) { while (__builtin_amdgcn_readfirstlane(umin4v(*(const volatile LAS u32x4*)(lds + G_CTL + 16))) <= (unsigned)i) {} asm volatile("" ::: "memory"); }
        fetch(2 * i + 4);
      }
      SEG(5);
    }
  } else {
    bool pending = false;
    int Rp = 0;
    for (int i = 0; i < NBLK; ++i) {
      const int b = blk(i);
      BAR();
      SEG(0);
      const unsigned rv = *(const volatile LAS unsigned*)(lds + G_CTL + 4);
      u32x4 res[2];
      float isd;
      if (SG == SG_W2) {
#pragma unroll
        for (int u = 0; u < 2; ++u) {
          const int t = 32 * b - 2 + 16 * u + (lane >> 2);
          dma16x(rs, (t >= 0 && t < S_) ? (unsigned)(t * D_ + col0 + ((lane ^ (lane >> 3)) & 3) * 8) * 2u : 0xF0000000u, WS_H2, lbase + G_K + wl * 2048 + u * 1024);
        }
      }
      f32x16 acc;
      int R = 0;
      LAS unsigned char* xr = lad(l16, lbase + G_XCH + wl * 4096);
      f32x4 xa[4];
      unsigned xf;
      auto take = [&]() {
        asm volatile("" ::: "memory");
        xf = *(const volatile LAS unsigned*)(lds + G_CTL + 16 + wl * 4);
        asm volatile("" ::: "memory");
#pragma unroll
        for (int q = 0; q < 4; ++q) xa[q] = *(const LAS f32x4*)(xr + q * 1024);
        if (SG == SG_W3) isd = *(const LAS float*)(lds + G_K + 1024 + wl * 256 + lane * 4);
        asm volatile("" ::: "memory");
      };
      products(i, acc, [&](int k) {
      }, [&]() {
        take();
      }, [&]() {
        SEG(5);
        R = __builtin_amdgcn_readfirstlane(rv);
        if (R >= i + 3 || Rp >= i + 2) {
          WAIT_V(0);
          LBAR();
          if (pending && tid == 256) stx4(rs, 0, myword, (unsigned)blk(i - 1) + 1u);
        }
      }, false);
      const bool more = R >= i + 3 || Rp >= i + 2;
      Rp = R;
      SEG(1);
      while (__builtin_amdgcn_readfirstlane(xf) <= (unsigned)i) take();
#pragma unroll
      for (int q = 0; q < 16; ++q) { acc[q] += xa[q >> 2][q & 3]; if (SG == SG_W3) acc[q] *= isd; }
      SEG(2);
      const unsigned fo = (unsigned)((col0 >> 5) * 2048 + hf * 256), ro = (unsigned)((row >> 4) * 1024 + (row & 15) * 16);
      u32x4 v[2];
      if (SG == SG_W1 || SG == SG_W3) {
#pragma unroll
        for (int q = 0; q < 16; q += 2) {
          float g0 = gelu_tanh(acc[q]), g1 = gelu_tanh(acc[q + 1]);
          asm volatile("" : "+v"(g0), "+v"(g1));
          acc[q] = g0; acc[q + 1] = g1;
          piece(2 * i + 5, q >> 1, R >= i + 3 && i >= 2);
        }
      }
      widen32(acc, v);
      if (SG == SG_WO || SG == SG_W4) {
        LAS unsigned char* tw = lds + G_XCH + wl * 4096 + row * 64;
        *(LAS u32x4*)(tw + ((hf ^ (row >> 1)) & 3) * 16) = v[0]; *(LAS u32x4*)(tw + (((2 + hf) ^ (row >> 1)) & 3) * 16) = v[1];
        asm volatile("" ::: "memory");
        LAS unsigned char* tr = lds + G_XCH + wl * 4096 + (lane >> 2) * 64 + ((lane ^ (lane >> 3)) & 3) * 16;
        const unsigned so = (SG == SG_WO ? WS_P2 : WS_P8) + (unsigned)(b * 4 + kq) * 65536u, vo = (unsigned)((lane >> 2) * D_ + col0 + (lane & 3) * 8) * 2u;
        stx16(rs, vo, so, *(const LAS u32x4*)(tr)); stx16(rs, vo, so + 32768, *(const LAS u32x4*)(tr + 1024));
      } else if (PROJ) {
        const unsigned so = dst + (unsigned)(b * NH + cb) * 8192 + wl * 1024, vo = (unsigned)((row >> 4) * 4096 + hf * 256 + (row & 15) * 16);
        stx16(rs, vo, so, v[0]); stx16(rs, vo, so + 512, v[1]);
      } else if (SG == SG_W1) {
        const int grp = cb >> 1, sh = (grp & 1) ? -(1 + (grp >> 1)) : (1 + (grp >> 1));
        const int rho = 32 * b + row + sh + 2;
        const unsigned vo = (rho >= 2 && rho < S_ + 2) ? (unsigned)(rho >> 5) * 65536u + fo + (unsigned)((rho >> 4) & 1) * 1024u + (unsigned)(rho & 15) * 16u : 0xF0000000u;
        stx16(rs, vo, WS_H1S, v[0]); stx16(rs, vo, WS_H1S + 512, v[1]);
      } else if (SG == SG_W2) {
        if (!more) WAIT_V(0);
        res[0] = *(const LAS u32x4*)(lds + G_K + wl * 2048 + row * 64 + ((hf ^ (row >> 1)) & 3) * 16);
        res[1] = *(const LAS u32x4*)(lds + G_K + wl * 2048 + row * 64 + (((2 + hf) ^ (row >> 1)) & 3) * 16);
        float zs = 0.f, q2 = 0.f;
#pragma unroll
        for (int q = 0; q < 8; ++q) {
          const unsigned x = pack2(lo2f(res[q >> 2][q & 3]) + lo2f(v[q >> 2][q & 3]), hi2f(res[q >> 2][q & 3]) + hi2f(v[q >> 2][q & 3]));
          v[q >> 2][q & 3] = x; zs += lo2f(x) + hi2f(x); q2 = fmaf(lo2f(x), lo2f(x), fmaf(hi2f(x), hi2f(x), q2));
          asm volatile("" : "+v"(zs), "+v"(q2));
          piece(2 * i + 5, q, R >= i + 3 && i >= 2);
        }
        stx16(rs, fo + ro, WS_X2 + (unsigned)b * 65536u, v[0]); stx16(rs, fo + ro, WS_X2 + (unsigned)b * 65536u + 512, v[1]);
        const float mw = hsum(zs);
        q2 = hsum(q2);
        if (hf == 0) stx8(rs, (unsigned)((cb * 2 + (wl >> 1)) * 512 + row * 16 + (wl & 1) * 8), WS_ST5 + (unsigned)b * 8192, u32x2{f2u(mw), f2u(q2)});
      } else {
        stx16(rs, fo + ro, WS_U + (unsigned)b * 229376u, v[0]); stx16(rs, fo + ro, WS_U + (unsigned)b * 229376u + 512, v[1]);
      }
      if (PROJ && bstr == 3) {
#pragma unroll
        for (int q = 0; q < 2; ++q) {
          const int pc = (wk * 4 + wl) * 2 + q;
          if (pc < 672 && 3 * i + ZA + pc / 224 <= NSEG) stx16(rs, l16, X_REC + (unsigned)(3 * i + ZA) * RSTR + (unsigned)pc * 1024, u32x4{0u, 0u, 0u, 0u});
        }
      }
      SEG(3);
      pending = R >= i + 3 && i >= 2;
      if (!pending) { WAIT_V(0); announce(i); }
      if (R >= i + 3 && !(WEAVE && i >= 2)) fetch(2 * i + 5);
      if (!PROJ) WST(rs, wk, 16 + b);
      SEG(4);
    }
  }
  LBAR();
}

__device__ __forceinline__ void wait32(__amdgpu_buffer_rsrc_t rs, int first, int mine, int lane, unsigned need, unsigned& cap) {
  wait_ge(rs, 0, F_ST + (unsigned)(first + mine) * 128u, need - 1u, cap);
  wait_ge(rs, (unsigned)(lane & 31) * 128u, F_ST + (unsigned)first * 128u, need, cap);
}
__device__ __forceinline__ void sum_partials(__amdgpu_buffer_rsrc_t rs, unsigned l16, unsigned so, float (&a)[16]) {
  u32x4 pp[4][2];
#pragma unroll
  for (int k = 0; k < 4; ++k) { pp[k][0] = ldx16(rs, l16, so + k * 65536u); pp[k][1] = ldx16(rs, l16, so + k * 65536u + 1024); }
#pragma unroll
  for (int i = 0; i < 8; ++i) {
    f32x2 t = {0.f, 0.f};
#pragma unroll
    for (int k = 0; k < 4; ++k) t += f32x2{lo2f(pp[k][i >> 2][i & 3]), hi2f(pp[k][i >> 2][i & 3])};
    a[2 * i] = t[0]; a[2 * i + 1] = t[1];
  }
}
__device__ __forceinline__ void put_row(__amdgpu_buffer_rsrc_t rs, unsigned l16, unsigned so, const float (&v)[16]) {
  stx16(rs, l16, so, u32x4{pack2(v[0], v[1]), pack2(v[2], v[3]), pack2(v[4], v[5]), pack2(v[6], v[7])});
  stx16(rs, l16, so + 1024, u32x4{pack2(v[8], v[9]), pack2(v[10], v[11]), pack2(v[12], v[13]), pack2(v[14], v[15])});
}
__device__ __forceinline__ void put_round(__amdgpu_buffer_rsrc_t rs, unsigned l16, unsigned so, float (&v)[16]) {
  u32x4 t[2];
#pragma unroll
  for (int i = 0; i < 8; ++i) { const unsigned x = pack2(v[2 * i], v[2 * i + 1]); t[i >> 2][i & 3] = x; v[2 * i] = lo2f(x); v[2 * i + 1] = hi2f(x); }
  stx16(rs, l16, so, t[0]); stx16(rs, l16, so + 1024, t[1]);
}
__device__ __forceinline__ void load_mod(__amdgpu_buffer_rsrc_t rs, int part, int lane, float (&v)[16], unsigned& cap) {
  for (;;) {
    asm volatile("" ::: "memory");
    u32x4 t[4];
#pragma unroll
    for (int i = 0; i < 4; ++i) t[i] = ldx16(rs, (unsigned)lane * 32, X_MOD + part * 4096 + (i >> 1) * 2048 + (i & 1) * 16);
    unsigned mn = 0xFFFFFFFFu;
#pragma unroll
    for (int i = 0; i < 16; ++i) { mn = min(mn, t[i >> 2][i & 3]); v[i] = hi2f(t[i >> 2][i & 3]); }
    if (__all(mn != 0u) || cap == 0u) break;
    cap = __builtin_amdgcn_readfirstlane(cap - 1u);
    __builtin_amdgcn_s_sleep(4);
  }
  cap = __builtin_amdgcn_readfirstlane(cap);
}
template <int NO>
__device__ __forceinline__ void mod_part(__amdgpu_buffer_rsrc_t rs, const u16* wmod, const u16* cv, LAS float* red, int col) {
  constexpr int KG = 512 / NO, NI = 1024 / KG;
  const int tid = threadIdx.x, kg = tid / NO, c8 = tid % NO;
  u32x4 wv[NI]; u16 cc[NI];
#pragma unroll
  for (int i = 0; i < NI; ++i) cc[i] = cv[kg + KG * i];
#pragma unroll
  for (int i = 0; i < NI; ++i) wv[i] = *(const u32x4*)(wmod + (size_t)(kg + KG * i) * 6144 + col + c8 * 8);
  SCHED();
  float acc[8] = {0.f, 0.f, 0.f, 0.f, 0.f, 0.f, 0.f, 0.f};
#pragma unroll
  for (int i = 0; i < NI; ++i) {
    const float v = bf2f(cc[i]), sc = rbf(v * __builtin_amdgcn_rcpf(1.0f + __builtin_amdgcn_exp2f(-1.4426950408889634f * v)));
#pragma unroll
    for (int j = 0; j < 4; ++j) { acc[2 * j] += sc * lo2f(wv[i][j]); acc[2 * j + 1] += sc * hi2f(wv[i][j]); }
  }
#pragma unroll
  for (int j = 0; j < 8; ++j) acc[j] = gsum(NO == 4 ? dpp_add<0x128>(dpp_add<0x124>(acc[j])) : acc[j]);
  if ((tid & 63) < NO) {
    LAS float* d = red + (tid >> 6) * 8 * NO + c8 * 8;
    *(LAS f32x4*)(d) = f32x4{acc[0], acc[1], acc[2], acc[3]}; *(LAS f32x4*)(d + 4) = f32x4{acc[4], acc[5], acc[6], acc[7]};
  }
  LBAR();
  if (tid < 8 * NO) {
    float u = 0.f;
#pragma unroll
    for (int i = 0; i < 8; ++i) u += red[i * 8 * NO + tid];
    stx4(rs, (unsigned)tid * 4, X_MOD + (unsigned)col * 4, f2u(rbf(u)) | 1u);
  }
}

__device__ __forceinline__ void stage_mid(__amdgpu_buffer_rsrc_t rs, const u16* x, int wk, int cu, unsigned& cap) {
  const int tid = threadIdx.x, lane = tid & 63, w = __builtin_amdgcn_readfirstlane(tid >> 6);
  const int id = w * 12 + cu, grp = id >> 5, rl = id & 31;
  const unsigned l16 = lane * 16;
  float ga[16], sc[16], sh[16];
  load_mod(rs, 2, lane, ga, cap); load_mod(rs, 4, lane, sc, cap); load_mod(rs, 3, lane, sh, cap);
#pragma unroll
  for (int i = 0; i < 16; ++i) sc[i] = rbf(1.0f + sc[i]);
  for (int seg = grp; seg < NSEG; seg += 3) {
    const int row = seg * 32 + rl;
    float v[16], a[16];
    load_row16(x + (size_t)row * D_, lane, v);
    wait32(rs, P_S2, rl, lane, (unsigned)seg + 1u, cap);
    sum_partials(rs, l16, WS_P2 + (unsigned)seg * 262144u + (unsigned)rl * 2048, a);
    float m, isd;
#pragma unroll
    for (int i = 0; i < 16; ++i) v[i] = fmaf(ga[i], a[i], v[i]);
    put_round(rs, l16, WS_X1 + (unsigned)row * 2048, v);
    ln_stats(v, m, isd);
#pragma unroll
    for (int i = 0; i < 16; ++i) v[i] = fmaf((v[i] - m) * isd, sc[i], sh[i]);
    put_round(rs, l16, WS_H2 + (unsigned)row * 2048, v);
    ln_stats(v, m, isd);
#pragma unroll
    for (int i = 0; i < 16; ++i) v[i] = (v[i] - m) * isd;
    put_row(rs, l16, WS_G + (unsigned)row * 2048, v);
    WAIT_V(0);
    if (lane == 0) stx4(rs, 0, F_ST + (unsigned)(P_S3 + grp * 32 + rl) * 128, (unsigned)seg + 1u);
    WST(rs, wk, 16 + seg);
  }
}

__device__ __forceinline__ void stage_out(__amdgpu_buffer_rsrc_t rs, u16* out, int wk, int cu, unsigned& cap) {
  const int tid = threadIdx.x, lane = tid & 63, w = __builtin_amdgcn_readfirstlane(tid >> 6);
  const int grp = w >> 2, rl = cu * 4 + (w & 3);
  const unsigned l16 = lane * 16;
  float gb[16];
  load_mod(rs, 5, lane, gb, cap);
  for (int b = grp; b < 65; b += 2) {
    const int t = 32 * b - 2 + rl;
    if (t < 0 || t >= S_) continue;
    wait_ge(rs, (unsigned)(lane & 7) * 128u, F_ST + P_S5 * 128u, (unsigned)b + 1u, cap);
    const unsigned xo = (unsigned)((lane >> 2) * 2048 + (lane & 3) * 256), xs = WS_X2 + (unsigned)b * 65536u + (unsigned)((rl >> 4) * 1024 + (rl & 15) * 16);
    const u32x4 x2a = ldx16(rs, xo, xs), x2b = ldx16(rs, xo, xs + 32768);
    const u32x4 x1a = ldx16(rs, l16, WS_X1 + (unsigned)t * 2048), x1b = ldx16(rs, l16, WS_X1 + (unsigned)t * 2048 + 1024);
    wait32(rs, P_S8, rl, lane, (unsigned)b + 1u, cap);
    float a[16];
    sum_partials(rs, l16, WS_P8 + (unsigned)b * 262144u + (unsigned)rl * 2048, a);
#pragma unroll
    for (int i = 0; i < 4; ++i) {
      a[2 * i] = fmaf(gb[2 * i], lo2f(x2a[i]) + a[2 * i], lo2f(x1a[i])); a[2 * i + 1] = fmaf(gb[2 * i + 1], hi2f(x2a[i]) + a[2 * i + 1], hi2f(x1a[i]));
      a[8 + 2 * i] = fmaf(gb[8 + 2 * i], lo2f(x2b[i]) + a[8 + 2 * i], lo2f(x1b[i])); a[9 + 2 * i] = fmaf(gb[9 + 2 * i], hi2f(x2b[i]) + a[9 + 2 * i], hi2f(x1b[i]));
    }
    store_row16(out + (size_t)t * D_, lane, a);
    WST(rs, wk, 16 + b);
  }
}

__device__ __forceinline__ void worker(const MainP& p, LAS unsigned char* lds, int xw, int j) {
  const int tid = threadIdx.x, lane = tid & 63, w = __builtin_amdgcn_readfirstlane(tid >> 6);
  const __amdgpu_buffer_rsrc_t rs = mk_rs(p.ws, WS_END);
  unsigned cap = SPIN_CAP;
  const int wk = xw * 32 + j;
  WST(rs, wk, 0);
  {
    const int c = xw < 3 ? j * 3 + xw : 90 + j * 2 + xw + 3, r0 = c * 8 + 2 * (w & 3);
    const bool two = r0 + 8 * NLN < S_;
    float v[4][16];
    if (c >= NLN - 64) mod_part<4>(rs, p.wmod, p.c, (LAS float*)(lds + G_XCH), (c - (NLN - 64)) * 32);
    WST(rs, wk, 5);
    if (w < 4) {
#pragma unroll
      for (int q = 0; q < 4; ++q) load_row16(p.x + (size_t)(r0 + (q & 1) + (two ? (q >> 1) * 8 * NLN : 0)) * D_, lane, v[q]);
    }
    if (w >= 4) {
      if (xw == 1 || xw == 2) {
        const __amdgpu_buffer_rsrc_t rsO = mk_rs(p.Wo, D_ * HD * 2);
#pragma unroll
        for (int q = 0; q < 32; ++q) dma16(rsO, (unsigned)lane * 16, (unsigned)((q * 64 + wk - 32) * 4 + (w & 3)) * 1024u, (unsigned)(size_t)lds + G_K + (w & 3) * 1024);
        wait_v<24>();
      }
      const __amdgpu_buffer_rsrc_t rsW = mk_rs(xw < 3 ? p.Wk : p.Wq, D_ * HD * 2);
      const int ub = lane >> 3;
      const unsigned vB = (unsigned)(((ub & 3) + 8 * (ub >> 2)) * HD + 16 * (((lane >> 1) & 3) ^ (ub >> 1)) + 8 * (lane & 1)) * 2u, lbase = (unsigned)(size_t)lds;
#pragma unroll
      for (int q = 0; q < 16; ++q) {
        const int wq = (w & 3) + 4 * (q >> 3);
        const unsigned so = (unsigned)((32 * (wq >> 2) + 16 * ((wq >> 1) & 1) + 4 * (wq & 1) + (((q >> 2) & 1) * 8 + (q & 3)) * 64) * HD + j * 128) * 2u;
        const unsigned d = lbase + ((q >> 2) & 1) * 65536 + (q & 3) * 16384 + wq * 1024;
        dma16n<(NTM & 16) != 0>(rsW, vB, so, d); dma16n<(NTM & 16) != 0>(rsW, vB, so + 128, d + 8192);
      }
    } else {
#pragma unroll
      for (int q = 0; q < 4; ++q) ln_row(v[q]);
      float sc[16], sh[16];
      load_mod(rs, 1, lane, sc, cap); load_mod(rs, 0, lane, sh, cap);
      WST(rs, wk, 6);
#pragma unroll
      for (int i = 0; i < 16; ++i) sc[i] = rbf(1.0f + sc[i]);
#pragma unroll
      for (int q = 0; q < 4; ++q) {
#pragma unroll
        for (int i = 0; i < 16; ++i) v[q][i] = rbf(rbf(v[q][i] * sc[i]) + sh[i]);
        if (q < 2 || two) put_row(rs, (unsigned)lane * 16, WS_H + (unsigned)(r0 + (q & 1) + (q >> 1) * 8 * NLN) * 2048, v[q]);
      }
      WAIT_V(0);
      if (lane == 0) stx4(rs, 0, F_LN + c * 128 + w * 4, 1u);
    }
    WST(rs, wk, 1);
  }
  {
    stage_gemm<SG_PJ>(lds, rs, xw < 3 ? p.Wk : p.Wq, wk, xw < 3 ? xw : xw == 3 ? QB0 : 0, j, P_PJ + min(xw, 3) * 32 + j + (xw == 4 ? 400 : 0), cap, xw < 3 ? WS_K : WS_Q, xw < 3 ? 3 : 1,
                      xw < 2 ? 20 : xw == 2 ? 24 : xw == 3 ? NSEG - QB0 : QB0);
    WST(rs, wk, 3);
  }
  typedef __attribute__((address_space(4))) const MainP* KArg;
  KArg kp = (KArg)__builtin_amdgcn_kernarg_segment_ptr();
  asm volatile("" : "+s"(kp));
  if (xw == 4) {
    auto sweep = [&](const u16* W, int kb) {
      const __amdgpu_buffer_rsrc_t rw = mk_rs(W, (unsigned)kb * 262144u);
      for (int q = 0; q < kb; ++q) dma16(rw, (unsigned)lane * 16, (unsigned)((q * 32 + j) * 8 + w) * 1024u, (unsigned)(size_t)lds + w * 1024);
    };
    sweep(kp->w1, 8); sweep(kp->w2, 8);
    mod_part<16>(rs, kp->wmod, kp->c, (LAS float*)(lds + G_XCH), 2048 + j * 128);
    sweep(kp->w3, 28);
  }
  if (xw == 0) stage_gemm<SG_WO>(lds, rs, kp->Wo, wk, j >> 3, j & 7, P_S2 + j, cap);
  else if (xw == 3) stage_gemm<SG_W4>(lds, rs, kp->w4, wk, j >> 3, j & 7, P_S8 + j, cap);
  else if (xw == 4) {
    unsigned chs = 0u;
    for (int sg = w >> 1; sg < NSEG; sg += 4) {
      attend(rs, sg, j, w & 1, lane, chs, cap);
      if ((sg >> 2) >= 3 && (sg >> 2) < 7) {
        const __amdgpu_buffer_rsrc_t rw = mk_rs(kp->w4, 28u * 262144u);
        for (int q = 0; q < 7; ++q) dma16(rw, (unsigned)lane * 16, (unsigned)((((sg >> 2) - 3) * 7 + q) * 256 + j * 8 + w) * 1024u, (unsigned)(size_t)lds + w * 1024);
      }
      WST(rs, wk, 16 + sg);
    }
  }
  else if (j >= 28) stage_out(rs, kp->out, wk, (xw - 1) * 4 + j - 28, cap);
  else if (xw == 2) stage_gemm<SG_W3>(lds, rs, kp->w3, wk, 0, j, P_S7 + j, cap);
  else if (j < 8) stage_gemm<SG_W1>(lds, rs, kp->w1, wk, 0, j, P_S4 + j, cap);
  else if (j < 16) stage_gemm<SG_W2>(lds, rs, kp->w2, wk, 0, j - 8, P_S5 + j - 8, cap);
  else stage_mid(rs, kp->x, wk, j - 16, cap);
  if (cap == 0u && lane == 0) stx4(rs, 0, X_CTL, 1u);
}

#define ST_DECL
#define ST0()
#define ST(i)
#define ST_FLUSH()
#define VSL 1
#define NL 2
constexpr int R_W = 0, R_MEM = 131072, R_QK = R_MEM + 16384, R_KT = R_QK + 8192, R_RED = R_MEM + 16384, R_LDS = 163840;
__device__ __forceinline__ void bar_lo(int w) { asm volatile("s_cmp_ge_u32 %0, 4\n\ts_cbranch_scc1 1f\n\ts_barrier\n\t1:" :: "s"(w) : "scc", "memory"); }
__device__ __forceinline__ void bar_hi(int w) { asm volatile("s_cmp_lt_u32 %0, 4\n\ts_cbranch_scc1 1f\n\ts_setprio 3\n\ts_barrier\n\t1:" :: "s"(w) : "scc", "memory"); }
#define ISSUE(R) asm volatile("buffer_load_dwordx4 v[" R "], %[vo], %[rs], %[so] offen sc1" : "=&{v[" R "]}"(a) : [vo] "v"(vo), [rs] "s"(rs), [so] "s"(so) : "memory")
template <int I> __device__ __forceinline__ void issue(__amdgpu_buffer_rsrc_t rs, unsigned vo, unsigned so, u32x4& a) {
  if (I == 0) ISSUE("240:243"); else if (I == 1) ISSUE("244:247"); else if (I == 2) ISSUE("248:251"); else ISSUE("252:255");
}
#define ARRIVE(R0, R1, R2, R3, CNT) \
  asm volatile("s_waitcnt vmcnt(" #CNT ")\n\t" \
               "1:\n\t" \
               "v_min3_u32 %[t], v" #R0 ", v" #R1 ", v" #R2 "\n\t" \
               "v_min_u32 %[t], %[t], v" #R3 "\n\t" \
               "v_cmp_eq_u32 vcc, 0, %[t]\n\t" \
               "s_cbranch_vccz 2f\n\t" \
               "s_mov_b64 exec, vcc\n\t" \
               "buffer_load_dwordx4 v[" #R0 ":" #R3 "], %[vo], %[rs], %[so] offen sc1\n\t" \
               "s_mov_b64 exec, -1\n\t" \
               "s_waitcnt vmcnt(0)\n\t" \
               "s_sub_u32 %[cap], %[cap], 1\n\t" \
               "s_cbranch_scc0 1b\n\t" \
               "s_mov_b32 %[cap], 0\n\t" \
               "2:" \
               : "+{v[" #R0 ":" #R3 "]}"(a), [t] "=&v"(t), [cap] "+s"(cap) : [vo] "v"(vo), [rs] "s"(rs), [so] "s"(so) : "vcc", "scc", "memory")
template <int I> __device__ __forceinline__ void arrive(__amdgpu_buffer_rsrc_t rs, unsigned vo, unsigned so, u32x4& a, unsigned& cap) {
  unsigned t;
  if (I == 0) ARRIVE(240, 241, 242, 243, 3);
  else if (I == 1) ARRIVE(244, 245, 246, 247, 2);
  else if (I == 2) ARRIVE(248, 249, 250, 251, 1);
  else ARRIVE(252, 253, 254, 255, 0);
}
__device__ __forceinline__ unsigned half_of(unsigned v) { unsigned r; asm volatile("v_lshrrev_b32 %0, 1, %1" : "=v"(r) : "v"(v)); return r; }
__device__ __forceinline__ void issue2(__amdgpu_buffer_rsrc_t rs, unsigned l8, unsigned so, int w, u32x4& a) {
  asm volatile("s_cmp_ge_u32 %[w], 4\n\ts_cbranch_scc1 1f\n\tv_lshrrev_b32 v255, 1, %[l8]\n\t"
               "buffer_load_dwordx2 v[252:253], v255, %[rs], %[so] offen sc1\n\tbuffer_load_dwordx2 v[254:255], v255, %[rs], %[so] offen offset:512 sc1\n\t1:"
               : "=&{v[252:255]}"(a) : [l8] "v"(l8), [rs] "s"(rs), [so] "s"(so), [w] "s"(w) : "scc", "memory");
}
__device__ __forceinline__ void arrive2(__amdgpu_buffer_rsrc_t rs, unsigned l8, unsigned so, u32x4& a, unsigned& cap) {
  unsigned t;
  asm volatile("s_waitcnt vmcnt(0)\n\t"
               "1:\n\t"
               "v_min3_u32 %[t], v252, v253, v254\n\t"
               "v_min_u32 %[t], %[t], v255\n\t"
               "v_cmp_eq_u32 vcc, 0, %[t]\n\t"
               "s_cbranch_vccz 2f\n\t"
               "v_lshrrev_b32 %[t], 1, %[l8]\n\t"
               "buffer_load_dwordx2 v[252:253], %[t], %[rs], %[so] offen sc1\n\t"
               "buffer_load_dwordx2 v[254:255], %[t], %[rs], %[so] offen offset:512 sc1\n\t"
               "s_waitcnt vmcnt(0)\n\t"
               "s_sub_u32 %[cap], %[cap], 1\n\t"
               "s_cbranch_scc0 1b\n\t"
               "s_mov_b32 %[cap], 0\n\t"
               "2:"
               : "+{v[252:255]}"(a), [t] "=&v"(t), [cap] "+s"(cap) : [l8] "v"(l8), [rs] "s"(rs), [so] "s"(so) : "vcc", "scc", "memory");
}
__device__ __forceinline__ unsigned ror8(unsigned v) { return __builtin_amdgcn_update_dpp(0u, v, 0x128, 0xF, 0xF, true); }
template <class F>
__device__ __forceinline__ void gather_mem(__amdgpu_buffer_rsrc_t rs, unsigned lbase, int s, int w, unsigned l16, unsigned& cap, F&& meanwhile) {
  const unsigned so = X_MEM + s * RSTR + w * 2048;
  u32x4 a0, a1;
  issue<2>(rs, l16, so, a0); issue<3>(rs, l16, so + 1024, a1);
  meanwhile();
  LAS unsigned char* d = lad(l16, lbase + R_MEM + w * 2048);
  arrive<2>(rs, l16, so, a0, cap);
  *(LAS u32x4*)(d) = a0;
  arrive<3>(rs, l16, so + 1024, a1, cap);
  *(LAS u32x4*)(d + 1024) = a1;
}
__device__ __forceinline__ unsigned mem_off(int lane) { return (unsigned)((lane & 1) * 512 + (((lane >> 1) ^ ((lane & 1) * 16)) * 8)); }
__device__ __forceinline__ bf16x8 mem_frag(LAS unsigned char* mp, int chunk) { return cat8(trread(mp + chunk * 1024), trread(mp + chunk * 1024 + 256)); }
__device__ __forceinline__ bf16x16 w_frag(LAS unsigned char* p) {
  return cat16(cat8(trread(p), trread(p + 2048)), cat8(trread(p + 4096), trread(p + 6144)));
}
__device__ __forceinline__ bf16x16 l_frag(LAS unsigned char* p, int i) { return cat16(*(const LAS bf16x8*)(p + i * 2048), *(const LAS bf16x8*)(p + i * 2048 + 1024)); }
__device__ __forceinline__ void make_resident(LAS unsigned char* wb, unsigned lo, unsigned l16) {
#pragma unroll
  for (int c = 0; c < 2; ++c) {
    bf16x16 f[4];
#pragma unroll
    for (int t = 0; t < 4; ++t) f[t] = w_frag(wb + c * 8192 + (lo ^ (t << 5)));
    WAIT_L0(); SCHED();
#pragma unroll
    for (int t = 0; t < 4; ++t) {
      *(LAS bf16x8*)(wb + (c * 4 + t) * 2048 + l16) = __builtin_shufflevector(f[t], f[t], 0, 1, 2, 3, 4, 5, 6, 7);
      *(LAS bf16x8*)(wb + (c * 4 + t) * 2048 + 1024 + l16) = __builtin_shufflevector(f[t], f[t], 8, 9, 10, 11, 12, 13, 14, 15);
    }
    WAIT_L0(); SCHED();
  }
}
__device__ __forceinline__ f32x4 fold(f32x4 a, f32x4 b) {
  f32x4 r;
#pragma unroll
  for (int i = 0; i < 4; ++i) {
    const auto t = __builtin_amdgcn_permlane32_swap(f2u(a[i]), f2u(b[i]), false, false);
    r[i] = u2f(t[0]) + u2f(t[1]);
  }
  return r;
}

struct Sched { unsigned tau, og, sa; int ea, eb; };
template <int CTRL> __device__ __forceinline__ int dpp_max(int v) { return max(v, (int)__builtin_amdgcn_update_dpp((unsigned)v, (unsigned)v, CTRL, 0xF, 0xF, false)); }
__device__ __forceinline__ int row_max(int v) { v = dpp_max<0xB1>(v); v = dpp_max<0x4E>(v); v = dpp_max<0x141>(v); return dpp_max<0x140>(v); }
__device__ __forceinline__ void times_issue(__amdgpu_buffer_rsrc_t rs, unsigned l16, int s, unsigned& xb, unsigned& xa) {
  asm volatile("v_lshlrev_b32 %1, 3, %2\n\tbuffer_load_dword %0, %1, %3, %4 offen sc1\n\tbuffer_load_dword %1, %1, %3, %5 offen sc1"
               : "=&v"(xb), "=&v"(xa) : "v"(l16), "s"(rs), "s"(X_TS + (unsigned)s * RSTR), "s"(X_TS + (unsigned)s * RSTR + 8192) : "memory");
}
__device__ __forceinline__ void retime(__amdgpu_buffer_rsrc_t rs, unsigned l16, int s, unsigned xb, unsigned xa, Sched& t, const MainP& p, unsigned& cap) {
  while (!__all(s > 0 ? min(xa, xb) != 0u : (xa != 0u || l16 < 512)) && cap != 0u) {
    times_issue(rs, l16, s, xb, xa);
    asm volatile("s_waitcnt vmcnt(0)" : "+v"(xb), "+v"(xa) :: "memory");
    cap = __builtin_amdgcn_readfirstlane(cap - 1u);
  }
  cap = __builtin_amdgcn_readfirstlane(cap);
  const unsigned base = __builtin_amdgcn_readlane(xa, 32);
  const int vb = row_max((int)(xb - base)), va = row_max((int)(xa - base));
  const unsigned sa = base + max(__builtin_amdgcn_readlane(va, 32), __builtin_amdgcn_readlane(va, 48));
  if (s > 1) {
    const int ta = (int)(base - t.sa) + max(__builtin_amdgcn_readlane(va, 0), __builtin_amdgcn_readlane(va, 16));
    const int tb = (int)(base - t.og) + max(max(__builtin_amdgcn_readlane(vb, 0), __builtin_amdgcn_readlane(vb, 16)), max(__builtin_amdgcn_readlane(vb, 32), __builtin_amdgcn_readlane(vb, 48)));
    if (ta + p.d3 <= (int)(t.og - t.sa)) t.eb = max(t.eb - p.d4, min(tb * 8, t.eb + p.d4));
    t.ea = max(t.ea - p.d4, min(ta * 8, t.ea + p.d4));
  }
  t.sa = sa;
  t.og = sa + (t.ea >> 3) + p.d1;
  t.tau = t.og + (t.eb >> 3) + p.d2;
}

__device__ __forceinline__ void role_a(const MainP& p, LAS unsigned char* lds, int h) {
  const int tid = threadIdx.x, lane = tid & 63, w = __builtin_amdgcn_readfirstlane(tid >> 6);
  const int cg = w & 3, kh = w >> 2;
  const __amdgpu_buffer_rsrc_t rs = mk_rs(p.ws, WS_END);
  const unsigned lbase = (unsigned)(size_t)lds;
  unsigned l16 = lane * 16;
  asm volatile("" : "+v"(l16));
  const int sidx = (lane & 8) ? 0xEEEE : 0x4444;
  bf16x16 W[24];
  unsigned qoff;
  {
    const int fr = lane & 15, g = lane >> 4, tu = 4 * (g & 1) + (fr >> 2), ub = lane >> 3;
    const unsigned lo = (g >> 1) * 1024 + tu * 128 + ((tu >> 1) << 5) + (fr & 3) * 8;
    qoff = R_QK + ((fr >> 1) & 1) * 512 + (g >> 1) * 256 + (fr & 1) * 128 + (8 * (g & 1) + (fr >> 2)) * 8;
    const __amdgpu_buffer_rsrc_t rw = mk_rs(cg < 2 ? p.Wq : p.Wk, D_ * HD * 2);
    const unsigned vo = (unsigned)(((ub & 3) + 8 * (ub >> 2)) * HD + 16 * (((lane >> 1) & 3) ^ (ub >> 1)) + 8 * (lane & 1)) * 2u;
    const unsigned so = (unsigned)(kh * 512 * HD + h * DH + (cg & 1) * 64) * 2u;
    auto stage = [&](int c) {
#pragma unroll
      for (int rho = 0; rho < 8; ++rho) dma16n<(NTM & 2) != 0>(rw, vo, so + (unsigned)((c * 64 + 32 * (rho >> 2) + 16 * (rho & 1) + 4 * ((rho >> 1) & 1)) * HD) * 2u, lbase + R_W + w * 16384 + (c & 1) * 8192 + rho * 1024);
    };
    stage(0); stage(1);
#pragma unroll
    for (int c = 0; c < 6; ++c) {
      WAIT_V(8);
#pragma unroll
      for (int t = 0; t < 4; ++t) W[4 * c + t] = w_frag(lds + R_W + w * 16384 + (c & 1) * 8192 + (lo ^ (t << 5)));
      WAIT_L0(); SCHED();
      stage(c + 2);
    }
    WAIT_V(0);
    make_resident(lds + R_W + w * 16384, lo, l16);
  }
  WST(rs, 160 + (h >> 1), 4);
  unsigned cap = SPIN_CAP;
  wait_ge(rs, 0, F_ST + (unsigned)(P_PJ + h) * 128, 1u, cap);
  dma16x(rs, l16, WS_K + h * 8192 + w * 1024, lbase + R_KT + w * 1024);
  Sched tt; tt.tau = rt32(); tt.og = tt.sa = tt.tau; tt.ea = 127 * 8; tt.eb = 108 * 8;
  const int cu_ = h; (void)cu_;
  ST_DECL
  for (int s = 0; s < NSEG; ++s) {
    LAS unsigned char* lp = lad(l16, lbase + R_W + w * 16384);
    bf16x16 L[NL];
#pragma unroll
    for (int i = 0; i < NL; ++i) L[i] = l_frag(lp, i);
    unsigned lz = l16;
    asm volatile("" : "+v"(lz));
    LAS unsigned char* mp = lad(mem_off((int)(lz >> 4)), lbase + R_MEM + kh * 8192);
    SCHED();
    if (w == 7) wait_until(tt.tau);
    BAR();
    ST0();
    gather_mem(rs, lbase, s, (w + h) & 7, l16, cap, []() {});
    ST(1);
    LBAR();
    asm volatile("s_cmp_lg_u32 %0, 3\n\ts_cbranch_scc1 1f\n\ts_memrealtime vcc\n\ts_waitcnt lgkmcnt(0)\n\ts_max_u32 vcc_lo, vcc_lo, 1\n\tv_mov_b32 v255, vcc_lo\n\t"
                 "s_mov_b64 exec, 1\n\tbuffer_store_dword v255, off, %1, %2 sc1\n\ts_mov_b64 exec, -1\n\t1:"
                 :: "s"(w), "s"(rs), "s"(X_TS + (unsigned)s * RSTR + (unsigned)(96 + h) * 128) : "vcc", "scc", "v255", "memory");
    ST(2);
    f32x4 c[4];
#pragma unroll
    for (int t = 0; t < 4; ++t) c[t] = f32x4{0.f, 0.f, 0.f, 0.f};
    bf16x8 al, af[2];
    u32x4 vt0, vt1;
    u32x4 vmv;
    const unsigned vso = X_VM + (unsigned)s * RSTR + h * 4096 + cg * 1024;
    al = mem_frag(mp, 6); af[0] = mem_frag(mp, 0); af[1] = mem_frag(mp, 1);
#pragma unroll
    for (int i = 0; i < 8; ++i) {
      c[i & 3] = smf(al, L[i % NL], c[i & 3], sidx);
      SCHED();
      if (i == 3) al = mem_frag(mp, 7);
      if (i + NL < 8) L[i % NL] = l_frag(lp, i + NL);
      if (i == 8 - NL) {
        const unsigned vo = WS_VT + (unsigned)(s * NH + h) * 8192 + cg * 2048;
        asm volatile("s_cmp_ge_u32 %5, 4\n\ts_cbranch_scc1 1f\n\t"
                     "buffer_load_dwordx4 %0, %2, %3, %4 offen sc1\n\tbuffer_load_dwordx4 %1, %2, %3, %4 offen offset:1024 sc1\n\t1:"
                     : "=&v"(vt0), "=&v"(vt1) : "v"(l16), "s"(rs), "s"(vo), "s"(w) : "scc", "memory");
      }
      SCHED();
#pragma unroll
      for (int r = 3 * i; r < 3 * i + 3; ++r) {
        c[(r + 1) & 3] = smf(af[(r >> 2) & 1], W[(r & ~3) + ((r + 1) & 3)], c[(r + 1) & 3], sidx);
        SCHED();
        if ((r & 3) == 3 && (r >> 2) + 2 < 6) af[(r >> 2) & 1] = mem_frag(mp, (r >> 2) + 2);
        SCHED();
      }
    }
    {
      LAS unsigned char* qw = lad(half_of(l16), lbase + R_QK + cg * 2048 + kh * 512);
      const f32x4 f0 = fold(c[0], c[1]);
      *(LAS u32x2*)(qw) = u32x2{pack2(f0[0], f0[1]), pack2(f0[2], f0[3])};
      const f32x4 f1 = fold(c[2], c[3]);
      *(LAS u32x2*)(qw + 1024) = u32x2{pack2(f1[0], f1[1]), pack2(f1[2], f1[3])};
    }
    ST(3);
    if (w < 4) {
      bf16x8 k1[4], k2[4];
      LAS unsigned char* kb = lad(l16, lbase + R_KT);
#pragma unroll
      for (int ks = 0; ks < 2; ++ks) { k1[ks] = *(const LAS bf16x8*)(kb + ks * 1024); k2[ks] = *(const LAS bf16x8*)(kb + 4096 + ks * 1024); }
      unsigned ov;
      {
        unsigned ot;
        asm volatile("v_and_b32 %0, 0x30, %2\n\tv_lshrrev_b32 %1, 2, %2\n\tv_and_b32 %1, 0xC0, %1\n\tv_or_b32 %0, %0, %1\n\t"
                     "v_and_b32 %1, 0x40, %2\n\tv_lshl_or_b32 %0, %1, 9, %0\n\tv_and_b32 %1, 0x80, %2\n\tv_lshl_or_b32 %0, %1, 24, %0"
                     : "=&v"(ov), "=&v"(ot) : "v"(l16));
      }
      if (VSL >= 0) { if (VSL > 0) __builtin_amdgcn_s_sleep(VSL > 0 ? VSL : 1); issue2(rs, l16, vso, w, vmv); }
      LBAR();
      ST(4);
      if (VSL < 0) issue2(rs, l16, vso, w, vmv);
      f32x4 t0 = {0.f, 0.f, 0.f, 0.f}, t1 = t0, t2 = t0;
      LAS unsigned char* qb = lad(qoff, lbase);
      bf16x8 qf[4], kf[4];
#pragma unroll
      for (int ks = 0; ks < 2; ++ks) {
        qf[ks] = cat8(trread(qb + ks * 1024), trread(qb + ks * 1024 + 32));
        kf[ks] = cat8(trread(qb + 4096 + ks * 1024), trread(qb + 4096 + ks * 1024 + 32));
      }
      SCHED();
#pragma unroll
      for (int ks = 0; ks < 4; ++ks) {
        t1 = mfma32(k1[ks], qf[ks], t1); t2 = mfma32(k2[ks], qf[ks], t2); t0 = mfma32(kf[ks], qf[ks], t0);
        SCHED();
        if (ks < 2) {
          qf[ks + 2] = cat8(trread(qb + (ks + 2) * 1024), trread(qb + (ks + 2) * 1024 + 32));
          k1[ks + 2] = *(const LAS bf16x8*)(kb + (ks + 2) * 1024); k2[ks + 2] = *(const LAS bf16x8*)(kb + 4096 + (ks + 2) * 1024);
          kf[ks + 2] = cat8(trread(qb + 4096 + (ks + 2) * 1024), trread(qb + 4096 + (ks + 2) * 1024 + 32));
        }
        SCHED();
      }
      ST(5);
      asm volatile("s_waitcnt vmcnt(2)" : "+v"(vt0), "+v"(vt1) :: "memory");
      {
        const float sl = 0.08838834764831845f * 1.4426950408889634f;
        float z[4] = {t1[0], t1[1], t1[2], t1[3]}, y = t0[0], mx;
        {
          float u1 = t0[1], u2 = t0[2], u3 = t0[3];
#define M8(Z, B) "v_add_f32_dpp " Z ", " Z ", " Z " row_ror:8 row_mask:0xf bank_mask:0x3\n\tv_add_f32_dpp " Z ", " B ", " B " row_ror:8 row_mask:0xf bank_mask:0xc\n\t"
          asm volatile("s_nop 7\n\t"
              "v_permlane32_swap_b32 %4, %6\n\tv_permlane32_swap_b32 %5, %7\n\t"
              M8("%0", "%9") "v_add_f32 %4, %4, %6\n\tv_add_f32 %5, %5, %7\n\t" M8("%1", "%10") M8("%2", "%11") M8("%4", "%5") M8("%3", "%12")
              "v_max3_f32 %8, %0, %1, %2\n\tv_max3_f32 %8, %8, %4, %3\n\ts_nop 1\n\t"
              "v_max_f32_dpp %8, %8, %8 row_ror:8 row_mask:0xf bank_mask:0xf\n\tv_mov_b32 %5, %8\n\ts_nop 1\n\t"
              "v_permlane16_swap_b32 %8, %5\n\tv_max_f32 %8, %8, %5\n\tv_mov_b32 %5, %8\n\ts_nop 1\n\t"
              "v_permlane32_swap_b32 %8, %5\n\tv_max_f32 %8, %8, %5"
              : "+v"(z[0]), "+v"(z[1]), "+v"(z[2]), "+v"(z[3]), "+v"(y), "+v"(u1), "+v"(u2), "+v"(u3), "=&v"(mx) : "v"(t2[0]), "v"(t2[1]), "v"(t2[2]), "v"(t2[3]));
#undef M8
        }
        const float nm = -mx * sl;
        y = __builtin_amdgcn_exp2f(fmaf(y, sl, nm));
#pragma unroll
        for (int r = 0; r < 4; ++r) z[r] = __builtin_amdgcn_exp2f(fmaf(z[r], sl, nm));
        const unsigned pm1 = pack2(y, u2f(ror8(f2u(y))));
        const auto pms = __builtin_amdgcn_permlane32_swap(pm1, pm1, false, false);
        const u32x2 pm = {pms[0], pms[1]};
        const unsigned p0 = pack2(z[0], z[1]), p1 = pack2(z[2], z[3]);
        const u32x4 pt = {p0, p1, ror8(p0), ror8(p1)};
        f32x4 o0 = mfma32(__builtin_bit_cast(bf16x8, vt0), __builtin_bit_cast(bf16x8, pt), f32x4{0.f, 0.f, 0.f, 0.f});
        f32x4 o1 = mfma32(__builtin_bit_cast(bf16x8, vt1), __builtin_bit_cast(bf16x8, pt), f32x4{0.f, 0.f, 0.f, 0.f});
        float inv = __builtin_amdgcn_rcpf(gsum(dpp_add<0x128>(((z[0] + z[1]) + (z[2] + z[3])) + y)));
        asm volatile("" : "+v"(inv));
        arrive2(rs, l16, vso, vmv, cap);
        const f32x4 n0 = mfma16n(u32x2{vmv[0], vmv[1]}, pm, f32x4{0.f, 0.f, 0.f, 0.f}), n1 = mfma16n(u32x2{vmv[2], vmv[3]}, pm, f32x4{0.f, 0.f, 0.f, 0.f});
        SCHED();
        o0 *= inv; o1 *= inv;
        {
          const u32x4 v = {nz(pack2(fmaf(n0[0], inv, o0[0]), fmaf(n0[1], inv, o0[1]))), nz(pack2(fmaf(n0[2], inv, o0[2]), fmaf(n0[3], inv, o0[3]))),
                           nz(pack2(fmaf(n1[0], inv, o1[0]), fmaf(n1[1], inv, o1[1]))), nz(pack2(fmaf(n1[2], inv, o1[2]), fmaf(n1[3], inv, o1[3])))};
          stx16(rs, ov, X_OX + s * RSTR + h * 1024 + w * 256, v);
        }
        if (w == 3) { const unsigned t = max(rt32(), 1u); if (l16 == 0) stx4(rs, 0, X_TS + (unsigned)(s + 1) * RSTR + (unsigned)(64 + h) * 128, t); }
        ST(8);
      }
    } else {
      LBAR();
      LAS unsigned char* qb = lad(qoff, lbase + 4096 + (w - 4) * 1024);
      u32x4 kf = __builtin_bit_cast(u32x4, cat8(trread(qb), trread(qb + 32)));
#pragma unroll
      for (int i = 0; i < 4; ++i) kf[i] = pack2(dpp_add<0x128>(lo2f(kf[i])), dpp_add<0x128>(hi2f(kf[i])));
      stx16(rs, l16, WS_KM + (unsigned)(s * NH + h) * 4096 + (w - 4) * 1024, kf);
      WAIT_V(0);
    }
    LBAR();
    if (w == 4 && l16 == 0) stx4(rs, 0, F_CH + h * 128, (unsigned)s + 1u);
    ST(9);
    if (w == 7) {
      unsigned xb, xa;
      times_issue(rs, l16, s, xb, xa);
      asm volatile("s_waitcnt vmcnt(0)" : "+v"(xb), "+v"(xa) :: "memory");
      retime(rs, l16, s, xb, xa, tt, p, cap);
    }
    if (s + 1 < NSEG) {
      unsigned fo;
      asm volatile("v_mov_b32 %0, 0" : "=v"(fo));
      wait_ge(rs, fo, F_ST + (unsigned)(P_PJ + k_owner(s + 1) * 32 + h) * 128, (unsigned)s + 2u, cap);
      dma16x(rs, l16, WS_K + (unsigned)((s + 1) * NH + h) * 8192 + w * 1024, lbase + R_KT + w * 1024);
    }
  }
  ST_FLUSH();
  if (cap == 0u && l16 == 0) stx4(rs, 0, X_CTL, 1u);
}

__device__ __forceinline__ void role_vb(const MainP& p, LAS unsigned char* lds, int rg, int cb) {
  const int tid = threadIdx.x, lane = tid & 63, w = __builtin_amdgcn_readfirstlane(tid >> 6);
  const int kh = w >> 2, wr = (w + cb) & 7;
  const __amdgpu_buffer_rsrc_t rs = mk_rs(p.ws, WS_END);
  const unsigned lbase = (unsigned)(size_t)lds;
  unsigned l16 = lane * 16, moff = mem_off(lane), roff, soff = (unsigned)((lane >> 5) * 256 + (lane & 31) * 4);
  const int sidx = (lane & 8) ? 0xEEEE : 0x4444;
  bf16x16 WV[8], WB[16];
  {
    const int fr = lane & 15, g = lane >> 4, tu = 4 * (g & 1) + (fr >> 2), ub = lane >> 3;
    { const int sl = mem_slot(16 * (g >> 1) + fr); roff = R_RED + w * 512 + ((sl >> 5) * 32 + (sl & 15) * 2 + (g & 1)) * 8; }
#pragma unroll
    for (int q = 0; q < 4; ++q) {
      const unsigned pc = 16u + (unsigned)(q * 512 + (rg * 32 + cb) * 8 + w);
      if (pc < ZA * 224u) stx16(rs, l16, X_REC + pc * 1024u, u32x4{0u, 0u, 0u, 0u});
    }
    const __amdgpu_buffer_rsrc_t rv = mk_rs(p.Wv, D_ * HD * 2), ro = mk_rs(p.Wo, D_ * HD * 2);
    const unsigned vv = (unsigned)(((ub & 3) + 8 * (ub >> 2)) * HD + 16 * (((lane >> 1) & 3) ^ (ub >> 1)) + 8 * (lane & 1)) * 2u;
    const unsigned sv = (unsigned)((32 * (w >> 2) + 16 * (w & 1) + 4 * ((w >> 1) & 1)) * HD + cb * DH + rg * 64) * 2u;
#pragma unroll
    for (int c = 0; c < 16; ++c) dma16n<(NTM & 1) != 0>(rv, vv, sv + (unsigned)c * 64 * HD * 2, lbase + R_W + c * 8192 + w * 1024);
    WAIT_V(0); BAR();
    const unsigned lo = (g >> 1) * 1024 + tu * 128 + (((tu >> 1) ^ (w & 3)) << 5) + (fr & 3) * 8;
#pragma unroll
    for (int c = 0; c < 8; ++c) WV[c] = w_frag(lds + R_W + (kh * 8 + c) * 8192 + lo);
    LBAR();
    const int u = (lane >> 2) & 7;
    const unsigned vw = (unsigned)((16 * (u >> 2) + 8 * ((u >> 1) & 1) + 4 * (lane >> 5) + (u & 1)) * D_ + (lane & 3) * 8) * 2u;
    const unsigned sw = (unsigned)(wr * 512 * D_ + cb * 32) * 2u;
    const unsigned bo = (g >> 1) * 1024 + tu * 64 + (fr & 3) * 8;
    auto stageo = [&](int hd) {
#pragma unroll
      for (int q = 0; q < 8; ++q)
        dma16n<(NTM & 4) != 0>(ro, vw, sw + (unsigned)((hd * 128 + 64 * (q >> 2) + 32 * (q & 1) + 2 * ((q >> 1) & 1)) * D_) * 2u, lbase + R_W + w * 16384 + (hd & 1) * 8192 + q * 1024);
    };
    stageo(0); stageo(1);
#pragma unroll
    for (int hd = 0; hd < 4; ++hd) {
      if (hd < 3) wait_v<8>(); else wait_v<0>();
#pragma unroll
      for (int f = 0; f < 4; ++f) WB[hd * 4 + f] = w_frag(lds + R_W + w * 16384 + (hd & 1) * 8192 + (f >> 1) * 512 + (f & 1) * 32 + bo);
      WAIT_L0(); SCHED();
      if (hd < 2) stageo(hd + 2);
    }
  }
  WST(rs, 176 + rg * 8 + (cb >> 2), 4);
  unsigned hsrc = (unsigned)((lane >> 3) * 2048 + (((lane & 7) ^ (lane >> 3)) << 4)), hoff = (unsigned)((lane & 15) * 128 + (((lane >> 4) ^ (lane & 7)) << 4));
  asm volatile("" : "+v"(l16), "+v"(moff), "+v"(roff), "+v"(soff), "+v"(hsrc), "+v"(hoff));
  unsigned cap = SPIN_CAP;
  auto h_piece = [&](int b, int q) { dma16x(rs, hsrc, WS_H + (unsigned)b * 65536u + (w & 3) * 16384 + (2 * q + kh) * 128, lbase + R_W + (b & 1) * 65536 + (q * 8 + w) * 1024); };
  auto h_fetch = [&](int b) {
#pragma unroll
    for (int q = 0; q < 8; ++q) h_piece(b, q);
  };
  f32x4 x0, x1;
  auto v_part = [&](int b, auto part, auto&& each) {
    constexpr int K0 = decltype(part)::value * 8;
    LAS unsigned char* a0 = lad(hoff, lbase + R_W + (b & 1) * 65536 + kh * 32768);
    LAS unsigned char* a1 = lad<64>(hoff, lbase + R_W + (b & 1) * 65536 + kh * 32768);
    bf16x8 af[3][2];
    auto rd = [&](int k) { LAS unsigned char* q = ((k & 1) ? a1 : a0) + (k >> 1) * 4096; af[k % 3][0] = *(const LAS bf16x8*)(q); af[k % 3][1] = *(const LAS bf16x8*)(q + 2048); };
    rd(K0); rd(K0 + 1);
    SCHED();
#pragma unroll
    for (int k = K0; k < K0 + 8; ++k) {
      if (k + 2 < K0 + 8) rd(k + 2);
      const bf16x8 wf = (k & 1) ? __builtin_shufflevector(WV[k >> 1], WV[k >> 1], 8, 9, 10, 11, 12, 13, 14, 15) : __builtin_shufflevector(WV[k >> 1], WV[k >> 1], 0, 1, 2, 3, 4, 5, 6, 7);
      x0 = mfma32(af[k % 3][0], wf, k ? x0 : f32x4{0.f, 0.f, 0.f, 0.f}); x1 = mfma32(af[k % 3][1], wf, k ? x1 : f32x4{0.f, 0.f, 0.f, 0.f});
      asm volatile("" : "+v"(x0), "+v"(x1));
      SCHED();
      each(k);
      SCHED();
    }
    if (K0) {
      LAS unsigned char* xv = lad(l16, lbase + R_RED + 4096 + (w & 3) * 2048);
      asm volatile("s_nop 7\n\ts_cmp_lt_u32 %3, 4\n\ts_cbranch_scc1 1f\n\tds_write_b128 %0, %1\n\tds_write_b128 %0, %2 offset:1024\n\t1:" :: "v"(xv), "v"(x0), "v"(x1), "s"(w) : "scc", "memory");
    }
  };
  auto v_out = [&](int b) {
    if (!kh && b < NSEG) {
      LAS unsigned char* xv = lad(l16, lbase + R_RED + 4096 + (w & 3) * 2048);
      x0 += *(const LAS f32x4*)(xv); x1 += *(const LAS f32x4*)(xv + 1024);
      stx16(rs, l16, WS_VT + (unsigned)(b * NH + cb) * 8192 + (rg * 4 + w) * 1024, u32x4{pack2(x0[0], x0[1]), pack2(x0[2], x0[3]), pack2(x1[0], x1[1]), pack2(x1[2], x1[3])});
    }
  };
  typedef std::integral_constant<int, 0> P0; typedef std::integral_constant<int, 1> P1;
  BAR();
  wait_ge(rs, (l16 & 0xF0) * 8 + (l16 >> 8) * 4, F_LN, 1u, cap);
  h_fetch(0); h_fetch(1);
  WAIT_V(8); BAR();
  v_part(0, P0{}, [](int) {}); v_part(0, P1{}, [](int) {});
  WAIT_V(0); LBAR();
  v_out(0); h_fetch(2);
  LBAR();
  v_part(1, P0{}, [](int) {}); v_part(1, P1{}, [](int) {});
  WAIT_V(0); LBAR();
  v_out(1); h_fetch(3);
  v_part(2, P0{}, [](int) {});
  wait_ge(rs, (l16 & 0xF0) * 8 + (l16 >> 8) * 4, F_LN + (unsigned)(1 + w) * 2048, 1u, cap);
  if (w == 0) wait_ge(rs, (l16 & 0xF0) * 8 + (l16 >> 8) * 4, F_LN + 9 * 2048, 1u, cap);
  WAIT_V(0); BAR();
  if (w == 0) {
    const int ln = l16 >> 4, sl = (ln >> 1) & 15, col = cb * 32 + (sl >> 3) * 16 + (ln >> 5) * 8 + (sl & 4) + (sl & 1) * 2 + ((sl >> 1) & 1), r = rg * 4 + (ln & 1) * 2;
    stx4(rs, soff, X_MEM + (cb >> 1) * 1024 + rg * 512 + ((cb ^ rg) & 1) * 128, nz((unsigned)p.mem0[r * D_ + col] | ((unsigned)p.mem0[(r + 1) * D_ + col] << 16)));
  }
  Sched tt; tt.tau = rt32(); tt.og = tt.sa = tt.tau; tt.ea = 127 * 8; tt.eb = 108 * 8;
  const int cu_ = 32 + (rg * 32 + cb) / 2; (void)cu_;
  ST_DECL
  for (int s = 0; s < NSEG; ++s) {
    if (w == 7) wait_until(tt.tau);
    BAR();
    ST0();
    gather_mem(rs, lbase, s, wr, l16, cap, [&]() { v_part(s + 2, P1{}, [](int) {}); });
    ST(1);
    LBAR();
    ST(2);
    {
      f32x4 c0 = {0.f, 0.f, 0.f, 0.f};
      LAS unsigned char* mp = lad(moff, lbase + R_MEM + kh * 8192);
      bf16x8 af[6];
#pragma unroll
      for (int c = 0; c < 6; ++c) af[c] = mem_frag(mp, c);
      SCHED();
#pragma unroll
      for (int c = 0; c < 8; ++c) { c0 = smf(af[c % 6], WV[c], c0, sidx); SCHED(); if (c < 2) { af[c] = mem_frag(mp, 6 + c); SCHED(); } }
      const f32x4 f = fold(c0, c0);
      if (l16 < 512) stx8(rs, half_of(l16), X_VM + (unsigned)s * RSTR + cb * 4096 + (rg * 4 + (w & 3)) * 512 + kh * 256, u32x2{nz(pack2(f[0], f[1])), nz(pack2(f[2], f[3]))});
    }
    ST(3);
    {
      unsigned xb, xa;
      times_issue(rs, w == 7 ? l16 : 0x1E000000u, s, xb, xa);
      h_fetch(min(s + 4, NSEG - 2 + (s & 1)));
      v_out(s + 2);
      asm volatile("s_waitcnt vmcnt(8)" : "+v"(xb), "+v"(xa) :: "memory");
      if (w == 7) retime(rs, l16, s, xb, xa, tt, p, cap);
    }
    if (s + 1 < NSEG) {
      u32x4 a[4];
      const unsigned so = X_OX + s * RSTR + rg * 32768 + wr * 4096;
      if (w == 7) wait_until(tt.og);
      BAR();
      ST(4);
      issue<0>(rs, l16, so, a[0]);
      v_part(s + 3, P0{}, [&](int k) {
        if (k == 0) issue<1>(rs, l16, so + 1024, a[1]);
        if (k == 2) issue<2>(rs, l16, so + 2048, a[2]);
        if (k == 4) issue<3>(rs, l16, so + 3072, a[3]);
      });
      f32x4 c[4];
#pragma unroll
      for (int t = 0; t < 4; ++t) c[t] = f32x4{0.f, 0.f, 0.f, 0.f};
#pragma unroll
      for (int i = 0; i < 4; ++i) {
        if (i == 0) arrive<0>(rs, l16, so, a[0], cap); else if (i == 1) arrive<1>(rs, l16, so + 1024, a[1], cap);
        else if (i == 2) arrive<2>(rs, l16, so + 2048, a[2], cap); else arrive<3>(rs, l16, so + 3072, a[3], cap);
#pragma unroll
        for (int t = 0; t < 4; ++t) c[t] = smf(__builtin_bit_cast(bf16x8, a[i]), WB[i * 4 + t], c[t], sidx);
        SCHED();
      }
      ST(5);
      {
        const bool odd = (l16 & 256) != 0;
        const f32x4 f = fold(odd ? c[2] : c[0], odd ? c[3] : c[1]);
        const auto e0 = __builtin_amdgcn_permlane16_swap(f2u(f[0]), f2u(f[2]), false, false), e1 = __builtin_amdgcn_permlane16_swap(f2u(f[1]), f2u(f[3]), false, false);
        *(LAS f32x2*)(lad(roff, lbase)) = f32x2{u2f(e0[0]) + u2f(e0[1]), u2f(e1[0]) + u2f(e1[1])};
      }
      ST(6);
      LBAR();
      ST(7);
      if (w == 0) {
        LAS unsigned char* rp = lad(half_of(l16), lbase + R_RED);
        f32x2 v[8];
#pragma unroll
        for (int i = 0; i < 8; ++i) v[i] = *(const LAS f32x2*)(rp + i * 512);
        const f32x2 sum = ((v[0] + v[1]) + (v[2] + v[3])) + ((v[4] + v[5]) + (v[6] + v[7]));
        stx4(rs, soff, X_MEM + (s + 1) * RSTR + (cb >> 1) * 1024 + rg * 512 + ((cb ^ rg) & 1) * 128, nz(pack2(sum[0], sum[1])));
        const unsigned t = max(rt32(), 1u);
        if (l16 == 0) stx4(rs, 0, X_TS + (unsigned)(s + 1) * RSTR + (unsigned)(rg * 32 + cb) * 128, t);
      }
      ST(8);
    }
  }
  ST_FLUSH();
  if (cap == 0u && l16 == 0) stx4(rs, 0, X_CTL, 1u);
}

#if defined(__HIP_DEVICE_COMPILE__)
__attribute__((target("no-packed-fp32-ops")))
#endif
__global__ void __launch_bounds__(512, 2) k_main(MainP p) {
  extern __shared__ __attribute__((aligned(16))) unsigned char smem[];
  const int b = blockIdx.x, x = b & 7, j = b >> 3;
  if (x >= 3) worker(p, (LAS unsigned char*)smem, x - 3, j);
  else if (x == 0) role_a(p, (LAS unsigned char*)smem, j);
  else role_vb(p, (LAS unsigned char*)smem, x - 1, j);
}

extern "C" void kernel_launch(void* const* d_in, const int* in_sizes, int n_in, void* d_out, int out_size, void* d_ws, size_t ws_size,
                              hipStream_t stream) {
  unsigned char* ws = (unsigned char*)d_ws;
  (void)hipMemsetAsync(ws, 0, X_ZERO, stream);
  MainP p;
  p.x = (const u16*)d_in[0]; p.mem0 = (const u16*)d_in[1]; p.c = (const u16*)d_in[12]; p.wmod = (const u16*)d_in[13]; p.Wq = (const u16*)d_in[2]; p.Wk = (const u16*)d_in[3]; p.Wv = (const u16*)d_in[4]; p.Wo = (const u16*)d_in[5];
  p.w1 = (const u16*)d_in[8]; p.w2 = (const u16*)d_in[9]; p.w3 = (const u16*)d_in[10]; p.w4 = (const u16*)d_in[11];
  p.out = (u16*)d_out; p.ws = ws; p.d1 = 6; p.d2 = 8; p.d3 = 2; p.d4 = 12;
  (void)hipFuncSetAttribute((const void*)k_main, hipFuncAttributeMaxDynamicSharedMemorySize, R_LDS);
  k_main<<<256, 512, R_LDS, stream>>>(p);
}
```

```cpp
#include <hip/hip_runtime.h>
#include <hip/hip_bf16.h>
#include <cstdlib>
#include <type_traits>
#pragma clang diagnostic ignored "-Winline-asm"

#define LAS __attribute__((address_space(3)))
#define GAS __attribute__((address_space(1)))
typedef unsigned short u16;
typedef __bf16 bf16x2 __attribute__((ext_vector_type(2)));
typedef __bf16 bf16x4 __attribute__((ext_vector_type(4)));
typedef __bf16 bf16x8 __attribute__((ext_vector_type(8)));
typedef __bf16 bf16x16 __attribute__((ext_vector_type(16)));
typedef short s16x4 __attribute__((ext_vector_type(4)));
typedef float f32x2 __attribute__((ext_vector_type(2)));
typedef float f32x4 __attribute__((ext_vector_type(4)));
typedef float f32x16 __attribute__((ext_vector_type(16)));
typedef unsigned u32x2 __attribute__((ext_vector_type(2)));
typedef unsigned u32x4 __attribute__((ext_vector_type(4)));

constexpr int S_ = 2048, D_ = 1024, NH = 32, DH = 128, HD = 4096, NSEG = 64, FF = 3584;

constexpr unsigned MB = 1u << 20;
constexpr unsigned F_LN = 0;
constexpr unsigned F_ST = 24576;
constexpr unsigned F_CH = F_ST + 98304;
constexpr unsigned X_CTL = F_CH + 4096;
constexpr unsigned X_MOD = X_CTL + 4096;
constexpr unsigned X_REC = X_MOD + 24576;
constexpr unsigned RSTR = 229376;
constexpr int ZA = 8;
constexpr unsigned X_MEM = X_REC;
constexpr unsigned X_OX = X_REC + 16384;
constexpr unsigned X_VM = X_REC + 81920;
constexpr unsigned X_TS = X_REC + 212992;
constexpr unsigned X_ZERO = X_REC + 16384;
constexpr unsigned WS_H = 16 * MB;
constexpr unsigned WS_Q = 20 * MB;
constexpr unsigned WS_K = 36 * MB;
constexpr unsigned WS_VT = 52 * MB;
constexpr unsigned WS_O = 68 * MB;
constexpr unsigned WS_X1 = 84 * MB;
constexpr unsigned WS_H2 = 88 * MB;
constexpr unsigned WS_G = 92 * MB;
constexpr unsigned WS_H1S = 96 * MB;
constexpr unsigned WS_X2 = 101 * MB;
constexpr unsigned WS_ST5 = 106 * MB;
constexpr unsigned WS_U = 108 * MB;
constexpr unsigned WS_KM = 124 * MB;
constexpr unsigned WS_P2 = 132 * MB;
constexpr unsigned WS_P8 = 164 * MB;
constexpr unsigned WS_END = 198 * MB;

__device__ __forceinline__ float bf2f(unsigned short b) { return __builtin_bit_cast(float, (unsigned)b << 16); }
__device__ __forceinline__ unsigned f2u(float f) { return __builtin_bit_cast(unsigned, f); }
__device__ __forceinline__ float u2f(unsigned u) { return __builtin_bit_cast(float, u); }
__device__ __forceinline__ float lo2f(unsigned w) { return __builtin_bit_cast(float, w << 16); }
__device__ __forceinline__ float hi2f(unsigned w) { return __builtin_bit_cast(float, w & 0xFFFF0000u); }
__device__ __forceinline__ unsigned pack2(float a, float b) { bf16x2 v; v[0] = (__bf16)a; v[1] = (__bf16)b; return __builtin_bit_cast(unsigned, v); }
__device__ __forceinline__ float rbf(float a) { return (float)(__bf16)a; }
__device__ __forceinline__ unsigned nz(unsigned w) { return max(w, 1u); }
__device__ __forceinline__ float gelu_tanh(float x) {
  const float e = __builtin_amdgcn_exp2f(x * fmaf(x * x, -0.1029432397f, -2.302208198f));
  return x * __builtin_amdgcn_rcpf(1.0f + e);
}
template <int CTRL> __device__ __forceinline__ float dpp_add(float v) { return v + u2f(__builtin_amdgcn_update_dpp(0u, f2u(v), CTRL, 0xF, 0xF, true)); }
__device__ __forceinline__ float wave_sum(float v) {
  v = dpp_add<0xB1>(v); v = dpp_add<0x4E>(v); v = dpp_add<0x141>(v); v = dpp_add<0x140>(v);
  auto t = __builtin_amdgcn_permlane16_swap(f2u(v), f2u(v), false, false);
  v = u2f(t[0]) + u2f(t[1]);
  t = __builtin_amdgcn_permlane32_swap(f2u(v), f2u(v), false, false);
  return u2f(t[0]) + u2f(t[1]);
}
#define WAIT_V(n) asm volatile("s_waitcnt vmcnt(" #n ")" ::: "memory")
#define WAIT_L0() asm volatile("s_waitcnt lgkmcnt(0)" ::: "memory")
#define BAR() do { asm volatile("" ::: "memory"); __builtin_amdgcn_s_barrier(); asm volatile("" ::: "memory"); } while (0)
#define LBAR() do { WAIT_L0(); BAR(); } while (0)
template <int N> __device__ __forceinline__ void wait_v() { asm volatile("s_waitcnt vmcnt(%0)" :: "n"(N) : "memory"); }
__device__ __forceinline__ void dma16(__amdgpu_buffer_rsrc_t rs, unsigned voff, unsigned soff, unsigned ldsa) {
  asm volatile("s_mov_b32 m0, %0\n\ts_nop 3\n\tbuffer_load_dwordx4 %1, %2, %3 offen lds" :: "s"(ldsa), "v"(voff), "s"(rs), "s"(soff) : "m0", "memory");
}
template <bool NT> __device__ __forceinline__ void dma16n(__amdgpu_buffer_rsrc_t rs, unsigned voff, unsigned soff, unsigned ldsa) {
  if (NT) asm volatile("s_mov_b32 m0, %0\n\ts_nop 3\n\tbuffer_load_dwordx4 %1, %2, %3 offen nt lds" :: "s"(ldsa), "v"(voff), "s"(rs), "s"(soff) : "m0", "memory");
  else dma16(rs, voff, soff, ldsa);
}
#define NTM 1
__device__ __forceinline__ void dma16x(__amdgpu_buffer_rsrc_t rs, unsigned voff, unsigned soff, unsigned ldsa) {
  asm volatile("s_mov_b32 m0, %0\n\ts_nop 3\n\tbuffer_load_dwordx4 %1, %2, %3 offen sc1 lds" :: "s"(ldsa), "v"(voff), "s"(rs), "s"(soff) : "m0", "memory");
}
__device__ __forceinline__ u32x4 ldx16(__amdgpu_buffer_rsrc_t rs, unsigned voff, unsigned soff) { return __builtin_amdgcn_raw_buffer_load_b128(rs, voff, soff, 16); }
__device__ __forceinline__ u32x2 ldx8(__amdgpu_buffer_rsrc_t rs, unsigned voff, unsigned soff) { return __builtin_amdgcn_raw_buffer_load_b64(rs, voff, soff, 16); }
__device__ __forceinline__ unsigned ldx4(__amdgpu_buffer_rsrc_t rs, unsigned voff, unsigned soff) { return __builtin_amdgcn_raw_buffer_load_b32(rs, voff, soff, 16); }
__device__ __forceinline__ void stx16(__amdgpu_buffer_rsrc_t rs, unsigned voff, unsigned soff, u32x4 v) {
  __builtin_amdgcn_raw_buffer_store_b128(v, rs, voff, soff, 16);
  asm volatile("s_nop 1" :: "v"(v) : "memory");
}
__device__ __forceinline__ void stx8(__amdgpu_buffer_rsrc_t rs, unsigned voff, unsigned soff, u32x2 v) { __builtin_amdgcn_raw_buffer_store_b64(v, rs, voff, soff, 16); }
__device__ __forceinline__ void stx4(__amdgpu_buffer_rsrc_t rs, unsigned voff, unsigned soff, unsigned v) { __builtin_amdgcn_raw_buffer_store_b32(v, rs, voff, soff, 16); }
__device__ __forceinline__ void wait_ge(__amdgpu_buffer_rsrc_t rs, unsigned voff, unsigned soff, unsigned need, unsigned& cap) {
  unsigned t;
  asm volatile("s_nop 4\n\t"
               "1:\n\t"
               "buffer_load_dword %[t], %[vo], %[rs], %[so] offen sc1\n\t"
               "s_waitcnt vmcnt(0)\n\t"
               "v_cmp_gt_u32 vcc, %[need], %[t]\n\t"
               "s_cmp_eq_u64 vcc, 0\n\t"
               "s_cbranch_scc1 2f\n\t"
               "s_sleep 2\n\t"
               "s_sub_u32 %[cap], %[cap], 1\n\t"
               "s_cbranch_scc0 1b\n\t"
               "s_mov_b32 %[cap], 0\n\t"
               "2:"
               : [t] "=&v"(t), [cap] "+s"(cap) : [vo] "v"(voff), [rs] "s"(rs), [so] "s"(soff), [need] "s"(need) : "vcc", "scc", "memory");
}
__device__ __forceinline__ void wait_seen(__amdgpu_buffer_rsrc_t rs, unsigned voff, unsigned soff, unsigned need, unsigned& seen, unsigned& cap) {
  if (seen >= need) return;
  unsigned t;
  asm volatile("s_nop 4\n\t"
               "1:\n\t"
               "buffer_load_dword %[t], %[vo], %[rs], %[so] offen sc1\n\t"
               "s_waitcnt vmcnt(0)\n\t"
               "v_cmp_gt_u32 vcc, %[need], %[t]\n\t"
               "s_cbranch_vccz 2f\n\t"
               "s_sleep 1\n\t"
               "s_sub_u32 %[cap], %[cap], 1\n\t"
               "s_cbranch_scc0 1b\n\t"
               "s_mov_b32 %[cap], 0\n\t"
               "2:"
               : [t] "=&v"(t), [cap] "+s"(cap) : [vo] "v"(voff), [rs] "s"(rs), [so] "s"(soff), [need] "s"(need) : "vcc", "scc", "memory");
  seen = min((unsigned)__builtin_amdgcn_readlane(t, 0), (unsigned)__builtin_amdgcn_readlane(t, 1));
}
__device__ __forceinline__ __amdgpu_buffer_rsrc_t mk_rs(const void* p, unsigned bytes) { return __builtin_amdgcn_make_buffer_rsrc((void*)p, 0, (int)bytes, 0x00020000); }
#define SCHED() __builtin_amdgcn_sched_barrier(0)
__device__ __forceinline__ unsigned now32() { unsigned long long t; asm volatile("s_memtime %0\n\ts_waitcnt lgkmcnt(0)" : "=s"(t) :: "memory"); return (unsigned)t; }
__device__ __forceinline__ unsigned rt32() { unsigned long long t; asm volatile("s_memrealtime %0\n\ts_waitcnt lgkmcnt(0)" : "=s"(t) :: "memory"); return (unsigned)t; }
__device__ __forceinline__ void wait_until(unsigned t) { while ((int)(rt32() - t) < 0) {} }
template <int X = 0> __device__ __forceinline__ LAS unsigned char* lad(unsigned v, unsigned u) {
  unsigned a;
  if (X == 0) asm volatile("v_add_u32 %0, %1, %2" : "=v"(a) : "s"(u), "v"(v));
  else asm volatile("v_xor_b32 %0, %3, %2\n\tv_add_u32 %0, %1, %0" : "=&v"(a) : "s"(u), "v"(v), "n"(X));
  return (LAS unsigned char*)(size_t)a;
}
__device__ __forceinline__ bf16x4 trread(LAS unsigned char* p) { return __builtin_amdgcn_ds_read_tr16_b64_v4bf16((LAS bf16x4*)p); }
__device__ __forceinline__ bf16x8 cat8(bf16x4 a, bf16x4 b) { return __builtin_shufflevector(a, b, 0, 1, 2, 3, 4, 5, 6, 7); }
__device__ __forceinline__ bf16x16 cat16(bf16x8 a, bf16x8 b) { return __builtin_shufflevector(a, b, 0, 1, 2, 3, 4, 5, 6, 7, 8, 9, 10, 11, 12, 13, 14, 15); }
__device__ __forceinline__ f32x4 mfma32(bf16x8 a, bf16x8 b, f32x4 c) { return __builtin_amdgcn_mfma_f32_16x16x32_bf16(a, b, c, 0, 0, 0); }
__device__ __forceinline__ f32x4 mfma16(u32x2 a, u32x2 b, f32x4 c) {
  return mfma32(__builtin_bit_cast(bf16x8, u32x4{a[0], a[1], 0u, 0u}), __builtin_bit_cast(bf16x8, u32x4{b[0], b[1], 0u, 0u}), c);
}
__device__ __forceinline__ f32x4 mfma16n(u32x2 a, u32x2 b, f32x4 c) { return __builtin_amdgcn_mfma_f32_16x16x16bf16_1k(__builtin_bit_cast(s16x4, a), __builtin_bit_cast(s16x4, b), c, 0, 0, 0); }
__device__ __forceinline__ f32x4 smf(bf16x8 a, bf16x16 b, f32x4 c, int idx) { return __builtin_amdgcn_smfmac_f32_16x16x64_bf16(a, b, c, idx, 0, 0); }

__device__ __forceinline__ void ln_row(float (&x)[16]) {
  float s = 0.f;
#pragma unroll
  for (int i = 0; i < 16; ++i) s += x[i];
  const float mean = wave_sum(s) * (1.0f / 1024.0f);
  float q = 0.f;
#pragma unroll
  for (int i = 0; i < 16; ++i) { const float d = x[i] - mean; q += d * d; }
  const float var = wave_sum(q) * (1.0f / 1024.0f);
  const float m = rbf(mean), isd = __builtin_amdgcn_rcpf(rbf(__builtin_amdgcn_sqrtf(rbf(rbf(var) + 1e-5f))));
#pragma unroll
  for (int i = 0; i < 16; ++i) x[i] = rbf(rbf(x[i] - m) * isd);
}
__device__ __forceinline__ void ln_stats(const float (&x)[16], float& m, float& isd) {
  float s = 0.f;
#pragma unroll
  for (int i = 0; i < 16; i += 4) s += (x[i] + x[i + 1]) + (x[i + 2] + x[i + 3]);
  const float mean = wave_sum(s) * (1.0f / 1024.0f);
  float q0 = 0.f, q1 = 0.f;
#pragma unroll
  for (int i = 0; i < 16; i += 2) { const float d0 = x[i] - mean, d1 = x[i + 1] - mean; q0 = fmaf(d0, d0, q0); q1 = fmaf(d1, d1, q1); }
  const float var = wave_sum(q0 + q1) * (1.0f / 1024.0f);
  m = rbf(mean); isd = __builtin_amdgcn_rcpf(rbf(__builtin_amdgcn_sqrtf(rbf(rbf(var) + 1e-5f))));
}
__device__ __forceinline__ void load_row16(const u16* rowp, int lane, float (&x)[16]) {
  const u32x4 a = *(const u32x4*)(rowp + lane * 8), b = *(const u32x4*)(rowp + 512 + lane * 8);
#pragma unroll
  for (int i = 0; i < 4; ++i) { x[2 * i] = lo2f(a[i]); x[2 * i + 1] = hi2f(a[i]); x[8 + 2 * i] = lo2f(b[i]); x[9 + 2 * i] = hi2f(b[i]); }
}
__device__ __forceinline__ void store_row16(u16* rowp, int lane, const float (&x)[16]) {
  u32x4 a, b;
#pragma unroll
  for (int i = 0; i < 4; ++i) { a[i] = pack2(x[2 * i], x[2 * i + 1]); b[i] = pack2(x[8 + 2 * i], x[9 + 2 * i]); }
  *(u32x4*)(rowp + lane * 8) = a; *(u32x4*)(rowp + 512 + lane * 8) = b;
}
__host__ __device__ __forceinline__ int mem_slot(int dl) { return ((dl >> 3) & 1) * 32 + (dl >> 4) * 8 + (dl & 4) + (dl & 1) * 2 + ((dl >> 1) & 1); }

#define WST(rs, wk, i)
struct MainP { const u16 *x, *mem0, *Wq, *Wk, *Wv, *Wo, *w1, *w2, *w3, *w4, *c, *wmod; u16* out; unsigned char* ws; int d1, d2, d3, d4; };
constexpr unsigned SPIN_CAP = 1u << 18;

__device__ __forceinline__ float gmax(float v) {
  auto t = __builtin_amdgcn_permlane16_swap(f2u(v), f2u(v), false, false);
  v = fmaxf(u2f(t[0]), u2f(t[1]));
  t = __builtin_amdgcn_permlane32_swap(f2u(v), f2u(v), false, false);
  return fmaxf(u2f(t[0]), u2f(t[1]));
}
__device__ __forceinline__ float gsum(float v) {
  auto t = __builtin_amdgcn_permlane16_swap(f2u(v), f2u(v), false, false);
  v = u2f(t[0]) + u2f(t[1]);
  t = __builtin_amdgcn_permlane32_swap(f2u(v), f2u(v), false, false);
  return u2f(t[0]) + u2f(t[1]);
}
__device__ __forceinline__ void publish(__amdgpu_buffer_rsrc_t rs, unsigned word, unsigned val) {
  WAIT_V(0); BAR();
  if (threadIdx.x == 0) stx4(rs, 0, word, val);
}

__device__ __forceinline__ int k_owner(int b) { return b < 60 ? b % 3 : 2; }
constexpr int QB0 = 10;
constexpr int P_S2 = 0, P_S8 = 32, P_S7 = 64, P_S4 = 96, P_S5 = 104, P_S3 = 112, P_PJ = 208, P_S1 = 336;
constexpr int NLN = 160;

enum { SG_WO = 0, SG_W1 = 1, SG_W2 = 2, SG_W3 = 3, SG_W4 = 4, SG_PJ = 5 };
#define RUSH0 52
constexpr int G_XCH = 131072, G_K = G_XCH + 16384, G_CTL = G_K + 8192;
__device__ __forceinline__ f32x16 mfma3232(bf16x8 a, bf16x8 b, f32x16 c) { return __builtin_amdgcn_mfma_f32_32x32x16_bf16(a, b, c, 0, 0, 0); }
__device__ __forceinline__ void widen32(const f32x16& a, u32x4 (&v)[2]) {
#pragma unroll
  for (int u = 0; u < 2; ++u) {
    const auto r0 = __builtin_amdgcn_permlane32_swap(pack2(a[8 * u], a[8 * u + 1]), pack2(a[8 * u + 4], a[8 * u + 5]), false, false);
    const auto r1 = __builtin_amdgcn_permlane32_swap(pack2(a[8 * u + 2], a[8 * u + 3]), pack2(a[8 * u + 6], a[8 * u + 7]), false, false);
    v[u] = u32x4{r0[0], r1[0], r0[1], r1[1]};
  }
}
__device__ __forceinline__ unsigned umin4v(u32x4 v) { return min(min(v[0], v[1]), min(v[2], v[3])); }
__device__ __forceinline__ float hsum(float v) { const auto t = __builtin_amdgcn_permlane32_swap(f2u(v), f2u(v), false, false); return u2f(t[0]) + u2f(t[1]); }

__device__ __forceinline__ void attend(__amdgpu_buffer_rsrc_t rs, int seg, int h, int t, int lane, unsigned& chs, unsigned& cap) {
  const int fr = lane & 15, g = lane >> 4;
  const unsigned l16 = lane * 16, ub = (unsigned)(seg * NH + h);
  wait_seen(rs, 0, F_CH + h * 128, (unsigned)max(seg, 1), chs, cap);
  wait_ge(rs, (lane & 1) && seg >= QB0 ? 96u * 128u : (unsigned)k_owner(seg) * 4096u, F_ST + (unsigned)(P_PJ + h) * 128, (unsigned)seg + 1u, cap);
  f32x4 s0 = {0.f, 0.f, 0.f, 0.f}, s1 = s0, s2 = s0;
  u32x4 q[4], kmf[4];
  u32x2 vmf[8];
  const unsigned em = (unsigned)(fr >> 3) * 512 + (unsigned)g * 128 + (unsigned)(8 * ((fr >> 2) & 1) + (fr & 3)) * 8;
  auto mem_load = [&](unsigned off) {
#pragma unroll
    for (int ks = 0; ks < 4; ++ks) kmf[ks] = ldx16(rs, l16 + off, WS_KM + ub * 4096 + ks * 1024);
#pragma unroll
    for (int i = 0; i < 8; ++i) vmf[i] = ldx8(rs, em + off, X_VM + (unsigned)seg * RSTR + h * 4096 + (i >> 1) * 1024 + (i & 1) * 32);
  };
  const bool have = chs > (unsigned)seg;
  f32x4 o[8];
  float mt = -3.0e38f, lt = 0.f;
  const float scale = 0.08838834764831845f, L2E = 1.4426950408889634f;
  {
    u32x4 k0[4], k1[4], vv[8];
#pragma unroll
    for (int ks = 0; ks < 4; ++ks) {
      q[ks] = ldx16(rs, l16, WS_Q + ub * 8192 + t * 4096 + ks * 1024);
      k0[ks] = ldx16(rs, l16, WS_K + ub * 8192 + ks * 1024); k1[ks] = ldx16(rs, t ? l16 : 0xF0000000u, WS_K + ub * 8192 + 4096 + ks * 1024);
    }
    const unsigned ev = (unsigned)(fr >> 3) * 1024 + (unsigned)(g * 16 + 8 * ((fr >> 2) & 1) + (fr & 3)) * 16;
#pragma unroll
    for (int i = 0; i < 8; ++i) vv[i] = ldx16(rs, ev, WS_VT + ub * 8192 + (i >> 1) * 2048 + (i & 1) * 64);
#pragma unroll
    for (int ks = 0; ks < 4; ++ks) {
      s1 = mfma32(__builtin_bit_cast(bf16x8, k0[ks]), __builtin_bit_cast(bf16x8, q[ks]), s1); s2 = mfma32(__builtin_bit_cast(bf16x8, k1[ks]), __builtin_bit_cast(bf16x8, q[ks]), s2);
    }
    SCHED();
    mem_load(have ? 0u : 0xF0000000u);
    SCHED();
    float b[4], c[4];
#pragma unroll
    for (int r = 0; r < 4; ++r) {
      const int j = 4 * g + r;
      b[r] = (t == 1 || j <= fr) ? rbf(rbf(s1[r]) * scale) : -3.0e38f;
      c[r] = (t == 1 && j <= fr) ? rbf(rbf(s2[r]) * scale) : -3.0e38f;
      mt = fmaxf(mt, fmaxf(b[r], c[r]));
    }
    mt = gmax(mt);
#pragma unroll
    for (int r = 0; r < 4; ++r) { b[r] = __builtin_amdgcn_exp2f((b[r] - mt) * L2E); c[r] = __builtin_amdgcn_exp2f((c[r] - mt) * L2E); lt += b[r] + c[r]; }
    const u32x4 pt = {pack2(b[0], b[1]), pack2(b[2], b[3]), pack2(c[0], c[1]), pack2(c[2], c[3])};
#pragma unroll
    for (int i = 0; i < 8; ++i) o[i] = mfma32(__builtin_bit_cast(bf16x8, vv[i]), __builtin_bit_cast(bf16x8, pt), f32x4{0.f, 0.f, 0.f, 0.f});
    lt = gsum(lt);
  }
  asm volatile("" : "+v"(o[0]), "+v"(o[1]), "+v"(o[2]), "+v"(o[3]), "+v"(o[4]), "+v"(o[5]), "+v"(o[6]), "+v"(o[7]), "+v"(lt));
  if (!have) { wait_seen(rs, 0, F_CH + h * 128, (unsigned)seg + 1u, chs, cap); mem_load(0u); }
#pragma unroll
  for (int ks = 0; ks < 4; ++ks) s0 = mfma32(__builtin_bit_cast(bf16x8, kmf[ks]), __builtin_bit_cast(bf16x8, q[ks]), s0);
  float a[4];
#pragma unroll
  for (int r = 0; r < 4; ++r) a[r] = rbf(rbf(s0[r]) * scale);
  const float mx = fmaxf(mt, gmax(fmaxf(fmaxf(a[0], a[1]), fmaxf(a[2], a[3]))));
#pragma unroll
  for (int r = 0; r < 4; ++r) a[r] = __builtin_amdgcn_exp2f((a[r] - mx) * L2E);
  const u32x2 pm = {pack2(a[0], a[1]), pack2(a[2], a[3])};
  const float al = __builtin_amdgcn_exp2f((mt - mx) * L2E);
  const float inv = __builtin_amdgcn_rcpf(fmaf(al, lt, 0.5f * gsum((a[0] + a[1]) + (a[2] + a[3])))), ai = al * inv;
#pragma unroll
  for (int u = 0; u < 4; ++u) {
    const f32x4 x = o[2 * u] * ai + mfma16n(vmf[2 * u], pm, f32x4{0.f, 0.f, 0.f, 0.f}) * inv, y = o[2 * u + 1] * ai + mfma16n(vmf[2 * u + 1], pm, f32x4{0.f, 0.f, 0.f, 0.f}) * inv;
    stx16(rs, l16, WS_O + (unsigned)seg * 262144 + (unsigned)((h * 4 + u) * 2 + t) * 1024, u32x4{pack2(x[0], x[1]), pack2(x[2], x[3]), pack2(y[0], y[1]), pack2(y[2], y[3])});
  }
  WAIT_V(0);
  if (lane == 0) stx4(rs, 0, F_ST + (unsigned)(P_S1 + (seg & 3) * 64 + h * 2 + t) * 128, (unsigned)seg + 1u);
}

template <int SG>
__device__ __forceinline__ void stage_gemm(LAS unsigned char* lds, __amdgpu_buffer_rsrc_t rs, const u16* Wg, int wk, int kq, int cb, int pid, unsigned& cap,
                                           unsigned dst = 0, int bstr = 1, int nb = 0) {
  constexpr bool PROJ = SG == SG_PJ, ROWM = (SG == SG_W1 || PROJ);
  constexpr int KS = (SG == SG_W4) ? 28 : 32;
  constexpr int LDW = PROJ ? HD : (SG == SG_W3) ? FF : D_;
  constexpr int WROWS = (SG == SG_WO) ? HD : (SG == SG_W4 ? FF : D_);
  const int NBLK = PROJ ? nb : (SG == SG_WO || SG == SG_W1) ? 64 : 65;
  constexpr unsigned ABASE = PROJ ? WS_H : SG == SG_WO ? WS_O : SG == SG_W1 ? WS_G : SG == SG_W2 ? WS_H1S : SG == SG_W3 ? WS_X2 : WS_U;
  constexpr unsigned ASTR = SG == SG_WO ? 262144u : SG == SG_W4 ? 229376u : 65536u;
  constexpr int NPC = KS / 4;
  const unsigned myword = F_ST + (unsigned)pid * 128u;
  int tid = threadIdx.x; asm volatile("" : "+v"(tid));
  const int lane = tid & 63, w = __builtin_amdgcn_readfirstlane(tid >> 6), row = lane & 31, hf = lane >> 5;
  const int kh = w >> 2, wl = w & 3;
  const int koff = PROJ ? 0 : kq;
  const unsigned lbase = (unsigned)(size_t)lds, l16 = lane * 16;
  const int col0 = cb * 128 + wl * 32;
  bf16x8 Wf[KS];
  {
    const __amdgpu_buffer_rsrc_t rsW = mk_rs(Wg, (unsigned)WROWS * LDW * 2u);
    const int ub = lane >> 3, tu = 4 * hf + ((lane & 15) >> 2);
    const unsigned vB = (unsigned)(((ub & 3) + 8 * (ub >> 2)) * LDW + 16 * (((lane >> 1) & 3) ^ (ub >> 1)) + 8 * (lane & 1)) * 2u;
    const unsigned sB = (unsigned)((koff * KS * 32 + 32 * (w >> 2) + 16 * ((w >> 1) & 1) + 4 * (w & 1)) * LDW + cb * 128) * 2u;
    const int boff = kh * 65536 + (wl >> 1) * 8192 + tu * 128 + (((tu >> 1) ^ (2 * (wl & 1) + ((lane >> 4) & 1))) << 5) + (lane & 3) * 8;
#pragma unroll
    for (int ps = 0; ps < 2; ++ps) {
      BAR();
#pragma unroll
      for (int hh = 0; hh < 2; ++hh)
#pragma unroll
        for (int i = 0; i < 4; ++i)
          if (4 * ps + i < NPC && !(PROJ && ps == 0)) {
            const unsigned so = sB + (unsigned)((hh * NPC + 4 * ps + i) * 64 * LDW) * 2u, d = lbase + hh * 65536 + i * 16384 + w * 1024;
            dma16n<(NTM & 16) != 0>(rsW, vB, so, d); dma16n<(NTM & 16) != 0>(rsW, vB, so + 128, d + 8192);
          }
      WAIT_V(0); BAR();
#pragma unroll
      for (int i = 0; i < 4; ++i)
        if (4 * ps + i < NPC) {
#pragma unroll
          for (int k = 0; k < 4; ++k) { LAS unsigned char* q = lds + (boff + i * 16384 + k * 2048); Wf[(4 * ps + i) * 4 + k] = cat8(trread(q), trread(q + 1024)); }
        }
      WAIT_L0();
    }
    if (SG == SG_W1) {
      const u32x4 z = {0u, 0u, 0u, 0u};
      stx16(rs, (unsigned)tid * 16, WS_H1S + cb * 8192, z); stx16(rs, (unsigned)tid * 16, WS_H1S + 64 * 65536 + cb * 8192, z);
      WAIT_V(0);
    }
    if (tid < 8) *(LAS unsigned*)(lds + G_CTL + tid * 4) = 0u;
    LBAR();
  }
  const f32x16 zero16 = {0.f, 0.f, 0.f, 0.f, 0.f, 0.f, 0.f, 0.f, 0.f, 0.f, 0.f, 0.f, 0.f, 0.f, 0.f, 0.f};
  auto put16 = [&](LAS unsigned char* p, const f32x16& a) {
#pragma unroll
    for (int q = 0; q < 4; ++q) *(LAS f32x4*)(p + q * 1024) = f32x4{a[4 * q], a[4 * q + 1], a[4 * q + 2], a[4 * q + 3]};
  };
  if (SG == SG_W3) {
    const u32x4 one4 = {0x3F803F80u, 0x3F803F80u, 0x3F803F80u, 0x3F803F80u};
    f32x16 cs = zero16;
#pragma unroll
    for (int k = 0; k < KS; ++k) cs = mfma3232(Wf[k], __builtin_bit_cast(bf16x8, one4), cs);
    if (kh == 0) put16(lds + wl * 4096 + l16, cs);
    LBAR();
    if (kh == 1 && row == 0) {
#pragma unroll
      for (int q = 0; q < 4; ++q)
        *(LAS f32x4*)(lds + G_K + wl * 128 + hf * 64 + q * 16) = *(const LAS f32x4*)(lds + wl * 4096 + q * 1024 + l16) + f32x4{cs[4 * q], cs[4 * q + 1], cs[4 * q + 2], cs[4 * q + 3]};
    }
    LBAR();
  }
  auto blk = [&](int i) -> int { return PROJ ? ((bstr == 3 && i >= 20) ? 40 + i : bstr * i + kq) : i; };
  const unsigned aoff = ROWM ? (unsigned)(row * 128 + ((hf ^ ((row >> 1) & 7)) << 4)) : (unsigned)((row >> 4) * 1024 + hf * 256 + (row & 15) * 16);
  const unsigned vA = ROWM ? (unsigned)((lane >> 3) * 2048 + (((lane & 7) ^ ((4 * wl + (lane >> 4)) & 7)) << 4)) : l16;
  auto piece = [&](int hb, int q, bool on) {
    const int i = hb >> 1, hh = hb & 1;
    const unsigned so = ABASE + (unsigned)blk(i) * ASTR + (unsigned)koff * (KS * 2048) + (ROWM ? wl * 16384 + hh * 1024 : (hh * KS + wl) * 1024);
    if (on) dma16x(rs, vA, so + q * (ROWM ? 128 : 4096), lbase + (i & 1) * 65536 + hh * 32768 + wl * 1024 + q * 4096);
  };
  auto fetch = [&](int hb) {
#pragma unroll
    for (int q = 0; q < NPC; ++q) piece(hb, q, true);
  };
  auto fetch_own = [&](int i) {
    const unsigned so = __builtin_amdgcn_readfirstlane(ABASE + (unsigned)i * ASTR + (unsigned)koff * (KS * 2048) + (ROWM ? wl * 16384 + kh * 1024 : (kh * KS + wl) * 1024));
    const unsigned d = __builtin_amdgcn_readfirstlane(lbase + (i & 1) * 65536 + kh * 32768 + wl * 1024);
#pragma unroll
    for (int q = 0; q < NPC; ++q) dma16x(rs, vA, so + q * (ROWM ? 128 : 4096), d + q * 4096);
  };
  constexpr bool WEAVE = (SG == SG_W1 || SG == SG_W2 || SG == SG_W3);
  auto products = [&](int i, f32x16& acc, auto&& first, auto&& early, auto&& handover, bool soon) {
    const unsigned ub = lbase + (i & 1) * 65536 + kh * 32768;
    LAS unsigned char* ab[4];
    ab[0] = lad(aoff, ub);
    if (ROWM) { ab[1] = lad<32>(aoff, ub); ab[2] = lad<64>(aoff, ub); ab[3] = lad<96>(aoff, ub); }
    constexpr int PD = 10;
    bf16x8 af[PD + 1];
    auto rd = [&](int k) { af[k % (PD + 1)] = ROWM ? *(const LAS bf16x8*)(ab[k & 3] + (k >> 2) * 4096) : *(const LAS bf16x8*)(ab[0] + (k >> 1) * 2048 + (k & 1) * 512); };
#pragma unroll
    for (int k = 0; k < PD; ++k) rd(k);
    SCHED();
#pragma unroll
    for (int k = 0; k < KS; ++k) {
      if (k + PD < KS) rd(k + PD);
      acc = mfma3232(Wf[k], af[k % (PD + 1)], k ? acc : zero16);
      SCHED();
      first(k);
      SCHED();
      if (k == KS - 10) { early(); SCHED(); }
      if (k == 9) { if (soon) handover(); SCHED(); }
      if (k == KS - 5) { if (!soon) handover(); SCHED(); }
    }
    asm volatile("" : "+v"(acc));
  };
  WST(rs, wk, PROJ ? 2 : 10);
#define SEG(x)
  auto announce = [&](int i) {
    if (lane == 0) {
      const unsigned c = __hip_atomic_fetch_add((LAS unsigned*)(lds + G_CTL), 1u, __ATOMIC_RELAXED, __HIP_MEMORY_SCOPE_WORKGROUP);
      if ((c & 3u) == 3u) stx4(rs, 0, myword, (unsigned)blk(i) + 1u);
    }
  };
  auto pvo = [&](int i) -> unsigned {
    const int b = blk(i);
    if (PROJ) return F_LN + (unsigned)((b % (NLN / 4)) * 4 + (lane & 3)) * 128u + (lane & 12);
    if (SG == SG_WO) return F_ST + (unsigned)(P_S1 + (b & 3) * 64 + kq * 16 + (lane & 15)) * 128u;
    if (SG == SG_W1) return F_ST + (unsigned)(P_S3 + (b % 3) * 32 + (lane & 31)) * 128u;
    if (SG == SG_W2) return F_ST + (unsigned)(P_S4 + (lane & 7)) * 128u;
    if (SG == SG_W3) return F_ST + (unsigned)(P_S5 + (lane & 7)) * 128u;
    return F_ST + (unsigned)(P_S7 + kq * 7 + min(lane & 7, 6)) * 128u;
  };
  auto need = [&](int i) -> unsigned { return PROJ ? 1u : (SG == SG_W2 && i == 64) ? 64u : (unsigned)i + 1u; };
  const int inext = PROJ ? NBLK : RUSH0;
  if (kh == 0) {
    unsigned nl = 0;
    int R = 0, Rp = 0;
    for (int i = 0; i < inext; ++i) {
      if (R <= i) { wait_ge(rs, pvo(i), 0, need(i), cap); SEG(6); fetch(2 * i); fetch(2 * i + 1); SEG(7); }
      const unsigned pk = w == 0 ? (lane < 32 ? pvo(i + 1) : pvo(i + 2)) : 0xF0000000u;
      if (Rp <= i) { if (R <= i + 1) wait_v<NPC>(); else wait_v<2 * NPC>(); }
      if (R >= i + 2 || Rp > i) BAR();
      else {
        nl += 4;
        if (lane == 0) (void)__hip_atomic_fetch_add((LAS unsigned*)(lds + G_CTL + 8), 1u, __ATOMIC_RELAXED, __HIP_MEMORY_SCOPE_WORKGROUP);
        while (__builtin_amdgcn_readfirstlane(*(const volatile LAS unsigned*)(lds + G_CTL + 8)) < nl) {}
        asm volatile("" ::: "memory");
      }
      SEG(1);
      f32x16 acc;
      unsigned peek;
      asm volatile("buffer_load_dword %0, %1, %2, 0 offen sc1" : "=v"(peek) : "v"(pk), "s"(rs) : "memory");
      u32x4 st[8];
      float mean = 0.f, q2 = 0.f;
      if (SG == SG_W3) {
#pragma unroll
        for (int q = 0; q < 8; ++q) st[q] = ldx16(rs, l16, WS_ST5 + (unsigned)i * 8192 + q * 1024);
      }
      products(i, acc, [&](int k) {
        if (SG == SG_W3) {
          if (k >= 18 && k < 26) { mean += u2f(st[k - 18][0]) + u2f(st[k - 18][2]); q2 += u2f(st[k - 18][1]) + u2f(st[k - 18][3]); asm volatile("" : "+v"(mean), "+v"(q2)); }
          if (k == 26) { mean = hsum(mean) * (1.0f / 1024.0f); q2 = hsum(q2); asm volatile("" : "+v"(mean), "+v"(q2)); }
          if (k == 28) { q2 = rbf(rbf(fmaxf(fmaf(-mean, mean, q2 * (1.0f / 1024.0f)), 0.f)) + 1e-5f); asm volatile("" : "+v"(q2)); }
          if (k == 29) { q2 = __builtin_amdgcn_rcpf(rbf(__builtin_amdgcn_sqrtf(q2))); mean = rbf(mean); asm volatile("" : "+v"(q2), "+v"(mean)); }
        }
      }, []() {}, [&]() {
        SEG(4);
        WAIT_V(0);
        SEG(0);
        asm volatile("" : "+v"(peek));
        if (w == 0) {
          const unsigned long long ok = __ballot(peek >= (lane < 32 ? need(i + 1) : need(i + 2)));
          const bool r1 = (R >= i + 2 || (unsigned)ok == 0xFFFFFFFFu) && i + 1 < inext, r2 = r1 && (unsigned)(ok >> 32) == 0xFFFFFFFFu && i + 2 < inext;
          if (lane == 0) *(volatile LAS unsigned*)(lds + G_CTL + 4) = (unsigned)(i + 1 + (r1 ? 1 : 0) + (r2 ? 1 : 0));
        }
        LBAR();
      }, Rp <= i);
      SEG(2);
      const unsigned rv = *(const volatile LAS unsigned*)(lds + G_CTL + 4);
      if (SG == SG_W3) {
        const float m = mean;
        *(LAS float*)(lds + G_K + 1024 + wl * 256 + lane * 4) = q2;
#pragma unroll
        for (int q = 0; q < 4; ++q) {
          const f32x4 cs = *(const LAS f32x4*)(lds + G_K + wl * 128 + hf * 64 + q * 16);
#pragma unroll
          for (int e = 0; e < 4; ++e) acc[4 * q + e] = fmaf(-m, cs[e], acc[4 * q + e]);
        }
      }
      put16(lad(l16, lbase + G_XCH + wl * 4096), acc);
      asm volatile("" ::: "memory");
      if (lane == 0) *(volatile LAS unsigned*)(lds + G_CTL + 16 + wl * 4) = (unsigned)i + 1u;
      const bool soon = Rp <= i;
      Rp = R; R = __builtin_amdgcn_readfirstlane(rv);
      SEG(3);
      if (R >= i + 2 && Rp < i + 2) { fetch(2 * i + 2); fetch(2 * i + 3); }
      if (R >= i + 3) {
        if (soon) { while (__builtin_amdgcn_readfirstlane(umin4v(*(const volatile LAS u32x4*)(lds + G_CTL + 16))) <= (unsigned)i) {} asm volatile("" ::: "memory"); }
        fetch(2 * i + 4);
      }
      SEG(5);
    }
  } else {
    bool pending = false;
    int Rp = 0;
    for (int i = 0; i < inext; ++i) {
      const int b = blk(i);
      BAR();
      SEG(0);
      const unsigned rv = *(const volatile LAS unsigned*)(lds + G_CTL + 4);
      u32x4 res[2];
      float isd;
      if (SG == SG_W2) {
#pragma unroll
        for (int u = 0; u < 2; ++u) {
          const int t = 32 * b - 2 + 16 * u + (lane >> 2);
          dma16x(rs, (t >= 0 && t < S_) ? (unsigned)(t * D_ + col0 + ((lane ^ (lane >> 3)) & 3) * 8) * 2u : 0xF0000000u, WS_H2, lbase + G_K + wl * 2048 + u * 1024);
        }
      }
      f32x16 acc;
      int R = 0;
      LAS unsigned char* xr = lad(l16, lbase + G_XCH + wl * 4096);
      f32x4 xa[4];
      unsigned xf;
      auto take = [&]() {
        asm volatile("" ::: "memory");
        xf = *(const volatile LAS unsigned*)(lds + G_CTL + 16 + wl * 4);
        asm volatile("" ::: "memory");
#pragma unroll
        for (int q = 0; q < 4; ++q) xa[q] = *(const LAS f32x4*)(xr + q * 1024);
        if (SG == SG_W3) isd = *(const LAS float*)(lds + G_K + 1024 + wl * 256 + lane * 4);
        asm volatile("" ::: "memory");
      };
      products(i, acc, [&](int k) {
      }, [&]() {
        take();
      }, [&]() {
        SEG(5);
        R = __builtin_amdgcn_readfirstlane(rv);
        if (R >= i + 3 || Rp >= i + 2) {
          WAIT_V(0);
          LBAR();
          if (pending && tid == 256) stx4(rs, 0, myword, (unsigned)blk(i - 1) + 1u);
        }
      }, false);
      const bool more = R >= i + 3 || Rp >= i + 2;
      Rp = R;
      SEG(1);
      while (__builtin_amdgcn_readfirstlane(xf) <= (unsigned)i) take();
#pragma unroll
      for (int q = 0; q < 16; ++q) { acc[q] += xa[q >> 2][q & 3]; if (SG == SG_W3) acc[q] *= isd; }
      SEG(2);
      const unsigned fo = (unsigned)((col0 >> 5) * 2048 + hf * 256), ro = (unsigned)((row >> 4) * 1024 + (row & 15) * 16);
      u32x4 v[2];
      if (SG == SG_W1 || SG == SG_W3) {
#pragma unroll
        for (int q = 0; q < 16; q += 2) {
          float g0 = gelu_tanh(acc[q]), g1 = gelu_tanh(acc[q + 1]);
          asm volatile("" : "+v"(g0), "+v"(g1));
          acc[q] = g0; acc[q + 1] = g1;
          piece(2 * i + 5, q >> 1, R >= i + 3 && i >= 2);
        }
      }
      widen32(acc, v);
      if (SG == SG_WO || SG == SG_W4) {
        LAS unsigned char* tw = lds + G_XCH + wl * 4096 + row * 64;
        *(LAS u32x4*)(tw + ((hf ^ (row >> 1)) & 3) * 16) = v[0]; *(LAS u32x4*)(tw + (((2 + hf) ^ (row >> 1)) & 3) * 16) = v[1];
        asm volatile("" ::: "memory");
        LAS unsigned char* tr = lds + G_XCH + wl * 4096 + (lane >> 2) * 64 + ((lane ^ (lane >> 3)) & 3) * 16;
        const unsigned so = (SG == SG_WO ? WS_P2 : WS_P8) + (unsigned)(b * 4 + kq) * 65536u, vo = (unsigned)((lane >> 2) * D_ + col0 + (lane & 3) * 8) * 2u;
        stx16(rs, vo, so, *(const LAS u32x4*)(tr)); stx16(rs, vo, so + 32768, *(const LAS u32x4*)(tr + 1024));
      } else if (PROJ) {
        const unsigned so = dst + (unsigned)(b * NH + cb) * 8192 + wl * 1024, vo = (unsigned)((row >> 4) * 4096 + hf * 256 + (row & 15) * 16);
        stx16(rs, vo, so, v[0]); stx16(rs, vo, so + 512, v[1]);
      } else if (SG == SG_W1) {
        const int grp = cb >> 1, sh = (grp & 1) ? -(1 + (grp >> 1)) : (1 + (grp >> 1));
        const int rho = 32 * b + row + sh + 2;
        const unsigned vo = (rho >= 2 && rho < S_ + 2) ? (unsigned)(rho >> 5) * 65536u + fo + (unsigned)((rho >> 4) & 1) * 1024u + (unsigned)(rho & 15) * 16u : 0xF0000000u;
        stx16(rs, vo, WS_H1S, v[0]); stx16(rs, vo, WS_H1S + 512, v[1]);
      } else if (SG == SG_W2) {
        if (!more) WAIT_V(0);
        res[0] = *(const LAS u32x4*)(lds + G_K + wl * 2048 + row * 64 + ((hf ^ (row >> 1)) & 3) * 16);
        res[1] = *(const LAS u32x4*)(lds + G_K + wl * 2048 + row * 64 + (((2 + hf) ^ (row >> 1)) & 3) * 16);
        float zs = 0.f, q2 = 0.f;
#pragma unroll
        for (int q = 0; q < 8; ++q) {
          const unsigned x = pack2(lo2f(res[q >> 2][q & 3]) + lo2f(v[q >> 2][q & 3]), hi2f(res[q >> 2][q & 3]) + hi2f(v[q >> 2][q & 3]));
          v[q >> 2][q & 3] = x; zs += lo2f(x) + hi2f(x); q2 = fmaf(lo2f(x), lo2f(x), fmaf(hi2f(x), hi2f(x), q2));
          asm volatile("" : "+v"(zs), "+v"(q2));
          piece(2 * i + 5, q, R >= i + 3 && i >= 2);
        }
        stx16(rs, fo + ro, WS_X2 + (unsigned)b * 65536u, v[0]); stx16(rs, fo + ro, WS_X2 + (unsigned)b * 65536u + 512, v[1]);
        const float mw = hsum(zs);
        q2 = hsum(q2);
        if (hf == 0) stx8(rs, (unsigned)((cb * 2 + (wl >> 1)) * 512 + row * 16 + (wl & 1) * 8), WS_ST5 + (unsigned)b * 8192, u32x2{f2u(mw), f2u(q2)});
      } else {
        stx16(rs, fo + ro, WS_U + (unsigned)b * 229376u, v[0]); stx16(rs, fo + ro, WS_U + (unsigned)b * 229376u + 512, v[1]);
      }
      if (PROJ && bstr == 3) {
#pragma unroll
        for (int q = 0; q < 2; ++q) {
          const int pc = (wk * 4 + wl) * 2 + q;
          if (pc < 672 && 3 * i + ZA + pc / 224 <= NSEG) stx16(rs, l16, X_REC + (unsigned)(3 * i + ZA) * RSTR + (unsigned)pc * 1024, u32x4{0u, 0u, 0u, 0u});
        }
      }
      SEG(3);
      pending = R >= i + 3 && i >= 2;
      if (!pending) { WAIT_V(0); announce(i); }
      if (R >= i + 3 && !(WEAVE && i >= 2)) fetch(2 * i + 5);
      if (!PROJ) WST(rs, wk, 16 + b);
      SEG(4);
    }
  }
  LBAR();
  if (!PROJ) {
    constexpr bool ROWS = (SG == SG_WO || SG == SG_W4 || SG == SG_W2);
    bool got = false;
    for (int i = inext; i < NBLK; ++i) {
      if (!got) { wait_ge(rs, pvo(i), 0, need(i), cap); SEG(6); fetch_own(i); }
      const unsigned pk = w == 0 ? pvo(i + 1) : 0xF0000000u;
      f32x16 acc;
      unsigned peek;
      u32x4 st;
      {
        const unsigned ub = lbase + (i & 1) * 65536 + kh * 32768;
        LAS unsigned char* ab[4];
        ab[0] = lad(aoff, ub);
        if (ROWM) { ab[1] = lad<32>(aoff, ub); ab[2] = lad<64>(aoff, ub); ab[3] = lad<96>(aoff, ub); }
        bf16x8 af[12];
        auto rd = [&](int k) { af[k % 12] = ROWM ? *(const LAS bf16x8*)(ab[k & 3] + (k >> 2) * 4096) : *(const LAS bf16x8*)(ab[0] + (k >> 1) * 2048 + (k & 1) * 512); };
#pragma unroll
        for (int k = -4; k < KS; ++k) {
          const int kr = k + 4;
          if (kr == 0) { wait_v<NPC - 1>(); BAR(); SEG(1); }
          if (kr == 4) { wait_v<NPC - 3>(); BAR(); }
          if (kr == 12) { wait_v<NPC - 5>(); BAR(); }
          if (kr == 20) {
            wait_v<0>(); BAR();
            asm volatile("buffer_load_dword %0, %1, %2, 0 offen sc1" : "=v"(peek) : "v"(pk), "s"(rs) : "memory");
            if (SG == SG_W2) {
              const int t = 32 * i - 2 + 16 * kh + (lane >> 2);
              dma16x(rs, (t >= 0 && t < S_) ? (unsigned)(t * D_ + col0 + ((lane ^ (lane >> 3)) & 3) * 8) * 2u : 0xF0000000u, WS_H2, lbase + G_K + w * 1024);
            }
            if (SG == SG_W3) asm volatile("buffer_load_dwordx4 %0, %1, %2, %3 offen sc1" : "=v"(st) : "v"(l16), "s"(rs), "s"(WS_ST5 + (unsigned)i * 8192 + w * 1024) : "memory");
          }
          if (kr < KS) rd(kr);
          if (k >= 0) acc = mfma3232(Wf[k], af[k % 12], k ? acc : zero16);
          if (k >= -1) SCHED();
        }
        asm volatile("" : "+v"(acc));
      }
      SEG(2);
      float y[8];
      if (ROWS) {
        u32x4 v[2];
        widen32(acc, v);
        LAS unsigned char* tw = lds + G_XCH + w * 2048 + row * 64;
        *(LAS u32x4*)(tw + ((hf ^ (row >> 1)) & 3) * 16) = v[0]; *(LAS u32x4*)(tw + (((2 + hf) ^ (row >> 1)) & 3) * 16) = v[1];
      } else {
        LAS unsigned char* xw = lad(l16, lbase + G_XCH + w * 2048);
#pragma unroll
        for (int q = 0; q < 2; ++q) {
          f32x4 give;
#pragma unroll
          for (int e = 0; e < 4; ++e) { give[e] = kh ? acc[q * 4 + e] : acc[8 + q * 4 + e]; y[4 * q + e] = kh ? acc[8 + q * 4 + e] : acc[q * 4 + e]; }
          *(LAS f32x4*)(xw + q * 1024) = give;
        }
      }
      WAIT_V(0);
      asm volatile("" : "+v"(peek));
      if (SG == SG_W3) {
        asm volatile("" : "+v"(st));
        const float s1 = hsum(u2f(st[0]) + u2f(st[2])), s2 = hsum(u2f(st[1]) + u2f(st[3]));
        if (hf == 0) *(LAS f32x2*)(lds + G_K + 1024 + w * 256 + row * 8) = f32x2{s1, s2};
      }
      if (w == 0) { const unsigned r = (__all(peek >= need(i + 1)) && i + 1 < NBLK) ? 1u : 0u; if (lane == 0) *(LAS unsigned*)(lds + G_CTL + 4) = r; }
      LBAR();
      const unsigned rdv = *(const LAS unsigned*)(lds + G_CTL + 4);
      if (SG == SG_WO || SG == SG_W4) {
        LAS unsigned char* tr = lds + G_XCH + wl * 2048 + kh * 1024 + (lane >> 2) * 64 + ((lane ^ (lane >> 3)) & 3) * 16;
        const u32x4 p0 = *(const LAS u32x4*)(tr), p1 = *(const LAS u32x4*)(tr + 8192);
        u32x4 v;
#pragma unroll
        for (int q = 0; q < 4; ++q) v[q] = pack2(lo2f(p0[q]) + lo2f(p1[q]), hi2f(p0[q]) + hi2f(p1[q]));
        stx16(rs, (unsigned)((16 * kh + (lane >> 2)) * D_ + col0 + (lane & 3) * 8) * 2u, (SG == SG_WO ? WS_P2 : WS_P8) + (unsigned)(i * 4 + kq) * 65536u, v);
      } else if (SG == SG_W2) {
        const int ro = (lane & 15) * 64 + (((lane >> 4) ^ (lane >> 1)) & 3) * 16;
        const u32x4 p0 = *(const LAS u32x4*)(lds + G_XCH + wl * 2048 + kh * 1024 + ro), p1 = *(const LAS u32x4*)(lds + G_XCH + 8192 + wl * 2048 + kh * 1024 + ro);
        const u32x4 res = *(const LAS u32x4*)(lds + G_K + w * 1024 + ro);
        u32x4 v;
        float s1 = 0.f, s2 = 0.f;
#pragma unroll
        for (int q = 0; q < 4; ++q) {
          v[q] = pack2(lo2f(res[q]) + (lo2f(p0[q]) + lo2f(p1[q])), hi2f(res[q]) + (hi2f(p0[q]) + hi2f(p1[q])));
          const float z0 = lo2f(v[q]), z1 = hi2f(v[q]);
          s1 += z0 + z1; s2 = fmaf(z0, z0, fmaf(z1, z1, s2));
        }
        stx16(rs, l16, WS_X2 + (unsigned)i * 65536u + (unsigned)((col0 >> 5) * 2048 + kh * 1024), v);
        s1 = gsum(s1); s2 = gsum(s2);
        if (lane < 16) stx8(rs, (unsigned)((cb * 2 + (wl >> 1)) * 512 + (16 * kh + lane) * 16 + (wl & 1) * 8), WS_ST5 + (unsigned)i * 8192, u32x2{f2u(s1), f2u(s2)});
      } else {
        LAS unsigned char* xr = lad(l16, lbase + G_XCH + (w ^ 4) * 2048);
        const f32x4 x0 = *(const LAS f32x4*)(xr), x1 = *(const LAS f32x4*)(xr + 1024);
        if (SG == SG_W3) {
          f32x2 t[4];
#pragma unroll
          for (int q = 0; q < 4; ++q) t[q] = *(const LAS f32x2*)(lds + G_K + 1024 + (hf * 4 + q) * 256 + row * 8);
          const f32x4 c0 = *(const LAS f32x4*)(lds + G_K + wl * 128 + hf * 64 + kh * 32), c1 = *(const LAS f32x4*)(lds + G_K + wl * 128 + hf * 64 + kh * 32 + 16);
          const float mean = hsum((t[0][0] + t[1][0]) + (t[2][0] + t[3][0])) * (1.0f / 1024.0f);
          const float var = fmaxf(fmaf(-mean, mean, hsum((t[0][1] + t[1][1]) + (t[2][1] + t[3][1])) * (1.0f / 1024.0f)), 0.f);
          const float m = rbf(mean), isd = __builtin_amdgcn_rcpf(rbf(__builtin_amdgcn_sqrtf(rbf(rbf(var) + 1e-5f))));
#pragma unroll
          for (int e = 0; e < 4; ++e) { y[e] = fmaf(-m, c0[e], y[e] + x0[e]) * isd; y[4 + e] = fmaf(-m, c1[e], y[4 + e] + x1[e]) * isd; }
        } else {
#pragma unroll
          for (int e = 0; e < 4; ++e) { y[e] += x0[e]; y[4 + e] += x1[e]; }
        }
#pragma unroll
        for (int e = 0; e < 8; ++e) y[e] = gelu_tanh(y[e]);
        const auto r0 = __builtin_amdgcn_permlane32_swap(pack2(y[0], y[1]), pack2(y[4], y[5]), false, false), r1 = __builtin_amdgcn_permlane32_swap(pack2(y[2], y[3]), pack2(y[6], y[7]), false, false);
        const u32x4 v = {r0[0], r1[0], r0[1], r1[1]};
        const unsigned fo = (unsigned)((col0 >> 5) * 2048 + kh * 512 + hf * 256);
        if (SG == SG_W1) {
          const int grp = cb >> 1, sh = (grp & 1) ? -(1 + (grp >> 1)) : (1 + (grp >> 1));
          const int rho = 32 * i + row + sh + 2;
          stx16(rs, (rho >= 2 && rho < S_ + 2) ? (unsigned)(rho >> 5) * 65536u + fo + (unsigned)((rho >> 4) & 1) * 1024u + (unsigned)(rho & 15) * 16u : 0xF0000000u, WS_H1S, v);
        } else stx16(rs, fo + (unsigned)((row >> 4) * 1024 + (row & 15) * 16), WS_U + (unsigned)i * 229376u, v);
      }
      SEG(3);
      got = __builtin_amdgcn_readfirstlane(rdv) != 0u;
      if (got) { fetch_own(i + 1); wait_v<NPC>(); } else wait_v<0>();
      LBAR();
      if (tid == 448) stx4(rs, 0, myword, (unsigned)i + 1u);
      WST(rs, wk, 16 + i);
      SEG(4);
    }
  }
}

__device__ __forceinline__ void wait32(__amdgpu_buffer_rsrc_t rs, int first, int mine, int lane, unsigned need, unsigned& cap) {
  wait_ge(rs, 0, F_ST + (unsigned)(first + mine) * 128u, need - 1u, cap);
  wait_ge(rs, (unsigned)(lane & 31) * 128u, F_ST + (unsigned)first * 128u, need, cap);
}
__device__ __forceinline__ void sum_partials(__amdgpu_buffer_rsrc_t rs, unsigned l16, unsigned so, float (&a)[16]) {
  u32x4 pp[4][2];
#pragma unroll
  for (int k = 0; k < 4; ++k) { pp[k][0] = ldx16(rs, l16, so + k * 65536u); pp[k][1] = ldx16(rs, l16, so + k * 65536u + 1024); }
#pragma unroll
  for (int i = 0; i < 8; ++i) {
    f32x2 t = {0.f, 0.f};
#pragma unroll
    for (int k = 0; k < 4; ++k) t += f32x2{lo2f(pp[k][i >> 2][i & 3]), hi2f(pp[k][i >> 2][i & 3])};
    a[2 * i] = t[0]; a[2 * i + 1] = t[1];
  }
}
__device__ __forceinline__ void put_row(__amdgpu_buffer_rsrc_t rs, unsigned l16, unsigned so, const float (&v)[16]) {
  stx16(rs, l16, so, u32x4{pack2(v[0], v[1]), pack2(v[2], v[3]), pack2(v[4], v[5]), pack2(v[6], v[7])});
  stx16(rs, l16, so + 1024, u32x4{pack2(v[8], v[9]), pack2(v[10], v[11]), pack2(v[12], v[13]), pack2(v[14], v[15])});
}
__device__ __forceinline__ void put_round(__amdgpu_buffer_rsrc_t rs, unsigned l16, unsigned so, float (&v)[16]) {
  u32x4 t[2];
#pragma unroll
  for (int i = 0; i < 8; ++i) { const unsigned x = pack2(v[2 * i], v[2 * i + 1]); t[i >> 2][i & 3] = x; v[2 * i] = lo2f(x); v[2 * i + 1] = hi2f(x); }
  stx16(rs, l16, so, t[0]); stx16(rs, l16, so + 1024, t[1]);
}
__device__ __forceinline__ void load_mod(__amdgpu_buffer_rsrc_t rs, int part, int lane, float (&v)[16], unsigned& cap) {
  for (;;) {
    asm volatile("" ::: "memory");
    u32x4 t[4];
#pragma unroll
    for (int i = 0; i < 4; ++i) t[i] = ldx16(rs, (unsigned)lane * 32, X_MOD + part * 4096 + (i >> 1) * 2048 + (i & 1) * 16);
    unsigned mn = 0xFFFFFFFFu;
#pragma unroll
    for (int i = 0; i < 16; ++i) { mn = min(mn, t[i >> 2][i & 3]); v[i] = hi2f(t[i >> 2][i & 3]); }
    if (__all(mn != 0u) || cap == 0u) break;
    cap = __builtin_amdgcn_readfirstlane(cap - 1u);
    __builtin_amdgcn_s_sleep(4);
  }
  cap = __builtin_amdgcn_readfirstlane(cap);
}
template <int NO>
__device__ __forceinline__ void mod_part(__amdgpu_buffer_rsrc_t rs, const u16* wmod, const u16* cv, LAS float* red, int col) {
  constexpr int KG = 512 / NO, NI = 1024 / KG;
  const int tid = threadIdx.x, kg = tid / NO, c8 = tid % NO;
  u32x4 wv[NI]; u16 cc[NI];
#pragma unroll
  for (int i = 0; i < NI; ++i) cc[i] = cv[kg + KG * i];
#pragma unroll
  for (int i = 0; i < NI; ++i) wv[i] = *(const u32x4*)(wmod + (size_t)(kg + KG * i) * 6144 + col + c8 * 8);
  SCHED();
  float acc[8] = {0.f, 0.f, 0.f, 0.f, 0.f, 0.f, 0.f, 0.f};
#pragma unroll
  for (int i = 0; i < NI; ++i) {
    const float v = bf2f(cc[i]), sc = rbf(v * __builtin_amdgcn_rcpf(1.0f + __builtin_amdgcn_exp2f(-1.4426950408889634f * v)));
#pragma unroll
    for (int j = 0; j < 4; ++j) { acc[2 * j] += sc * lo2f(wv[i][j]); acc[2 * j + 1] += sc * hi2f(wv[i][j]); }
  }
#pragma unroll
  for (int j = 0; j < 8; ++j) acc[j] = gsum(NO == 4 ? dpp_add<0x128>(dpp_add<0x124>(acc[j])) : acc[j]);
  if ((tid & 63) < NO) {
    LAS float* d = red + (tid >> 6) * 8 * NO + c8 * 8;
    *(LAS f32x4*)(d) = f32x4{acc[0], acc[1], acc[2], acc[3]}; *(LAS f32x4*)(d + 4) = f32x4{acc[4], acc[5], acc[6], acc[7]};
  }
  LBAR();
  if (tid < 8 * NO) {
    float u = 0.f;
#pragma unroll
    for (int i = 0; i < 8; ++i) u += red[i * 8 * NO + tid];
    stx4(rs, (unsigned)tid * 4, X_MOD + (unsigned)col * 4, f2u(rbf(u)) | 1u);
  }
}

__device__ __forceinline__ void stage_mid(__amdgpu_buffer_rsrc_t rs, const u16* x, int wk, int cu, unsigned& cap) {
  const int tid = threadIdx.x, lane = tid & 63, w = __builtin_amdgcn_readfirstlane(tid >> 6);
  const int id = w * 12 + cu, grp = id >> 5, rl = id & 31;
  const unsigned l16 = lane * 16;
  float ga[16], sc[16], sh[16];
  load_mod(rs, 2, lane, ga, cap); load_mod(rs, 4, lane, sc, cap); load_mod(rs, 3, lane, sh, cap);
#pragma unroll
  for (int i = 0; i < 16; ++i) sc[i] = rbf(1.0f + sc[i]);
  for (int seg = grp; seg < NSEG; seg += 3) {
    const int row = seg * 32 + rl;
    float v[16], a[16];
    load_row16(x + (size_t)row * D_, lane, v);
    wait32(rs, P_S2, rl, lane, (unsigned)seg + 1u, cap);
    sum_partials(rs, l16, WS_P2 + (unsigned)seg * 262144u + (unsigned)rl * 2048, a);
    float m, isd;
#pragma unroll
    for (int i = 0; i < 16; ++i) v[i] = fmaf(ga[i], a[i], v[i]);
    put_round(rs, l16, WS_X1 + (unsigned)row * 2048, v);
    ln_stats(v, m, isd);
#pragma unroll
    for (int i = 0; i < 16; ++i) v[i] = fmaf((v[i] - m) * isd, sc[i], sh[i]);
    put_round(rs, l16, WS_H2 + (unsigned)row * 2048, v);
    ln_stats(v, m, isd);
#pragma unroll
    for (int i = 0; i < 16; ++i) v[i] = (v[i] - m) * isd;
    put_row(rs, l16, WS_G + (unsigned)row * 2048, v);
    WAIT_V(0);
    if (lane == 0) stx4(rs, 0, F_ST + (unsigned)(P_S3 + grp * 32 + rl) * 128, (unsigned)seg + 1u);
    WST(rs, wk, 16 + seg);
  }
}

__device__ __forceinline__ void stage_out(__amdgpu_buffer_rsrc_t rs, u16* out, int wk, int cu, unsigned& cap) {
  const int tid = threadIdx.x, lane = tid & 63, w = __builtin_amdgcn_readfirstlane(tid >> 6);
  const int grp = w >> 2, rl = cu * 4 + (w & 3);
  const unsigned l16 = lane * 16;
  float gb[16];
  load_mod(rs, 5, lane, gb, cap);
  for (int b = grp; b < 65; b += 2) {
    const int t = 32 * b - 2 + rl;
    if (t < 0 || t >= S_) continue;
    wait_ge(rs, (unsigned)(lane & 7) * 128u, F_ST + P_S5 * 128u, (unsigned)b + 1u, cap);
    const unsigned xo = (unsigned)((lane >> 2) * 2048 + (lane & 3) * 256), xs = WS_X2 + (unsigned)b * 65536u + (unsigned)((rl >> 4) * 1024 + (rl & 15) * 16);
    const u32x4 x2a = ldx16(rs, xo, xs), x2b = ldx16(rs, xo, xs + 32768);
    const u32x4 x1a = ldx16(rs, l16, WS_X1 + (unsigned)t * 2048), x1b = ldx16(rs, l16, WS_X1 + (unsigned)t * 2048 + 1024);
    wait32(rs, P_S8, rl, lane, (unsigned)b + 1u, cap);
    float a[16];
    sum_partials(rs, l16, WS_P8 + (unsigned)b * 262144u + (unsigned)rl * 2048, a);
#pragma unroll
    for (int i = 0; i < 4; ++i) {
      a[2 * i] = fmaf(gb[2 * i], lo2f(x2a[i]) + a[2 * i], lo2f(x1a[i])); a[2 * i + 1] = fmaf(gb[2 * i + 1], hi2f(x2a[i]) + a[2 * i + 1], hi2f(x1a[i]));
      a[8 + 2 * i] = fmaf(gb[8 + 2 * i], lo2f(x2b[i]) + a[8 + 2 * i], lo2f(x1b[i])); a[9 + 2 * i] = fmaf(gb[9 + 2 * i], hi2f(x2b[i]) + a[9 + 2 * i], hi2f(x1b[i]));
    }
    store_row16(out + (size_t)t * D_, lane, a);
    WST(rs, wk, 16 + b);
  }
}

__device__ __forceinline__ void worker(const MainP& p, LAS unsigned char* lds, int xw, int j) {
  const int tid = threadIdx.x, lane = tid & 63, w = __builtin_amdgcn_readfirstlane(tid >> 6);
  const __amdgpu_buffer_rsrc_t rs = mk_rs(p.ws, WS_END);
  unsigned cap = SPIN_CAP;
  const int wk = xw * 32 + j;
  WST(rs, wk, 0);
  {
    const int c = xw < 3 ? j * 3 + xw : 90 + j * 2 + xw + 3, r0 = c * 8 + 2 * (w & 3);
    const bool two = r0 + 8 * NLN < S_;
    float v[4][16];
    if (c >= NLN - 64) mod_part<4>(rs, p.wmod, p.c, (LAS float*)(lds + G_XCH), (c - (NLN - 64)) * 32);
    WST(rs, wk, 5);
    if (w < 4) {
#pragma unroll
      for (int q = 0; q < 4; ++q) load_row16(p.x + (size_t)(r0 + (q & 1) + (two ? (q >> 1) * 8 * NLN : 0)) * D_, lane, v[q]);
    }
    if (w >= 4) {
      if (xw == 1 || xw == 2) {
        const __amdgpu_buffer_rsrc_t rsO = mk_rs(p.Wo, D_ * HD * 2);
#pragma unroll
        for (int q = 0; q < 32; ++q) dma16(rsO, (unsigned)lane * 16, (unsigned)((q * 64 + wk - 32) * 4 + (w & 3)) * 1024u, (unsigned)(size_t)lds + G_K + (w & 3) * 1024);
        wait_v<24>();
      }
      const __amdgpu_buffer_rsrc_t rsW = mk_rs(xw < 3 ? p.Wk : p.Wq, D_ * HD * 2);
      const int ub = lane >> 3;
      const unsigned vB = (unsigned)(((ub & 3) + 8 * (ub >> 2)) * HD + 16 * (((lane >> 1) & 3) ^ (ub >> 1)) + 8 * (lane & 1)) * 2u, lbase = (unsigned)(size_t)lds;
#pragma unroll
      for (int q = 0; q < 16; ++q) {
        const int wq = (w & 3) + 4 * (q >> 3);
        const unsigned so = (unsigned)((32 * (wq >> 2) + 16 * ((wq >> 1) & 1) + 4 * (wq & 1) + (((q >> 2) & 1) * 8 + (q & 3)) * 64) * HD + j * 128) * 2u;
        const unsigned d = lbase + ((q >> 2) & 1) * 65536 + (q & 3) * 16384 + wq * 1024;
        dma16n<(NTM & 16) != 0>(rsW, vB, so, d); dma16n<(NTM & 16) != 0>(rsW, vB, so + 128, d + 8192);
      }
    } else {
#pragma unroll
      for (int q = 0; q < 4; ++q) ln_row(v[q]);
      float sc[16], sh[16];
      load_mod(rs, 1, lane, sc, cap); load_mod(rs, 0, lane, sh, cap);
      WST(rs, wk, 6);
#pragma unroll
      for (int i = 0; i < 16; ++i) sc[i] = rbf(1.0f + sc[i]);
#pragma unroll
      for (int q = 0; q < 4; ++q) {
#pragma unroll
        for (int i = 0; i < 16; ++i) v[q][i] = rbf(rbf(v[q][i] * sc[i]) + sh[i]);
        if (q < 2 || two) put_row(rs, (unsigned)lane * 16, WS_H + (unsigned)(r0 + (q & 1) + (q >> 1) * 8 * NLN) * 2048, v[q]);
      }
      WAIT_V(0);
      if (lane == 0) stx4(rs, 0, F_LN + c * 128 + w * 4, 1u);
    }
    WST(rs, wk, 1);
  }
  {
    stage_gemm<SG_PJ>(lds, rs, xw < 3 ? p.Wk : p.Wq, wk, xw < 3 ? xw : xw == 3 ? QB0 : 0, j, P_PJ + min(xw, 3) * 32 + j + (xw == 4 ? 400 : 0), cap, xw < 3 ? WS_K : WS_Q, xw < 3 ? 3 : 1,
                      xw < 2 ? 20 : xw == 2 ? 24 : xw == 3 ? NSEG - QB0 : QB0);
    WST(rs, wk, 3);
  }
  typedef __attribute__((address_space(4))) const MainP* KArg;
  KArg kp = (KArg)__builtin_amdgcn_kernarg_segment_ptr();
  asm volatile("" : "+s"(kp));
  if (xw == 4) {
    auto sweep = [&](const u16* W, int kb) {
      const __amdgpu_buffer_rsrc_t rw = mk_rs(W, (unsigned)kb * 262144u);
      for (int q = 0; q < kb; ++q) dma16(rw, (unsigned)lane * 16, (unsigned)((q * 32 + j) * 8 + w) * 1024u, (unsigned)(size_t)lds + w * 1024);
    };
    sweep(kp->w1, 8); sweep(kp->w2, 8);
    mod_part<16>(rs, kp->wmod, kp->c, (LAS float*)(lds + G_XCH), 2048 + j * 128);
    sweep(kp->w3, 28);
  }
  if (xw == 0) stage_gemm<SG_WO>(lds, rs, kp->Wo, wk, j >> 3, j & 7, P_S2 + j, cap);
  else if (xw == 3) stage_gemm<SG_W4>(lds, rs, kp->w4, wk, j >> 3, j & 7, P_S8 + j, cap);
  else if (xw == 4) {
    unsigned chs = 0u;
    for (int sg = w >> 1; sg < NSEG; sg += 4) {
      attend(rs, sg, j, w & 1, lane, chs, cap);
      if ((sg >> 2) >= 3 && (sg >> 2) < 7) {
        const __amdgpu_buffer_rsrc_t rw = mk_rs(kp->w4, 28u * 262144u);
        for (int q = 0; q < 7; ++q) dma16(rw, (unsigned)lane * 16, (unsigned)((((sg >> 2) - 3) * 7 + q) * 256 + j * 8 + w) * 1024u, (unsigned)(size_t)lds + w * 1024);
      }
      WST(rs, wk, 16 + sg);
    }
  }
  else if (j >= 28) stage_out(rs, kp->out, wk, (xw - 1) * 4 + j - 28, cap);
  else if (xw == 2) stage_gemm<SG_W3>(lds, rs, kp->w3, wk, 0, j, P_S7 + j, cap);
  else if (j < 8) stage_gemm<SG_W1>(lds, rs, kp->w1, wk, 0, j, P_S4 + j, cap);
  else if (j < 16) stage_gemm<SG_W2>(lds, rs, kp->w2, wk, 0, j - 8, P_S5 + j - 8, cap);
  else stage_mid(rs, kp->x, wk, j - 16, cap);
  if (cap == 0u && lane == 0) stx4(rs, 0, X_CTL, 1u);
}

#define ST_DECL
#define ST0()
#define ST(i)
#define ST_FLUSH()
#define VSL 1
#define NL 2
constexpr int R_W = 0, R_MEM = 131072, R_QK = R_MEM + 16384, R_KT = R_QK + 8192, R_RED = R_MEM + 16384, R_LDS = 163840;
__device__ __forceinline__ void bar_lo(int w) { asm volatile("s_cmp_ge_u32 %0, 4\n\ts_cbranch_scc1 1f\n\ts_barrier\n\t1:" :: "s"(w) : "scc", "memory"); }
__device__ __forceinline__ void bar_hi(int w) { asm volatile("s_cmp_lt_u32 %0, 4\n\ts_cbranch_scc1 1f\n\ts_setprio 3\n\ts_barrier\n\t1:" :: "s"(w) : "scc", "memory"); }
#define ISSUE(R) asm volatile("buffer_load_dwordx4 v[" R "], %[vo], %[rs], %[so] offen sc1" : "=&{v[" R "]}"(a) : [vo] "v"(vo), [rs] "s"(rs), [so] "s"(so) : "memory")
template <int I> __device__ __forceinline__ void issue(__amdgpu_buffer_rsrc_t rs, unsigned vo, unsigned so, u32x4& a) {
  if (I == 0) ISSUE("240:243"); else if (I == 1) ISSUE("244:247"); else if (I == 2) ISSUE("248:251"); else ISSUE("252:255");
}
#define ARRIVE(R0, R1, R2, R3, CNT) \
  asm volatile("s_waitcnt vmcnt(" #CNT ")\n\t" \
               "1:\n\t" \
               "v_min3_u32 %[t], v" #R0 ", v" #R1 ", v" #R2 "\n\t" \
               "v_min_u32 %[t], %[t], v" #R3 "\n\t" \
               "v_cmp_eq_u32 vcc, 0, %[t]\n\t" \
               "s_cbranch_vccz 2f\n\t" \
               "s_mov_b64 exec, vcc\n\t" \
               "buffer_load_dwordx4 v[" #R0 ":" #R3 "], %[vo], %[rs], %[so] offen sc1\n\t" \
               "s_mov_b64 exec, -1\n\t" \
               "s_waitcnt vmcnt(0)\n\t" \
               "s_sub_u32 %[cap], %[cap], 1\n\t" \
               "s_cbranch_scc0 1b\n\t" \
               "s_mov_b32 %[cap], 0\n\t" \
               "2:" \
               : "+{v[" #R0 ":" #R3 "]}"(a), [t] "=&v"(t), [cap] "+s"(cap) : [vo] "v"(vo), [rs] "s"(rs), [so] "s"(so) : "vcc", "scc", "memory")
template <int I> __device__ __forceinline__ void arrive(__amdgpu_buffer_rsrc_t rs, unsigned vo, unsigned so, u32x4& a, unsigned& cap) {
  unsigned t;
  if (I == 0) ARRIVE(240, 241, 242, 243, 3);
  else if (I == 1) ARRIVE(244, 245, 246, 247, 2);
  else if (I == 2) ARRIVE(248, 249, 250, 251, 1);
  else ARRIVE(252, 253, 254, 255, 0);
}
__device__ __forceinline__ unsigned half_of(unsigned v) { unsigned r; asm volatile("v_lshrrev_b32 %0, 1, %1" : "=v"(r) : "v"(v)); return r; }
__device__ __forceinline__ void issue2(__amdgpu_buffer_rsrc_t rs, unsigned l8, unsigned so, int w, u32x4& a) {
  asm volatile("s_cmp_ge_u32 %[w], 4\n\ts_cbranch_scc1 1f\n\tv_lshrrev_b32 v255, 1, %[l8]\n\t"
               "buffer_load_dwordx2 v[252:253], v255, %[rs], %[so] offen sc1\n\tbuffer_load_dwordx2 v[254:255], v255, %[rs], %[so] offen offset:512 sc1\n\t1:"
               : "=&{v[252:255]}"(a) : [l8] "v"(l8), [rs] "s"(rs), [so] "s"(so), [w] "s"(w) : "scc", "memory");
}
__device__ __forceinline__ void arrive2(__amdgpu_buffer_rsrc_t rs, unsigned l8, unsigned so, u32x4& a, unsigned& cap) {
  unsigned t;
  asm volatile("s_waitcnt vmcnt(0)\n\t"
               "1:\n\t"
               "v_min3_u32 %[t], v252, v253, v254\n\t"
               "v_min_u32 %[t], %[t], v255\n\t"
               "v_cmp_eq_u32 vcc, 0, %[t]\n\t"
               "s_cbranch_vccz 2f\n\t"
               "v_lshrrev_b32 %[t], 1, %[l8]\n\t"
               "buffer_load_dwordx2 v[252:253], %[t], %[rs], %[so] offen sc1\n\t"
               "buffer_load_dwordx2 v[254:255], %[t], %[rs], %[so] offen offset:512 sc1\n\t"
               "s_waitcnt vmcnt(0)\n\t"
               "s_sub_u32 %[cap], %[cap], 1\n\t"
               "s_cbranch_scc0 1b\n\t"
               "s_mov_b32 %[cap], 0\n\t"
               "2:"
               : "+{v[252:255]}"(a), [t] "=&v"(t), [cap] "+s"(cap) : [l8] "v"(l8), [rs] "s"(rs), [so] "s"(so) : "vcc", "scc", "memory");
}
__device__ __forceinline__ unsigned ror8(unsigned v) { return __builtin_amdgcn_update_dpp(0u, v, 0x128, 0xF, 0xF, true); }
template <class F>
__device__ __forceinline__ void gather_mem(__amdgpu_buffer_rsrc_t rs, unsigned lbase, int s, int w, unsigned l16, unsigned& cap, F&& meanwhile) {
  const unsigned so = X_MEM + s * RSTR + w * 2048;
  u32x4 a0, a1;
  issue<2>(rs, l16, so, a0); issue<3>(rs, l16, so + 1024, a1);
  meanwhile();
  LAS unsigned char* d = lad(l16, lbase + R_MEM + w * 2048);
  arrive<2>(rs, l16, so, a0, cap);
  *(LAS u32x4*)(d) = a0;
  arrive<3>(rs, l16, so + 1024, a1, cap);
  *(LAS u32x4*)(d + 1024) = a1;
}
__device__ __forceinline__ unsigned mem_off(int lane) { return (unsigned)((lane & 1) * 512 + (((lane >> 1) ^ ((lane & 1) * 16)) * 8)); }
__device__ __forceinline__ bf16x8 mem_frag(LAS unsigned char* mp, int chunk) { return cat8(trread(mp + chunk * 1024), trread(mp + chunk * 1024 + 256)); }
__device__ __forceinline__ bf16x16 w_frag(LAS unsigned char* p) {
  return cat16(cat8(trread(p), trread(p + 2048)), cat8(trread(p + 4096), trread(p + 6144)));
}
__device__ __forceinline__ bf16x16 l_frag(LAS unsigned char* p, int i) { return cat16(*(const LAS bf16x8*)(p + i * 2048), *(const LAS bf16x8*)(p + i * 2048 + 1024)); }
__device__ __forceinline__ void make_resident(LAS unsigned char* wb, unsigned lo, unsigned l16) {
#pragma unroll
  for (int c = 0; c < 2; ++c) {
    bf16x16 f[4];
#pragma unroll
    for (int t = 0; t < 4; ++t) f[t] = w_frag(wb + c * 8192 + (lo ^ (t << 5)));
    WAIT_L0(); SCHED();
#pragma unroll
    for (int t = 0; t < 4; ++t) {
      *(LAS bf16x8*)(wb + (c * 4 + t) * 2048 + l16) = __builtin_shufflevector(f[t], f[t], 0, 1, 2, 3, 4, 5, 6, 7);
      *(LAS bf16x8*)(wb + (c * 4 + t) * 2048 + 1024 + l16) = __builtin_shufflevector(f[t], f[t], 8, 9, 10, 11, 12, 13, 14, 15);
    }
    WAIT_L0(); SCHED();
  }
}
__device__ __forceinline__ f32x4 fold(f32x4 a, f32x4 b) {
  f32x4 r;
#pragma unroll
  for (int i = 0; i < 4; ++i) {
    const auto t = __builtin_amdgcn_permlane32_swap(f2u(a[i]), f2u(b[i]), false, false);
    r[i] = u2f(t[0]) + u2f(t[1]);
  }
  return r;
}

struct Sched { unsigned tau, og, sa; int ea, eb; };
template <int CTRL> __device__ __forceinline__ int dpp_max(int v) { return max(v, (int)__builtin_amdgcn_update_dpp((unsigned)v, (unsigned)v, CTRL, 0xF, 0xF, false)); }
__device__ __forceinline__ int row_max(int v) { v = dpp_max<0xB1>(v); v = dpp_max<0x4E>(v); v = dpp_max<0x141>(v); return dpp_max<0x140>(v); }
__device__ __forceinline__ void times_issue(__amdgpu_buffer_rsrc_t rs, unsigned l16, int s, unsigned& xb, unsigned& xa) {
  asm volatile("v_lshlrev_b32 %1, 3, %2\n\tbuffer_load_dword %0, %1, %3, %4 offen sc1\n\tbuffer_load_dword %1, %1, %3, %5 offen sc1"
               : "=&v"(xb), "=&v"(xa) : "v"(l16), "s"(rs), "s"(X_TS + (unsigned)s * RSTR), "s"(X_TS + (unsigned)s * RSTR + 8192) : "memory");
}
__device__ __forceinline__ void retime(__amdgpu_buffer_rsrc_t rs, unsigned l16, int s, unsigned xb, unsigned xa, Sched& t, const MainP& p, unsigned& cap) {
  while (!__all(s > 0 ? min(xa, xb) != 0u : (xa != 0u || l16 < 512)) && cap != 0u) {
    times_issue(rs, l16, s, xb, xa);
    asm volatile("s_waitcnt vmcnt(0)" : "+v"(xb), "+v"(xa) :: "memory");
    cap = __builtin_amdgcn_readfirstlane(cap - 1u);
  }
  cap = __builtin_amdgcn_readfirstlane(cap);
  const unsigned base = __builtin_amdgcn_readlane(xa, 32);
  const int vb = row_max((int)(xb - base)), va = row_max((int)(xa - base));
  const unsigned sa = base + max(__builtin_amdgcn_readlane(va, 32), __builtin_amdgcn_readlane(va, 48));
  if (s > 1) {
    const int ta = (int)(base - t.sa) + max(__builtin_amdgcn_readlane(va, 0), __builtin_amdgcn_readlane(va, 16));
    const int tb = (int)(base - t.og) + max(max(__builtin_amdgcn_readlane(vb, 0), __builtin_amdgcn_readlane(vb, 16)), max(__builtin_amdgcn_readlane(vb, 32), __builtin_amdgcn_readlane(vb, 48)));
    if (ta + p.d3 <= (int)(t.og - t.sa)) t.eb = max(t.eb - p.d4, min(tb * 8, t.eb + p.d4));
    t.ea = max(t.ea - p.d4, min(ta * 8, t.ea + p.d4));
  }
  t.sa = sa;
  t.og = sa + (t.ea >> 3) + p.d1;
  t.tau = t.og + (t.eb >> 3) + p.d2;
}

__device__ __forceinline__ void role_a(const MainP& p, LAS unsigned char* lds, int h) {
  const int tid = threadIdx.x, lane = tid & 63, w = __builtin_amdgcn_readfirstlane(tid >> 6);
  const int cg = w & 3, kh = w >> 2;
  const __amdgpu_buffer_rsrc_t rs = mk_rs(p.ws, WS_END);
  const unsigned lbase = (unsigned)(size_t)lds;
  unsigned l16 = lane * 16;
  asm volatile("" : "+v"(l16));
  const int sidx = (lane & 8) ? 0xEEEE : 0x4444;
  bf16x16 W[24];
  unsigned qoff;
  {
    const int fr = lane & 15, g = lane >> 4, tu = 4 * (g & 1) + (fr >> 2), ub = lane >> 3;
    const unsigned lo = (g >> 1) * 1024 + tu * 128 + ((tu >> 1) << 5) + (fr & 3) * 8;
    qoff = R_QK + ((fr >> 1) & 1) * 512 + (g >> 1) * 256 + (fr & 1) * 128 + (8 * (g & 1) + (fr >> 2)) * 8;
    const __amdgpu_buffer_rsrc_t rw = mk_rs(cg < 2 ? p.Wq : p.Wk, D_ * HD * 2);
    const unsigned vo = (unsigned)(((ub & 3) + 8 * (ub >> 2)) * HD + 16 * (((lane >> 1) & 3) ^ (ub >> 1)) + 8 * (lane & 1)) * 2u;
    const unsigned so = (unsigned)(kh * 512 * HD + h * DH + (cg & 1) * 64) * 2u;
    auto stage = [&](int c) {
#pragma unroll
      for (int rho = 0; rho < 8; ++rho) dma16n<(NTM & 2) != 0>(rw, vo, so + (unsigned)((c * 64 + 32 * (rho >> 2) + 16 * (rho & 1) + 4 * ((rho >> 1) & 1)) * HD) * 2u, lbase + R_W + w * 16384 + (c & 1) * 8192 + rho * 1024);
    };
    stage(0); stage(1);
#pragma unroll
    for (int c = 0; c < 6; ++c) {
      WAIT_V(8);
#pragma unroll
      for (int t = 0; t < 4; ++t) W[4 * c + t] = w_frag(lds + R_W + w * 16384 + (c & 1) * 8192 + (lo ^ (t << 5)));
      WAIT_L0(); SCHED();
      stage(c + 2);
    }
    WAIT_V(0);
    make_resident(lds + R_W + w * 16384, lo, l16);
  }
  WST(rs, 160 + (h >> 1), 4);
  unsigned cap = SPIN_CAP;
  wait_ge(rs, 0, F_ST + (unsigned)(P_PJ + h) * 128, 1u, cap);
  dma16x(rs, l16, WS_K + h * 8192 + w * 1024, lbase + R_KT + w * 1024);
  Sched tt; tt.tau = rt32(); tt.og = tt.sa = tt.tau; tt.ea = 127 * 8; tt.eb = 108 * 8;
  const int cu_ = h; (void)cu_;
  ST_DECL
  for (int s = 0; s < NSEG; ++s) {
    LAS unsigned char* lp = lad(l16, lbase + R_W + w * 16384);
    bf16x16 L[NL];
#pragma unroll
    for (int i = 0; i < NL; ++i) L[i] = l_frag(lp, i);
    unsigned lz = l16;
    asm volatile("" : "+v"(lz));
    LAS unsigned char* mp = lad(mem_off((int)(lz >> 4)), lbase + R_MEM + kh * 8192);
    SCHED();
    if (w == 7) wait_until(tt.tau);
    BAR();
    ST0();
    gather_mem(rs, lbase, s, (w + h) & 7, l16, cap, []() {});
    ST(1);
    LBAR();
    asm volatile("s_cmp_lg_u32 %0, 3\n\ts_cbranch_scc1 1f\n\ts_memrealtime vcc\n\ts_waitcnt lgkmcnt(0)\n\ts_max_u32 vcc_lo, vcc_lo, 1\n\tv_mov_b32 v255, vcc_lo\n\t"
                 "s_mov_b64 exec, 1\n\tbuffer_store_dword v255, off, %1, %2 sc1\n\ts_mov_b64 exec, -1\n\t1:"
                 :: "s"(w), "s"(rs), "s"(X_TS + (unsigned)s * RSTR + (unsigned)(96 + h) * 128) : "vcc", "scc", "v255", "memory");
    ST(2);
    f32x4 c[4];
#pragma unroll
    for (int t = 0; t < 4; ++t) c[t] = f32x4{0.f, 0.f, 0.f, 0.f};
    bf16x8 al, af[2];
    u32x4 vt0, vt1;
    u32x4 vmv;
    const unsigned vso = X_VM + (unsigned)s * RSTR + h * 4096 + cg * 1024;
    al = mem_frag(mp, 6); af[0] = mem_frag(mp, 0); af[1] = mem_frag(mp, 1);
#pragma unroll
    for (int i = 0; i < 8; ++i) {
      c[i & 3] = smf(al, L[i % NL], c[i & 3], sidx);
      SCHED();
      if (i == 3) al = mem_frag(mp, 7);
      if (i + NL < 8) L[i % NL] = l_frag(lp, i + NL);
      if (i == 8 - NL) {
        const unsigned vo = WS_VT + (unsigned)(s * NH + h) * 8192 + cg * 2048;
        asm volatile("s_cmp_ge_u32 %5, 4\n\ts_cbranch_scc1 1f\n\t"
                     "buffer_load_dwordx4 %0, %2, %3, %4 offen sc1\n\tbuffer_load_dwordx4 %1, %2, %3, %4 offen offset:1024 sc1\n\t1:"
                     : "=&v"(vt0), "=&v"(vt1) : "v"(l16), "s"(rs), "s"(vo), "s"(w) : "scc", "memory");
      }
      SCHED();
#pragma unroll
      for (int r = 3 * i; r < 3 * i + 3; ++r) {
        c[(r + 1) & 3] = smf(af[(r >> 2) & 1], W[(r & ~3) + ((r + 1) & 3)], c[(r + 1) & 3], sidx);
        SCHED();
        if ((r & 3) == 3 && (r >> 2) + 2 < 6) af[(r >> 2) & 1] = mem_frag(mp, (r >> 2) + 2);
        SCHED();
      }
    }
    {
      LAS unsigned char* qw = lad(half_of(l16), lbase + R_QK + cg * 2048 + kh * 512);
      const f32x4 f0 = fold(c[0], c[1]);
      *(LAS u32x2*)(qw) = u32x2{pack2(f0[0], f0[1]), pack2(f0[2], f0[3])};
      const f32x4 f1 = fold(c[2], c[3]);
      *(LAS u32x2*)(qw + 1024) = u32x2{pack2(f1[0], f1[1]), pack2(f1[2], f1[3])};
    }
    ST(3);
    if (w < 4) {
      bf16x8 k1[4], k2[4];
      LAS unsigned char* kb = lad(l16, lbase + R_KT);
#pragma unroll
      for (int ks = 0; ks < 2; ++ks) { k1[ks] = *(const LAS bf16x8*)(kb + ks * 1024); k2[ks] = *(const LAS bf16x8*)(kb + 4096 + ks * 1024); }
      unsigned ov;
      {
        unsigned ot;
        asm volatile("v_and_b32 %0, 0x30, %2\n\tv_lshrrev_b32 %1, 2, %2\n\tv_and_b32 %1, 0xC0, %1\n\tv_or_b32 %0, %0, %1\n\t"
                     "v_and_b32 %1, 0x40, %2\n\tv_lshl_or_b32 %0, %1, 9, %0\n\tv_and_b32 %1, 0x80, %2\n\tv_lshl_or_b32 %0, %1, 24, %0"
                     : "=&v"(ov), "=&v"(ot) : "v"(l16));
      }
      if (VSL >= 0) { if (VSL > 0) __builtin_amdgcn_s_sleep(VSL > 0 ? VSL : 1); issue2(rs, l16, vso, w, vmv); }
      LBAR();
      ST(4);
      if (VSL < 0) issue2(rs, l16, vso, w, vmv);
      f32x4 t0 = {0.f, 0.f, 0.f, 0.f}, t1 = t0, t2 = t0;
      LAS unsigned char* qb = lad(qoff, lbase);
      bf16x8 qf[4], kf[4];
#pragma unroll
      for (int ks = 0; ks < 2; ++ks) {
        qf[ks] = cat8(trread(qb + ks * 1024), trread(qb + ks * 1024 + 32));
        kf[ks] = cat8(trread(qb + 4096 + ks * 1024), trread(qb + 4096 + ks * 1024 + 32));
      }
      SCHED();
#pragma unroll
      for (int ks = 0; ks < 4; ++ks) {
        t1 = mfma32(k1[ks], qf[ks], t1); t2 = mfma32(k2[ks], qf[ks], t2); t0 = mfma32(kf[ks], qf[ks], t0);
        SCHED();
        if (ks < 2) {
          qf[ks + 2] = cat8(trread(qb + (ks + 2) * 1024), trread(qb + (ks + 2) * 1024 + 32));
          k1[ks + 2] = *(const LAS bf16x8*)(kb + (ks + 2) * 1024); k2[ks + 2] = *(const LAS bf16x8*)(kb + 4096 + (ks + 2) * 1024);
          kf[ks + 2] = cat8(trread(qb + 4096 + (ks + 2) * 1024), trread(qb + 4096 + (ks + 2) * 1024 + 32));
        }
        SCHED();
      }
      ST(5);
      asm volatile("s_waitcnt vmcnt(2)" : "+v"(vt0), "+v"(vt1) :: "memory");
      {
        const float sl = 0.08838834764831845f * 1.4426950408889634f;
        float z[4] = {t1[0], t1[1], t1[2], t1[3]}, y = t0[0], mx;
        {
          float u1 = t0[1], u2 = t0[2], u3 = t0[3];
#define M8(Z, B) "v_add_f32_dpp " Z ", " Z ", " Z " row_ror:8 row_mask:0xf bank_mask:0x3\n\tv_add_f32_dpp " Z ", " B ", " B " row_ror:8 row_mask:0xf bank_mask:0xc\n\t"
          asm volatile("s_nop 7\n\t"
              "v_permlane32_swap_b32 %4, %6\n\tv_permlane32_swap_b32 %5, %7\n\t"
              M8("%0", "%9") "v_add_f32 %4, %4, %6\n\tv_add_f32 %5, %5, %7\n\t" M8("%1", "%10") M8("%2", "%11") M8("%4", "%5") M8("%3", "%12")
              "v_max3_f32 %8, %0, %1, %2\n\tv_max3_f32 %8, %8, %4, %3\n\ts_nop 1\n\t"
              "v_max_f32_dpp %8, %8, %8 row_ror:8 row_mask:0xf bank_mask:0xf\n\tv_mov_b32 %5, %8\n\ts_nop 1\n\t"
              "v_permlane16_swap_b32 %8, %5\n\tv_max_f32 %8, %8, %5\n\tv_mov_b32 %5, %8\n\ts_nop 1\n\t"
              "v_permlane32_swap_b32 %8, %5\n\tv_max_f32 %8, %8, %5"
              : "+v"(z[0]), "+v"(z[1]), "+v"(z[2]), "+v"(z[3]), "+v"(y), "+v"(u1), "+v"(u2), "+v"(u3), "=&v"(mx) : "v"(t2[0]), "v"(t2[1]), "v"(t2[2]), "v"(t2[3]));
#undef M8
        }
        const float nm = -mx * sl;
        y = __builtin_amdgcn_exp2f(fmaf(y, sl, nm));
#pragma unroll
        for (int r = 0; r < 4; ++r) z[r] = __builtin_amdgcn_exp2f(fmaf(z[r], sl, nm));
        const unsigned pm1 = pack2(y, u2f(ror8(f2u(y))));
        const auto pms = __builtin_amdgcn_permlane32_swap(pm1, pm1, false, false);
        const u32x2 pm = {pms[0], pms[1]};
        const unsigned p0 = pack2(z[0], z[1]), p1 = pack2(z[2], z[3]);
        const u32x4 pt = {p0, p1, ror8(p0), ror8(p1)};
        f32x4 o0 = mfma32(__builtin_bit_cast(bf16x8, vt0), __builtin_bit_cast(bf16x8, pt), f32x4{0.f, 0.f, 0.f, 0.f});
        f32x4 o1 = mfma32(__builtin_bit_cast(bf16x8, vt1), __builtin_bit_cast(bf16x8, pt), f32x4{0.f, 0.f, 0.f, 0.f});
        float inv = __builtin_amdgcn_rcpf(gsum(dpp_add<0x128>(((z[0] + z[1]) + (z[2] + z[3])) + y)));
        asm volatile("" : "+v"(inv));
        arrive2(rs, l16, vso, vmv, cap);
        const f32x4 n0 = mfma16n(u32x2{vmv[0], vmv[1]}, pm, f32x4{0.f, 0.f, 0.f, 0.f}), n1 = mfma16n(u32x2{vmv[2], vmv[3]}, pm, f32x4{0.f, 0.f, 0.f, 0.f});
        SCHED();
        o0 *= inv; o1 *= inv;
        {
          const u32x4 v = {nz(pack2(fmaf(n0[0], inv, o0[0]), fmaf(n0[1], inv, o0[1]))), nz(pack2(fmaf(n0[2], inv, o0[2]), fmaf(n0[3], inv, o0[3]))),
                           nz(pack2(fmaf(n1[0], inv, o1[0]), fmaf(n1[1], inv, o1[1]))), nz(pack2(fmaf(n1[2], inv, o1[2]), fmaf(n1[3], inv, o1[3])))};
          stx16(rs, ov, X_OX + s * RSTR + h * 1024 + w * 256, v);
        }
        if (w == 3) { const unsigned t = max(rt32(), 1u); if (l16 == 0) stx4(rs, 0, X_TS + (unsigned)(s + 1) * RSTR + (unsigned)(64 + h) * 128, t); }
        ST(8);
      }
    } else {
      LBAR();
      LAS unsigned char* qb = lad(qoff, lbase + 4096 + (w - 4) * 1024);
      u32x4 kf = __builtin_bit_cast(u32x4, cat8(trread(qb), trread(qb + 32)));
#pragma unroll
      for (int i = 0; i < 4; ++i) kf[i] = pack2(dpp_add<0x128>(lo2f(kf[i])), dpp_add<0x128>(hi2f(kf[i])));
      stx16(rs, l16, WS_KM + (unsigned)(s * NH + h) * 4096 + (w - 4) * 1024, kf);
      WAIT_V(0);
    }
    LBAR();
    if (w == 4 && l16 == 0) stx4(rs, 0, F_CH + h * 128, (unsigned)s + 1u);
    ST(9);
    if (w == 7) {
      unsigned xb, xa;
      times_issue(rs, l16, s, xb, xa);
      asm volatile("s_waitcnt vmcnt(0)" : "+v"(xb), "+v"(xa) :: "memory");
      retime(rs, l16, s, xb, xa, tt, p, cap);
    }
    if (s + 1 < NSEG) {
      unsigned fo;
      asm volatile("v_mov_b32 %0, 0" : "=v"(fo));
      wait_ge(rs, fo, F_ST + (unsigned)(P_PJ + k_owner(s + 1) * 32 + h) * 128, (unsigned)s + 2u, cap);
      dma16x(rs, l16, WS_K + (unsigned)((s + 1) * NH + h) * 8192 + w * 1024, lbase + R_KT + w * 1024);
    }
  }
  ST_FLUSH();
  if (cap == 0u && l16 == 0) stx4(rs, 0, X_CTL, 1u);
}

__device__ __forceinline__ void role_vb(const MainP& p, LAS unsigned char* lds, int rg, int cb) {
  const int tid = threadIdx.x, lane = tid & 63, w = __builtin_amdgcn_readfirstlane(tid >> 6);
  const int kh = w >> 2, wr = (w + cb) & 7;
  const __amdgpu_buffer_rsrc_t rs = mk_rs(p.ws, WS_END);
  const unsigned lbase = (unsigned)(size_t)lds;
  unsigned l16 = lane * 16, moff = mem_off(lane), roff, soff = (unsigned)((lane >> 5) * 256 + (lane & 31) * 4);
  const int sidx = (lane & 8) ? 0xEEEE : 0x4444;
  bf16x16 WV[8], WB[16];
  {
    const int fr = lane & 15, g = lane >> 4, tu = 4 * (g & 1) + (fr >> 2), ub = lane >> 3;
    { const int sl = mem_slot(16 * (g >> 1) + fr); roff = R_RED + w * 512 + ((sl >> 5) * 32 + (sl & 15) * 2 + (g & 1)) * 8; }
#pragma unroll
    for (int q = 0; q < 4; ++q) {
      const unsigned pc = 16u + (unsigned)(q * 512 + (rg * 32 + cb) * 8 + w);
      if (pc < ZA * 224u) stx16(rs, l16, X_REC + pc * 1024u, u32x4{0u, 0u, 0u, 0u});
    }
    const __amdgpu_buffer_rsrc_t rv = mk_rs(p.Wv, D_ * HD * 2), ro = mk_rs(p.Wo, D_ * HD * 2);
    const unsigned vv = (unsigned)(((ub & 3) + 8 * (ub >> 2)) * HD + 16 * (((lane >> 1) & 3) ^ (ub >> 1)) + 8 * (lane & 1)) * 2u;
    const unsigned sv = (unsigned)((32 * (w >> 2) + 16 * (w & 1) + 4 * ((w >> 1) & 1)) * HD + cb * DH + rg * 64) * 2u;
#pragma unroll
    for (int c = 0; c < 16; ++c) dma16n<(NTM & 1) != 0>(rv, vv, sv + (unsigned)c * 64 * HD * 2, lbase + R_W + c * 8192 + w * 1024);
    WAIT_V(0); BAR();
    const unsigned lo = (g >> 1) * 1024 + tu * 128 + (((tu >> 1) ^ (w & 3)) << 5) + (fr & 3) * 8;
#pragma unroll
    for (int c = 0; c < 8; ++c) WV[c] = w_frag(lds + R_W + (kh * 8 + c) * 8192 + lo);
    LBAR();
    const int u = (lane >> 2) & 7;
    const unsigned vw = (unsigned)((16 * (u >> 2) + 8 * ((u >> 1) & 1) + 4 * (lane >> 5) + (u & 1)) * D_ + (lane & 3) * 8) * 2u;
    const unsigned sw = (unsigned)(wr * 512 * D_ + cb * 32) * 2u;
    const unsigned bo = (g >> 1) * 1024 + tu * 64 + (fr & 3) * 8;
    auto stageo = [&](int hd) {
#pragma unroll
      for (int q = 0; q < 8; ++q)
        dma16n<(NTM & 4) != 0>(ro, vw, sw + (unsigned)((hd * 128 + 64 * (q >> 2) + 32 * (q & 1) + 2 * ((q >> 1) & 1)) * D_) * 2u, lbase + R_W + w * 16384 + (hd & 1) * 8192 + q * 1024);
    };
    stageo(0); stageo(1);
#pragma unroll
    for (int hd = 0; hd < 4; ++hd) {
      if (hd < 3) wait_v<8>(); else wait_v<0>();
#pragma unroll
      for (int f = 0; f < 4; ++f) WB[hd * 4 + f] = w_frag(lds + R_W + w * 16384 + (hd & 1) * 8192 + (f >> 1) * 512 + (f & 1) * 32 + bo);
      WAIT_L0(); SCHED();
      if (hd < 2) stageo(hd + 2);
    }
  }
  WST(rs, 176 + rg * 8 + (cb >> 2), 4);
  unsigned hsrc = (unsigned)((lane >> 3) * 2048 + (((lane & 7) ^ (lane >> 3)) << 4)), hoff = (unsigned)((lane & 15) * 128 + (((lane >> 4) ^ (lane & 7)) << 4));
  asm volatile("" : "+v"(l16), "+v"(moff), "+v"(roff), "+v"(soff), "+v"(hsrc), "+v"(hoff));
  unsigned cap = SPIN_CAP;
  auto h_piece = [&](int b, int q) { dma16x(rs, hsrc, WS_H + (unsigned)b * 65536u + (w & 3) * 16384 + (2 * q + kh) * 128, lbase + R_W + (b & 1) * 65536 + (q * 8 + w) * 1024); };
  auto h_fetch = [&](int b) {
#pragma unroll
    for (int q = 0; q < 8; ++q) h_piece(b, q);
  };
  f32x4 x0, x1;
  auto v_part = [&](int b, auto part, auto&& each) {
    constexpr int K0 = decltype(part)::value * 8;
    LAS unsigned char* a0 = lad(hoff, lbase + R_W + (b & 1) * 65536 + kh * 32768);
    LAS unsigned char* a1 = lad<64>(hoff, lbase + R_W + (b & 1) * 65536 + kh * 32768);
    bf16x8 af[3][2];
    auto rd = [&](int k) { LAS unsigned char* q = ((k & 1) ? a1 : a0) + (k >> 1) * 4096; af[k % 3][0] = *(const LAS bf16x8*)(q); af[k % 3][1] = *(const LAS bf16x8*)(q + 2048); };
    rd(K0); rd(K0 + 1);
    SCHED();
#pragma unroll
    for (int k = K0; k < K0 + 8; ++k) {
      if (k + 2 < K0 + 8) rd(k + 2);
      const bf16x8 wf = (k & 1) ? __builtin_shufflevector(WV[k >> 1], WV[k >> 1], 8, 9, 10, 11, 12, 13, 14, 15) : __builtin_shufflevector(WV[k >> 1], WV[k >> 1], 0, 1, 2, 3, 4, 5, 6, 7);
      x0 = mfma32(af[k % 3][0], wf, k ? x0 : f32x4{0.f, 0.f, 0.f, 0.f}); x1 = mfma32(af[k % 3][1], wf, k ? x1 : f32x4{0.f, 0.f, 0.f, 0.f});
      asm volatile("" : "+v"(x0), "+v"(x1));
      SCHED();
      each(k);
      SCHED();
    }
    if (K0) {
      LAS unsigned char* xv = lad(l16, lbase + R_RED + 4096 + (w & 3) * 2048);
      asm volatile("s_nop 7\n\ts_cmp_lt_u32 %3, 4\n\ts_cbranch_scc1 1f\n\tds_write_b128 %0, %1\n\tds_write_b128 %0, %2 offset:1024\n\t1:" :: "v"(xv), "v"(x0), "v"(x1), "s"(w) : "scc", "memory");
    }
  };
  auto v_out = [&](int b) {
    if (!kh && b < NSEG) {
      LAS unsigned char* xv = lad(l16, lbase + R_RED + 4096 + (w & 3) * 2048);
      x0 += *(const LAS f32x4*)(xv); x1 += *(const LAS f32x4*)(xv + 1024);
      stx16(rs, l16, WS_VT + (unsigned)(b * NH + cb) * 8192 + (rg * 4 + w) * 1024, u32x4{pack2(x0[0], x0[1]), pack2(x0[2], x0[3]), pack2(x1[0], x1[1]), pack2(x1[2], x1[3])});
    }
  };
  typedef std::integral_constant<int, 0> P0; typedef std::integral_constant<int, 1> P1;
  BAR();
  wait_ge(rs, (l16 & 0xF0) * 8 + (l16 >> 8) * 4, F_LN, 1u, cap);
  h_fetch(0); h_fetch(1);
  WAIT_V(8); BAR();
  v_part(0, P0{}, [](int) {}); v_part(0, P1{}, [](int) {});
  WAIT_V(0); LBAR();
  v_out(0); h_fetch(2);
  LBAR();
  v_part(1, P0{}, [](int) {}); v_part(1, P1{}, [](int) {});
  WAIT_V(0); LBAR();
  v_out(1); h_fetch(3);
  v_part(2, P0{}, [](int) {});
  wait_ge(rs, (l16 & 0xF0) * 8 + (l16 >> 8) * 4, F_LN + (unsigned)(1 + w) * 2048, 1u, cap);
  if (w == 0) wait_ge(rs, (l16 & 0xF0) * 8 + (l16 >> 8) * 4, F_LN + 9 * 2048, 1u, cap);
  WAIT_V(0); BAR();
  if (w == 0) {
    const int ln = l16 >> 4, sl = (ln >> 1) & 15, col = cb * 32 + (sl >> 3) * 16 + (ln >> 5) * 8 + (sl & 4) + (sl & 1) * 2 + ((sl >> 1) & 1), r = rg * 4 + (ln & 1) * 2;
    stx4(rs, soff, X_MEM + (cb >> 1) * 1024 + rg * 512 + ((cb ^ rg) & 1) * 128, nz((unsigned)p.mem0[r * D_ + col] | ((unsigned)p.mem0[(r + 1) * D_ + col] << 16)));
  }
  Sched tt; tt.tau = rt32(); tt.og = tt.sa = tt.tau; tt.ea = 127 * 8; tt.eb = 108 * 8;
  const int cu_ = 32 + (rg * 32 + cb) / 2; (void)cu_;
  ST_DECL
  for (int s = 0; s < NSEG; ++s) {
    if (w == 7) wait_until(tt.tau);
    BAR();
    ST0();
    gather_mem(rs, lbase, s, wr, l16, cap, [&]() { v_part(s + 2, P1{}, [](int) {}); });
    ST(1);
    LBAR();
    ST(2);
    {
      f32x4 c0 = {0.f, 0.f, 0.f, 0.f};
      LAS unsigned char* mp = lad(moff, lbase + R_MEM + kh * 8192);
      bf16x8 af[6];
#pragma unroll
      for (int c = 0; c < 6; ++c) af[c] = mem_frag(mp, c);
      SCHED();
#pragma unroll
      for (int c = 0; c < 8; ++c) { c0 = smf(af[c % 6], WV[c], c0, sidx); SCHED(); if (c < 2) { af[c] = mem_frag(mp, 6 + c); SCHED(); } }
      const f32x4 f = fold(c0, c0);
      if (l16 < 512) stx8(rs, half_of(l16), X_VM + (unsigned)s * RSTR + cb * 4096 + (rg * 4 + (w & 3)) * 512 + kh * 256, u32x2{nz(pack2(f[0], f[1])), nz(pack2(f[2], f[3]))});
    }
    ST(3);
    {
      unsigned xb, xa;
      times_issue(rs, w == 7 ? l16 : 0x1E000000u, s, xb, xa);
      h_fetch(min(s + 4, NSEG - 2 + (s & 1)));
      v_out(s + 2);
      asm volatile("s_waitcnt vmcnt(8)" : "+v"(xb), "+v"(xa) :: "memory");
      if (w == 7) retime(rs, l16, s, xb, xa, tt, p, cap);
    }
    if (s + 1 < NSEG) {
      u32x4 a[4];
      const unsigned so = X_OX + s * RSTR + rg * 32768 + wr * 4096;
      if (w == 7) wait_until(tt.og);
      BAR();
      ST(4);
      issue<0>(rs, l16, so, a[0]);
      v_part(s + 3, P0{}, [&](int k) {
        if (k == 0) issue<1>(rs, l16, so + 1024, a[1]);
        if (k == 2) issue<2>(rs, l16, so + 2048, a[2]);
        if (k == 4) issue<3>(rs, l16, so + 3072, a[3]);
      });
      f32x4 c[4];
#pragma unroll
      for (int t = 0; t < 4; ++t) c[t] = f32x4{0.f, 0.f, 0.f, 0.f};
#pragma unroll
      for (int i = 0; i < 4; ++i) {
        if (i == 0) arrive<0>(rs, l16, so, a[0], cap); else if (i == 1) arrive<1>(rs, l16, so + 1024, a[1], cap);
        else if (i == 2) arrive<2>(rs, l16, so + 2048, a[2], cap); else arrive<3>(rs, l16, so + 3072, a[3], cap);
#pragma unroll
        for (int t = 0; t < 4; ++t) c[t] = smf(__builtin_bit_cast(bf16x8, a[i]), WB[i * 4 + t], c[t], sidx);
        SCHED();
      }
      ST(5);
      {
        const bool odd = (l16 & 256) != 0;
        const f32x4 f = fold(odd ? c[2] : c[0], odd ? c[3] : c[1]);
        const auto e0 = __builtin_amdgcn_permlane16_swap(f2u(f[0]), f2u(f[2]), false, false), e1 = __builtin_amdgcn_permlane16_swap(f2u(f[1]), f2u(f[3]), false, false);
        *(LAS f32x2*)(lad(roff, lbase)) = f32x2{u2f(e0[0]) + u2f(e0[1]), u2f(e1[0]) + u2f(e1[1])};
      }
      ST(6);
      LBAR();
      ST(7);
      if (w == 0) {
        LAS unsigned char* rp = lad(half_of(l16), lbase + R_RED);
        f32x2 v[8];
#pragma unroll
        for (int i = 0; i < 8; ++i) v[i] = *(const LAS f32x2*)(rp + i * 512);
        const f32x2 sum = ((v[0] + v[1]) + (v[2] + v[3])) + ((v[4] + v[5]) + (v[6] + v[7]));
        stx4(rs, soff, X_MEM + (s + 1) * RSTR + (cb >> 1) * 1024 + rg * 512 + ((cb ^ rg) & 1) * 128, nz(pack2(sum[0], sum[1])));
        const unsigned t = max(rt32(), 1u);
        if (l16 == 0) stx4(rs, 0, X_TS + (unsigned)(s + 1) * RSTR + (unsigned)(rg * 32 + cb) * 128, t);
      }
      ST(8);
    }
  }
  ST_FLUSH();
  if (cap == 0u && l16 == 0) stx4(rs, 0, X_CTL, 1u);
}

#if defined(__HIP_DEVICE_COMPILE__)
__attribute__((target("no-packed-fp32-ops")))
#endif
__global__ void __launch_bounds__(512, 2) k_main(MainP p) {
  extern __shared__ __attribute__((aligned(16))) unsigned char smem[];
  const int b = blockIdx.x, x = b & 7, j = b >> 3;
  if (x >= 3) worker(p, (LAS unsigned char*)smem, x - 3, j);
  else if (x == 0) role_a(p, (LAS unsigned char*)smem, j);
  else role_vb(p, (LAS unsigned char*)smem, x - 1, j);
}

extern "C" void kernel_launch(void* const* d_in, const int* in_sizes, int n_in, void* d_out, int out_size, void* d_ws, size_t ws_size,
                              hipStream_t stream) {
  unsigned char* ws = (unsigned char*)d_ws;
  (void)hipMemsetAsync(ws, 0, X_ZERO, stream);
  MainP p;
  p.x = (const u16*)d_in[0]; p.mem0 = (const u16*)d_in[1]; p.c = (const u16*)d_in[12]; p.wmod = (const u16*)d_in[13]; p.Wq = (const u16*)d_in[2]; p.Wk = (const u16*)d_in[3]; p.Wv = (const u16*)d_in[4]; p.Wo = (const u16*)d_in[5];
  p.w1 = (const u16*)d_in[8]; p.w2 = (const u16*)d_in[9]; p.w3 = (const u16*)d_in[10]; p.w4 = (const u16*)d_in[11];
  p.out = (u16*)d_out; p.ws = ws; p.d1 = 6; p.d2 = 8; p.d3 = 2; p.d4 = 12;
  (void)hipFuncSetAttribute((const void*)k_main, hipFuncAttributeMaxDynamicSharedMemorySize, R_LDS);
  k_main<<<256, 512, R_LDS, stream>>>(p);
}
```
